```python
import torch

def _softmax(x, axis=-1):
    return torch.softmax(x, dim=axis)

def _gelu(x):
    return torch.nn.functional.gelu(x, approximate='tanh')

def _silu(x):
    return torch.nn.functional.silu(x)

def _sigmoid(x):
    return torch.sigmoid(x)

def _layernorm(x, axis=-1, eps=1e-05):
    m = x.mean(dim=axis, keepdim=True)
    v = x.var(dim=axis, keepdim=True, unbiased=False)
    return (x - m) / torch.sqrt(v + eps)

def _scatter_add(tgt, idx, src, axis=0):
    return tgt.clone().index_add_(axis, idx, src)

def _segment_sum(src, seg, n):
    out = torch.zeros((n,) + src.shape[1:], dtype=src.dtype)
    return out.index_add_(0, seg, src)

def _pad(x, pw):
    flat = tuple((v for pair in reversed(pw) for v in pair))
    return torch.nn.functional.pad(x, flat)
kernel_name = 'recurrent_scan__softmax_full_mha__mixer_gmlp__prompt'

def setup_inputs():

    _w = lambda a: (lambda t: t.to(torch.bfloat16) if t.is_floating_point() else t)(torch.as_tensor(a))

    def _A_setup():
        B, S, D = (1, 2048, 512)
        return {'x': _w((lambda *s: torch.randn(*s, dtype=torch.bfloat16) * 0.05)(B, S, D)), 'wqkv': _w((lambda *s: torch.randn(*s, dtype=torch.bfloat16) * 0.05)(D, 3 * D)), 'wo': _w((lambda *s: torch.randn(*s, dtype=torch.bfloat16) * 0.05)(D, D))}
    _A_in = _A_setup()

    def _B_setup():
        B, S, D, F = (1, 2048, 512, 1792)
        Fh = F // 2
        return {'x': _w((lambda *s: torch.randn(*s, dtype=torch.bfloat16) * 0.05)(B, S, D)), 'w_in': _w((lambda *s: torch.randn(*s, dtype=torch.bfloat16) * 0.8)(D, 2 * Fh)), 'w_sp': _w(0.01 * (lambda *s: torch.randn(*s, dtype=torch.bfloat16) * 0.8)(S, S)), 'b_sp': _w(1.0 + 0.01 * (lambda *s: torch.randn(*s, dtype=torch.bfloat16) * 0.05)(S)), 'w_out': _w((lambda *s: torch.randn(*s, dtype=torch.bfloat16) * 0.05)(Fh, D))}
    _B_in = _B_setup()
    return {'A_x': _A_in['x'], 'A_wqkv': _A_in['wqkv'], 'A_wo': _A_in['wo'], 'B_x': _B_in['x'], 'B_w_in': _B_in['w_in'], 'B_w_sp': _B_in['w_sp'], 'B_b_sp': _B_in['b_sp'], 'B_w_out': _B_in['w_out']}

def reference(A_x, A_wqkv, A_wo, B_x, B_w_in, B_w_sp, B_b_sp, B_w_out):

    def _A_ref(x, wqkv, wo):
        B, S, D = x.shape
        H = 32
        Dh = D // H
        qkv = (lambda x, *d: x.permute(*d))((x @ wqkv).reshape(B, S, 3, H, Dh), 2, 0, 3, 1, 4)
        q, k, v = (qkv[0], qkv[1], qkv[2])
        scale = Dh ** (-0.5)
        scores = torch.einsum('bhqd,bhkd->bhqk', q, k) * scale
        p = _softmax(scores, axis=-1)
        o = torch.einsum('bhqk,bhkd->bhqd', p, v)
        return (lambda x, *d: x.permute(*d))(o, 0, 2, 1, 3).reshape(B, S, D) @ wo

    def _B_ref(x, w_in, w_sp, b_sp, w_out):
        B, S, D = x.shape
        F = w_in.shape[-1]
        Fh = F // 2
        z = _gelu(torch.einsum('bsd,df->bsf', x, w_in))
        z1 = z[..., :Fh].clone()
        z2 = _layernorm(z[..., Fh:], axis=-1)
        g = torch.einsum('ts,bsf->btf', w_sp, z2) + b_sp[None, :, None]
        return torch.einsum('bsf,fd->bsd', z1 * g, w_out)
    x = A_x
    xn = _layernorm(x, axis=-1)
    s = _A_ref(xn, A_wqkv, A_wo)
    g = _silu(_B_ref(xn, B_w_in, B_w_sp, B_b_sp, B_w_out))
    return x + s * g
```

```cpp
#include <hip/hip_runtime.h>
#include <hip/hip_bf16.h>
#include <stdint.h>

typedef short bf16x8 __attribute__((ext_vector_type(8)));
typedef short s16x4 __attribute__((ext_vector_type(4)));
typedef __bf16 bf16x2_t __attribute__((ext_vector_type(2)));
typedef float f32x16 __attribute__((ext_vector_type(16)));
typedef float f32x4 __attribute__((ext_vector_type(4)));
typedef float f32x2 __attribute__((ext_vector_type(2)));
typedef unsigned u32x4 __attribute__((ext_vector_type(4)));
typedef unsigned u32x2 __attribute__((ext_vector_type(2)));
typedef unsigned long long u64;
#define GAS __attribute__((address_space(1)))
#define LAS __attribute__((address_space(3)))
typedef LAS const char* lds_cptr;
typedef short v4i16_t __attribute__((ext_vector_type(4)));
#define RLX_AGENT __ATOMIC_RELAXED, __HIP_MEMORY_SCOPE_AGENT

constexpr int SEQ = 2048, DM = 512, NQKV = 1536, NIN = 1792, FH = 896;
constexpr float QSCALE = 0.25f * 1.4426950408889634f;
constexpr float LN_EPS = 1e-5f;
constexpr size_t MB = 1024 * 1024;
constexpr size_t WS_QF = 0, WS_KV = 2 * MB, WS_Z1 = 6 * MB, WS_Z2N = 10 * MB, WS_U = 14 * MB, WS_OF = 18 * MB;
constexpr size_t WS_KM = 20 * MB, WS_ST = 20 * MB + 65536, WS_FLAG = 21 * MB;

#define MFMA(a, b, c) __builtin_amdgcn_mfma_f32_32x32x16_bf16(a, b, c, 0, 0, 0)
#define SBAR() __builtin_amdgcn_sched_barrier(0)
__device__ __forceinline__ unsigned cvtpk(float lo, float hi) { unsigned r; asm("v_cvt_pk_bf16_f32 %0, %1, %2" : "=v"(r) : "v"(lo), "v"(hi)); return r; }
__device__ __forceinline__ float bflo(unsigned u) { return __uint_as_float(u << 16); }
__device__ __forceinline__ float bfhi(unsigned u) { return __uint_as_float(u & 0xffff0000u); }
__device__ __forceinline__ float dot2(unsigned a, unsigned b, float acc) {
    return __builtin_amdgcn_fdot2_f32_bf16(__builtin_bit_cast(bf16x2_t, a), __builtin_bit_cast(bf16x2_t, b), acc, false); }
__device__ __forceinline__ void glds16(const void* g, unsigned lds_base) {
    asm volatile("s_mov_b32 m0, %1\n\ts_nop 0\n\tglobal_load_lds_dwordx4 %0, off" :: "v"(g), "s"(lds_base) : "memory", "m0"); }
__device__ __forceinline__ s16x4 trread(lds_cptr p) { return __builtin_bit_cast(s16x4, __builtin_amdgcn_ds_read_tr16_b64_v4i16((LAS v4i16_t*)p)); }
__device__ __forceinline__ bf16x8 ldsfrag(lds_cptr p) { return *(LAS const bf16x8*)p; }
__device__ __forceinline__ bf16x8 cat(s16x4 a, s16x4 b) { return (bf16x8){a[0], a[1], a[2], a[3], b[0], b[1], b[2], b[3]}; }
__device__ __forceinline__ float swap32_add(float v) { auto rr = __builtin_amdgcn_permlane32_swap(__float_as_uint(v), __float_as_uint(v), false, false); return __uint_as_float(rr[0]) + __uint_as_float(rr[1]); }
__device__ __forceinline__ float swap32_max(float v) { auto rr = __builtin_amdgcn_permlane32_swap(__float_as_uint(v), __float_as_uint(v), false, false); return fmaxf(__uint_as_float(rr[0]), __uint_as_float(rr[1])); }
__device__ __forceinline__ float gelu_t(float x) {
    const float c1 = -2.3022082f, c2 = -0.10294324f;
    float e = __builtin_amdgcn_exp2f(x * __builtin_fmaf(x * x, c2, c1));
    return x * __builtin_amdgcn_rcpf(1.0f + e); }
__device__ __forceinline__ float silu_f(float x) { return x * __builtin_amdgcn_rcpf(1.0f + __builtin_amdgcn_exp2f(-1.4426950408889634f * x)); }
#define STAMP(ws, slot) do {} while (0)
#define WAITV(n) asm volatile("s_waitcnt vmcnt(" #n ")" ::: "memory")
#define WAIT_BAR(n) asm volatile("s_waitcnt vmcnt(" #n ")\n\ts_barrier" ::: "memory")
#define LGKM_BAR() do { __builtin_amdgcn_s_waitcnt(0xC07F); asm volatile("" ::: "memory"); __builtin_amdgcn_s_barrier(); asm volatile("" ::: "memory"); } while (0)
__device__ __forceinline__ unsigned wsw(unsigned row) { return ((row & 3) << 2) | ((row >> 2) & 3); }

#define STAMP_BASE 0
constexpr int P_SLOT = 16384, P_LDS = 4 * P_SLOT + 2048;
__device__ __forceinline__ void proj_load(const uint16_t* __restrict__ x, const uint16_t* __restrict__ W, int ldw, int row0, int col0, char* lds, int rot) {
    const int lane = threadIdx.x & 63, wid = __builtin_amdgcn_readfirstlane(threadIdx.x >> 6) - 4;
    const unsigned lds0 = (unsigned)(uintptr_t)lds;
    const int xr0 = 32 * wid + (lane >> 2), xr1 = xr0 + 16;
    const uint16_t* xs0 = x + (size_t)(row0 + xr0) * DM + (((lane & 3) ^ ((xr0 >> 2) & 3)) * 8);
    const uint16_t* xs1 = x + (size_t)(row0 + xr1) * DM + (((lane & 3) ^ ((xr1 >> 2) & 3)) * 8);
    const int wk0 = 8 * wid + (lane >> 4), wk1 = wk0 + 4;
    const uint16_t* ws0 = W + (size_t)wk0 * ldw + col0 + (((lane & 15) ^ wsw(wk0)) * 8);
    const uint16_t* ws1 = W + (size_t)wk1 * ldw + col0 + (((lane & 15) ^ wsw(wk1)) * 8);
    const unsigned xd = lds0 + wid * 2048, wd = lds0 + 8192 + wid * 2048;
    auto issue = [&](int it) {
        const unsigned so = (it & 3) * P_SLOT; const int kk = (it + rot) & 15;
        glds16(xs0 + kk * 32, xd + so); glds16(xs1 + kk * 32, xd + so + 1024);
        glds16(ws0 + (size_t)kk * 32 * ldw, wd + so); glds16(ws1 + (size_t)kk * 32 * ldw, wd + so + 1024); };
    issue(0); issue(1); issue(2); issue(3);
    WAIT_BAR(12);
#pragma unroll 1
    for (int i = 1; i < 14; ++i) { WAIT_BAR(8); if (i + 3 < 16) issue(i + 3); }
    WAIT_BAR(4);
    WAIT_BAR(0);
    asm volatile("s_barrier" ::: "memory");
}
struct PFrag { bf16x8 x[2], w[2]; };
template <bool SWAP>
__device__ __forceinline__ void proj_compute(char* lds, f32x16 (&acc)[2][2], char* ws) {
    const int lane = threadIdx.x & 63, wid = __builtin_amdgcn_readfirstlane(threadIdx.x >> 6), wr = wid >> 1, wc = wid & 1, l31 = lane & 31, hh = lane >> 5;
    unsigned xoff[2][2], woff[2][2];
#pragma unroll
    for (int rt = 0; rt < 2; ++rt) { const int r = wr * 64 + (rt ^ wc) * 32 + l31; xoff[rt][0] = r * 64 + ((hh ^ ((r >> 2) & 3)) * 16); xoff[rt][1] = xoff[rt][0] ^ 32; }
    const int blk = (lane >> 4) & 1, q = (lane & 15) >> 2, p = lane & 3;
#pragma unroll
    for (int ct = 0; ct < 2; ++ct) { const int kr = 8 * hh + q; const int ch = 4 * (wc * 2 + (ct ^ wr)) + 2 * blk + (p >> 1); woff[ct][0] = 8192 + kr * 256 + ((ch ^ wsw(kr)) * 16) + 8 * (p & 1); woff[ct][1] = (woff[ct][0] + 1024) ^ 16; }
    auto rd = [&](PFrag& f, int slot, int ks) {
        lds_cptr sb = (lds_cptr)lds + slot * P_SLOT;
#pragma unroll
        for (int rt = 0; rt < 2; ++rt) f.x[rt] = ldsfrag(sb + xoff[rt][ks]);
#pragma unroll
        for (int ct = 0; ct < 2; ++ct) f.w[ct] = cat(trread(sb + woff[ct][0] + ks * 4096), trread(sb + woff[ct][1] + ks * 4096)); };
    float sx = 0.f, sq = 0.f, sc = 0.f;
    const unsigned ONES = 0x3f803f80u;
    auto mm = [&](const PFrag& f) {
#pragma unroll
        for (int rt = 0; rt < 2; ++rt)
#pragma unroll
            for (int ct = 0; ct < 2; ++ct) acc[rt][ct] = SWAP ? MFMA(f.x[rt], f.w[ct], acc[rt][ct]) : MFMA(f.w[ct], f.x[rt], acc[rt][ct]);
        const u32x4 xs = __builtin_bit_cast(u32x4, f.x[0]), wv = __builtin_bit_cast(u32x4, f.w[0]);
#pragma unroll
        for (int i = 0; i < 4; ++i) { sx = dot2(xs[i], ONES, sx); sq = dot2(xs[i], xs[i], sq); sc = dot2(wv[i], ONES, sc); } };
#pragma unroll
    for (int a = 0; a < 2; ++a)
#pragma unroll
        for (int b = 0; b < 2; ++b) acc[a][b] = f32x16{};
    PFrag f0, f1;
    STAMP(ws, 4);
    asm volatile("s_barrier" ::: "memory");
    STAMP(ws, 5);
    rd(f0, 0, 0);
#pragma unroll 1
    for (int io = 0; io < 4; ++io) {
#pragma unroll
        for (int sl = 0; sl < 4; ++sl) {
            rd(f1, sl, 1); SBAR();
            mm(f0); SBAR();
            LGKM_BAR(); rd(f0, (sl + 1) & 3, 0); SBAR();
            mm(f1); SBAR();
        }
        if (io == 0) STAMP(ws, 6); if (io == 1) STAMP(ws, 7);
    }
    float* st = (float*)(lds + 4 * P_SLOT);
    sx = swap32_add(sx); sq = swap32_add(sq); sc = swap32_add(sc);
    if (hh == 0) {
        const float mu = sx * (1.0f / DM); const float var = fmaxf(sq * (1.0f / DM) - mu * mu, 0.f);
        st[wr * 64 + wc * 32 + l31] = mu; st[128 + wr * 64 + wc * 32 + l31] = __builtin_amdgcn_rsqf(var + LN_EPS);
        st[256 + wc * 64 + wr * 32 + l31] = sc; }
}

__global__ __launch_bounds__(512, 4) void k_proj(const uint16_t* __restrict__ x, const uint16_t* __restrict__ wqkv, const uint16_t* __restrict__ win, char* __restrict__ ws) {
    extern __shared__ __attribute__((aligned(16))) char lds[];
    const int b = blockIdx.x, xcd = b & 7, i = b >> 3;
    const int rb = 4 * (xcd >> 1) + (i & 3), cb = 13 * (xcd & 1) + (i >> 2);
    const int tid = threadIdx.x, lane = tid & 63, wid = __builtin_amdgcn_readfirstlane(tid >> 6), wr = (wid >> 1) & 1, wc = wid & 1, l31 = lane & 31, hh = lane >> 5;
    const int row0 = rb * 128;
    u64 tag = 0;
    GAS u64* flags = (GAS u64*)(ws + WS_FLAG);
    GAS u64* epw = (GAS u64*)(ws + WS_FLAG + 65536 + rb * 128);
    if (cb >= 19) tag = __hip_atomic_load(epw, RLX_AGENT) + 1;
    STAMP(ws, 0);
    f32x16 acc[2][2];
    const bool isq = cb < 12;
    const uint16_t* W = isq ? wqkv : win; const int ldw = isq ? NQKV : NIN, col0 = isq ? cb * 128 : (cb - 12) * 128;
    const int rot = ((i & 3) * 4 + (i >> 2) * 3) & 15;
    if (wid >= 4) {
        proj_load(x, W, ldw, row0, col0, lds, rot);
        if (cb < 19) return;
        __syncthreads(); __syncthreads(); __syncthreads(); __syncthreads(); __syncthreads(); __syncthreads(); __syncthreads();
        return;
    }
    if (cb >= 8 && cb < 12) proj_compute<true>(lds, acc, ws); else proj_compute<false>(lds, acc, ws);
    if (cb < 19) asm volatile("s_waitcnt lgkmcnt(0)\n\ts_barrier" ::: "memory"); else __syncthreads();
    STAMP(ws, 1);
    const float* st = (const float*)(lds + 4 * P_SLOT);
    if (cb >= 8 && cb < 12) {
#pragma unroll
        for (int rt = 0; rt < 2; ++rt) {
            float mu[16], rs[16];
#pragma unroll
            for (int q = 0; q < 4; ++q) { const f32x4 m = *(const f32x4*)(st + wr * 64 + (rt ^ wc) * 32 + 8 * q + 4 * hh), r = *(const f32x4*)(st + 128 + wr * 64 + (rt ^ wc) * 32 + 8 * q + 4 * hh);
#pragma unroll
                for (int e = 0; e < 4; ++e) { mu[4 * q + e] = m[e]; rs[4 * q + e] = r[e]; } }
            const int tile = rb * 4 + wr * 2 + (rt ^ wc);
#pragma unroll
            for (int ct = 0; ct < 2; ++ct) {
                const float cs = st[256 + wc * 64 + (ct ^ wr) * 32 + l31];
                unsigned pk[8];
#pragma unroll
                for (int r = 0; r < 16; r += 2) pk[r >> 1] = cvtpk((acc[rt][ct][r] - mu[r] * cs) * rs[r], (acc[rt][ct][r + 1] - mu[r + 1] * cs) * rs[r + 1]);
                const int head = (cb - 8) * 8 + wc * 4 + (ct ^ wr) * 2 + (l31 >> 4);
                char* dst = ws + WS_KV + (size_t)head * 131072 + (tile >> 3) * 16384 + 8192 + (tile & 7) * 1024 + hh * 256 + (l31 & 15) * 16;
                *(u32x4*)dst = (u32x4){pk[0], pk[1], pk[2], pk[3]}; *(u32x4*)(dst + 512) = (u32x4){pk[4], pk[5], pk[6], pk[7]};
            } }
        STAMP(ws, 3);
        return;
    }
    if (cb < 8) {
        const bool isK = cb >= 4; const float sc = isK ? 1.0f : QSCALE;
        float kmx[2][2] = {{0.f, 0.f}, {0.f, 0.f}};
#pragma unroll
        for (int rt = 0; rt < 2; ++rt) { const int tile = rb * 4 + wr * 2 + (rt ^ wc);
            const float mu = st[wr * 64 + (rt ^ wc) * 32 + l31], rs = st[128 + wr * 64 + (rt ^ wc) * 32 + l31] * sc;
#pragma unroll
            for (int ct = 0; ct < 2; ++ct)
#pragma unroll
                for (int hb = 0; hb < 2; ++hb) {
                    float v[8];
#pragma unroll
                    for (int q = 0; q < 2; ++q) { const f32x4 c = *(const f32x4*)(st + 256 + wc * 64 + (ct ^ wr) * 32 + 16 * hb + 8 * q + 4 * hh);
#pragma unroll
                        for (int e = 0; e < 4; ++e) v[4 * q + e] = (acc[rt][ct][8 * hb + 4 * q + e] - mu * c[e]) * rs; }
                    u32x4 pk;
#pragma unroll
                    for (int e = 0; e < 4; ++e) pk[e] = cvtpk(v[2 * e], v[2 * e + 1]);
                    const int head = (cb & 3) * 8 + wc * 4 + (ct ^ wr) * 2 + hb;
                    char* dst = isK ? ws + WS_KV + (size_t)head * 131072 + (tile >> 3) * 16384 + (tile & 7) * 1024 + lane * 16 : ws + WS_QF + (size_t)head * 65536 + tile * 1024 + lane * 16;
                    *(u32x4*)dst = pk;
                    float n2 = 0.f;
#pragma unroll
                    for (int e = 0; e < 4; ++e) n2 = dot2(pk[e], pk[e], n2);
                    kmx[ct][hb] = fmaxf(kmx[ct][hb], swap32_add(n2));
                } }
        if (isK) {
#pragma unroll
            for (int ct = 0; ct < 2; ++ct)
#pragma unroll
                for (int hb = 0; hb < 2; ++hb) { float m = kmx[ct][hb];
#pragma unroll
                    for (int o = 16; o >= 1; o >>= 1) m = fmaxf(m, __shfl_xor(m, o));
                    if (lane == 0) ((float*)(ws + WS_KM))[((cb & 3) * 8 + wc * 4 + (ct ^ wr) * 2 + hb) * 32 + rb * 2 + wr] = m; } }
        STAMP(ws, 3);
        return;
    }
    unsigned zp[2][2][8];
#pragma unroll
    for (int rt = 0; rt < 2; ++rt) { const float mu = st[wr * 64 + (rt ^ wc) * 32 + l31], rs = st[128 + wr * 64 + (rt ^ wc) * 32 + l31];
#pragma unroll
        for (int ct = 0; ct < 2; ++ct)
#pragma unroll
            for (int q = 0; q < 4; ++q) { const f32x4 c = *(const f32x4*)(st + 256 + wc * 64 + (ct ^ wr) * 32 + 8 * q + 4 * hh);
                zp[rt][ct][2 * q] = cvtpk(gelu_t((acc[rt][ct][4 * q] - mu * c[0]) * rs), gelu_t((acc[rt][ct][4 * q + 1] - mu * c[1]) * rs));
                zp[rt][ct][2 * q + 1] = cvtpk(gelu_t((acc[rt][ct][4 * q + 2] - mu * c[2]) * rs), gelu_t((acc[rt][ct][4 * q + 3] - mu * c[3]) * rs)); } }
    if (cb < 19) {
#pragma unroll
        for (int rt = 0; rt < 2; ++rt)
#pragma unroll
            for (int ct = 0; ct < 2; ++ct) { char* dst = ws + WS_Z1 + ((size_t)(rb * 4 + wr * 2 + (rt ^ wc)) * 28 + (cb - 12) * 4 + wc * 2 + (ct ^ wr)) * 2048 + lane * 32;
                *(u32x4*)dst = (u32x4){zp[rt][ct][0], zp[rt][ct][1], zp[rt][ct][2], zp[rt][ct][3]}; *(u32x4*)(dst + 16) = (u32x4){zp[rt][ct][4], zp[rt][ct][5], zp[rt][ct][6], zp[rt][ct][7]}; }
        STAMP(ws, 3);
        return;
    }
    const int cj = cb - 19;
    float* pst = (float*)(lds);
    const unsigned ONES = 0x3f803f80u;
#pragma unroll
    for (int rt = 0; rt < 2; ++rt) { float s1 = 0.f, s2 = 0.f;
#pragma unroll
        for (int ct = 0; ct < 2; ++ct)
#pragma unroll
            for (int e = 0; e < 8; ++e) { s1 = dot2(zp[rt][ct][e], ONES, s1); s2 = dot2(zp[rt][ct][e], zp[rt][ct][e], s2); }
        s1 = swap32_add(s1); s2 = swap32_add(s2);
        if (hh == 0) *(f32x2*)(pst + (wc * 128 + wr * 64 + (rt ^ wc) * 32 + l31) * 2) = (f32x2){s1, s2}; }
    __syncthreads();
    STAMP(ws, 2);
    GAS u64* gst = (GAS u64*)(ws + WS_ST) + (size_t)rb * 7 * 128;
    if (tid < 128) { const f32x2 a = *(const f32x2*)(pst + tid * 2), c = *(const f32x2*)(pst + (128 + tid) * 2); const f32x2 s = a + c;
        __hip_atomic_store(gst + cj * 128 + tid, __builtin_bit_cast(u64, s), RLX_AGENT); }
    WAITV(0);
    __syncthreads();
    if (tid == 0) __hip_atomic_store(flags + (rb * 8 + cj) * 16, tag, RLX_AGENT);
    if (wid == 0) {
        for (unsigned spins = 0; spins < (1u << 22); ++spins) {
            const u64 v = lane < 7 ? __hip_atomic_load(flags + (rb * 8 + lane) * 16, RLX_AGENT) : tag;
            if (__all(v == tag)) break;
            __builtin_amdgcn_s_sleep(1); }
        if (cj == 0 && lane == 0) __hip_atomic_store(epw, tag, RLX_AGENT);
    }
    __syncthreads();
    float* fin = (float*)(lds + 2048);
    if (tid < 128) { f32x2 s = {0.f, 0.f};
#pragma unroll
        for (int j = 0; j < 7; ++j) s += __builtin_bit_cast(f32x2, __hip_atomic_load(gst + j * 128 + tid, RLX_AGENT));
        const float mu = s[0] * (1.0f / FH); const float var = fmaxf(s[1] * (1.0f / FH) - mu * mu, 0.f);
        fin[tid] = mu; fin[128 + tid] = __builtin_amdgcn_rsqf(var + LN_EPS); }
    __syncthreads();
    char* img = lds + 4096;
#pragma unroll
    for (int rt = 0; rt < 2; ++rt) { const int tok = wr * 64 + (rt ^ wc) * 32 + l31; const float mu = fin[tok], rs = fin[128 + tok];
#pragma unroll
        for (int ct = 0; ct < 2; ++ct)
#pragma unroll
            for (int q = 0; q < 4; ++q) { const unsigned a = zp[rt][ct][2 * q], c = zp[rt][ct][2 * q + 1];
                const u32x2 o = {cvtpk((bflo(a) - mu) * rs, (bfhi(a) - mu) * rs), cvtpk((bflo(c) - mu) * rs, (bfhi(c) - mu) * rs)};
                const int f = wc * 64 + (ct ^ wr) * 32 + 8 * q + 4 * hh;
                *(u32x2*)(img + tok * 256 + (((f >> 3) ^ wsw(tok)) * 16) + (f & 4) * 2) = o; } }
    __syncthreads();
    { const int blk = (lane >> 4) & 1, q = (lane & 15) >> 2, p = lane & 3;
#pragma unroll
      for (int ks = 0; ks < 8; ++ks) { s16x4 t2[2];
#pragma unroll
          for (int t = 0; t < 2; ++t) { const int kr = 16 * ks + 8 * hh + 4 * t + q; const int ch = 4 * wid + 2 * blk + (p >> 1);
              t2[t] = trread((lds_cptr)img + kr * 256 + ((ch ^ wsw(kr)) * 16) + 8 * (p & 1)); }
          *(bf16x8*)(ws + WS_Z2N + ((size_t)(cj * 4 + wid) * 128 + rb * 8 + ks) * 1024 + lane * 16) = cat(t2[0], t2[1]); } }
    __syncthreads();
    STAMP(ws, 3);
}

#undef STAMP_BASE
#define STAMP_BASE 512
constexpr int A_ONES = 3 * 16384, A_LDS = A_ONES + 8192 + 512;
__device__ __forceinline__ void attn_role(char* __restrict__ ws, char* lds, int b) {
    const int tid = threadIdx.x, lane = tid & 63, wid = __builtin_amdgcn_readfirstlane(tid >> 6);
    const int head = (b & 7) * 4 + ((b >> 3) & 3), jb = b >> 5;
    const unsigned lds0 = (unsigned)(uintptr_t)lds;
    const char* kvb = ws + WS_KV + (size_t)head * 131072;
    STAMP(ws, 0);
    unsigned goff = wid * 2048 + lane * 16;
#pragma unroll
    for (int c = 0; c < 2; ++c)
#pragma unroll
        for (int k = 0; k < 2; ++k) glds16(kvb + c * 16384 + goff + k * 1024, lds0 + c * 16384 + wid * 2048 + k * 1024);
    for (int i = tid; i < (8192 + 512) / 16; i += 512) { const unsigned o1 = ((i & 11) == 0) ? 0x3f803f80u : 0u; *(u32x4*)(lds + A_ONES + i * 16) = (u32x4){o1, o1, o1, o1}; }
    const int tile0 = jb * 8 + wid;
    const u32x4 qa = *(const u32x4*)(ws + WS_QF + (size_t)head * 65536 + tile0 * 1024 + lane * 16);
    float km = ((const float*)(ws + WS_KM))[head * 32 + (lane & 31)];
#pragma unroll
    for (int o = 16; o >= 1; o >>= 1) km = fmaxf(km, __shfl_xor(km, o));
    float na = 0.f;
#pragma unroll
    for (int e = 0; e < 4; ++e) na = dot2(qa[e], qa[e], na);
    na = swap32_add(na);
    float ma = __builtin_amdgcn_sqrtf(na * km);
    if (__any(!(ma <= 40.0f))) {
        float xa = -3.0e38f;
        for (int t = 0; t < 64; ++t) {
            const bf16x8 kf = *(const bf16x8*)(kvb + (t >> 3) * 16384 + (t & 7) * 1024 + lane * 16);
            const f32x16 sa = MFMA(kf, __builtin_bit_cast(bf16x8, qa), f32x16{});
#pragma unroll
            for (int r = 0; r < 16; ++r) xa = fmaxf(xa, sa[r]); }
        ma = swap32_max(xa);
    }
    const float nma = -ma;
    const unsigned kaddr = lds0 + lane * 16;
    const bool ones = (lane & 16) != 0;
    const unsigned vaddr = ones ? lds0 + A_ONES + (lane & 15) * 16 : lds0 + 8192 + (lane >> 5) * 256 + (lane & 15) * 16;
    const unsigned vsel = ones ? 0u : 0xffffffffu;
    goff += 2 * 16384;
    unsigned dbeg = lds0 + wid * 2048, dend = dbeg + 3 * 16384, dslot = dbeg + 2 * 16384, nslot = 0x4000, cnt, m0save;
    unsigned oa0, oa1, oa2, oa3;
    STAMP(ws, 1);
    asm volatile(
    "s_mov_b32 %[m0save], m0\n"
    "v_mov_b32 v96, %[qa0]\n"
    "v_mov_b32 v97, %[qa1]\n"
    "v_mov_b32 v98, %[qa2]\n"
    "v_mov_b32 v99, %[qa3]\n"
    "v_mov_b32 v72, %[nma]\n"
    "v_mov_b32 v56, 0\n"
    "v_mov_b32 v73, %[nma]\n"
    "v_mov_b32 v57, 0\n"
    "v_mov_b32 v74, %[nma]\n"
    "v_mov_b32 v58, 0\n"
    "v_mov_b32 v75, %[nma]\n"
    "v_mov_b32 v59, 0\n"
    "v_mov_b32 v76, %[nma]\n"
    "v_mov_b32 v60, 0\n"
    "v_mov_b32 v77, %[nma]\n"
    "v_mov_b32 v61, 0\n"
    "v_mov_b32 v78, %[nma]\n"
    "v_mov_b32 v62, 0\n"
    "v_mov_b32 v79, %[nma]\n"
    "v_mov_b32 v63, 0\n"
    "v_mov_b32 v80, %[nma]\n"
    "v_mov_b32 v64, 0\n"
    "v_mov_b32 v81, %[nma]\n"
    "v_mov_b32 v65, 0\n"
    "v_mov_b32 v82, %[nma]\n"
    "v_mov_b32 v66, 0\n"
    "v_mov_b32 v83, %[nma]\n"
    "v_mov_b32 v67, 0\n"
    "v_mov_b32 v84, %[nma]\n"
    "v_mov_b32 v68, 0\n"
    "v_mov_b32 v85, %[nma]\n"
    "v_mov_b32 v69, 0\n"
    "v_mov_b32 v86, %[nma]\n"
    "v_mov_b32 v70, 0\n"
    "v_mov_b32 v87, %[nma]\n"
    "v_mov_b32 v71, 0\n"
    "v_mov_b32 v120, 0\n"
    "v_mov_b32 v121, 0\n"
    "v_mov_b32 v122, 0\n"
    "v_mov_b32 v123, 0\n"
    "v_mov_b32 v88, 0\n"
    "v_mov_b32 v89, 0\n"
    "v_mov_b32 v90, 0\n"
    "v_mov_b32 v91, 0\n"
    "v_mov_b32 v92, 0\n"
    "v_mov_b32 v93, 0\n"
    "v_mov_b32 v94, 0\n"
    "v_mov_b32 v95, 0\n"
    "v_mov_b32 v124, %[kaddr]\n"
    "v_mov_b32 v125, %[vaddr]\n"
    "v_add_u32 v126, 0x4000, v124\n"
    "v_and_b32 v127, 0x4000, %[vsel]\n"
    "v_add_u32 v127, v127, v125\n"
    "s_waitcnt vmcnt(2) lgkmcnt(0)\n"
    "s_barrier\n"
    "ds_read_b128 v[100:103], v124\n"
    "ds_read_b128 v[104:107], v124 offset:1024\n"
    "ds_read_b128 v[108:111], v125\n"
    "ds_read_b128 v[112:115], v125 offset:512\n"
    "s_mov_b32 %[cnt], 0\n"
    "s_waitcnt lgkmcnt(0)\n"
    "v_mfma_f32_32x32x16_bf16 v[24:39], v[100:103], v[96:99], v[72:87]\n"
    "s_nop 15\n"
    ".Lloop%=:\n"
    "v_exp_f32 v24, v24\n"
    "v_exp_f32 v25, v25\n"
    "v_mfma_f32_32x32x16_bf16 v[56:71], v[120:123], v[92:95], v[56:71]\n"
    "v_exp_f32 v26, v26\n"
    "v_exp_f32 v27, v27\n"
    "v_cvt_pk_bf16_f32 v88, v24, v25\n"
    "v_exp_f32 v28, v28\n"
    "s_waitcnt lgkmcnt(0)\n"
    "v_mfma_f32_32x32x16_bf16 v[40:55], v[104:107], v[96:99], v[72:87]\n"
    "v_exp_f32 v29, v29\n"
    "v_cvt_pk_bf16_f32 v89, v26, v27\n"
    "ds_read_b128 v[100:103], v124 offset:2048\n"
    "v_exp_f32 v30, v30\n"
    "ds_read_b128 v[116:119], v125 offset:1024\n"
    "v_exp_f32 v31, v31\n"
    "v_cvt_pk_bf16_f32 v90, v28, v29\n"
    "ds_read_b128 v[120:123], v125 offset:1536\n"
    "v_exp_f32 v32, v32\n"
    "v_exp_f32 v33, v33\n"
    "v_cvt_pk_bf16_f32 v91, v30, v31\n"
    "v_exp_f32 v34, v34\n"
    "v_exp_f32 v35, v35\n"
    "v_mfma_f32_32x32x16_bf16 v[56:71], v[108:111], v[88:91], v[56:71]\n"
    "v_cvt_pk_bf16_f32 v92, v32, v33\n"
    "v_exp_f32 v36, v36\n"
    "v_exp_f32 v37, v37\n"
    "v_cvt_pk_bf16_f32 v93, v34, v35\n"
    "v_exp_f32 v38, v38\n"
    "v_exp_f32 v39, v39\n"
    "v_cvt_pk_bf16_f32 v94, v36, v37\n"
    "v_cvt_pk_bf16_f32 v95, v38, v39\n"
    "v_exp_f32 v40, v40\n"
    "v_exp_f32 v41, v41\n"
    "v_mfma_f32_32x32x16_bf16 v[56:71], v[112:115], v[92:95], v[56:71]\n"
    "v_exp_f32 v42, v42\n"
    "v_exp_f32 v43, v43\n"
    "v_cvt_pk_bf16_f32 v88, v40, v41\n"
    "v_exp_f32 v44, v44\n"
    "s_waitcnt lgkmcnt(0)\n"
    "v_mfma_f32_32x32x16_bf16 v[24:39], v[100:103], v[96:99], v[72:87]\n"
    "v_exp_f32 v45, v45\n"
    "v_cvt_pk_bf16_f32 v89, v42, v43\n"
    "ds_read_b128 v[104:107], v124 offset:3072\n"
    "v_exp_f32 v46, v46\n"
    "ds_read_b128 v[108:111], v125 offset:2048\n"
    "v_exp_f32 v47, v47\n"
    "v_cvt_pk_bf16_f32 v90, v44, v45\n"
    "ds_read_b128 v[112:115], v125 offset:2560\n"
    "v_exp_f32 v48, v48\n"
    "v_exp_f32 v49, v49\n"
    "v_cvt_pk_bf16_f32 v91, v46, v47\n"
    "v_exp_f32 v50, v50\n"
    "v_exp_f32 v51, v51\n"
    "v_mfma_f32_32x32x16_bf16 v[56:71], v[116:119], v[88:91], v[56:71]\n"
    "v_cvt_pk_bf16_f32 v92, v48, v49\n"
    "v_exp_f32 v52, v52\n"
    "v_exp_f32 v53, v53\n"
    "v_cvt_pk_bf16_f32 v93, v50, v51\n"
    "v_exp_f32 v54, v54\n"
    "v_exp_f32 v55, v55\n"
    "v_cvt_pk_bf16_f32 v94, v52, v53\n"
    "v_cvt_pk_bf16_f32 v95, v54, v55\n"
    "v_exp_f32 v24, v24\n"
    "v_exp_f32 v25, v25\n"
    "v_mfma_f32_32x32x16_bf16 v[56:71], v[120:123], v[92:95], v[56:71]\n"
    "v_exp_f32 v26, v26\n"
    "v_exp_f32 v27, v27\n"
    "v_cvt_pk_bf16_f32 v88, v24, v25\n"
    "v_exp_f32 v28, v28\n"
    "s_waitcnt lgkmcnt(0)\n"
    "v_mfma_f32_32x32x16_bf16 v[40:55], v[104:107], v[96:99], v[72:87]\n"
    "v_exp_f32 v29, v29\n"
    "v_cvt_pk_bf16_f32 v89, v26, v27\n"
    "ds_read_b128 v[100:103], v124 offset:4096\n"
    "v_exp_f32 v30, v30\n"
    "ds_read_b128 v[116:119], v125 offset:3072\n"
    "v_exp_f32 v31, v31\n"
    "v_cvt_pk_bf16_f32 v90, v28, v29\n"
    "ds_read_b128 v[120:123], v125 offset:3584\n"
    "v_exp_f32 v32, v32\n"
    "v_exp_f32 v33, v33\n"
    "v_cvt_pk_bf16_f32 v91, v30, v31\n"
    "v_exp_f32 v34, v34\n"
    "v_exp_f32 v35, v35\n"
    "v_mfma_f32_32x32x16_bf16 v[56:71], v[108:111], v[88:91], v[56:71]\n"
    "v_cvt_pk_bf16_f32 v92, v32, v33\n"
    "v_exp_f32 v36, v36\n"
    "v_exp_f32 v37, v37\n"
    "v_cvt_pk_bf16_f32 v93, v34, v35\n"
    "v_exp_f32 v38, v38\n"
    "v_exp_f32 v39, v39\n"
    "v_cvt_pk_bf16_f32 v94, v36, v37\n"
    "v_cvt_pk_bf16_f32 v95, v38, v39\n"
    "v_exp_f32 v40, v40\n"
    "v_exp_f32 v41, v41\n"
    "v_mfma_f32_32x32x16_bf16 v[56:71], v[112:115], v[92:95], v[56:71]\n"
    "v_exp_f32 v42, v42\n"
    "v_exp_f32 v43, v43\n"
    "v_cvt_pk_bf16_f32 v88, v40, v41\n"
    "v_exp_f32 v44, v44\n"
    "s_waitcnt lgkmcnt(0)\n"
    "v_mfma_f32_32x32x16_bf16 v[24:39], v[100:103], v[96:99], v[72:87]\n"
    "v_exp_f32 v45, v45\n"
    "v_cvt_pk_bf16_f32 v89, v42, v43\n"
    "ds_read_b128 v[104:107], v124 offset:5120\n"
    "v_exp_f32 v46, v46\n"
    "ds_read_b128 v[108:111], v125 offset:4096\n"
    "v_exp_f32 v47, v47\n"
    "v_cvt_pk_bf16_f32 v90, v44, v45\n"
    "ds_read_b128 v[112:115], v125 offset:4608\n"
    "v_exp_f32 v48, v48\n"
    "v_exp_f32 v49, v49\n"
    "v_cvt_pk_bf16_f32 v91, v46, v47\n"
    "v_exp_f32 v50, v50\n"
    "v_exp_f32 v51, v51\n"
    "v_mfma_f32_32x32x16_bf16 v[56:71], v[116:119], v[88:91], v[56:71]\n"
    "v_cvt_pk_bf16_f32 v92, v48, v49\n"
    "v_exp_f32 v52, v52\n"
    "v_exp_f32 v53, v53\n"
    "v_cvt_pk_bf16_f32 v93, v50, v51\n"
    "v_exp_f32 v54, v54\n"
    "v_exp_f32 v55, v55\n"
    "v_cvt_pk_bf16_f32 v94, v52, v53\n"
    "v_cvt_pk_bf16_f32 v95, v54, v55\n"
    "v_exp_f32 v24, v24\n"
    "v_exp_f32 v25, v25\n"
    "v_mfma_f32_32x32x16_bf16 v[56:71], v[120:123], v[92:95], v[56:71]\n"
    "v_exp_f32 v26, v26\n"
    "v_exp_f32 v27, v27\n"
    "v_cvt_pk_bf16_f32 v88, v24, v25\n"
    "v_exp_f32 v28, v28\n"
    "s_waitcnt lgkmcnt(0)\n"
    "v_mfma_f32_32x32x16_bf16 v[40:55], v[104:107], v[96:99], v[72:87]\n"
    "v_exp_f32 v29, v29\n"
    "v_cvt_pk_bf16_f32 v89, v26, v27\n"
    "ds_read_b128 v[100:103], v124 offset:6144\n"
    "v_exp_f32 v30, v30\n"
    "ds_read_b128 v[116:119], v125 offset:5120\n"
    "v_exp_f32 v31, v31\n"
    "v_cvt_pk_bf16_f32 v90, v28, v29\n"
    "ds_read_b128 v[120:123], v125 offset:5632\n"
    "v_exp_f32 v32, v32\n"
    "v_exp_f32 v33, v33\n"
    "v_cvt_pk_bf16_f32 v91, v30, v31\n"
    "v_exp_f32 v34, v34\n"
    "v_exp_f32 v35, v35\n"
    "v_mfma_f32_32x32x16_bf16 v[56:71], v[108:111], v[88:91], v[56:71]\n"
    "v_cvt_pk_bf16_f32 v92, v32, v33\n"
    "v_exp_f32 v36, v36\n"
    "v_exp_f32 v37, v37\n"
    "v_cvt_pk_bf16_f32 v93, v34, v35\n"
    "v_exp_f32 v38, v38\n"
    "v_exp_f32 v39, v39\n"
    "v_cvt_pk_bf16_f32 v94, v36, v37\n"
    "v_cvt_pk_bf16_f32 v95, v38, v39\n"
    "v_exp_f32 v40, v40\n"
    "v_exp_f32 v41, v41\n"
    "v_mfma_f32_32x32x16_bf16 v[56:71], v[112:115], v[92:95], v[56:71]\n"
    "v_exp_f32 v42, v42\n"
    "v_exp_f32 v43, v43\n"
    "v_cvt_pk_bf16_f32 v88, v40, v41\n"
    "v_exp_f32 v44, v44\n"
    "s_waitcnt lgkmcnt(0)\n"
    "v_mfma_f32_32x32x16_bf16 v[24:39], v[100:103], v[96:99], v[72:87]\n"
    "v_exp_f32 v45, v45\n"
    "v_cvt_pk_bf16_f32 v89, v42, v43\n"
    "ds_read_b128 v[104:107], v124 offset:7168\n"
    "v_exp_f32 v46, v46\n"
    "ds_read_b128 v[108:111], v125 offset:6144\n"
    "v_exp_f32 v47, v47\n"
    "v_cvt_pk_bf16_f32 v90, v44, v45\n"
    "ds_read_b128 v[112:115], v125 offset:6656\n"
    "v_exp_f32 v48, v48\n"
    "v_exp_f32 v49, v49\n"
    "v_cvt_pk_bf16_f32 v91, v46, v47\n"
    "v_exp_f32 v50, v50\n"
    "v_exp_f32 v51, v51\n"
    "v_mfma_f32_32x32x16_bf16 v[56:71], v[116:119], v[88:91], v[56:71]\n"
    "v_cvt_pk_bf16_f32 v92, v48, v49\n"
    "v_exp_f32 v52, v52\n"
    "v_exp_f32 v53, v53\n"
    "v_cvt_pk_bf16_f32 v93, v50, v51\n"
    "v_exp_f32 v54, v54\n"
    "v_exp_f32 v55, v55\n"
    "v_cvt_pk_bf16_f32 v94, v52, v53\n"
    "v_cvt_pk_bf16_f32 v95, v54, v55\n"
    "v_exp_f32 v24, v24\n"
    "v_exp_f32 v25, v25\n"
    "v_mfma_f32_32x32x16_bf16 v[56:71], v[120:123], v[92:95], v[56:71]\n"
    "v_exp_f32 v26, v26\n"
    "v_exp_f32 v27, v27\n"
    "v_cvt_pk_bf16_f32 v88, v24, v25\n"
    "v_exp_f32 v28, v28\n"
    "s_waitcnt lgkmcnt(0)\n"
    "v_mfma_f32_32x32x16_bf16 v[40:55], v[104:107], v[96:99], v[72:87]\n"
    "v_exp_f32 v29, v29\n"
    "v_cvt_pk_bf16_f32 v89, v26, v27\n"
    "s_waitcnt vmcnt(0)\n"
    "s_barrier\n"
    "ds_read_b128 v[100:103], v126 offset:0\n"
    "v_exp_f32 v30, v30\n"
    "ds_read_b128 v[116:119], v125 offset:7168\n"
    "v_exp_f32 v31, v31\n"
    "v_cvt_pk_bf16_f32 v90, v28, v29\n"
    "ds_read_b128 v[120:123], v125 offset:7680\n"
    "v_exp_f32 v32, v32\n"
    "v_exp_f32 v33, v33\n"
    "v_cvt_pk_bf16_f32 v91, v30, v31\n"
    "v_exp_f32 v34, v34\n"
    "v_exp_f32 v35, v35\n"
    "v_mfma_f32_32x32x16_bf16 v[56:71], v[108:111], v[88:91], v[56:71]\n"
    "s_cmp_ge_u32 %[cnt], 6\n"
    "s_cbranch_scc1 .Lnodma%=\n"
    "s_mov_b32 m0, %[dslot]\n"
    "s_nop 0\n"
    "global_load_lds_dwordx4 %[goff], %[gbase] offset:0\n"
    "global_load_lds_dwordx4 %[goff], %[gbase] offset:1024\n"
    ".Lnodma%=:\n"
    "v_cvt_pk_bf16_f32 v92, v32, v33\n"
    "v_exp_f32 v36, v36\n"
    "v_exp_f32 v37, v37\n"
    "v_cvt_pk_bf16_f32 v93, v34, v35\n"
    "v_exp_f32 v38, v38\n"
    "v_exp_f32 v39, v39\n"
    "v_cvt_pk_bf16_f32 v94, v36, v37\n"
    "v_cvt_pk_bf16_f32 v95, v38, v39\n"
    "v_exp_f32 v40, v40\n"
    "v_exp_f32 v41, v41\n"
    "v_mfma_f32_32x32x16_bf16 v[56:71], v[112:115], v[92:95], v[56:71]\n"
    "v_exp_f32 v42, v42\n"
    "v_exp_f32 v43, v43\n"
    "v_cvt_pk_bf16_f32 v88, v40, v41\n"
    "v_exp_f32 v44, v44\n"
    "s_waitcnt lgkmcnt(0)\n"
    "v_mfma_f32_32x32x16_bf16 v[24:39], v[100:103], v[96:99], v[72:87]\n"
    "v_exp_f32 v45, v45\n"
    "v_cvt_pk_bf16_f32 v89, v42, v43\n"
    "ds_read_b128 v[104:107], v126 offset:1024\n"
    "v_exp_f32 v46, v46\n"
    "ds_read_b128 v[108:111], v127 offset:0\n"
    "v_exp_f32 v47, v47\n"
    "v_cvt_pk_bf16_f32 v90, v44, v45\n"
    "ds_read_b128 v[112:115], v127 offset:512\n"
    "v_exp_f32 v48, v48\n"
    "v_exp_f32 v49, v49\n"
    "v_cvt_pk_bf16_f32 v91, v46, v47\n"
    "v_exp_f32 v50, v50\n"
    "v_exp_f32 v51, v51\n"
    "v_mfma_f32_32x32x16_bf16 v[56:71], v[116:119], v[88:91], v[56:71]\n"
    "v_cvt_pk_bf16_f32 v92, v48, v49\n"
    "v_exp_f32 v52, v52\n"
    "v_exp_f32 v53, v53\n"
    "v_cvt_pk_bf16_f32 v93, v50, v51\n"
    "v_exp_f32 v54, v54\n"
    "v_exp_f32 v55, v55\n"
    "v_cvt_pk_bf16_f32 v94, v52, v53\n"
    "v_cvt_pk_bf16_f32 v95, v54, v55\n"
    "v_mov_b32 v124, v126\n"
    "v_mov_b32 v125, v127\n"
    "s_add_u32 %[nslot], %[nslot], 0x4000\n"
    "s_cmp_eq_u32 %[nslot], 0xc000\n"
    "s_cselect_b32 %[nslot], 0, %[nslot]\n"
    "v_add_u32 v126, %[nslot], %[kaddr]\n"
    "v_and_b32 v127, %[nslot], %[vsel]\n"
    "v_add_u32 v127, v127, %[vaddr]\n"
    "s_add_u32 %[dslot], %[dslot], 0x4000\n"
    "s_cmp_eq_u32 %[dslot], %[dend]\n"
    "s_cselect_b32 %[dslot], %[dbeg], %[dslot]\n"
    "v_add_u32 %[goff], 0x4000, %[goff]\n"
    "s_add_u32 %[cnt], %[cnt], 1\n"
    "s_cmp_lt_u32 %[cnt], 8\n"
    "s_cbranch_scc1 .Lloop%=\n"
    "s_nop 1\n"
    "v_mfma_f32_32x32x16_bf16 v[56:71], v[120:123], v[92:95], v[56:71]\n"
    "s_nop 15\n"
    "s_nop 3\n"
    "v_rcp_f32 v24, v64\n"
    "s_nop 0\n"
    "v_mul_f32 v56, v56, v24\n"
    "v_mul_f32 v57, v57, v24\n"
    "v_mul_f32 v58, v58, v24\n"
    "v_mul_f32 v59, v59, v24\n"
    "v_mul_f32 v60, v60, v24\n"
    "v_mul_f32 v61, v61, v24\n"
    "v_mul_f32 v62, v62, v24\n"
    "v_mul_f32 v63, v63, v24\n"
    "v_cvt_pk_bf16_f32 %[oa0], v56, v57\n"
    "v_cvt_pk_bf16_f32 %[oa1], v58, v59\n"
    "v_cvt_pk_bf16_f32 %[oa2], v60, v61\n"
    "v_cvt_pk_bf16_f32 %[oa3], v62, v63\n"
    "s_waitcnt lgkmcnt(0)\n"
    "s_mov_b32 m0, %[m0save]\n"
        : [oa0] "=&v"(oa0), [oa1] "=&v"(oa1), [oa2] "=&v"(oa2), [oa3] "=&v"(oa3),
          [goff] "+v"(goff), [dslot] "+s"(dslot), [nslot] "+s"(nslot), [cnt] "=&s"(cnt), [m0save] "=&s"(m0save)
        : [qa0] "v"(qa[0]), [qa1] "v"(qa[1]), [qa2] "v"(qa[2]), [qa3] "v"(qa[3]),
          [nma] "v"(nma), [kaddr] "v"(kaddr), [vaddr] "v"(vaddr), [vsel] "v"(vsel), [gbase] "s"(kvb), [dbeg] "s"(dbeg), [dend] "s"(dend), [psel] "s"(0x07060302u)
        : "memory", "scc", "vcc",
          "v24", "v25", "v26", "v27", "v28", "v29", "v30", "v31", "v32", "v33", "v34", "v35", "v36", "v37", "v38", "v39", "v40", "v41", "v42", "v43", "v44", "v45", "v46", "v47", "v48", "v49", "v50", "v51", "v52", "v53", "v54", "v55", "v56", "v57", "v58", "v59", "v60", "v61", "v62", "v63", "v64", "v65", "v66", "v67", "v68", "v69", "v70", "v71", "v72", "v73", "v74", "v75", "v76", "v77", "v78", "v79", "v80", "v81", "v82", "v83", "v84", "v85", "v86", "v87", "v88", "v89", "v90", "v91", "v92", "v93", "v94", "v95", "v96", "v97", "v98", "v99", "v100", "v101", "v102", "v103", "v104", "v105", "v106", "v107", "v108", "v109", "v110", "v111", "v112", "v113", "v114", "v115", "v116", "v117", "v118", "v119", "v120", "v121", "v122", "v123", "v124", "v125", "v126", "v127"
    );
    STAMP(ws, 2);
    char* od = ws + WS_OF + ((size_t)tile0 * 32 + head) * 1024 + lane * 16;
    *(u32x4*)od = (u32x4){oa0, oa1, oa2, oa3};
    STAMP(ws, 3);
}

constexpr int S_SLOT = 24576, S_LDS = 3 * S_SLOT;
__device__ __forceinline__ void spatial_role(const uint16_t* __restrict__ wsp, const uint16_t* __restrict__ bsp, char* __restrict__ ws, char* lds, int g) {
    const int tid = threadIdx.x, lane = tid & 63, wid8 = __builtin_amdgcn_readfirstlane(tid >> 6), wid = wid8 & 3, l31 = lane & 31, hh = lane >> 5;
    const int i = g >> 3, tb = 4 * (g & 7) + (i & 3), fb = i >> 2;
    const unsigned lds0 = (unsigned)(uintptr_t)lds;
    STAMP(ws, 0);
    if (wid8 >= 4) {
        const int r0 = 16 * wid + (lane >> 3), r1 = r0 + 8;
        const uint16_t* s0 = wsp + (size_t)(tb * 64 + r0) * SEQ + (((lane & 7) ^ ((r0 >> 1) & 7)) * 8);
        const uint16_t* s1 = wsp + (size_t)(tb * 64 + r1) * SEQ + (((lane & 7) ^ ((r1 >> 1) & 7)) * 8);
        const char* zs = ws + WS_Z2N + (size_t)(fb * 4 + wid) * 131072 + lane * 16;
        const int rot = ((i & 3) * 8 + (i >> 2) * 4) & 31;
        auto issue = [&](int it_, int slot) { const unsigned so = lds0 + slot * S_SLOT; const int it = (it_ + rot) & 31;
            glds16(s0 + it * 64, so + wid * 2048); glds16(s1 + it * 64, so + wid * 2048 + 1024);
#pragma unroll
            for (int k = 0; k < 4; ++k) glds16(zs + (size_t)(it * 4 + k) * 1024, so + 8192 + wid * 4096 + k * 1024); };
        issue(0, 0); issue(1, 1); issue(2, 2);
        WAIT_BAR(12);
        int slot = 0;
#pragma unroll 1
        for (int it = 1; it < 31; ++it) { WAIT_BAR(6); if (it + 2 < 32) issue(it + 2, slot); slot = slot == 2 ? 0 : slot + 1; }
        WAIT_BAR(0);
        asm volatile("s_barrier" ::: "memory");
        return;
    }
    unsigned boff[2][4];
#pragma unroll
    for (int rt = 0; rt < 2; ++rt)
#pragma unroll
        for (int ks = 0; ks < 4; ++ks) { const int r = rt * 32 + l31; boff[rt][ks] = (r * 128 + ((hh ^ ((r >> 1) & 7)) * 16)) ^ (ks * 32); }
    const unsigned aoff = 8192 + wid * 4096 + lane * 16;
    struct SF { bf16x8 a, b[2]; };
    auto rd = [&](SF& f, int slot, int ks) { lds_cptr sb = (lds_cptr)lds + slot * S_SLOT;
        f.a = ldsfrag(sb + aoff + ks * 1024); f.b[0] = ldsfrag(sb + boff[0][ks]); f.b[1] = ldsfrag(sb + boff[1][ks]); };
    f32x16 acc[2] = {f32x16{}, f32x16{}};
    auto mm = [&](const SF& f) { acc[0] = MFMA(f.a, f.b[0], acc[0]); acc[1] = MFMA(f.a, f.b[1], acc[1]); };
    SF f0, f1;
    asm volatile("s_barrier" ::: "memory");
    rd(f0, 0, 0);
    auto step = [&](int sl, int nsl) {
        rd(f1, sl, 1); SBAR(); mm(f0); SBAR();
        rd(f0, sl, 2); SBAR(); mm(f1); SBAR();
        rd(f1, sl, 3); SBAR(); mm(f0); SBAR();
        LGKM_BAR(); rd(f0, nsl, 0); SBAR();
        mm(f1); SBAR(); };
#pragma unroll 1
    for (int io = 0; io < 10; ++io) { step(0, 1); step(1, 2); step(2, 0); if (io == 0) STAMP(ws, 1); }
    step(0, 1); step(1, 2);
    STAMP(ws, 2);
#pragma unroll
    for (int rt = 0; rt < 2; ++rt) { const int tt = tb * 2 + rt; const float bias = __uint_as_float((unsigned)bsp[tt * 32 + l31] << 16);
        const char* z1p = ws + WS_Z1 + ((size_t)tt * 28 + fb * 4 + wid) * 2048 + lane * 32;
        const u32x4 za = *(const u32x4*)z1p, zb = *(const u32x4*)(z1p + 16);
        u32x4 ua, ub;
#pragma unroll
        for (int e = 0; e < 4; ++e) {
            ua[e] = cvtpk(bflo(za[e]) * (acc[rt][2 * e] + bias), bfhi(za[e]) * (acc[rt][2 * e + 1] + bias));
            ub[e] = cvtpk(bflo(zb[e]) * (acc[rt][8 + 2 * e] + bias), bfhi(zb[e]) * (acc[rt][8 + 2 * e + 1] + bias)); }
        char* ud = ws + WS_U + ((size_t)tt * 56 + (fb * 4 + wid) * 2) * 1024 + lane * 16;
        *(u32x4*)ud = ua; *(u32x4*)(ud + 1024) = ub; }
    STAMP(ws, 3);
}

__global__ __launch_bounds__(512, 4) void k_mid(const uint16_t* __restrict__ wsp, const uint16_t* __restrict__ bsp, char* __restrict__ ws) {
    extern __shared__ __attribute__((aligned(16))) char lds[];
    const int b = blockIdx.x;
    if (b < 256) attn_role(ws, lds, b); else spatial_role(wsp, bsp, ws, lds, b - 256);
}

#undef STAMP_BASE
#define STAMP_BASE 1024
constexpr int F_SLOT = 8192 + 8192, F_LDS = 4 * F_SLOT;
__global__ __launch_bounds__(512, 4) void k_fin(const uint16_t* __restrict__ x, const uint16_t* __restrict__ wo, const uint16_t* __restrict__ wout, const char* __restrict__ ws, uint16_t* __restrict__ out) {
    extern __shared__ __attribute__((aligned(16))) char lds[];
    const int b = blockIdx.x, tb = 4 * (b & 7) + ((b >> 3) & 3), db = b >> 5;
    const int tid = threadIdx.x, lane = tid & 63, wid8 = __builtin_amdgcn_readfirstlane(tid >> 6), wid = wid8 & 3, wr = wid >> 1, wc = wid & 1, l31 = lane & 31, hh = lane >> 5;
    const unsigned lds0 = (unsigned)(uintptr_t)lds;
    auto wswz = [](int r) { return ((r & 2) << 1) | ((r >> 2) & 3); };
    STAMP(ws, 0);
    if (wid8 >= 4) {
        const int k0 = 16 * wid + (lane >> 3), k1 = k0 + 8;
        const size_t wo0 = (size_t)k0 * DM + db * 64 + (((lane & 7) ^ wswz(k0)) * 8), wo1 = (size_t)k1 * DM + db * 64 + (((lane & 7) ^ wswz(k1)) * 8);
        const int att = tb * 2 + (wid & 1), aks = 2 * (wid >> 1);
        auto issue = [&](int it) { const unsigned so = lds0 + (it & 3) * F_SLOT;
            const uint16_t* wb = it < 8 ? wo + (size_t)it * 64 * DM : wout + (size_t)(it - 8) * 64 * DM;
            glds16(wb + wo0, so + wid * 2048); glds16(wb + wo1, so + wid * 2048 + 1024);
            const char* ab = it < 8 ? ws + WS_OF + ((size_t)att * 32 + it * 4 + aks) * 1024 : ws + WS_U + ((size_t)att * 56 + (it - 8) * 4 + aks) * 1024;
            glds16(ab + lane * 16, so + 8192 + (wid & 1) * 4096 + aks * 1024); glds16(ab + 1024 + lane * 16, so + 8192 + (wid & 1) * 4096 + aks * 1024 + 1024); };
        issue(0); issue(1); issue(2); issue(3);
        WAIT_BAR(12);
#pragma unroll 1
        for (int i = 1; i < 20; ++i) { WAIT_BAR(8); if (i + 3 < 22) issue(i + 3); }
        WAIT_BAR(4);
        WAIT_BAR(0);
        asm volatile("s_barrier" ::: "memory");
        return;
    }
    unsigned woff[2];
    { const int blk = (lane >> 4) & 1, q = (lane & 15) >> 2, p = lane & 3; const int kr = 4 * hh + q; const int ch = 4 * wc + 2 * blk + (p >> 1);
      woff[0] = kr * 128 + ((ch ^ wswz(kr)) * 16) + 8 * (p & 1); woff[1] = (woff[0] + 1024) ^ 32; }
    const unsigned aoff = 8192 + wr * 4096 + lane * 16;
    struct FF { bf16x8 w, a; };
    auto rd = [&](FF& f, int slot, int ks) { lds_cptr sb = (lds_cptr)lds + slot * F_SLOT;
        f.w = cat(trread(sb + woff[0] + ks * 2048), trread(sb + woff[1] + ks * 2048)); f.a = ldsfrag(sb + aoff + ks * 1024); };
    f32x16 as = f32x16{}, ag = f32x16{};
    FF f0, f1;
    asm volatile("s_barrier" ::: "memory");
    rd(f0, 0, 0);
    auto step = [&](int sl, f32x16& acc) {
        rd(f1, sl, 1); SBAR(); acc = MFMA(f0.w, f0.a, acc); SBAR();
        rd(f0, sl, 2); SBAR(); acc = MFMA(f1.w, f1.a, acc); SBAR();
        rd(f1, sl, 3); SBAR(); acc = MFMA(f0.w, f0.a, acc); SBAR();
        LGKM_BAR(); rd(f0, (sl + 1) & 3, 0); SBAR();
        acc = MFMA(f1.w, f1.a, acc); SBAR(); };
#pragma unroll 1
    for (int io = 0; io < 2; ++io) { step(0, as); step(1, as); step(2, as); step(3, as); }
#pragma unroll 1
    for (int io = 0; io < 3; ++io) { step(0, ag); step(1, ag); step(2, ag); step(3, ag); }
    step(0, ag); step(1, ag);
    STAMP(ws, 2);
    const int tok = tb * 64 + wr * 32 + l31;
#pragma unroll
    for (int q = 0; q < 4; ++q) { const size_t o = (size_t)tok * DM + db * 64 + wc * 32 + 8 * q + 4 * hh;
        const u32x2 xv = *(const u32x2*)(x + o);
        const float r0 = bflo(xv[0]) + as[4 * q] * silu_f(ag[4 * q]), r1 = bfhi(xv[0]) + as[4 * q + 1] * silu_f(ag[4 * q + 1]);
        const float r2 = bflo(xv[1]) + as[4 * q + 2] * silu_f(ag[4 * q + 2]), r3 = bfhi(xv[1]) + as[4 * q + 3] * silu_f(ag[4 * q + 3]);
        *(u32x2*)(out + o) = (u32x2){cvtpk(r0, r1), cvtpk(r2, r3)}; }
    STAMP(ws, 3);
}

extern "C" void kernel_launch(void* const* d_in, const int* in_sizes, int n_in, void* d_out, int out_size, void* d_ws, size_t ws_size, hipStream_t stream) {
    const uint16_t* x = (const uint16_t*)d_in[0]; const uint16_t* wqkv = (const uint16_t*)d_in[1]; const uint16_t* wo = (const uint16_t*)d_in[2];
    const uint16_t* win = (const uint16_t*)d_in[4]; const uint16_t* wsp = (const uint16_t*)d_in[5]; const uint16_t* bsp = (const uint16_t*)d_in[6]; const uint16_t* wout = (const uint16_t*)d_in[7];
    char* ws = (char*)d_ws;
    constexpr int MID_LDS = S_LDS > A_LDS ? S_LDS : A_LDS;
    (void)hipFuncSetAttribute((const void*)k_proj, hipFuncAttributeMaxDynamicSharedMemorySize, P_LDS);
    (void)hipFuncSetAttribute((const void*)k_mid, hipFuncAttributeMaxDynamicSharedMemorySize, MID_LDS);
    (void)hipFuncSetAttribute((const void*)k_fin, hipFuncAttributeMaxDynamicSharedMemorySize, F_LDS);
    k_proj<<<416, 512, P_LDS, stream>>>(x, wqkv, win, ws);
    k_mid<<<480, 512, MID_LDS, stream>>>(wsp, bsp, ws);
    k_fin<<<256, 512, F_LDS, stream>>>(x, wo, wout, ws, (uint16_t*)d_out);
}
```

```cpp
#include <hip/hip_runtime.h>
#include <hip/hip_bf16.h>
#include <stdint.h>

typedef short bf16x8 __attribute__((ext_vector_type(8)));
typedef short s16x4 __attribute__((ext_vector_type(4)));
typedef __bf16 bf16x2_t __attribute__((ext_vector_type(2)));
typedef float f32x16 __attribute__((ext_vector_type(16)));
typedef float f32x4 __attribute__((ext_vector_type(4)));
typedef float f32x2 __attribute__((ext_vector_type(2)));
typedef unsigned u32x4 __attribute__((ext_vector_type(4)));
typedef unsigned u32x2 __attribute__((ext_vector_type(2)));
typedef unsigned long long u64;
#define GAS __attribute__((address_space(1)))
#define LAS __attribute__((address_space(3)))
typedef LAS const char* lds_cptr;
typedef short v4i16_t __attribute__((ext_vector_type(4)));
#define RLX_AGENT __ATOMIC_RELAXED, __HIP_MEMORY_SCOPE_AGENT

constexpr int SEQ = 2048, DM = 512, NQKV = 1536, NIN = 1792, FH = 896;
constexpr float QSCALE = 0.25f * 1.4426950408889634f;
constexpr float LN_EPS = 1e-5f;
constexpr size_t MB = 1024 * 1024;
constexpr size_t WS_QF = 0, WS_KV = 2 * MB, WS_Z1 = 6 * MB, WS_Z2N = 10 * MB, WS_U = 14 * MB, WS_OF = 18 * MB;
constexpr size_t WS_KM = 20 * MB, WS_ST = 20 * MB + 65536, WS_RS = 20 * MB + 196608, WS_CS = 20 * MB + 229376  , WS_FLAG = 21 * MB;
constexpr int FL_RS = 0, FL_CS = 64, FL_QKV = 128, FL_ST = 320, FL_Z = 432, FL_O = 656, FL_U = 912, FL_G = 1136, FL_EPOCH = 1392;

#define MFMA(a, b, c) __builtin_amdgcn_mfma_f32_32x32x16_bf16(a, b, c, 0, 0, 0)
#define SBAR() __builtin_amdgcn_sched_barrier(0)
__device__ __forceinline__ unsigned cvtpk(float lo, float hi) { unsigned r; asm("v_cvt_pk_bf16_f32 %0, %1, %2" : "=v"(r) : "v"(lo), "v"(hi)); return r; }
__device__ __forceinline__ float bflo(unsigned u) { return __uint_as_float(u << 16); }
__device__ __forceinline__ float bfhi(unsigned u) { return __uint_as_float(u & 0xffff0000u); }
__device__ __forceinline__ float dot2(unsigned a, unsigned b, float acc) {
    return __builtin_amdgcn_fdot2_f32_bf16(__builtin_bit_cast(bf16x2_t, a), __builtin_bit_cast(bf16x2_t, b), acc, false); }
__device__ __forceinline__ void glds16(const void* g, unsigned lds_base) {
    asm volatile("s_mov_b32 m0, %1\n\ts_nop 0\n\tglobal_load_lds_dwordx4 %0, off" :: "v"(g), "s"(lds_base) : "memory", "m0"); }
__device__ __forceinline__ s16x4 trread(lds_cptr p) { return __builtin_bit_cast(s16x4, __builtin_amdgcn_ds_read_tr16_b64_v4i16((LAS v4i16_t*)p)); }
__device__ __forceinline__ bf16x8 ldsfrag(lds_cptr p) { return *(LAS const bf16x8*)p; }
__device__ __forceinline__ bf16x8 cat(s16x4 a, s16x4 b) { return (bf16x8){a[0], a[1], a[2], a[3], b[0], b[1], b[2], b[3]}; }
__device__ __forceinline__ float swap32_add(float v) { auto rr = __builtin_amdgcn_permlane32_swap(__float_as_uint(v), __float_as_uint(v), false, false); return __uint_as_float(rr[0]) + __uint_as_float(rr[1]); }
__device__ __forceinline__ float swap32_max(float v) { auto rr = __builtin_amdgcn_permlane32_swap(__float_as_uint(v), __float_as_uint(v), false, false); return fmaxf(__uint_as_float(rr[0]), __uint_as_float(rr[1])); }
template <int CTRL> __device__ __forceinline__ float dppf(float v) { return __int_as_float(__builtin_amdgcn_update_dpp(0, __float_as_int(v), CTRL, 0xf, 0xf, false)); }
__device__ __forceinline__ float row16_add(float v) { v += dppf<0x128>(v); v += dppf<0x124>(v); v += dppf<0x122>(v); v += dppf<0x121>(v); return v; }
__device__ __forceinline__ float row16_max(float v) { v = fmaxf(v, dppf<0x128>(v)); v = fmaxf(v, dppf<0x124>(v)); v = fmaxf(v, dppf<0x122>(v)); v = fmaxf(v, dppf<0x121>(v)); return v; }
__device__ __forceinline__ float swap16_max(float v) { auto rr = __builtin_amdgcn_permlane16_swap(__float_as_uint(v), __float_as_uint(v), false, false); return fmaxf(__uint_as_float(rr[0]), __uint_as_float(rr[1])); }
__device__ __forceinline__ float gelu_t(float x) {
    const float c1 = -2.3022082f, c2 = -0.10294324f;
    float e = __builtin_amdgcn_exp2f(x * __builtin_fmaf(x * x, c2, c1));
    return x * __builtin_amdgcn_rcpf(1.0f + e); }
__device__ __forceinline__ float silu_f(float x) { return x * __builtin_amdgcn_rcpf(1.0f + __builtin_amdgcn_exp2f(-1.4426950408889634f * x)); }
#define STAMP(slot) do {} while (0)
#define PF_DIST 9
#define WAITV(n) asm volatile("s_waitcnt vmcnt(" #n ")" ::: "memory")
#define WAIT_BAR(n) asm volatile("s_waitcnt vmcnt(" #n ")\n\ts_barrier" ::: "memory")
#define BAR() asm volatile("s_barrier" ::: "memory")
#define LGKM_BAR() do { __builtin_amdgcn_s_waitcnt(0xC07F); asm volatile("" ::: "memory"); __builtin_amdgcn_s_barrier(); asm volatile("" ::: "memory"); } while (0)
__device__ __forceinline__ unsigned wsw(unsigned row) { return ((row & 3) << 2) | ((row >> 2) & 3); }
__device__ __forceinline__ void st16_wt(void* p, u32x4 v) { asm volatile("global_store_dwordx4 %0, %1, off sc1\n\ts_nop 1" :: "v"(p), "v"(v) : "memory"); }
__device__ __forceinline__ void st8_wt(void* p, u64 v) { __hip_atomic_store((GAS u64*)p, v, RLX_AGENT); }
__device__ __forceinline__ u64 ld8_wt(const void* p) { return __hip_atomic_load((GAS u64*)p, RLX_AGENT); }
__device__ __forceinline__ u32x4 ld16_wt(const void* p) { u32x4 v; asm volatile("global_load_dwordx4 %0, %1, off sc1\n\ts_waitcnt vmcnt(0)" : "=&v"(v) : "v"(p) : "memory"); return v; }
__device__ __forceinline__ void glds16_wt(const void* g, unsigned lds_base) {
    asm volatile("s_mov_b32 m0, %1\n\ts_nop 0\n\tglobal_load_lds_dwordx4 %0, off sc1" :: "v"(g), "s"(lds_base) : "memory", "m0"); }
__device__ __forceinline__ GAS u64* flagp(char* ws, int idx) { return (GAS u64*)(ws + WS_FLAG) + idx * 16; }
__device__ __forceinline__ void wait_flags(GAS u64* f, bool want, u64 tag) {
    for (unsigned spins = 0; spins < (1u << 20); ++spins) {
        const u64 v = want ? __hip_atomic_load(f, RLX_AGENT) : tag;
        if (__all(v == tag)) return;
        __builtin_amdgcn_s_sleep(2); } }
#define ACQUIRE() do { __builtin_amdgcn_fence(__ATOMIC_ACQUIRE, "agent"); asm volatile("s_waitcnt vmcnt(0)" ::: "memory"); } while (0)
#define PUBLISH(idx) do { WAITV(0); __syncthreads(); if (threadIdx.x == 0) __hip_atomic_store(flagp(ws, idx), tag, RLX_AGENT); } while (0)

__device__ __forceinline__ void stats_role(const uint16_t* __restrict__ x, const uint16_t* __restrict__ wqkv, const uint16_t* __restrict__ win, char* __restrict__ ws, char* lds, int j, u64 tag) {
    const int tid = threadIdx.x, lane = tid & 63, wid = tid >> 6, rr = lane >> 4, cc = lane & 15;
    const unsigned ONES = 0x3f803f80u;
    u32x4 xv[4], w[8];
    const int row = 32 * j + 4 * wid + rr;
#pragma unroll
    for (int i = 0; i < 4; ++i) xv[i] = *(const u32x4*)(x + (size_t)row * DM + i * 128 + cc * 8);
    const int cbk = j >> 1;
    if (j < 52) {
        const uint16_t* W = cbk < 12 ? wqkv + cbk * 128 : win + (cbk - 12) * 128; const int ldw = cbk < 12 ? NQKV : NIN;
        const uint16_t* p = W + (size_t)((j & 1) * 256 + 32 * wid + rr) * ldw + cc * 8;
#pragma unroll
        for (int i = 0; i < 8; ++i) w[i] = *(const u32x4*)(p + (size_t)(i * 4) * ldw);
    }
    float s1 = 0.f, s2 = 0.f;
#pragma unroll
    for (int i = 0; i < 4; ++i)
#pragma unroll
        for (int e = 0; e < 4; ++e) { s1 = dot2(xv[i][e], ONES, s1); s2 = dot2(xv[i][e], xv[i][e], s2); }
    s1 = row16_add(s1); s2 = row16_add(s2);
    { const float mu = s1 * (1.0f / DM), var = fmaxf(s2 * (1.0f / DM) - mu * mu, 0.f);
      if (cc == 0) st8_wt(ws + WS_RS + (size_t)row * 8, __builtin_bit_cast(u64, (f32x2){mu, __builtin_amdgcn_rsqf(var + LN_EPS)})); }
    if (j < 52) {
        float cs[8] = {0.f, 0.f, 0.f, 0.f, 0.f, 0.f, 0.f, 0.f};
        unsigned sel_lo = 0x00003f80u, sel_hi = 0x3f800000u;
        asm volatile("" : "+v"(sel_lo), "+v"(sel_hi));
#pragma unroll
        for (int i = 0; i < 8; ++i)
#pragma unroll
            for (int e = 0; e < 4; ++e) { cs[2 * e] = dot2(w[i][e], sel_lo, cs[2 * e]); cs[2 * e + 1] = dot2(w[i][e], sel_hi, cs[2 * e + 1]); }
        float* red = (float*)lds;
        *(f32x4*)(red + (wid * 4 + rr) * 128 + cc * 8) = (f32x4){cs[0], cs[1], cs[2], cs[3]}; *(f32x4*)(red + (wid * 4 + rr) * 128 + cc * 8 + 4) = (f32x4){cs[4], cs[5], cs[6], cs[7]};
        __syncthreads();
        if (tid < 64) { f32x2 a = {0.f, 0.f};
#pragma unroll
            for (int q = 0; q < 32; ++q) a += *(const f32x2*)(red + q * 128 + 2 * tid);
            st8_wt(ws + WS_CS + (size_t)((j & 1) * 3328 + cbk * 128 + 2 * tid) * 4, __builtin_bit_cast(u64, a)); }
    }
    STAMP(11);
    WAITV(0); __syncthreads();
    STAMP(1);
    if (tid == 0) { __hip_atomic_store(flagp(ws, FL_RS + j), tag, RLX_AGENT); if (j < 52) __hip_atomic_store(flagp(ws, FL_CS + j), tag, RLX_AGENT); }
}

constexpr int P_SLOT = 16384, P_ST = 4 * P_SLOT, P_LDS = 4 * P_SLOT + 4096;
struct PFrag { bf16x8 x[2], w; };
template <bool SWAP>
__device__ __forceinline__ void proj_main(const uint16_t* __restrict__ x, const uint16_t* __restrict__ W, int ldw, int rb, int col0, int cblk, char* __restrict__ ws, char* lds, int rot, u64 tag, f32x16 (&acc)[2]) {
    const int tid = threadIdx.x, lane = tid & 63, wid = __builtin_amdgcn_readfirstlane(tid >> 6), wr = wid >> 2, wc = wid & 3, l31 = lane & 31, hh = lane >> 5;
    const unsigned lds0 = (unsigned)(uintptr_t)lds;
    const int xr = 16 * wid + (lane >> 2), wk = 4 * wid + (lane >> 4);
    const uint16_t* xs = x + (size_t)(rb * 128 + xr) * DM + (((lane & 3) ^ ((xr >> 2) & 3)) * 8);
    const uint16_t* wsrc = W + (size_t)wk * ldw + col0 + (((lane & 15) ^ wsw(wk)) * 8);
    const unsigned xd = lds0 + wid * 1024, wd = lds0 + 8192 + wid * 1024;
    auto issue = [&](int it) { const unsigned so = (it & 3) * P_SLOT; const int kk = (it + rot) & 15;
        glds16(xs + kk * 32, xd + so); glds16(wsrc + (size_t)kk * 32 * ldw, wd + so); };
    issue(0); issue(1); issue(2); issue(3);
    unsigned xoff[2][2], woff[2][2];
    const int blk = (lane >> 4) & 1, q = (lane & 15) >> 2, p = lane & 3;
#pragma unroll
    for (int ks = 0; ks < 2; ++ks) {
#pragma unroll
        for (int rt = 0; rt < 2; ++rt) { const int r = wr * 64 + rt * 32 + l31; xoff[rt][ks] = r * 64 + (((2 * ks + hh) ^ ((r >> 2) & 3)) * 16); }
        const int kr = 8 * hh + q, ch = 4 * wc + 2 * blk + (p >> 1);
        woff[0][ks] = 8192 + ks * 4096 + kr * 256 + ((ch ^ wsw(kr)) * 16) + 8 * (p & 1); woff[1][ks] = (woff[0][ks] + 1024) ^ 16; }
    auto rd = [&](PFrag& f, int slot, int j) { lds_cptr sb = (lds_cptr)lds + slot * P_SLOT;
        f.x[0] = ldsfrag(sb + xoff[0][j]); f.x[1] = ldsfrag(sb + xoff[1][j]); f.w = cat(trread(sb + woff[0][j]), trread(sb + woff[1][j])); };
    auto mm = [&](const PFrag& f) {
#pragma unroll
        for (int rt = 0; rt < 2; ++rt) acc[rt] = SWAP ? MFMA(f.x[rt], f.w, acc[rt]) : MFMA(f.w, f.x[rt], acc[rt]); };
    acc[0] = f32x16{}; acc[1] = f32x16{};
    PFrag fa[2], fb[2];
    WAIT_BAR(6);
    STAMP(10);
    rd(fa[0], 0, 0); rd(fa[1], 0, 1);
    auto mm1 = [&](const PFrag& f, int rt) { acc[rt] = SWAP ? MFMA(f.x[rt], f.w, acc[rt]) : MFMA(f.w, f.x[rt], acc[rt]); SBAR(); };
    auto issue1 = [&](int it, int which) { const unsigned so = (it & 3) * P_SLOT; const int kk = (it + rot) & 15;
        if (which == 0) glds16(xs + kk * 32, xd + so); else glds16(wsrc + (size_t)kk * 32 * ldw, wd + so); SBAR(); };
    auto step = [&](int it, int sl, PFrag (&cur)[2], PFrag (&nxt)[2]) {
        __builtin_amdgcn_s_waitcnt(0xC07F);
        if (it < 13) WAITV(4); else if (it == 13) WAITV(2); else WAITV(0);
        __builtin_amdgcn_s_barrier(); asm volatile("" ::: "memory");
        lds_cptr sb = (lds_cptr)lds + ((sl + 1) & 3) * P_SLOT;
        mm1(cur[0], 0);
        nxt[0].x[0] = ldsfrag(sb + xoff[0][0]); nxt[0].x[1] = ldsfrag(sb + xoff[1][0]); SBAR();
        mm1(cur[0], 1);
        nxt[0].w = cat(trread(sb + woff[0][0]), trread(sb + woff[1][0])); SBAR();
        if (it + 4 < 16) issue1(it + 4, 0);
        mm1(cur[1], 0);
        nxt[1].x[0] = ldsfrag(sb + xoff[0][1]); nxt[1].x[1] = ldsfrag(sb + xoff[1][1]); SBAR();
        mm1(cur[1], 1);
        nxt[1].w = cat(trread(sb + woff[0][1]), trread(sb + woff[1][1])); SBAR();
        if (it + 4 < 16) issue1(it + 4, 1); };
    u64 stv = 0, stw = 0;
#pragma unroll 1
    for (int io = 0; io < 4; ++io) {
        step(io * 4, 0, fa, fb); step(io * 4 + 1, 1, fb, fa);
        if (io == 2 && wid < 3) {
            wait_flags(lane < 4 ? flagp(ws, FL_RS + rb * 4 + lane) : flagp(ws, FL_CS + 2 * cblk + (lane & 1)), lane < 6, tag);
            stv = ld8_wt(tid < 128 ? ws + WS_RS + (size_t)(rb * 128 + tid) * 8 : ws + WS_CS + (size_t)(cblk * 128 + 2 * (tid - 128)) * 4);
            stw = ld8_wt(ws + WS_CS + (size_t)(3328 + cblk * 128 + 2 * (tid & 63)) * 4); }
        step(io * 4 + 2, 2, fa, fb); step(io * 4 + 3, 3, fb, fa); }
    STAMP(11);
    float* st = (float*)(lds + P_ST);
    { const f32x2 v = __builtin_bit_cast(f32x2, stv);
      if (tid < 128) { st[tid] = v[0]; st[128 + tid] = v[1]; } else if (tid < 192) *(f32x2*)(st + 256 + 2 * (tid - 128)) = v + __builtin_bit_cast(f32x2, stw); }
    __syncthreads();
}

__device__ __forceinline__ void qkv_role(const uint16_t* __restrict__ x, const uint16_t* __restrict__ wqkv, char* __restrict__ ws, char* lds, int b, u64 tag) {
    const int xcd = b & 7, i = b >> 3;
    const int rb = 4 * (xcd >> 1) + (i & 3), cb = 6 * (xcd & 1) + (i >> 2);
    const int tid = threadIdx.x, lane = tid & 63, wid = __builtin_amdgcn_readfirstlane(tid >> 6), wr = wid >> 2, wc = wid & 3, l31 = lane & 31, hh = lane >> 5;
    f32x16 acc[2];
    const int rot = ((i & 3) * 4 + (i >> 2) * 3) & 15;
    if (cb >= 8) proj_main<true>(x, wqkv, NQKV, rb, cb * 128, cb, ws, lds, rot, tag, acc); else proj_main<false>(x, wqkv, NQKV, rb, cb * 128, cb, ws, lds, rot, tag, acc);
    STAMP(1);
    const float* st = (const float*)(lds + P_ST);
    if (cb >= 8) {
        const float cs = st[256 + wc * 32 + l31];
        const int head = (cb - 8) * 8 + wc * 2 + (l31 >> 4);
#pragma unroll
        for (int rt = 0; rt < 2; ++rt) { const int tr = wr * 64 + rt * 32;
            unsigned pk[8];
#pragma unroll
            for (int q = 0; q < 4; ++q) { const f32x4 m = *(const f32x4*)(st + tr + 8 * q + 4 * hh), r = *(const f32x4*)(st + 128 + tr + 8 * q + 4 * hh);
                pk[2 * q] = cvtpk((acc[rt][4 * q] - m[0] * cs) * r[0], (acc[rt][4 * q + 1] - m[1] * cs) * r[1]);
                pk[2 * q + 1] = cvtpk((acc[rt][4 * q + 2] - m[2] * cs) * r[2], (acc[rt][4 * q + 3] - m[3] * cs) * r[3]); }
            const int tile = rb * 4 + (tr >> 5);
            char* dst = ws + WS_KV + (size_t)head * 131072 + (tile >> 3) * 16384 + 8192 + (tile & 7) * 1024 + hh * 256 + (l31 & 15) * 16;
            st16_wt(dst, (u32x4){pk[0], pk[1], pk[2], pk[3]}); st16_wt(dst + 512, (u32x4){pk[4], pk[5], pk[6], pk[7]}); }
    } else {
        const bool isK = cb >= 4; const float sc = isK ? 1.0f : QSCALE;
        float kmx[2] = {0.f, 0.f};
#pragma unroll
        for (int rt = 0; rt < 2; ++rt) { const int tr = wr * 64 + rt * 32, tile = rb * 4 + (tr >> 5);
            const float mu = st[tr + l31], rs = st[128 + tr + l31] * sc;
#pragma unroll
            for (int hb = 0; hb < 2; ++hb) {
                float v[8];
#pragma unroll
                for (int q = 0; q < 2; ++q) { const f32x4 c = *(const f32x4*)(st + 256 + wc * 32 + 16 * hb + 8 * q + 4 * hh);
#pragma unroll
                    for (int e = 0; e < 4; ++e) v[4 * q + e] = (acc[rt][8 * hb + 4 * q + e] - mu * c[e]) * rs; }
                u32x4 pk;
#pragma unroll
                for (int e = 0; e < 4; ++e) pk[e] = cvtpk(v[2 * e], v[2 * e + 1]);
                const int head = (cb & 3) * 8 + wc * 2 + hb;
                char* dst = isK ? ws + WS_KV + (size_t)head * 131072 + (tile >> 3) * 16384 + (tile & 7) * 1024 + lane * 16 : ws + WS_QF + (size_t)head * 65536 + tile * 1024 + lane * 16;
                st16_wt(dst, pk);
                float n2 = 0.f;
#pragma unroll
                for (int e = 0; e < 4; ++e) n2 = dot2(pk[e], pk[e], n2);
                kmx[hb] = fmaxf(kmx[hb], swap32_add(n2));
            } }
        if (isK) {
#pragma unroll
            for (int hb = 0; hb < 2; ++hb) { const float m = swap16_max(row16_max(kmx[hb]));
                if (lane == 0) __hip_atomic_store((GAS unsigned*)(ws + WS_KM) + ((cb & 3) * 8 + wc * 2 + hb) * 32 + rb * 2 + wr, __float_as_uint(m), RLX_AGENT); } }
    }
    STAMP(12);
    PUBLISH(FL_QKV + cb * 16 + rb);
    STAMP(13);
}

constexpr int A_ONES = 3 * 16384, A_LDS = A_ONES + 8192 + 512;
__device__ __forceinline__ void attn_role(char* __restrict__ ws, char* lds, int b, u64 tag) {
    const int tid = threadIdx.x, lane = tid & 63, wid = __builtin_amdgcn_readfirstlane(tid >> 6);
    const int head = (b & 7) * 4 + ((b >> 3) & 3), jb = b >> 5;
    const unsigned lds0 = (unsigned)(uintptr_t)lds;
    const char* kvb = ws + WS_KV + (size_t)head * 131072;
    for (int i = tid; i < (8192 + 512) / 16; i += 512) { const unsigned o1 = ((i & 11) == 0) ? 0x3f803f80u : 0u; *(u32x4*)(lds + A_ONES + i * 16) = (u32x4){o1, o1, o1, o1}; }
    if (wid == 0) {
        const int hb = head >> 3;
        const int idx = lane < 16 ? (4 + hb) * 16 + lane : lane < 32 ? (8 + hb) * 16 + (lane - 16) : hb * 16 + 2 * jb + (lane & 1);
        wait_flags(flagp(ws, FL_QKV + idx), lane < 34, tag);
        STAMP(14);
        }
    __syncthreads();
    STAMP(2);
    unsigned goff = wid * 2048 + lane * 16;
#pragma unroll
    for (int c = 0; c < 2; ++c)
#pragma unroll
        for (int k = 0; k < 2; ++k) glds16_wt(kvb + c * 16384 + goff + k * 1024, lds0 + c * 16384 + wid * 2048 + k * 1024);
    const int tile0 = jb * 8 + wid;
    u32x4 qa; unsigned kmu;
    asm volatile("global_load_dwordx4 %0, %2, off sc1\n\tglobal_load_dword %1, %3, off sc1\n\ts_waitcnt vmcnt(0)" : "=&v"(qa), "=&v"(kmu)
                 : "v"(ws + WS_QF + (size_t)head * 65536 + tile0 * 1024 + lane * 16), "v"(ws + WS_KM + (size_t)(head * 32 + (lane & 31)) * 4) : "memory");
    const float km = swap16_max(row16_max(__uint_as_float(kmu)));
    float na = 0.f;
#pragma unroll
    for (int e = 0; e < 4; ++e) na = dot2(qa[e], qa[e], na);
    na = swap32_add(na);
    float ma = __builtin_amdgcn_sqrtf(na * km);
    if (__any(!(ma <= 40.0f))) {
        float xa = -3.0e38f;
        for (int t = 0; t < 64; ++t) {
            const bf16x8 kf = __builtin_bit_cast(bf16x8, ld16_wt(kvb + (t >> 3) * 16384 + (t & 7) * 1024 + lane * 16));
            const f32x16 sa = MFMA(kf, __builtin_bit_cast(bf16x8, qa), f32x16{});
#pragma unroll
            for (int r = 0; r < 16; ++r) xa = fmaxf(xa, sa[r]); }
        ma = swap32_max(xa);
    }
    const float nma = -ma;
    const unsigned kaddr = lds0 + lane * 16;
    const bool ones = (lane & 16) != 0;
    const unsigned vaddr = ones ? lds0 + A_ONES + (lane & 15) * 16 : lds0 + 8192 + (lane >> 5) * 256 + (lane & 15) * 16;
    const unsigned vsel = ones ? 0u : 0xffffffffu;
    goff += 2 * 16384;
    unsigned dbeg = lds0 + wid * 2048, dend = dbeg + 3 * 16384, dslot = dbeg + 2 * 16384, nslot = 0x4000, cnt, m0save;
    unsigned oa0, oa1, oa2, oa3;
    STAMP(3);
    asm volatile(
    "s_mov_b32 %[m0save], m0\n"
    "v_mov_b32 v96, %[qa0]\n"
    "v_mov_b32 v97, %[qa1]\n"
    "v_mov_b32 v98, %[qa2]\n"
    "v_mov_b32 v99, %[qa3]\n"
    "v_mov_b32 v72, %[nma]\n"
    "v_mov_b32 v56, 0\n"
    "v_mov_b32 v73, %[nma]\n"
    "v_mov_b32 v57, 0\n"
    "v_mov_b32 v74, %[nma]\n"
    "v_mov_b32 v58, 0\n"
    "v_mov_b32 v75, %[nma]\n"
    "v_mov_b32 v59, 0\n"
    "v_mov_b32 v76, %[nma]\n"
    "v_mov_b32 v60, 0\n"
    "v_mov_b32 v77, %[nma]\n"
    "v_mov_b32 v61, 0\n"
    "v_mov_b32 v78, %[nma]\n"
    "v_mov_b32 v62, 0\n"
    "v_mov_b32 v79, %[nma]\n"
    "v_mov_b32 v63, 0\n"
    "v_mov_b32 v80, %[nma]\n"
    "v_mov_b32 v64, 0\n"
    "v_mov_b32 v81, %[nma]\n"
    "v_mov_b32 v65, 0\n"
    "v_mov_b32 v82, %[nma]\n"
    "v_mov_b32 v66, 0\n"
    "v_mov_b32 v83, %[nma]\n"
    "v_mov_b32 v67, 0\n"
    "v_mov_b32 v84, %[nma]\n"
    "v_mov_b32 v68, 0\n"
    "v_mov_b32 v85, %[nma]\n"
    "v_mov_b32 v69, 0\n"
    "v_mov_b32 v86, %[nma]\n"
    "v_mov_b32 v70, 0\n"
    "v_mov_b32 v87, %[nma]\n"
    "v_mov_b32 v71, 0\n"
    "v_mov_b32 v120, 0\n"
    "v_mov_b32 v121, 0\n"
    "v_mov_b32 v122, 0\n"
    "v_mov_b32 v123, 0\n"
    "v_mov_b32 v88, 0\n"
    "v_mov_b32 v89, 0\n"
    "v_mov_b32 v90, 0\n"
    "v_mov_b32 v91, 0\n"
    "v_mov_b32 v92, 0\n"
    "v_mov_b32 v93, 0\n"
    "v_mov_b32 v94, 0\n"
    "v_mov_b32 v95, 0\n"
    "v_mov_b32 v124, %[kaddr]\n"
    "v_mov_b32 v125, %[vaddr]\n"
    "v_add_u32 v126, 0x4000, v124\n"
    "v_and_b32 v127, 0x4000, %[vsel]\n"
    "v_add_u32 v127, v127, v125\n"
    "s_waitcnt vmcnt(2) lgkmcnt(0)\n"
    "s_barrier\n"
    "ds_read_b128 v[100:103], v124\n"
    "ds_read_b128 v[104:107], v124 offset:1024\n"
    "ds_read_b128 v[108:111], v125\n"
    "ds_read_b128 v[112:115], v125 offset:512\n"
    "s_mov_b32 %[cnt], 0\n"
    "s_waitcnt lgkmcnt(0)\n"
    "v_mfma_f32_32x32x16_bf16 v[24:39], v[100:103], v[96:99], v[72:87]\n"
    "s_nop 15\n"
    ".Lloop%=:\n"
    "v_exp_f32 v24, v24\n"
    "v_exp_f32 v25, v25\n"
    "v_mfma_f32_32x32x16_bf16 v[56:71], v[120:123], v[92:95], v[56:71]\n"
    "v_exp_f32 v26, v26\n"
    "v_exp_f32 v27, v27\n"
    "v_cvt_pk_bf16_f32 v88, v24, v25\n"
    "v_exp_f32 v28, v28\n"
    "s_waitcnt lgkmcnt(0)\n"
    "v_mfma_f32_32x32x16_bf16 v[40:55], v[104:107], v[96:99], v[72:87]\n"
    "v_exp_f32 v29, v29\n"
    "v_cvt_pk_bf16_f32 v89, v26, v27\n"
    "ds_read_b128 v[100:103], v124 offset:2048\n"
    "v_exp_f32 v30, v30\n"
    "ds_read_b128 v[116:119], v125 offset:1024\n"
    "v_exp_f32 v31, v31\n"
    "v_cvt_pk_bf16_f32 v90, v28, v29\n"
    "ds_read_b128 v[120:123], v125 offset:1536\n"
    "v_exp_f32 v32, v32\n"
    "v_exp_f32 v33, v33\n"
    "v_cvt_pk_bf16_f32 v91, v30, v31\n"
    "v_exp_f32 v34, v34\n"
    "v_exp_f32 v35, v35\n"
    "v_mfma_f32_32x32x16_bf16 v[56:71], v[108:111], v[88:91], v[56:71]\n"
    "v_cvt_pk_bf16_f32 v92, v32, v33\n"
    "v_exp_f32 v36, v36\n"
    "v_exp_f32 v37, v37\n"
    "v_cvt_pk_bf16_f32 v93, v34, v35\n"
    "v_exp_f32 v38, v38\n"
    "v_exp_f32 v39, v39\n"
    "v_cvt_pk_bf16_f32 v94, v36, v37\n"
    "v_cvt_pk_bf16_f32 v95, v38, v39\n"
    "v_exp_f32 v40, v40\n"
    "v_exp_f32 v41, v41\n"
    "v_mfma_f32_32x32x16_bf16 v[56:71], v[112:115], v[92:95], v[56:71]\n"
    "v_exp_f32 v42, v42\n"
    "v_exp_f32 v43, v43\n"
    "v_cvt_pk_bf16_f32 v88, v40, v41\n"
    "v_exp_f32 v44, v44\n"
    "s_waitcnt lgkmcnt(0)\n"
    "v_mfma_f32_32x32x16_bf16 v[24:39], v[100:103], v[96:99], v[72:87]\n"
    "v_exp_f32 v45, v45\n"
    "v_cvt_pk_bf16_f32 v89, v42, v43\n"
    "ds_read_b128 v[104:107], v124 offset:3072\n"
    "v_exp_f32 v46, v46\n"
    "ds_read_b128 v[108:111], v125 offset:2048\n"
    "v_exp_f32 v47, v47\n"
    "v_cvt_pk_bf16_f32 v90, v44, v45\n"
    "ds_read_b128 v[112:115], v125 offset:2560\n"
    "v_exp_f32 v48, v48\n"
    "v_exp_f32 v49, v49\n"
    "v_cvt_pk_bf16_f32 v91, v46, v47\n"
    "v_exp_f32 v50, v50\n"
    "v_exp_f32 v51, v51\n"
    "v_mfma_f32_32x32x16_bf16 v[56:71], v[116:119], v[88:91], v[56:71]\n"
    "v_cvt_pk_bf16_f32 v92, v48, v49\n"
    "v_exp_f32 v52, v52\n"
    "v_exp_f32 v53, v53\n"
    "v_cvt_pk_bf16_f32 v93, v50, v51\n"
    "v_exp_f32 v54, v54\n"
    "v_exp_f32 v55, v55\n"
    "v_cvt_pk_bf16_f32 v94, v52, v53\n"
    "v_cvt_pk_bf16_f32 v95, v54, v55\n"
    "v_exp_f32 v24, v24\n"
    "v_exp_f32 v25, v25\n"
    "v_mfma_f32_32x32x16_bf16 v[56:71], v[120:123], v[92:95], v[56:71]\n"
    "v_exp_f32 v26, v26\n"
    "v_exp_f32 v27, v27\n"
    "v_cvt_pk_bf16_f32 v88, v24, v25\n"
    "v_exp_f32 v28, v28\n"
    "s_waitcnt lgkmcnt(0)\n"
    "v_mfma_f32_32x32x16_bf16 v[40:55], v[104:107], v[96:99], v[72:87]\n"
    "v_exp_f32 v29, v29\n"
    "v_cvt_pk_bf16_f32 v89, v26, v27\n"
    "ds_read_b128 v[100:103], v124 offset:4096\n"
    "v_exp_f32 v30, v30\n"
    "ds_read_b128 v[116:119], v125 offset:3072\n"
    "v_exp_f32 v31, v31\n"
    "v_cvt_pk_bf16_f32 v90, v28, v29\n"
    "ds_read_b128 v[120:123], v125 offset:3584\n"
    "v_exp_f32 v32, v32\n"
    "v_exp_f32 v33, v33\n"
    "v_cvt_pk_bf16_f32 v91, v30, v31\n"
    "v_exp_f32 v34, v34\n"
    "v_exp_f32 v35, v35\n"
    "v_mfma_f32_32x32x16_bf16 v[56:71], v[108:111], v[88:91], v[56:71]\n"
    "v_cvt_pk_bf16_f32 v92, v32, v33\n"
    "v_exp_f32 v36, v36\n"
    "v_exp_f32 v37, v37\n"
    "v_cvt_pk_bf16_f32 v93, v34, v35\n"
    "v_exp_f32 v38, v38\n"
    "v_exp_f32 v39, v39\n"
    "v_cvt_pk_bf16_f32 v94, v36, v37\n"
    "v_cvt_pk_bf16_f32 v95, v38, v39\n"
    "v_exp_f32 v40, v40\n"
    "v_exp_f32 v41, v41\n"
    "v_mfma_f32_32x32x16_bf16 v[56:71], v[112:115], v[92:95], v[56:71]\n"
    "v_exp_f32 v42, v42\n"
    "v_exp_f32 v43, v43\n"
    "v_cvt_pk_bf16_f32 v88, v40, v41\n"
    "v_exp_f32 v44, v44\n"
    "s_waitcnt lgkmcnt(0)\n"
    "v_mfma_f32_32x32x16_bf16 v[24:39], v[100:103], v[96:99], v[72:87]\n"
    "v_exp_f32 v45, v45\n"
    "v_cvt_pk_bf16_f32 v89, v42, v43\n"
    "ds_read_b128 v[104:107], v124 offset:5120\n"
    "v_exp_f32 v46, v46\n"
    "ds_read_b128 v[108:111], v125 offset:4096\n"
    "v_exp_f32 v47, v47\n"
    "v_cvt_pk_bf16_f32 v90, v44, v45\n"
    "ds_read_b128 v[112:115], v125 offset:4608\n"
    "v_exp_f32 v48, v48\n"
    "v_exp_f32 v49, v49\n"
    "v_cvt_pk_bf16_f32 v91, v46, v47\n"
    "v_exp_f32 v50, v50\n"
    "v_exp_f32 v51, v51\n"
    "v_mfma_f32_32x32x16_bf16 v[56:71], v[116:119], v[88:91], v[56:71]\n"
    "v_cvt_pk_bf16_f32 v92, v48, v49\n"
    "v_exp_f32 v52, v52\n"
    "v_exp_f32 v53, v53\n"
    "v_cvt_pk_bf16_f32 v93, v50, v51\n"
    "v_exp_f32 v54, v54\n"
    "v_exp_f32 v55, v55\n"
    "v_cvt_pk_bf16_f32 v94, v52, v53\n"
    "v_cvt_pk_bf16_f32 v95, v54, v55\n"
    "v_exp_f32 v24, v24\n"
    "v_exp_f32 v25, v25\n"
    "v_mfma_f32_32x32x16_bf16 v[56:71], v[120:123], v[92:95], v[56:71]\n"
    "v_exp_f32 v26, v26\n"
    "v_exp_f32 v27, v27\n"
    "v_cvt_pk_bf16_f32 v88, v24, v25\n"
    "v_exp_f32 v28, v28\n"
    "s_waitcnt lgkmcnt(0)\n"
    "v_mfma_f32_32x32x16_bf16 v[40:55], v[104:107], v[96:99], v[72:87]\n"
    "v_exp_f32 v29, v29\n"
    "v_cvt_pk_bf16_f32 v89, v26, v27\n"
    "ds_read_b128 v[100:103], v124 offset:6144\n"
    "v_exp_f32 v30, v30\n"
    "ds_read_b128 v[116:119], v125 offset:5120\n"
    "v_exp_f32 v31, v31\n"
    "v_cvt_pk_bf16_f32 v90, v28, v29\n"
    "ds_read_b128 v[120:123], v125 offset:5632\n"
    "v_exp_f32 v32, v32\n"
    "v_exp_f32 v33, v33\n"
    "v_cvt_pk_bf16_f32 v91, v30, v31\n"
    "v_exp_f32 v34, v34\n"
    "v_exp_f32 v35, v35\n"
    "v_mfma_f32_32x32x16_bf16 v[56:71], v[108:111], v[88:91], v[56:71]\n"
    "v_cvt_pk_bf16_f32 v92, v32, v33\n"
    "v_exp_f32 v36, v36\n"
    "v_exp_f32 v37, v37\n"
    "v_cvt_pk_bf16_f32 v93, v34, v35\n"
    "v_exp_f32 v38, v38\n"
    "v_exp_f32 v39, v39\n"
    "v_cvt_pk_bf16_f32 v94, v36, v37\n"
    "v_cvt_pk_bf16_f32 v95, v38, v39\n"
    "v_exp_f32 v40, v40\n"
    "v_exp_f32 v41, v41\n"
    "v_mfma_f32_32x32x16_bf16 v[56:71], v[112:115], v[92:95], v[56:71]\n"
    "v_exp_f32 v42, v42\n"
    "v_exp_f32 v43, v43\n"
    "v_cvt_pk_bf16_f32 v88, v40, v41\n"
    "v_exp_f32 v44, v44\n"
    "s_waitcnt lgkmcnt(0)\n"
    "v_mfma_f32_32x32x16_bf16 v[24:39], v[100:103], v[96:99], v[72:87]\n"
    "v_exp_f32 v45, v45\n"
    "v_cvt_pk_bf16_f32 v89, v42, v43\n"
    "ds_read_b128 v[104:107], v124 offset:7168\n"
    "v_exp_f32 v46, v46\n"
    "ds_read_b128 v[108:111], v125 offset:6144\n"
    "v_exp_f32 v47, v47\n"
    "v_cvt_pk_bf16_f32 v90, v44, v45\n"
    "ds_read_b128 v[112:115], v125 offset:6656\n"
    "v_exp_f32 v48, v48\n"
    "v_exp_f32 v49, v49\n"
    "v_cvt_pk_bf16_f32 v91, v46, v47\n"
    "v_exp_f32 v50, v50\n"
    "v_exp_f32 v51, v51\n"
    "v_mfma_f32_32x32x16_bf16 v[56:71], v[116:119], v[88:91], v[56:71]\n"
    "v_cvt_pk_bf16_f32 v92, v48, v49\n"
    "v_exp_f32 v52, v52\n"
    "v_exp_f32 v53, v53\n"
    "v_cvt_pk_bf16_f32 v93, v50, v51\n"
    "v_exp_f32 v54, v54\n"
    "v_exp_f32 v55, v55\n"
    "v_cvt_pk_bf16_f32 v94, v52, v53\n"
    "v_cvt_pk_bf16_f32 v95, v54, v55\n"
    "v_exp_f32 v24, v24\n"
    "v_exp_f32 v25, v25\n"
    "v_mfma_f32_32x32x16_bf16 v[56:71], v[120:123], v[92:95], v[56:71]\n"
    "v_exp_f32 v26, v26\n"
    "v_exp_f32 v27, v27\n"
    "v_cvt_pk_bf16_f32 v88, v24, v25\n"
    "v_exp_f32 v28, v28\n"
    "s_waitcnt lgkmcnt(0)\n"
    "v_mfma_f32_32x32x16_bf16 v[40:55], v[104:107], v[96:99], v[72:87]\n"
    "v_exp_f32 v29, v29\n"
    "v_cvt_pk_bf16_f32 v89, v26, v27\n"
    "s_waitcnt vmcnt(0)\n"
    "s_barrier\n"
    "ds_read_b128 v[100:103], v126 offset:0\n"
    "v_exp_f32 v30, v30\n"
    "ds_read_b128 v[116:119], v125 offset:7168\n"
    "v_exp_f32 v31, v31\n"
    "v_cvt_pk_bf16_f32 v90, v28, v29\n"
    "ds_read_b128 v[120:123], v125 offset:7680\n"
    "v_exp_f32 v32, v32\n"
    "v_exp_f32 v33, v33\n"
    "v_cvt_pk_bf16_f32 v91, v30, v31\n"
    "v_exp_f32 v34, v34\n"
    "v_exp_f32 v35, v35\n"
    "v_mfma_f32_32x32x16_bf16 v[56:71], v[108:111], v[88:91], v[56:71]\n"
    "s_cmp_ge_u32 %[cnt], 6\n"
    "s_cbranch_scc1 .Lnodma%=\n"
    "s_mov_b32 m0, %[dslot]\n"
    "s_nop 0\n"
    "global_load_lds_dwordx4 %[goff], %[gbase] offset:0\n"
    "global_load_lds_dwordx4 %[goff], %[gbase] offset:1024\n"
    ".Lnodma%=:\n"
    "v_cvt_pk_bf16_f32 v92, v32, v33\n"
    "v_exp_f32 v36, v36\n"
    "v_exp_f32 v37, v37\n"
    "v_cvt_pk_bf16_f32 v93, v34, v35\n"
    "v_exp_f32 v38, v38\n"
    "v_exp_f32 v39, v39\n"
    "v_cvt_pk_bf16_f32 v94, v36, v37\n"
    "v_cvt_pk_bf16_f32 v95, v38, v39\n"
    "v_exp_f32 v40, v40\n"
    "v_exp_f32 v41, v41\n"
    "v_mfma_f32_32x32x16_bf16 v[56:71], v[112:115], v[92:95], v[56:71]\n"
    "v_exp_f32 v42, v42\n"
    "v_exp_f32 v43, v43\n"
    "v_cvt_pk_bf16_f32 v88, v40, v41\n"
    "v_exp_f32 v44, v44\n"
    "s_waitcnt lgkmcnt(0)\n"
    "v_mfma_f32_32x32x16_bf16 v[24:39], v[100:103], v[96:99], v[72:87]\n"
    "v_exp_f32 v45, v45\n"
    "v_cvt_pk_bf16_f32 v89, v42, v43\n"
    "ds_read_b128 v[104:107], v126 offset:1024\n"
    "v_exp_f32 v46, v46\n"
    "ds_read_b128 v[108:111], v127 offset:0\n"
    "v_exp_f32 v47, v47\n"
    "v_cvt_pk_bf16_f32 v90, v44, v45\n"
    "ds_read_b128 v[112:115], v127 offset:512\n"
    "v_exp_f32 v48, v48\n"
    "v_exp_f32 v49, v49\n"
    "v_cvt_pk_bf16_f32 v91, v46, v47\n"
    "v_exp_f32 v50, v50\n"
    "v_exp_f32 v51, v51\n"
    "v_mfma_f32_32x32x16_bf16 v[56:71], v[116:119], v[88:91], v[56:71]\n"
    "v_cvt_pk_bf16_f32 v92, v48, v49\n"
    "v_exp_f32 v52, v52\n"
    "v_exp_f32 v53, v53\n"
    "v_cvt_pk_bf16_f32 v93, v50, v51\n"
    "v_exp_f32 v54, v54\n"
    "v_exp_f32 v55, v55\n"
    "v_cvt_pk_bf16_f32 v94, v52, v53\n"
    "v_cvt_pk_bf16_f32 v95, v54, v55\n"
    "v_mov_b32 v124, v126\n"
    "v_mov_b32 v125, v127\n"
    "s_add_u32 %[nslot], %[nslot], 0x4000\n"
    "s_cmp_eq_u32 %[nslot], 0xc000\n"
    "s_cselect_b32 %[nslot], 0, %[nslot]\n"
    "v_add_u32 v126, %[nslot], %[kaddr]\n"
    "v_and_b32 v127, %[nslot], %[vsel]\n"
    "v_add_u32 v127, v127, %[vaddr]\n"
    "s_add_u32 %[dslot], %[dslot], 0x4000\n"
    "s_cmp_eq_u32 %[dslot], %[dend]\n"
    "s_cselect_b32 %[dslot], %[dbeg], %[dslot]\n"
    "v_add_u32 %[goff], 0x4000, %[goff]\n"
    "s_add_u32 %[cnt], %[cnt], 1\n"
    "s_cmp_lt_u32 %[cnt], 8\n"
    "s_cbranch_scc1 .Lloop%=\n"
    "s_nop 1\n"
    "v_mfma_f32_32x32x16_bf16 v[56:71], v[120:123], v[92:95], v[56:71]\n"
    "s_nop 15\n"
    "s_nop 3\n"
    "v_rcp_f32 v24, v64\n"
    "s_nop 0\n"
    "v_mul_f32 v56, v56, v24\n"
    "v_mul_f32 v57, v57, v24\n"
    "v_mul_f32 v58, v58, v24\n"
    "v_mul_f32 v59, v59, v24\n"
    "v_mul_f32 v60, v60, v24\n"
    "v_mul_f32 v61, v61, v24\n"
    "v_mul_f32 v62, v62, v24\n"
    "v_mul_f32 v63, v63, v24\n"
    "v_cvt_pk_bf16_f32 %[oa0], v56, v57\n"
    "v_cvt_pk_bf16_f32 %[oa1], v58, v59\n"
    "v_cvt_pk_bf16_f32 %[oa2], v60, v61\n"
    "v_cvt_pk_bf16_f32 %[oa3], v62, v63\n"
    "s_waitcnt lgkmcnt(0)\n"
    "s_mov_b32 m0, %[m0save]\n"
        : [oa0] "=&v"(oa0), [oa1] "=&v"(oa1), [oa2] "=&v"(oa2), [oa3] "=&v"(oa3),
          [goff] "+v"(goff), [dslot] "+s"(dslot), [nslot] "+s"(nslot), [cnt] "=&s"(cnt), [m0save] "=&s"(m0save)
        : [qa0] "v"(qa[0]), [qa1] "v"(qa[1]), [qa2] "v"(qa[2]), [qa3] "v"(qa[3]),
          [nma] "v"(nma), [kaddr] "v"(kaddr), [vaddr] "v"(vaddr), [vsel] "v"(vsel), [gbase] "s"(kvb), [dbeg] "s"(dbeg), [dend] "s"(dend), [psel] "s"(0x07060302u)
        : "memory", "scc", "vcc",
          "v24", "v25", "v26", "v27", "v28", "v29", "v30", "v31", "v32", "v33", "v34", "v35", "v36", "v37", "v38", "v39", "v40", "v41", "v42", "v43", "v44", "v45", "v46", "v47", "v48", "v49", "v50", "v51", "v52", "v53", "v54", "v55", "v56", "v57", "v58", "v59", "v60", "v61", "v62", "v63", "v64", "v65", "v66", "v67", "v68", "v69", "v70", "v71", "v72", "v73", "v74", "v75", "v76", "v77", "v78", "v79", "v80", "v81", "v82", "v83", "v84", "v85", "v86", "v87", "v88", "v89", "v90", "v91", "v92", "v93", "v94", "v95", "v96", "v97", "v98", "v99", "v100", "v101", "v102", "v103", "v104", "v105", "v106", "v107", "v108", "v109", "v110", "v111", "v112", "v113", "v114", "v115", "v116", "v117", "v118", "v119", "v120", "v121", "v122", "v123", "v124", "v125", "v126", "v127"
    );
    STAMP(4);
    st16_wt(ws + WS_OF + ((size_t)tile0 * 32 + head) * 1024 + lane * 16, (u32x4){oa0, oa1, oa2, oa3});
    PUBLISH(FL_O + jb * 32 + head);
}

__device__ __forceinline__ void z_role(const uint16_t* __restrict__ x, const uint16_t* __restrict__ win, char* __restrict__ ws, char* lds, int g, u64 tag) {
    const int tid = threadIdx.x, lane = tid & 63, wid = __builtin_amdgcn_readfirstlane(tid >> 6), wr = wid >> 2, wc = wid & 3, l31 = lane & 31, hh = lane >> 5;
    const int xcd = g & 7, i = g >> 3;
    const int rb = 4 * (xcd >> 1) + (i & 3), cbz = 7 * ((xcd & 1) ^ 1) + (i >> 2);
    f32x16 acc[2];
    proj_main<false>(x, win, NIN, rb, cbz * 128, 12 + cbz, ws, lds, ((i & 3) * 4 + (i >> 2) * 3) & 15, tag, acc);
    STAMP(1);
    const float* st = (const float*)(lds + P_ST);
    unsigned zp[2][8];
#pragma unroll
    for (int rt = 0; rt < 2; ++rt) { const int tr = wr * 64 + rt * 32; const float mu = st[tr + l31], rs = st[128 + tr + l31];
#pragma unroll
        for (int q = 0; q < 4; ++q) { const f32x4 c = *(const f32x4*)(st + 256 + wc * 32 + 8 * q + 4 * hh);
            zp[rt][2 * q] = cvtpk(gelu_t((acc[rt][4 * q] - mu * c[0]) * rs), gelu_t((acc[rt][4 * q + 1] - mu * c[1]) * rs));
            zp[rt][2 * q + 1] = cvtpk(gelu_t((acc[rt][4 * q + 2] - mu * c[2]) * rs), gelu_t((acc[rt][4 * q + 3] - mu * c[3]) * rs)); } }
    if (cbz < 7) {
#pragma unroll
        for (int rt = 0; rt < 2; ++rt) { const int tt = rb * 4 + wr * 2 + rt;
            char* dst = ws + WS_Z1 + ((size_t)tt * 28 + cbz * 4 + wc) * 2048 + lane * 32;
            st16_wt(dst, (u32x4){zp[rt][0], zp[rt][1], zp[rt][2], zp[rt][3]}); st16_wt(dst + 16, (u32x4){zp[rt][4], zp[rt][5], zp[rt][6], zp[rt][7]}); }
    } else {
        const int cj = cbz - 7;
        float* pst = (float*)lds;
        const unsigned ONES = 0x3f803f80u;
#pragma unroll
        for (int rt = 0; rt < 2; ++rt) { float s1 = 0.f, s2 = 0.f;
#pragma unroll
            for (int e = 0; e < 8; ++e) { s1 = dot2(zp[rt][e], ONES, s1); s2 = dot2(zp[rt][e], zp[rt][e], s2); }
            s1 = swap32_add(s1); s2 = swap32_add(s2);
            if (hh == 0) *(f32x2*)(pst + (wc * 128 + wr * 64 + rt * 32 + l31) * 2) = (f32x2){s1, s2}; }
        __syncthreads();
        char* gst = ws + WS_ST + (size_t)rb * 7 * 1024;
        if (tid < 128) { f32x2 s = *(const f32x2*)(pst + tid * 2);
#pragma unroll
            for (int j = 1; j < 4; ++j) s += *(const f32x2*)(pst + (j * 128 + tid) * 2);
            st8_wt(gst + (cj * 128 + tid) * 8, __builtin_bit_cast(u64, s)); }
        PUBLISH(FL_ST + rb * 7 + cj);
        if (wid == 0) wait_flags(flagp(ws, FL_ST + rb * 7 + lane), lane < 7, tag);
        __syncthreads();
        float* fin = (float*)(lds + 4096);
        if (tid < 128) { f32x2 s = {0.f, 0.f};
#pragma unroll
            for (int j = 0; j < 7; ++j) s += __builtin_bit_cast(f32x2, ld8_wt(gst + (j * 128 + tid) * 8));
            const float mu = s[0] * (1.0f / FH); const float var = fmaxf(s[1] * (1.0f / FH) - mu * mu, 0.f);
            fin[tid] = mu; fin[128 + tid] = __builtin_amdgcn_rsqf(var + LN_EPS); }
        __syncthreads();
        char* img = lds + 8192;
#pragma unroll
        for (int rt = 0; rt < 2; ++rt) { const int tok = wr * 64 + rt * 32 + l31; const float mu = fin[tok], rs = fin[128 + tok];
#pragma unroll
            for (int q = 0; q < 4; ++q) { const unsigned a = zp[rt][2 * q], c = zp[rt][2 * q + 1];
                const u32x2 o = {cvtpk((bflo(a) - mu) * rs, (bfhi(a) - mu) * rs), cvtpk((bflo(c) - mu) * rs, (bfhi(c) - mu) * rs)};
                const int f = wc * 32 + 8 * q + 4 * hh;
                *(u32x2*)(img + tok * 256 + (((f >> 3) ^ wsw(tok)) * 16) + (f & 4) * 2) = o; } }
        __syncthreads();
        { const int blk = (lane >> 4) & 1, q = (lane & 15) >> 2, p = lane & 3;
#pragma unroll
          for (int kq = 0; kq < 4; ++kq) { const int ks = wr * 4 + kq; s16x4 t2[2];
#pragma unroll
              for (int t = 0; t < 2; ++t) { const int kr = 16 * ks + 8 * hh + 4 * t + q; const int ch = 4 * wc + 2 * blk + (p >> 1);
                  t2[t] = trread((lds_cptr)img + kr * 256 + ((ch ^ wsw(kr)) * 16) + 8 * (p & 1)); }
              st16_wt(ws + WS_Z2N + ((size_t)(cj * 4 + wc) * 128 + rb * 8 + ks) * 1024 + lane * 16, __builtin_bit_cast(u32x4, cat(t2[0], t2[1]))); } }
    }
    PUBLISH(FL_Z + rb * 14 + cbz);
}

constexpr int S_SLOT = 24576, S_LDS = 3 * S_SLOT;
__device__ __forceinline__ void spatial_role(const uint16_t* __restrict__ wsp, const uint16_t* __restrict__ bsp, char* __restrict__ ws, char* lds, int g, u64 tag) {
    const int tid = threadIdx.x, lane = tid & 63, wid8 = __builtin_amdgcn_readfirstlane(tid >> 6), wid = wid8 & 3, l31 = lane & 31, hh = lane >> 5;
    const int i = g >> 3, tb = 4 * (g & 7) + (i & 3), fb = i >> 2;
    const unsigned lds0 = (unsigned)(uintptr_t)lds;
    if (wid8 == 0) {
        wait_flags(flagp(ws, lane < 16 ? FL_Z + lane * 14 + 7 + fb : FL_Z + (tb >> 1) * 14 + fb), lane < 17, tag); }
    __syncthreads();
    STAMP(3);
    if (wid8 >= 4) {
        const int r0 = 16 * wid + (lane >> 3), r1 = r0 + 8;
        const uint16_t* s0 = wsp + (size_t)(tb * 64 + r0) * SEQ + (((lane & 7) ^ ((r0 >> 1) & 7)) * 8);
        const uint16_t* s1 = wsp + (size_t)(tb * 64 + r1) * SEQ + (((lane & 7) ^ ((r1 >> 1) & 7)) * 8);
        const char* zs = ws + WS_Z2N + (size_t)(fb * 4 + wid) * 131072 + lane * 16;
        auto issue = [&](int it, int slot) { const unsigned so = lds0 + slot * S_SLOT;
            glds16(s0 + it * 64, so + wid * 2048); glds16(s1 + it * 64, so + wid * 2048 + 1024);
#pragma unroll
            for (int k = 0; k < 4; ++k) glds16_wt(zs + (size_t)(it * 4 + k) * 1024, so + 8192 + wid * 4096 + k * 1024); };
        issue(0, 0); issue(1, 1); issue(2, 2);
        WAIT_BAR(12);
        int slot = 0;
#pragma unroll 1
        for (int it = 1; it < 31; ++it) { WAIT_BAR(6); if (it + 2 < 32) issue(it + 2, slot); slot = slot == 2 ? 0 : slot + 1; }
        WAIT_BAR(0);
        BAR();
    } else {
        const int pid = i * 4 + wid;
        const char* pfp = nullptr; unsigned pfs = 0;
        if (pid < 4) { pfp = (const char*)wsp + (size_t)((4 * (g & 7) + pid) * 64 + lane) * (SEQ * 2); pfs = 128; }
        else if (pid < 18) { pfp = ws + WS_Z2N + (size_t)((pid - 4) * 2 + hh) * 131072 + l31 * 128; pfs = 4096; }
        unsigned junk = 0;
        auto pf = [&](int it) { if (pid < 18 && it < 32) asm volatile("global_load_dword %0, %1, off sc1" : "+v"(junk) : "v"(pfp + (size_t)it * pfs) : "memory"); };
#pragma unroll
        for (int it = 3; it < PF_DIST; ++it) pf(it);
        unsigned boff[2][4];
#pragma unroll
        for (int rt = 0; rt < 2; ++rt)
#pragma unroll
            for (int ks = 0; ks < 4; ++ks) { const int r = rt * 32 + l31; boff[rt][ks] = (r * 128 + ((hh ^ ((r >> 1) & 7)) * 16)) ^ (ks * 32); }
        const unsigned aoff = 8192 + wid * 4096 + lane * 16;
        struct SF { bf16x8 a, b[2]; };
        auto rd = [&](SF& f, int slot, int ks) { lds_cptr sb = (lds_cptr)lds + slot * S_SLOT;
            f.a = ldsfrag(sb + aoff + ks * 1024); f.b[0] = ldsfrag(sb + boff[0][ks]); f.b[1] = ldsfrag(sb + boff[1][ks]); };
        f32x16 acc[2] = {f32x16{}, f32x16{}};
        auto mm = [&](const SF& f) { acc[0] = MFMA(f.a, f.b[0], acc[0]); acc[1] = MFMA(f.a, f.b[1], acc[1]); };
        SF f[4];
        BAR();
#pragma unroll
        for (int ks = 0; ks < 4; ++ks) rd(f[ks], 0, ks);
        int itc = 0;
        auto step = [&](int nsl) {
            LGKM_BAR();
            pf(itc + PF_DIST); ++itc;
#pragma unroll
            for (int ks = 0; ks < 4; ++ks) { lds_cptr sb = (lds_cptr)lds + nsl * S_SLOT;
                acc[0] = MFMA(f[ks].a, f[ks].b[0], acc[0]); SBAR();
                f[ks].b[0] = ldsfrag(sb + boff[0][ks]); SBAR();
                acc[1] = MFMA(f[ks].a, f[ks].b[1], acc[1]); SBAR();
                f[ks].b[1] = ldsfrag(sb + boff[1][ks]); f[ks].a = ldsfrag(sb + aoff + ks * 1024); SBAR(); } };
#pragma unroll 1
        for (int io = 0; io < 10; ++io) { step(1); step(2); step(0); }
        step(1); step(2);
        asm volatile("s_waitcnt vmcnt(0)" : "+v"(junk) :: "memory");
#pragma unroll
        for (int rt = 0; rt < 2; ++rt) { const int tt = tb * 2 + rt; const float bias = __uint_as_float((unsigned)bsp[tt * 32 + l31] << 16);
            const char* z1p = ws + WS_Z1 + ((size_t)tt * 28 + fb * 4 + wid) * 2048 + lane * 32;
            const u32x4 za = ld16_wt(z1p), zb = ld16_wt(z1p + 16);
            u32x4 ua, ub;
#pragma unroll
            for (int e = 0; e < 4; ++e) {
                ua[e] = cvtpk(bflo(za[e]) * (acc[rt][2 * e] + bias), bfhi(za[e]) * (acc[rt][2 * e + 1] + bias));
                ub[e] = cvtpk(bflo(zb[e]) * (acc[rt][8 + 2 * e] + bias), bfhi(zb[e]) * (acc[rt][8 + 2 * e + 1] + bias)); }
            char* ud = ws + WS_U + ((size_t)tt * 56 + (fb * 4 + wid) * 2) * 1024 + lane * 16;
            st16_wt(ud, ua); st16_wt(ud + 1024, ub); }
    }
    PUBLISH(FL_U + tb * 7 + fb);
}

constexpr int F_SLOT = 8192 + 8192, F_LDS = 4 * F_SLOT;
__device__ __forceinline__ void final_role(const uint16_t* __restrict__ x, const uint16_t* __restrict__ wo, const uint16_t* __restrict__ wout, char* __restrict__ ws, uint16_t* __restrict__ out, char* lds, int g, u64 tag) {
    const int tb = 4 * (g & 7) + ((g >> 3) & 3), db = g >> 5;
    const int tid = threadIdx.x, lane = tid & 63, wid8 = __builtin_amdgcn_readfirstlane(tid >> 6), wid = wid8 & 3, wr = wid >> 1, wc = wid & 1, l31 = lane & 31, hh = lane >> 5;
    const unsigned lds0 = (unsigned)(uintptr_t)lds;
    auto wswz = [](int r) { return ((r & 2) << 1) | ((r >> 2) & 3); };
    f32x16 as = f32x16{}, ag = f32x16{};
    const int k0 = 16 * wid + (lane >> 3), k1 = k0 + 8;
    const size_t wo0 = (size_t)k0 * DM + db * 64 + (((lane & 7) ^ wswz(k0)) * 8), wo1 = (size_t)k1 * DM + db * 64 + (((lane & 7) ^ wswz(k1)) * 8);
    const int att = tb * 2 + (wid & 1), aks = 2 * (wid >> 1);
    unsigned woff[2];
    { const int blk = (lane >> 4) & 1, q = (lane & 15) >> 2, p = lane & 3; const int kr = 4 * hh + q; const int ch = 4 * wc + 2 * blk + (p >> 1);
      woff[0] = kr * 128 + ((ch ^ wswz(kr)) * 16) + 8 * (p & 1); woff[1] = (woff[0] + 1024) ^ 32; }
    const unsigned aoff = 8192 + wr * 4096 + lane * 16;
    struct FF { bf16x8 w, a; };
    auto rd = [&](FF& f, int slot, int ks) { lds_cptr sb = (lds_cptr)lds + slot * F_SLOT;
        f.w = cat(trread(sb + woff[0] + ks * 2048), trread(sb + woff[1] + ks * 2048)); f.a = ldsfrag(sb + aoff + ks * 1024); };
    auto pass = [&](const uint16_t* wb, const char* ab, int nfr, int n, f32x16& acc) {
        if (wid8 >= 4) {
            auto issue = [&](int it) { const unsigned so = lds0 + (it & 3) * F_SLOT; const uint16_t* w = wb + (size_t)it * 64 * DM;
                glds16(w + wo0, so + wid * 2048); glds16(w + wo1, so + wid * 2048 + 1024);
                const char* a = ab + ((size_t)att * nfr + it * 4 + aks) * 1024 + lane * 16;
                glds16_wt(a, so + 8192 + (wid & 1) * 4096 + aks * 1024); glds16_wt(a + 1024, so + 8192 + (wid & 1) * 4096 + aks * 1024 + 1024); };
            issue(0); issue(1); issue(2); issue(3);
            WAIT_BAR(12);
#pragma unroll 1
            for (int i = 1; i < n - 2; ++i) { WAIT_BAR(8); if (i + 3 < n) issue(i + 3); }
            WAIT_BAR(4);
            WAIT_BAR(0);
            BAR();
        } else {
            FF fa[4], fb[4];
            BAR();
#pragma unroll
            for (int ks = 0; ks < 4; ++ks) rd(fa[ks], 0, ks);
            auto step = [&](int nsl, FF (&cur)[4], FF (&nxt)[4]) {
                LGKM_BAR();
#pragma unroll
                for (int ks = 0; ks < 4; ++ks) { acc = MFMA(cur[ks].w, cur[ks].a, acc); SBAR(); rd(nxt[ks], nsl, ks); SBAR(); } };
#pragma unroll 1
            for (int it = 0; it < n; it += 2) { step((it + 1) & 3, fa, fb); step((it + 2) & 3, fb, fa); }
        } };
    if (wid8 == 0) wait_flags(flagp(ws, FL_U + tb * 7 + lane), lane < 7, tag);
    __syncthreads();
    STAMP(5);
    pass(wout, ws + WS_U, 56, 14, ag);
    STAMP(6);
    if (wid8 == 0) wait_flags(flagp(ws, FL_O + (tb >> 2) * 32 + lane), lane < 32, tag);
    __syncthreads();
    STAMP(7);
    pass(wo, ws + WS_OF, 32, 8, as);
    STAMP(8);
    if (wid8 < 4) {
        const int tok = tb * 64 + wr * 32 + l31;
#pragma unroll
        for (int q = 0; q < 4; ++q) { const size_t o = (size_t)tok * DM + db * 64 + wc * 32 + 8 * q + 4 * hh;
            const u32x2 xv = *(const u32x2*)(x + o);
            const float r0 = bflo(xv[0]) + as[4 * q] * silu_f(ag[4 * q]), r1 = bfhi(xv[0]) + as[4 * q + 1] * silu_f(ag[4 * q + 1]);
            const float r2 = bflo(xv[1]) + as[4 * q + 2] * silu_f(ag[4 * q + 2]), r3 = bfhi(xv[1]) + as[4 * q + 3] * silu_f(ag[4 * q + 3]);
            *(u32x2*)(out + o) = (u32x2){cvtpk(r0, r1), cvtpk(r2, r3)}; }
    }
}

#define GDELAY 200
#define GPRIO 2
constexpr int ALL_LDS = S_LDS;
__global__ __launch_bounds__(512, 4) void k_all(const uint16_t* __restrict__ x, const uint16_t* __restrict__ wqkv, const uint16_t* __restrict__ wo, const uint16_t* __restrict__ win,
                                                const uint16_t* __restrict__ wsp, const uint16_t* __restrict__ bsp, const uint16_t* __restrict__ wout, char* __restrict__ ws, uint16_t* __restrict__ out) {
    extern __shared__ __attribute__((aligned(16))) char lds[];
    const int b = blockIdx.x;
    const u64 ep = __hip_atomic_load(flagp(ws, FL_EPOCH), RLX_AGENT);
    const u64 tag = (((u64)(unsigned)__builtin_amdgcn_readfirstlane((unsigned)(ep >> 32)) << 32) | (unsigned)__builtin_amdgcn_readfirstlane((unsigned)ep)) + 1;
    STAMP(0);
    if (b < 256) {
        __builtin_amdgcn_s_setprio(3);
        if (b < 192) qkv_role(x, wqkv, ws, lds, b, tag); else stats_role(x, wqkv, win, ws, lds, b - 192, tag);
        __builtin_amdgcn_s_setprio(0);
        __syncthreads();
        attn_role(ws, lds, b, tag);
        STAMP(5);
    } else {
        const int g = b - 256;
        __builtin_amdgcn_s_setprio(GPRIO);
        if (threadIdx.x == 0) __hip_atomic_store(flagp(ws, FL_G + g), tag, RLX_AGENT);
        { const u64 t0 = __builtin_amdgcn_s_memrealtime(); while (__builtin_amdgcn_s_memrealtime() - t0 < GDELAY) __builtin_amdgcn_s_sleep(8); }
        if (g < 224) { z_role(x, win, ws, lds, g, tag); STAMP(2); __syncthreads(); spatial_role(wsp, bsp, ws, lds, g, tag); STAMP(4); __syncthreads(); }
        final_role(x, wo, wout, ws, out, lds, g, tag);
        if (g == 255 && threadIdx.x < 64) {
            const int lane = threadIdx.x;
#pragma unroll 1
            for (int c = 0; c < 4; ++c) { wait_flags(flagp(ws, FL_O + c * 64 + lane), true, tag); wait_flags(flagp(ws, FL_G + c * 64 + lane), true, tag); }
            if (lane == 0) __hip_atomic_store(flagp(ws, FL_EPOCH), tag, RLX_AGENT);
        }
        STAMP(9);
    }
}

extern "C" void kernel_launch(void* const* d_in, const int* in_sizes, int n_in, void* d_out, int out_size, void* d_ws, size_t ws_size, hipStream_t stream) {
    const uint16_t* x = (const uint16_t*)d_in[0]; const uint16_t* wqkv = (const uint16_t*)d_in[1]; const uint16_t* wo = (const uint16_t*)d_in[2];
    const uint16_t* win = (const uint16_t*)d_in[4]; const uint16_t* wsp = (const uint16_t*)d_in[5]; const uint16_t* bsp = (const uint16_t*)d_in[6]; const uint16_t* wout = (const uint16_t*)d_in[7];
    (void)hipFuncSetAttribute((const void*)k_all, hipFuncAttributeMaxDynamicSharedMemorySize, ALL_LDS);
    k_all<<<512, 512, ALL_LDS, stream>>>(x, wqkv, wo, win, wsp, bsp, wout, (char*)d_ws, (uint16_t*)d_out);
}
```

```cpp
#include <hip/hip_runtime.h>
#include <hip/hip_bf16.h>
#include <stdint.h>
#define PACE_N 2

typedef short bf16x8 __attribute__((ext_vector_type(8)));
typedef short s16x4 __attribute__((ext_vector_type(4)));
typedef __bf16 bf16x2_t __attribute__((ext_vector_type(2)));
typedef float f32x16 __attribute__((ext_vector_type(16)));
typedef float f32x4 __attribute__((ext_vector_type(4)));
typedef float f32x2 __attribute__((ext_vector_type(2)));
typedef unsigned u32x4 __attribute__((ext_vector_type(4)));
typedef unsigned u32x2 __attribute__((ext_vector_type(2)));
typedef unsigned long long u64;
#define GAS __attribute__((address_space(1)))
#define LAS __attribute__((address_space(3)))
typedef LAS const char* lds_cptr;
typedef short v4i16_t __attribute__((ext_vector_type(4)));
#define RLX_AGENT __ATOMIC_RELAXED, __HIP_MEMORY_SCOPE_AGENT

constexpr int SEQ = 2048, DM = 512, NQKV = 1536, NIN = 1792, FH = 896;
constexpr float QSCALE = 0.25f * 1.4426950408889634f;
constexpr float LN_EPS = 1e-5f;
constexpr size_t MB = 1024 * 1024;
constexpr size_t WS_QF = 0, WS_KV = 2 * MB, WS_Z1 = 6 * MB, WS_Z2N = 10 * MB, WS_U = 14 * MB, WS_OF = 18 * MB;
constexpr size_t WS_KM = 20 * MB, WS_ST = 20 * MB + 65536, WS_RS = 20 * MB + 196608, WS_CS = 20 * MB + 229376  , WS_FLAG = 21 * MB;
constexpr int FL_RS = 0, FL_CS = 64, FL_QKV = 128, FL_ST = 320, FL_Z = 432, FL_O = 656, FL_U = 912, FL_G = 1136, FL_EPOCH = 1392;

#define MFMA(a, b, c) __builtin_amdgcn_mfma_f32_32x32x16_bf16(a, b, c, 0, 0, 0)
#define SBAR() __builtin_amdgcn_sched_barrier(0)
__device__ __forceinline__ unsigned cvtpk(float lo, float hi) { unsigned r; asm("v_cvt_pk_bf16_f32 %0, %1, %2" : "=v"(r) : "v"(lo), "v"(hi)); return r; }
__device__ __forceinline__ float bflo(unsigned u) { return __uint_as_float(u << 16); }
__device__ __forceinline__ float bfhi(unsigned u) { return __uint_as_float(u & 0xffff0000u); }
__device__ __forceinline__ float dot2(unsigned a, unsigned b, float acc) {
    return __builtin_amdgcn_fdot2_f32_bf16(__builtin_bit_cast(bf16x2_t, a), __builtin_bit_cast(bf16x2_t, b), acc, false); }
__device__ __forceinline__ void glds16(const void* g, unsigned lds_base) {
    asm volatile("s_mov_b32 m0, %1\n\ts_nop 0\n\tglobal_load_lds_dwordx4 %0, off" :: "v"(g), "s"(lds_base) : "memory", "m0"); }
template <int IMM, bool WT> __device__ __forceinline__ void glds_s(const void* sbase, unsigned voff) {
    if (WT) asm volatile("global_load_lds_dwordx4 %0, %1 offset:%2 sc1" :: "v"(voff), "s"(sbase), "n"(IMM) : "memory");
    else asm volatile("global_load_lds_dwordx4 %0, %1 offset:%2" :: "v"(voff), "s"(sbase), "n"(IMM) : "memory"); }
__device__ __forceinline__ void set_m0(unsigned v) { asm volatile("s_mov_b32 m0, %0\n\ts_nop 0" :: "s"(v) : "memory", "m0"); }
__device__ __forceinline__ s16x4 trread(lds_cptr p) { return __builtin_bit_cast(s16x4, __builtin_amdgcn_ds_read_tr16_b64_v4i16((LAS v4i16_t*)p)); }
__device__ __forceinline__ bf16x8 ldsfrag(lds_cptr p) { return *(LAS const bf16x8*)p; }
__device__ __forceinline__ bf16x8 cat(s16x4 a, s16x4 b) { return (bf16x8){a[0], a[1], a[2], a[3], b[0], b[1], b[2], b[3]}; }
__device__ __forceinline__ float swap32_add(float v) { auto rr = __builtin_amdgcn_permlane32_swap(__float_as_uint(v), __float_as_uint(v), false, false); return __uint_as_float(rr[0]) + __uint_as_float(rr[1]); }
__device__ __forceinline__ float swap32_max(float v) { auto rr = __builtin_amdgcn_permlane32_swap(__float_as_uint(v), __float_as_uint(v), false, false); return fmaxf(__uint_as_float(rr[0]), __uint_as_float(rr[1])); }
template <int CTRL> __device__ __forceinline__ float dppf(float v) { return __int_as_float(__builtin_amdgcn_update_dpp(0, __float_as_int(v), CTRL, 0xf, 0xf, false)); }
__device__ __forceinline__ float row16_add(float v) { v += dppf<0x128>(v); v += dppf<0x124>(v); v += dppf<0x122>(v); v += dppf<0x121>(v); return v; }
__device__ __forceinline__ float row16_max(float v) { v = fmaxf(v, dppf<0x128>(v)); v = fmaxf(v, dppf<0x124>(v)); v = fmaxf(v, dppf<0x122>(v)); v = fmaxf(v, dppf<0x121>(v)); return v; }
__device__ __forceinline__ float swap16_max(float v) { auto rr = __builtin_amdgcn_permlane16_swap(__float_as_uint(v), __float_as_uint(v), false, false); return fmaxf(__uint_as_float(rr[0]), __uint_as_float(rr[1])); }
__device__ __forceinline__ float gelu_t(float x) {
    const float c1 = -2.3022082f, c2 = -0.10294324f;
    float e = __builtin_amdgcn_exp2f(x * __builtin_fmaf(x * x, c2, c1));
    return x * __builtin_amdgcn_rcpf(1.0f + e); }
__device__ __forceinline__ float silu_f(float x) { return x * __builtin_amdgcn_rcpf(1.0f + __builtin_amdgcn_exp2f(-1.4426950408889634f * x)); }
#define STAMP(slot) do {} while (0)
#define PF_DIST 9
#define PACE() do { asm volatile("s_nop %0" :: "n"(PACE_N)); SBAR(); } while (0)
#define WAITV(n) asm volatile("s_waitcnt vmcnt(" #n ")" ::: "memory")
#define WAIT_BAR(n) asm volatile("s_waitcnt vmcnt(" #n ")\n\ts_barrier" ::: "memory")
#define BAR() asm volatile("s_barrier" ::: "memory")
#define LGKM_BAR() do { __builtin_amdgcn_s_waitcnt(0xC07F); asm volatile("" ::: "memory"); __builtin_amdgcn_s_barrier(); asm volatile("" ::: "memory"); } while (0)
__device__ __forceinline__ unsigned wsw(unsigned row) { return ((row & 3) << 2) | ((row >> 2) & 3); }
__device__ __forceinline__ void st16_wt(void* p, u32x4 v) {
    asm volatile("global_store_dwordx4 %0, %1, off sc1\n\ts_nop 1" :: "v"(p), "v"(v)); }
__device__ __forceinline__ void st8_wt(void* p, u64 v) { __hip_atomic_store((GAS u64*)p, v, RLX_AGENT); }
__device__ __forceinline__ u64 ld8_wt(const void* p) { return __hip_atomic_load((GAS u64*)p, RLX_AGENT); }
__device__ __forceinline__ u32x4 ld16_wt(const void* p) { u32x4 v; asm volatile("global_load_dwordx4 %0, %1, off sc1\n\ts_waitcnt vmcnt(0)" : "=&v"(v) : "v"(p) : "memory"); return v; }
__device__ __forceinline__ void glds16_wt(const void* g, unsigned lds_base) {
    asm volatile("s_mov_b32 m0, %1\n\ts_nop 0\n\tglobal_load_lds_dwordx4 %0, off sc1" :: "v"(g), "s"(lds_base) : "memory", "m0"); }
__device__ __forceinline__ GAS u64* flagp(char* ws, int idx) { return (GAS u64*)(ws + WS_FLAG) + idx * 16; }
__device__ __forceinline__ void wait_flags(GAS u64* f, bool want, u64 tag) {
    for (unsigned spins = 0; spins < (1u << 20); ++spins) {
        const u64 v = want ? __hip_atomic_load(f, RLX_AGENT) : tag;
        if (__all(v == tag)) return;
        __builtin_amdgcn_s_sleep(2); } }
#define ACQUIRE() do { __builtin_amdgcn_fence(__ATOMIC_ACQUIRE, "agent"); asm volatile("s_waitcnt vmcnt(0)" ::: "memory"); } while (0)
#define PUBLISH(idx) do { WAITV(0); __syncthreads(); if (threadIdx.x == 0) __hip_atomic_store(flagp(ws, idx), tag, RLX_AGENT); } while (0)

__device__ __forceinline__ void stats_role(const uint16_t* __restrict__ x, const uint16_t* __restrict__ wqkv, const uint16_t* __restrict__ win, char* __restrict__ ws, char* lds, int j, u64 tag) {
    const int tid = threadIdx.x, lane = tid & 63, wid = tid >> 6, rr = lane >> 4, cc = lane & 15;
    const unsigned ONES = 0x3f803f80u;
    u32x4 xv[4], w[8];
    const int row = 32 * j + 4 * wid + rr;
#pragma unroll
    for (int i = 0; i < 4; ++i) xv[i] = *(const u32x4*)(x + (size_t)row * DM + i * 128 + cc * 8);
    const int cbk = j >> 1;
    if (j < 52) {
        const uint16_t* W = cbk < 12 ? wqkv + cbk * 128 : win + (cbk - 12) * 128; const int ldw = cbk < 12 ? NQKV : NIN;
        const uint16_t* p = W + (size_t)((j & 1) * 256 + 32 * wid + rr) * ldw + cc * 8;
#pragma unroll
        for (int i = 0; i < 8; ++i) w[i] = *(const u32x4*)(p + (size_t)(i * 4) * ldw);
    }
    float s1 = 0.f, s2 = 0.f;
#pragma unroll
    for (int i = 0; i < 4; ++i)
#pragma unroll
        for (int e = 0; e < 4; ++e) { s1 = dot2(xv[i][e], ONES, s1); s2 = dot2(xv[i][e], xv[i][e], s2); }
    s1 = row16_add(s1); s2 = row16_add(s2);
    { const float mu = s1 * (1.0f / DM), var = fmaxf(s2 * (1.0f / DM) - mu * mu, 0.f);
      if (cc == 0) st8_wt(ws + WS_RS + (size_t)row * 8, __builtin_bit_cast(u64, (f32x2){mu, __builtin_amdgcn_rsqf(var + LN_EPS)})); }
    if (j < 52) {
        float cs[8] = {0.f, 0.f, 0.f, 0.f, 0.f, 0.f, 0.f, 0.f};
        unsigned sel_lo = 0x00003f80u, sel_hi = 0x3f800000u;
        asm volatile("" : "+v"(sel_lo), "+v"(sel_hi));
#pragma unroll
        for (int i = 0; i < 8; ++i)
#pragma unroll
            for (int e = 0; e < 4; ++e) { cs[2 * e] = dot2(w[i][e], sel_lo, cs[2 * e]); cs[2 * e + 1] = dot2(w[i][e], sel_hi, cs[2 * e + 1]); }
        float* red = (float*)lds;
        *(f32x4*)(red + (wid * 4 + rr) * 128 + cc * 8) = (f32x4){cs[0], cs[1], cs[2], cs[3]}; *(f32x4*)(red + (wid * 4 + rr) * 128 + cc * 8 + 4) = (f32x4){cs[4], cs[5], cs[6], cs[7]};
        __syncthreads();
        if (tid < 64) { f32x2 a = {0.f, 0.f};
#pragma unroll
            for (int q = 0; q < 32; ++q) a += *(const f32x2*)(red + q * 128 + 2 * tid);
            st8_wt(ws + WS_CS + (size_t)((j & 1) * 3328 + cbk * 128 + 2 * tid) * 4, __builtin_bit_cast(u64, a)); }
    }
    STAMP(11);
    WAITV(0); __syncthreads();
    STAMP(1);
    if (tid == 0) { __hip_atomic_store(flagp(ws, FL_RS + j), tag, RLX_AGENT); if (j < 52) __hip_atomic_store(flagp(ws, FL_CS + j), tag, RLX_AGENT); }
}

constexpr int P_SLOT = 16384, P_ST = 4 * P_SLOT, P_LDS = 4 * P_SLOT + 4096;
struct PFrag { bf16x8 x[2], w; };
template <bool SWAP>
__device__ __forceinline__ void proj_main(const uint16_t* __restrict__ x, const uint16_t* __restrict__ W, int ldw, int rb, int col0, int cblk, char* __restrict__ ws, char* lds, int rot, u64 tag, f32x16 (&acc)[2]) {
    const int tid = threadIdx.x, lane = tid & 63, wid = __builtin_amdgcn_readfirstlane(tid >> 6), wr = wid >> 2, wc = wid & 3, l31 = lane & 31, hh = lane >> 5;
    const unsigned lds0 = (unsigned)(uintptr_t)lds;
    const int xr = 16 * wid + (lane >> 2), wk = 4 * wid + (lane >> 4);
    const char* xb = (const char*)(x + (size_t)rb * 128 * DM); const char* wb = (const char*)(W + col0);
    const unsigned xvo = (xr * DM + ((lane & 3) ^ ((xr >> 2) & 3)) * 8) * 2, wvo = (wk * ldw + ((lane & 15) ^ wsw(wk)) * 8) * 2;
    const unsigned xd = lds0 + wid * 1024, wd = lds0 + 8192 + wid * 1024;
    auto issue1 = [&](int it, int which) { const unsigned so = (it & 3) * P_SLOT; const int kk = (it + rot) & 15;
        if (which == 0) { set_m0(xd + so); glds_s<0, false>(xb + kk * 64, xvo); } else { set_m0(wd + so); glds_s<0, false>(wb + (size_t)kk * 64 * ldw, wvo); } SBAR(); };
#pragma unroll
    for (int it = 0; it < 4; ++it) { issue1(it, 0); issue1(it, 1); }
    unsigned xoff[2][2], woff[2][2];
    const int blk = (lane >> 4) & 1, q = (lane & 15) >> 2, p = lane & 3;
#pragma unroll
    for (int ks = 0; ks < 2; ++ks) {
#pragma unroll
        for (int rt = 0; rt < 2; ++rt) { const int r = wr * 64 + rt * 32 + l31; xoff[rt][ks] = r * 64 + (((2 * ks + hh) ^ ((r >> 2) & 3)) * 16); }
        const int kr = 8 * hh + q, ch = 4 * wc + 2 * blk + (p >> 1);
        woff[0][ks] = 8192 + ks * 4096 + kr * 256 + ((ch ^ wsw(kr)) * 16) + 8 * (p & 1); woff[1][ks] = (woff[0][ks] + 1024) ^ 16; }
    auto rd = [&](PFrag& f, int slot, int j) { lds_cptr sb = (lds_cptr)lds + slot * P_SLOT;
        f.x[0] = ldsfrag(sb + xoff[0][j]); f.x[1] = ldsfrag(sb + xoff[1][j]); f.w = cat(trread(sb + woff[0][j]), trread(sb + woff[1][j])); };
    auto mm = [&](const PFrag& f) {
#pragma unroll
        for (int rt = 0; rt < 2; ++rt) acc[rt] = SWAP ? MFMA(f.x[rt], f.w, acc[rt]) : MFMA(f.w, f.x[rt], acc[rt]); };
    acc[0] = f32x16{}; acc[1] = f32x16{};
    PFrag fa[2], fb[2];
    WAIT_BAR(6);
    STAMP(10);
    rd(fa[0], 0, 0); rd(fa[1], 0, 1);
    auto mm1 = [&](const PFrag& f, int rt) { acc[rt] = SWAP ? MFMA(f.x[rt], f.w, acc[rt]) : MFMA(f.w, f.x[rt], acc[rt]); SBAR(); };
    auto step = [&](int it, int sl, PFrag (&cur)[2], PFrag (&nxt)[2]) {
        __builtin_amdgcn_s_waitcnt(0xC07F);
        if (it < 13) WAITV(4); else if (it == 13) WAITV(2); else WAITV(0);
        __builtin_amdgcn_s_barrier(); asm volatile("" ::: "memory");
        lds_cptr sb = (lds_cptr)lds + ((sl + 1) & 3) * P_SLOT;
        mm1(cur[0], 0);
        nxt[0].x[0] = ldsfrag(sb + xoff[0][0]); nxt[0].x[1] = ldsfrag(sb + xoff[1][0]); SBAR();
        mm1(cur[0], 1);
        nxt[0].w = cat(trread(sb + woff[0][0]), trread(sb + woff[1][0])); SBAR();
        if (it + 4 < 16) issue1(it + 4, 0);
        mm1(cur[1], 0);
        nxt[1].x[0] = ldsfrag(sb + xoff[0][1]); nxt[1].x[1] = ldsfrag(sb + xoff[1][1]); SBAR();
        mm1(cur[1], 1);
        nxt[1].w = cat(trread(sb + woff[0][1]), trread(sb + woff[1][1])); SBAR();
        if (it + 4 < 16) issue1(it + 4, 1); };
#pragma unroll 1
    for (int io = 0; io < 2; ++io) { step(io * 4, 0, fa, fb); step(io * 4 + 1, 1, fb, fa); step(io * 4 + 2, 2, fa, fb); step(io * 4 + 3, 3, fb, fa); }
    const char* fa_ = (const char*)(lane < 4 ? flagp(ws, FL_RS + rb * 4 + lane) : flagp(ws, FL_CS + 2 * cblk + (lane & 1)));
    const char* sa_ = tid < 128 ? ws + WS_RS + (size_t)(rb * 128 + tid) * 8 : ws + WS_CS + (size_t)(cblk * 128 + 2 * (tid & 63)) * 4;
    const char* sb_ = ws + WS_CS + (size_t)(3328 + cblk * 128 + 2 * (tid & 63)) * 4;
    u64 fl, stv, stw;
    asm volatile("global_load_dwordx2 %0, %1, off sc1" : "=v"(fl) : "v"(fa_) : "memory");
    step(8, 0, fa, fb); step(9, 1, fb, fa); step(10, 2, fa, fb); step(11, 3, fb, fa);
    asm volatile("" : "+v"(fl));
    const bool got = __all(lane >= 6 || fl == tag);
    asm volatile("global_load_dwordx2 %0, %2, off sc1\n\tglobal_load_dwordx2 %1, %3, off sc1" : "=&v"(stv), "=&v"(stw) : "v"(sa_), "v"(sb_) : "memory");
    step(12, 0, fa, fb); step(13, 1, fb, fa); step(14, 2, fa, fb); step(15, 3, fb, fa);
    asm volatile("" : "+v"(stv), "+v"(stw));
    if (!got) { wait_flags((GAS u64*)fa_, lane < 6, tag); stv = ld8_wt(sa_); stw = ld8_wt(sb_); }
    STAMP(11);
    float* st = (float*)(lds + P_ST);
    { const f32x2 v = __builtin_bit_cast(f32x2, stv);
      if (tid < 128) { st[tid] = v[0]; st[128 + tid] = v[1]; } else if (tid < 192) *(f32x2*)(st + 256 + 2 * (tid - 128)) = v + __builtin_bit_cast(f32x2, stw); }
    __syncthreads();
}

__device__ __forceinline__ void qkv_role(const uint16_t* __restrict__ x, const uint16_t* __restrict__ wqkv, char* __restrict__ ws, char* lds, int b, u64 tag) {
    const int xcd = b & 7, i = b >> 3;
    const int rb = 4 * (xcd >> 1) + (i & 3), cb = 6 * (xcd & 1) + (i >> 2);
    const int tid = threadIdx.x, lane = tid & 63, wid = __builtin_amdgcn_readfirstlane(tid >> 6), wr = wid >> 2, wc = wid & 3, l31 = lane & 31, hh = lane >> 5;
    f32x16 acc[2];
    const int rot = ((i & 3) * 4 + (i >> 2) * 3) & 15;
    if (cb >= 8) proj_main<true>(x, wqkv, NQKV, rb, cb * 128, cb, ws, lds, rot, tag, acc); else proj_main<false>(x, wqkv, NQKV, rb, cb * 128, cb, ws, lds, rot, tag, acc);
    STAMP(1);
    const float* st = (const float*)(lds + P_ST);
    if (cb >= 8) {
        const float cs = st[256 + wc * 32 + l31];
        const int head = (cb - 8) * 8 + wc * 2 + (l31 >> 4);
#pragma unroll
        for (int rt = 0; rt < 2; ++rt) { const int tr = wr * 64 + rt * 32;
            unsigned pk[8];
#pragma unroll
            for (int q = 0; q < 4; ++q) { const f32x4 m = *(const f32x4*)(st + tr + 8 * q + 4 * hh), r = *(const f32x4*)(st + 128 + tr + 8 * q + 4 * hh);
                pk[2 * q] = cvtpk((acc[rt][4 * q] - m[0] * cs) * r[0], (acc[rt][4 * q + 1] - m[1] * cs) * r[1]);
                pk[2 * q + 1] = cvtpk((acc[rt][4 * q + 2] - m[2] * cs) * r[2], (acc[rt][4 * q + 3] - m[3] * cs) * r[3]); }
            const int tile = rb * 4 + (tr >> 5);
            char* dst = ws + WS_KV + (size_t)head * 131072 + (tile >> 3) * 16384 + 8192 + (tile & 7) * 1024 + hh * 256 + (l31 & 15) * 16;
            st16_wt(dst, (u32x4){pk[0], pk[1], pk[2], pk[3]}); st16_wt(dst + 512, (u32x4){pk[4], pk[5], pk[6], pk[7]}); }
    } else {
        const bool isK = cb >= 4; const float sc = isK ? 1.0f : QSCALE;
        float kmx[2] = {0.f, 0.f};
#pragma unroll
        for (int rt = 0; rt < 2; ++rt) { const int tr = wr * 64 + rt * 32, tile = rb * 4 + (tr >> 5);
            const float mu = st[tr + l31], rs = st[128 + tr + l31] * sc;
#pragma unroll
            for (int hb = 0; hb < 2; ++hb) {
                float v[8];
#pragma unroll
                for (int q = 0; q < 2; ++q) { const f32x4 c = *(const f32x4*)(st + 256 + wc * 32 + 16 * hb + 8 * q + 4 * hh);
#pragma unroll
                    for (int e = 0; e < 4; ++e) v[4 * q + e] = (acc[rt][8 * hb + 4 * q + e] - mu * c[e]) * rs; }
                u32x4 pk;
#pragma unroll
                for (int e = 0; e < 4; ++e) pk[e] = cvtpk(v[2 * e], v[2 * e + 1]);
                const int head = (cb & 3) * 8 + wc * 2 + hb;
                char* dst = isK ? ws + WS_KV + (size_t)head * 131072 + (tile >> 3) * 16384 + (tile & 7) * 1024 + lane * 16 : ws + WS_QF + (size_t)head * 65536 + tile * 1024 + lane * 16;
                st16_wt(dst, pk);
                float n2 = 0.f;
#pragma unroll
                for (int e = 0; e < 4; ++e) n2 = dot2(pk[e], pk[e], n2);
                kmx[hb] = fmaxf(kmx[hb], swap32_add(n2));
            } }
        if (isK) {
#pragma unroll
            for (int hb = 0; hb < 2; ++hb) { const float m = swap16_max(row16_max(kmx[hb]));
                if (lane == 0) __hip_atomic_store((GAS unsigned*)(ws + WS_KM) + ((cb & 3) * 8 + wc * 2 + hb) * 32 + rb * 2 + wr, __float_as_uint(m), RLX_AGENT); } }
    }
    STAMP(12);
    PUBLISH(FL_QKV + cb * 16 + rb);
    STAMP(13);
}

constexpr int A_ONES = 3 * 16384, A_LDS = A_ONES + 8192 + 512;
__device__ __forceinline__ void attn_role(char* __restrict__ ws, char* lds, int b, u64 tag) {
    const int tid = threadIdx.x, lane = tid & 63, wid = __builtin_amdgcn_readfirstlane(tid >> 6);
    const int head = (b & 7) * 4 + ((b >> 3) & 3), jb = b >> 5;
    const unsigned lds0 = (unsigned)(uintptr_t)lds;
    const char* kvb = ws + WS_KV + (size_t)head * 131072;
    for (int i = tid; i < (8192 + 512) / 16; i += 512) { const unsigned o1 = ((i & 11) == 0) ? 0x3f803f80u : 0u; *(u32x4*)(lds + A_ONES + i * 16) = (u32x4){o1, o1, o1, o1}; }
    if (wid == 0) {
        const int hb = head >> 3;
        const int idx = lane < 16 ? (4 + hb) * 16 + lane : lane < 32 ? (8 + hb) * 16 + (lane - 16) : hb * 16 + 2 * jb + (lane & 1);
        wait_flags(flagp(ws, FL_QKV + idx), lane < 34, tag);
        STAMP(14);
        }
    __syncthreads();
    STAMP(2);
    unsigned goff = wid * 2048 + lane * 16;
#pragma unroll
    for (int c = 0; c < 2; ++c)
#pragma unroll
        for (int k = 0; k < 2; ++k) glds16_wt(kvb + c * 16384 + goff + k * 1024, lds0 + c * 16384 + wid * 2048 + k * 1024);
    const int tile0 = jb * 8 + wid;
    u32x4 qa; unsigned kmu;
    asm volatile("global_load_dwordx4 %0, %2, off sc1\n\tglobal_load_dword %1, %3, off sc1\n\ts_waitcnt vmcnt(0)" : "=&v"(qa), "=&v"(kmu)
                 : "v"(ws + WS_QF + (size_t)head * 65536 + tile0 * 1024 + lane * 16), "v"(ws + WS_KM + (size_t)(head * 32 + (lane & 31)) * 4) : "memory");
    const float km = swap16_max(row16_max(__uint_as_float(kmu)));
    float na = 0.f;
#pragma unroll
    for (int e = 0; e < 4; ++e) na = dot2(qa[e], qa[e], na);
    na = swap32_add(na);
    float ma = __builtin_amdgcn_sqrtf(na * km);
    if (__any(!(ma <= 40.0f))) {
        float xa = -3.0e38f;
        for (int t = 0; t < 64; ++t) {
            const bf16x8 kf = __builtin_bit_cast(bf16x8, ld16_wt(kvb + (t >> 3) * 16384 + (t & 7) * 1024 + lane * 16));
            const f32x16 sa = MFMA(kf, __builtin_bit_cast(bf16x8, qa), f32x16{});
#pragma unroll
            for (int r = 0; r < 16; ++r) xa = fmaxf(xa, sa[r]); }
        ma = swap32_max(xa);
    }
    const float nma = -ma;
    const unsigned kaddr = lds0 + lane * 16;
    const bool ones = (lane & 16) != 0;
    const unsigned vaddr = ones ? lds0 + A_ONES + (lane & 15) * 16 : lds0 + 8192 + (lane >> 5) * 256 + (lane & 15) * 16;
    const unsigned vsel = ones ? 0u : 0xffffffffu;
    goff += 2 * 16384;
    unsigned dbeg = lds0 + wid * 2048, dend = dbeg + 3 * 16384, dslot = dbeg + 2 * 16384, nslot = 0x4000, cnt, m0save;
    unsigned oa0, oa1, oa2, oa3;
    STAMP(3);
    asm volatile(
    "s_mov_b32 %[m0save], m0\n"
    "v_mov_b32 v96, %[qa0]\n"
    "v_mov_b32 v97, %[qa1]\n"
    "v_mov_b32 v98, %[qa2]\n"
    "v_mov_b32 v99, %[qa3]\n"
    "v_mov_b32 v72, %[nma]\n"
    "v_mov_b32 v56, 0\n"
    "v_mov_b32 v73, %[nma]\n"
    "v_mov_b32 v57, 0\n"
    "v_mov_b32 v74, %[nma]\n"
    "v_mov_b32 v58, 0\n"
    "v_mov_b32 v75, %[nma]\n"
    "v_mov_b32 v59, 0\n"
    "v_mov_b32 v76, %[nma]\n"
    "v_mov_b32 v60, 0\n"
    "v_mov_b32 v77, %[nma]\n"
    "v_mov_b32 v61, 0\n"
    "v_mov_b32 v78, %[nma]\n"
    "v_mov_b32 v62, 0\n"
    "v_mov_b32 v79, %[nma]\n"
    "v_mov_b32 v63, 0\n"
    "v_mov_b32 v80, %[nma]\n"
    "v_mov_b32 v64, 0\n"
    "v_mov_b32 v81, %[nma]\n"
    "v_mov_b32 v65, 0\n"
    "v_mov_b32 v82, %[nma]\n"
    "v_mov_b32 v66, 0\n"
    "v_mov_b32 v83, %[nma]\n"
    "v_mov_b32 v67, 0\n"
    "v_mov_b32 v84, %[nma]\n"
    "v_mov_b32 v68, 0\n"
    "v_mov_b32 v85, %[nma]\n"
    "v_mov_b32 v69, 0\n"
    "v_mov_b32 v86, %[nma]\n"
    "v_mov_b32 v70, 0\n"
    "v_mov_b32 v87, %[nma]\n"
    "v_mov_b32 v71, 0\n"
    "v_mov_b32 v120, 0\n"
    "v_mov_b32 v121, 0\n"
    "v_mov_b32 v122, 0\n"
    "v_mov_b32 v123, 0\n"
    "v_mov_b32 v88, 0\n"
    "v_mov_b32 v89, 0\n"
    "v_mov_b32 v90, 0\n"
    "v_mov_b32 v91, 0\n"
    "v_mov_b32 v92, 0\n"
    "v_mov_b32 v93, 0\n"
    "v_mov_b32 v94, 0\n"
    "v_mov_b32 v95, 0\n"
    "v_mov_b32 v124, %[kaddr]\n"
    "v_mov_b32 v125, %[vaddr]\n"
    "v_add_u32 v126, 0x4000, v124\n"
    "v_and_b32 v127, 0x4000, %[vsel]\n"
    "v_add_u32 v127, v127, v125\n"
    "s_waitcnt vmcnt(2) lgkmcnt(0)\n"
    "s_barrier\n"
    "ds_read_b128 v[100:103], v124\n"
    "ds_read_b128 v[104:107], v124 offset:1024\n"
    "ds_read_b128 v[108:111], v125\n"
    "ds_read_b128 v[112:115], v125 offset:512\n"
    "s_mov_b32 %[cnt], 0\n"
    "s_waitcnt lgkmcnt(0)\n"
    "v_mfma_f32_32x32x16_bf16 v[24:39], v[100:103], v[96:99], v[72:87]\n"
    "s_nop 15\n"
    ".Lloop%=:\n"
    "v_exp_f32 v24, v24\n"
    "v_exp_f32 v25, v25\n"
    "v_mfma_f32_32x32x16_bf16 v[56:71], v[120:123], v[92:95], v[56:71]\n"
    "v_exp_f32 v26, v26\n"
    "v_exp_f32 v27, v27\n"
    "v_cvt_pk_bf16_f32 v88, v24, v25\n"
    "v_exp_f32 v28, v28\n"
    "s_waitcnt lgkmcnt(0)\n"
    "v_mfma_f32_32x32x16_bf16 v[40:55], v[104:107], v[96:99], v[72:87]\n"
    "v_exp_f32 v29, v29\n"
    "v_cvt_pk_bf16_f32 v89, v26, v27\n"
    "ds_read_b128 v[100:103], v124 offset:2048\n"
    "v_exp_f32 v30, v30\n"
    "ds_read_b128 v[116:119], v125 offset:1024\n"
    "v_exp_f32 v31, v31\n"
    "v_cvt_pk_bf16_f32 v90, v28, v29\n"
    "ds_read_b128 v[120:123], v125 offset:1536\n"
    "v_exp_f32 v32, v32\n"
    "v_exp_f32 v33, v33\n"
    "v_cvt_pk_bf16_f32 v91, v30, v31\n"
    "v_exp_f32 v34, v34\n"
    "v_exp_f32 v35, v35\n"
    "v_mfma_f32_32x32x16_bf16 v[56:71], v[108:111], v[88:91], v[56:71]\n"
    "v_cvt_pk_bf16_f32 v92, v32, v33\n"
    "v_exp_f32 v36, v36\n"
    "v_exp_f32 v37, v37\n"
    "v_cvt_pk_bf16_f32 v93, v34, v35\n"
    "v_exp_f32 v38, v38\n"
    "v_exp_f32 v39, v39\n"
    "v_cvt_pk_bf16_f32 v94, v36, v37\n"
    "v_cvt_pk_bf16_f32 v95, v38, v39\n"
    "v_exp_f32 v40, v40\n"
    "v_exp_f32 v41, v41\n"
    "v_mfma_f32_32x32x16_bf16 v[56:71], v[112:115], v[92:95], v[56:71]\n"
    "v_exp_f32 v42, v42\n"
    "v_exp_f32 v43, v43\n"
    "v_cvt_pk_bf16_f32 v88, v40, v41\n"
    "v_exp_f32 v44, v44\n"
    "s_waitcnt lgkmcnt(0)\n"
    "v_mfma_f32_32x32x16_bf16 v[24:39], v[100:103], v[96:99], v[72:87]\n"
    "v_exp_f32 v45, v45\n"
    "v_cvt_pk_bf16_f32 v89, v42, v43\n"
    "ds_read_b128 v[104:107], v124 offset:3072\n"
    "v_exp_f32 v46, v46\n"
    "ds_read_b128 v[108:111], v125 offset:2048\n"
    "v_exp_f32 v47, v47\n"
    "v_cvt_pk_bf16_f32 v90, v44, v45\n"
    "ds_read_b128 v[112:115], v125 offset:2560\n"
    "v_exp_f32 v48, v48\n"
    "v_exp_f32 v49, v49\n"
    "v_cvt_pk_bf16_f32 v91, v46, v47\n"
    "v_exp_f32 v50, v50\n"
    "v_exp_f32 v51, v51\n"
    "v_mfma_f32_32x32x16_bf16 v[56:71], v[116:119], v[88:91], v[56:71]\n"
    "v_cvt_pk_bf16_f32 v92, v48, v49\n"
    "v_exp_f32 v52, v52\n"
    "v_exp_f32 v53, v53\n"
    "v_cvt_pk_bf16_f32 v93, v50, v51\n"
    "v_exp_f32 v54, v54\n"
    "v_exp_f32 v55, v55\n"
    "v_cvt_pk_bf16_f32 v94, v52, v53\n"
    "v_cvt_pk_bf16_f32 v95, v54, v55\n"
    "v_exp_f32 v24, v24\n"
    "v_exp_f32 v25, v25\n"
    "v_mfma_f32_32x32x16_bf16 v[56:71], v[120:123], v[92:95], v[56:71]\n"
    "v_exp_f32 v26, v26\n"
    "v_exp_f32 v27, v27\n"
    "v_cvt_pk_bf16_f32 v88, v24, v25\n"
    "v_exp_f32 v28, v28\n"
    "s_waitcnt lgkmcnt(0)\n"
    "v_mfma_f32_32x32x16_bf16 v[40:55], v[104:107], v[96:99], v[72:87]\n"
    "v_exp_f32 v29, v29\n"
    "v_cvt_pk_bf16_f32 v89, v26, v27\n"
    "ds_read_b128 v[100:103], v124 offset:4096\n"
    "v_exp_f32 v30, v30\n"
    "ds_read_b128 v[116:119], v125 offset:3072\n"
    "v_exp_f32 v31, v31\n"
    "v_cvt_pk_bf16_f32 v90, v28, v29\n"
    "ds_read_b128 v[120:123], v125 offset:3584\n"
    "v_exp_f32 v32, v32\n"
    "v_exp_f32 v33, v33\n"
    "v_cvt_pk_bf16_f32 v91, v30, v31\n"
    "v_exp_f32 v34, v34\n"
    "v_exp_f32 v35, v35\n"
    "v_mfma_f32_32x32x16_bf16 v[56:71], v[108:111], v[88:91], v[56:71]\n"
    "v_cvt_pk_bf16_f32 v92, v32, v33\n"
    "v_exp_f32 v36, v36\n"
    "v_exp_f32 v37, v37\n"
    "v_cvt_pk_bf16_f32 v93, v34, v35\n"
    "v_exp_f32 v38, v38\n"
    "v_exp_f32 v39, v39\n"
    "v_cvt_pk_bf16_f32 v94, v36, v37\n"
    "v_cvt_pk_bf16_f32 v95, v38, v39\n"
    "v_exp_f32 v40, v40\n"
    "v_exp_f32 v41, v41\n"
    "v_mfma_f32_32x32x16_bf16 v[56:71], v[112:115], v[92:95], v[56:71]\n"
    "v_exp_f32 v42, v42\n"
    "v_exp_f32 v43, v43\n"
    "v_cvt_pk_bf16_f32 v88, v40, v41\n"
    "v_exp_f32 v44, v44\n"
    "s_waitcnt lgkmcnt(0)\n"
    "v_mfma_f32_32x32x16_bf16 v[24:39], v[100:103], v[96:99], v[72:87]\n"
    "v_exp_f32 v45, v45\n"
    "v_cvt_pk_bf16_f32 v89, v42, v43\n"
    "ds_read_b128 v[104:107], v124 offset:5120\n"
    "v_exp_f32 v46, v46\n"
    "ds_read_b128 v[108:111], v125 offset:4096\n"
    "v_exp_f32 v47, v47\n"
    "v_cvt_pk_bf16_f32 v90, v44, v45\n"
    "ds_read_b128 v[112:115], v125 offset:4608\n"
    "v_exp_f32 v48, v48\n"
    "v_exp_f32 v49, v49\n"
    "v_cvt_pk_bf16_f32 v91, v46, v47\n"
    "v_exp_f32 v50, v50\n"
    "v_exp_f32 v51, v51\n"
    "v_mfma_f32_32x32x16_bf16 v[56:71], v[116:119], v[88:91], v[56:71]\n"
    "v_cvt_pk_bf16_f32 v92, v48, v49\n"
    "v_exp_f32 v52, v52\n"
    "v_exp_f32 v53, v53\n"
    "v_cvt_pk_bf16_f32 v93, v50, v51\n"
    "v_exp_f32 v54, v54\n"
    "v_exp_f32 v55, v55\n"
    "v_cvt_pk_bf16_f32 v94, v52, v53\n"
    "v_cvt_pk_bf16_f32 v95, v54, v55\n"
    "v_exp_f32 v24, v24\n"
    "v_exp_f32 v25, v25\n"
    "v_mfma_f32_32x32x16_bf16 v[56:71], v[120:123], v[92:95], v[56:71]\n"
    "v_exp_f32 v26, v26\n"
    "v_exp_f32 v27, v27\n"
    "v_cvt_pk_bf16_f32 v88, v24, v25\n"
    "v_exp_f32 v28, v28\n"
    "s_waitcnt lgkmcnt(0)\n"
    "v_mfma_f32_32x32x16_bf16 v[40:55], v[104:107], v[96:99], v[72:87]\n"
    "v_exp_f32 v29, v29\n"
    "v_cvt_pk_bf16_f32 v89, v26, v27\n"
    "ds_read_b128 v[100:103], v124 offset:6144\n"
    "v_exp_f32 v30, v30\n"
    "ds_read_b128 v[116:119], v125 offset:5120\n"
    "v_exp_f32 v31, v31\n"
    "v_cvt_pk_bf16_f32 v90, v28, v29\n"
    "ds_read_b128 v[120:123], v125 offset:5632\n"
    "v_exp_f32 v32, v32\n"
    "v_exp_f32 v33, v33\n"
    "v_cvt_pk_bf16_f32 v91, v30, v31\n"
    "v_exp_f32 v34, v34\n"
    "v_exp_f32 v35, v35\n"
    "v_mfma_f32_32x32x16_bf16 v[56:71], v[108:111], v[88:91], v[56:71]\n"
    "v_cvt_pk_bf16_f32 v92, v32, v33\n"
    "v_exp_f32 v36, v36\n"
    "v_exp_f32 v37, v37\n"
    "v_cvt_pk_bf16_f32 v93, v34, v35\n"
    "v_exp_f32 v38, v38\n"
    "v_exp_f32 v39, v39\n"
    "v_cvt_pk_bf16_f32 v94, v36, v37\n"
    "v_cvt_pk_bf16_f32 v95, v38, v39\n"
    "v_exp_f32 v40, v40\n"
    "v_exp_f32 v41, v41\n"
    "v_mfma_f32_32x32x16_bf16 v[56:71], v[112:115], v[92:95], v[56:71]\n"
    "v_exp_f32 v42, v42\n"
    "v_exp_f32 v43, v43\n"
    "v_cvt_pk_bf16_f32 v88, v40, v41\n"
    "v_exp_f32 v44, v44\n"
    "s_waitcnt lgkmcnt(0)\n"
    "v_mfma_f32_32x32x16_bf16 v[24:39], v[100:103], v[96:99], v[72:87]\n"
    "v_exp_f32 v45, v45\n"
    "v_cvt_pk_bf16_f32 v89, v42, v43\n"
    "ds_read_b128 v[104:107], v124 offset:7168\n"
    "v_exp_f32 v46, v46\n"
    "ds_read_b128 v[108:111], v125 offset:6144\n"
    "v_exp_f32 v47, v47\n"
    "v_cvt_pk_bf16_f32 v90, v44, v45\n"
    "ds_read_b128 v[112:115], v125 offset:6656\n"
    "v_exp_f32 v48, v48\n"
    "v_exp_f32 v49, v49\n"
    "v_cvt_pk_bf16_f32 v91, v46, v47\n"
    "v_exp_f32 v50, v50\n"
    "v_exp_f32 v51, v51\n"
    "v_mfma_f32_32x32x16_bf16 v[56:71], v[116:119], v[88:91], v[56:71]\n"
    "v_cvt_pk_bf16_f32 v92, v48, v49\n"
    "v_exp_f32 v52, v52\n"
    "v_exp_f32 v53, v53\n"
    "v_cvt_pk_bf16_f32 v93, v50, v51\n"
    "v_exp_f32 v54, v54\n"
    "v_exp_f32 v55, v55\n"
    "v_cvt_pk_bf16_f32 v94, v52, v53\n"
    "v_cvt_pk_bf16_f32 v95, v54, v55\n"
    "v_exp_f32 v24, v24\n"
    "v_exp_f32 v25, v25\n"
    "v_mfma_f32_32x32x16_bf16 v[56:71], v[120:123], v[92:95], v[56:71]\n"
    "v_exp_f32 v26, v26\n"
    "v_exp_f32 v27, v27\n"
    "v_cvt_pk_bf16_f32 v88, v24, v25\n"
    "v_exp_f32 v28, v28\n"
    "s_waitcnt lgkmcnt(0)\n"
    "v_mfma_f32_32x32x16_bf16 v[40:55], v[104:107], v[96:99], v[72:87]\n"
    "v_exp_f32 v29, v29\n"
    "v_cvt_pk_bf16_f32 v89, v26, v27\n"
    "s_waitcnt vmcnt(0)\n"
    "s_barrier\n"
    "ds_read_b128 v[100:103], v126 offset:0\n"
    "v_exp_f32 v30, v30\n"
    "ds_read_b128 v[116:119], v125 offset:7168\n"
    "v_exp_f32 v31, v31\n"
    "v_cvt_pk_bf16_f32 v90, v28, v29\n"
    "ds_read_b128 v[120:123], v125 offset:7680\n"
    "v_exp_f32 v32, v32\n"
    "v_exp_f32 v33, v33\n"
    "v_cvt_pk_bf16_f32 v91, v30, v31\n"
    "v_exp_f32 v34, v34\n"
    "v_exp_f32 v35, v35\n"
    "v_mfma_f32_32x32x16_bf16 v[56:71], v[108:111], v[88:91], v[56:71]\n"
    "s_cmp_ge_u32 %[cnt], 6\n"
    "s_cbranch_scc1 .Lnodma%=\n"
    "s_mov_b32 m0, %[dslot]\n"
    "s_nop 0\n"
    "global_load_lds_dwordx4 %[goff], %[gbase] offset:0\n"
    "global_load_lds_dwordx4 %[goff], %[gbase] offset:1024\n"
    ".Lnodma%=:\n"
    "v_cvt_pk_bf16_f32 v92, v32, v33\n"
    "v_exp_f32 v36, v36\n"
    "v_exp_f32 v37, v37\n"
    "v_cvt_pk_bf16_f32 v93, v34, v35\n"
    "v_exp_f32 v38, v38\n"
    "v_exp_f32 v39, v39\n"
    "v_cvt_pk_bf16_f32 v94, v36, v37\n"
    "v_cvt_pk_bf16_f32 v95, v38, v39\n"
    "v_exp_f32 v40, v40\n"
    "v_exp_f32 v41, v41\n"
    "v_mfma_f32_32x32x16_bf16 v[56:71], v[112:115], v[92:95], v[56:71]\n"
    "v_exp_f32 v42, v42\n"
    "v_exp_f32 v43, v43\n"
    "v_cvt_pk_bf16_f32 v88, v40, v41\n"
    "v_exp_f32 v44, v44\n"
    "s_waitcnt lgkmcnt(0)\n"
    "v_mfma_f32_32x32x16_bf16 v[24:39], v[100:103], v[96:99], v[72:87]\n"
    "v_exp_f32 v45, v45\n"
    "v_cvt_pk_bf16_f32 v89, v42, v43\n"
    "ds_read_b128 v[104:107], v126 offset:1024\n"
    "v_exp_f32 v46, v46\n"
    "ds_read_b128 v[108:111], v127 offset:0\n"
    "v_exp_f32 v47, v47\n"
    "v_cvt_pk_bf16_f32 v90, v44, v45\n"
    "ds_read_b128 v[112:115], v127 offset:512\n"
    "v_exp_f32 v48, v48\n"
    "v_exp_f32 v49, v49\n"
    "v_cvt_pk_bf16_f32 v91, v46, v47\n"
    "v_exp_f32 v50, v50\n"
    "v_exp_f32 v51, v51\n"
    "v_mfma_f32_32x32x16_bf16 v[56:71], v[116:119], v[88:91], v[56:71]\n"
    "v_cvt_pk_bf16_f32 v92, v48, v49\n"
    "v_exp_f32 v52, v52\n"
    "v_exp_f32 v53, v53\n"
    "v_cvt_pk_bf16_f32 v93, v50, v51\n"
    "v_exp_f32 v54, v54\n"
    "v_exp_f32 v55, v55\n"
    "v_cvt_pk_bf16_f32 v94, v52, v53\n"
    "v_cvt_pk_bf16_f32 v95, v54, v55\n"
    "v_mov_b32 v124, v126\n"
    "v_mov_b32 v125, v127\n"
    "s_add_u32 %[nslot], %[nslot], 0x4000\n"
    "s_cmp_eq_u32 %[nslot], 0xc000\n"
    "s_cselect_b32 %[nslot], 0, %[nslot]\n"
    "v_add_u32 v126, %[nslot], %[kaddr]\n"
    "v_and_b32 v127, %[nslot], %[vsel]\n"
    "v_add_u32 v127, v127, %[vaddr]\n"
    "s_add_u32 %[dslot], %[dslot], 0x4000\n"
    "s_cmp_eq_u32 %[dslot], %[dend]\n"
    "s_cselect_b32 %[dslot], %[dbeg], %[dslot]\n"
    "v_add_u32 %[goff], 0x4000, %[goff]\n"
    "s_add_u32 %[cnt], %[cnt], 1\n"
    "s_cmp_lt_u32 %[cnt], 8\n"
    "s_cbranch_scc1 .Lloop%=\n"
    "s_nop 1\n"
    "v_mfma_f32_32x32x16_bf16 v[56:71], v[120:123], v[92:95], v[56:71]\n"
    "s_nop 15\n"
    "s_nop 3\n"
    "v_rcp_f32 v24, v64\n"
    "s_nop 0\n"
    "v_mul_f32 v56, v56, v24\n"
    "v_mul_f32 v57, v57, v24\n"
    "v_mul_f32 v58, v58, v24\n"
    "v_mul_f32 v59, v59, v24\n"
    "v_mul_f32 v60, v60, v24\n"
    "v_mul_f32 v61, v61, v24\n"
    "v_mul_f32 v62, v62, v24\n"
    "v_mul_f32 v63, v63, v24\n"
    "v_cvt_pk_bf16_f32 %[oa0], v56, v57\n"
    "v_cvt_pk_bf16_f32 %[oa1], v58, v59\n"
    "v_cvt_pk_bf16_f32 %[oa2], v60, v61\n"
    "v_cvt_pk_bf16_f32 %[oa3], v62, v63\n"
    "s_waitcnt lgkmcnt(0)\n"
    "s_mov_b32 m0, %[m0save]\n"
        : [oa0] "=&v"(oa0), [oa1] "=&v"(oa1), [oa2] "=&v"(oa2), [oa3] "=&v"(oa3),
          [goff] "+v"(goff), [dslot] "+s"(dslot), [nslot] "+s"(nslot), [cnt] "=&s"(cnt), [m0save] "=&s"(m0save)
        : [qa0] "v"(qa[0]), [qa1] "v"(qa[1]), [qa2] "v"(qa[2]), [qa3] "v"(qa[3]),
          [nma] "v"(nma), [kaddr] "v"(kaddr), [vaddr] "v"(vaddr), [vsel] "v"(vsel), [gbase] "s"(kvb), [dbeg] "s"(dbeg), [dend] "s"(dend), [psel] "s"(0x07060302u), [dbgaddr] "s"(lds0 + A_LDS + wid * 64)
        : "memory", "scc", "vcc",
          "v24", "v25", "v26", "v27", "v28", "v29", "v30", "v31", "v32", "v33", "v34", "v35", "v36", "v37", "v38", "v39", "v40", "v41", "v42", "v43", "v44", "v45", "v46", "v47", "v48", "v49", "v50", "v51", "v52", "v53", "v54", "v55", "v56", "v57", "v58", "v59", "v60", "v61", "v62", "v63", "v64", "v65", "v66", "v67", "v68", "v69", "v70", "v71", "v72", "v73", "v74", "v75", "v76", "v77", "v78", "v79", "v80", "v81", "v82", "v83", "v84", "v85", "v86", "v87", "v88", "v89", "v90", "v91", "v92", "v93", "v94", "v95", "v96", "v97", "v98", "v99", "v100", "v101", "v102", "v103", "v104", "v105", "v106", "v107", "v108", "v109", "v110", "v111", "v112", "v113", "v114", "v115", "v116", "v117", "v118", "v119", "v120", "v121", "v122", "v123", "v124", "v125", "v126", "v127"
    );
    STAMP(4);
    st16_wt(ws + WS_OF + ((size_t)tile0 * 32 + head) * 1024 + lane * 16, (u32x4){oa0, oa1, oa2, oa3});
    PUBLISH(FL_O + jb * 32 + head);
}

__device__ __forceinline__ void z_role(const uint16_t* __restrict__ x, const uint16_t* __restrict__ win, char* __restrict__ ws, char* lds, int g, u64 tag) {
    const int tid = threadIdx.x, lane = tid & 63, wid = __builtin_amdgcn_readfirstlane(tid >> 6), wr = wid >> 2, wc = wid & 3, l31 = lane & 31, hh = lane >> 5;
    const int xcd = g & 7, i = g >> 3;
    const int rb = 4 * (xcd >> 1) + (i & 3), cbz = 7 * ((xcd & 1) ^ 1) + (i >> 2);
    f32x16 acc[2];
    proj_main<false>(x, win, NIN, rb, cbz * 128, 12 + cbz, ws, lds, ((i & 3) * 4 + (i >> 2) * 3) & 15, tag, acc);
    STAMP(1);
    const float* st = (const float*)(lds + P_ST);
    unsigned zp[2][8];
#pragma unroll
    for (int rt = 0; rt < 2; ++rt) { const int tr = wr * 64 + rt * 32; const float mu = st[tr + l31], rs = st[128 + tr + l31];
#pragma unroll
        for (int q = 0; q < 4; ++q) { const f32x4 c = *(const f32x4*)(st + 256 + wc * 32 + 8 * q + 4 * hh);
            zp[rt][2 * q] = cvtpk(gelu_t((acc[rt][4 * q] - mu * c[0]) * rs), gelu_t((acc[rt][4 * q + 1] - mu * c[1]) * rs));
            zp[rt][2 * q + 1] = cvtpk(gelu_t((acc[rt][4 * q + 2] - mu * c[2]) * rs), gelu_t((acc[rt][4 * q + 3] - mu * c[3]) * rs)); } }
    if (cbz < 7) {
#pragma unroll
        for (int rt = 0; rt < 2; ++rt) { const int tt = rb * 4 + wr * 2 + rt;
            char* dst = ws + WS_Z1 + ((size_t)tt * 28 + cbz * 4 + wc) * 2048 + lane * 32;
            st16_wt(dst, (u32x4){zp[rt][0], zp[rt][1], zp[rt][2], zp[rt][3]}); st16_wt(dst + 16, (u32x4){zp[rt][4], zp[rt][5], zp[rt][6], zp[rt][7]}); }
    } else {
        const int cj = cbz - 7;
        float* pst = (float*)lds;
        const unsigned ONES = 0x3f803f80u;
#pragma unroll
        for (int rt = 0; rt < 2; ++rt) { float s1 = 0.f, s2 = 0.f;
#pragma unroll
            for (int e = 0; e < 8; ++e) { s1 = dot2(zp[rt][e], ONES, s1); s2 = dot2(zp[rt][e], zp[rt][e], s2); }
            s1 = swap32_add(s1); s2 = swap32_add(s2);
            if (hh == 0) *(f32x2*)(pst + (wc * 128 + wr * 64 + rt * 32 + l31) * 2) = (f32x2){s1, s2}; }
        __syncthreads();
        char* gst = ws + WS_ST + (size_t)rb * 7 * 1024;
        if (tid < 128) { f32x2 s = *(const f32x2*)(pst + tid * 2);
#pragma unroll
            for (int j = 1; j < 4; ++j) s += *(const f32x2*)(pst + (j * 128 + tid) * 2);
            st8_wt(gst + (cj * 128 + tid) * 8, __builtin_bit_cast(u64, s)); }
        PUBLISH(FL_ST + rb * 7 + cj);
        if (wid == 0) wait_flags(flagp(ws, FL_ST + rb * 7 + lane), lane < 7, tag);
        __syncthreads();
        float* fin = (float*)(lds + 4096);
        if (tid < 128) { f32x2 s = {0.f, 0.f};
#pragma unroll
            for (int j = 0; j < 7; ++j) s += __builtin_bit_cast(f32x2, ld8_wt(gst + (j * 128 + tid) * 8));
            const float mu = s[0] * (1.0f / FH); const float var = fmaxf(s[1] * (1.0f / FH) - mu * mu, 0.f);
            fin[tid] = mu; fin[128 + tid] = __builtin_amdgcn_rsqf(var + LN_EPS); }
        __syncthreads();
        char* img = lds + 8192;
#pragma unroll
        for (int rt = 0; rt < 2; ++rt) { const int tok = wr * 64 + rt * 32 + l31; const float mu = fin[tok], rs = fin[128 + tok];
#pragma unroll
            for (int q = 0; q < 4; ++q) { const unsigned a = zp[rt][2 * q], c = zp[rt][2 * q + 1];
                const u32x2 o = {cvtpk((bflo(a) - mu) * rs, (bfhi(a) - mu) * rs), cvtpk((bflo(c) - mu) * rs, (bfhi(c) - mu) * rs)};
                const int f = wc * 32 + 8 * q + 4 * hh;
                *(u32x2*)(img + tok * 256 + (((f >> 3) ^ wsw(tok)) * 16) + (f & 4) * 2) = o; } }
        __syncthreads();
        { const int blk = (lane >> 4) & 1, q = (lane & 15) >> 2, p = lane & 3;
#pragma unroll
          for (int kq = 0; kq < 4; ++kq) { const int ks = wr * 4 + kq; s16x4 t2[2];
#pragma unroll
              for (int t = 0; t < 2; ++t) { const int kr = 16 * ks + 8 * hh + 4 * t + q; const int ch = 4 * wc + 2 * blk + (p >> 1);
                  t2[t] = trread((lds_cptr)img + kr * 256 + ((ch ^ wsw(kr)) * 16) + 8 * (p & 1)); }
              st16_wt(ws + WS_Z2N + ((size_t)(cj * 4 + wc) * 128 + rb * 8 + ks) * 1024 + lane * 16, __builtin_bit_cast(u32x4, cat(t2[0], t2[1]))); } }
    }
    PUBLISH(FL_Z + rb * 14 + cbz);
}

constexpr int S_SLOT = 24576, S_LDS = 3 * S_SLOT;
__device__ __forceinline__ void spatial_role(const uint16_t* __restrict__ wsp, const uint16_t* __restrict__ bsp, char* __restrict__ ws, char* lds, int g, u64 tag) {
    const int tid = threadIdx.x, lane = tid & 63, wid8 = __builtin_amdgcn_readfirstlane(tid >> 6), wid = wid8 & 3, l31 = lane & 31, hh = lane >> 5;
    const int i = g >> 3, tb = 4 * (g & 7) + (i & 3), fb = i >> 2;
    const unsigned lds0 = (unsigned)(uintptr_t)lds;
    if (wid8 == 0) {
        wait_flags(flagp(ws, lane < 16 ? FL_Z + lane * 14 + 7 + fb : FL_Z + (tb >> 1) * 14 + fb), lane < 17, tag); }
    __syncthreads();
    STAMP(3);
    if (wid8 >= 4) {
        const int r0 = 16 * wid + (lane >> 3), r1 = r0 + 8;
        const char* sb_ = (const char*)(wsp + (size_t)tb * 64 * SEQ);
        const unsigned vo0 = (r0 * SEQ + ((lane & 7) ^ ((r0 >> 1) & 7)) * 8) * 2, vo1 = (r1 * SEQ + ((lane & 7) ^ ((r1 >> 1) & 7)) * 8) * 2;
        const char* zs = ws + WS_Z2N + (size_t)(fb * 4 + wid) * 131072; const unsigned zvo = lane * 16;
        auto issue = [&](int it, int slot) { const unsigned so = lds0 + slot * S_SLOT;
            set_m0(so + wid * 2048); glds_s<0, false>(sb_ + it * 128, vo0);
            set_m0(so + wid * 2048 + 1024); glds_s<0, false>(sb_ + it * 128, vo1);
            set_m0(so + 8192 + wid * 4096);
            glds_s<0, true>(zs + it * 4096, zvo); glds_s<1024, true>(zs + it * 4096, zvo); glds_s<2048, true>(zs + it * 4096, zvo); glds_s<3072, true>(zs + it * 4096, zvo); };
        issue(0, 0); issue(1, 1); issue(2, 2);
        WAIT_BAR(12);
        int slot = 0;
#pragma unroll 1
        for (int it = 1; it < 31; ++it) { WAIT_BAR(6); if (it + 2 < 32) issue(it + 2, slot); slot = slot == 2 ? 0 : slot + 1; }
        WAIT_BAR(0);
        BAR();
    } else {
        unsigned boff[2][4];
#pragma unroll
        for (int rt = 0; rt < 2; ++rt)
#pragma unroll
            for (int ks = 0; ks < 4; ++ks) { const int r = rt * 32 + l31; boff[rt][ks] = (r * 128 + ((hh ^ ((r >> 1) & 7)) * 16)) ^ (ks * 32); }
        const unsigned aoff = 8192 + wid * 4096 + lane * 16;
        struct SF { bf16x8 a, b[2]; };
        auto rd = [&](SF& f, int slot, int ks) { lds_cptr sb = (lds_cptr)lds + slot * S_SLOT;
            f.a = ldsfrag(sb + aoff + ks * 1024); f.b[0] = ldsfrag(sb + boff[0][ks]); f.b[1] = ldsfrag(sb + boff[1][ks]); };
        f32x16 acc[2] = {f32x16{}, f32x16{}};
        auto mm = [&](const SF& f) { acc[0] = MFMA(f.a, f.b[0], acc[0]); acc[1] = MFMA(f.a, f.b[1], acc[1]); };
        SF f[4];
        BAR();
#pragma unroll
        for (int ks = 0; ks < 4; ++ks) rd(f[ks], 0, ks);
        auto step = [&](int nsl) {
            LGKM_BAR();
#pragma unroll
            for (int ks = 0; ks < 4; ++ks) { lds_cptr sb = (lds_cptr)lds + nsl * S_SLOT;
                acc[0] = MFMA(f[ks].a, f[ks].b[0], acc[0]); SBAR();
                f[ks].b[0] = ldsfrag(sb + boff[0][ks]); SBAR();
                PACE();
                acc[1] = MFMA(f[ks].a, f[ks].b[1], acc[1]); SBAR();
                f[ks].b[1] = ldsfrag(sb + boff[1][ks]); f[ks].a = ldsfrag(sb + aoff + ks * 1024); SBAR();
                PACE(); } };
#pragma unroll 1
        for (int io = 0; io < 10; ++io) { step(1); step(2); step(0); }
        step(1); step(2);
#pragma unroll
        for (int rt = 0; rt < 2; ++rt) { const int tt = tb * 2 + rt; const float bias = __uint_as_float((unsigned)bsp[tt * 32 + l31] << 16);
            const char* z1p = ws + WS_Z1 + ((size_t)tt * 28 + fb * 4 + wid) * 2048 + lane * 32;
            const u32x4 za = ld16_wt(z1p), zb = ld16_wt(z1p + 16);
            u32x4 ua, ub;
#pragma unroll
            for (int e = 0; e < 4; ++e) {
                ua[e] = cvtpk(bflo(za[e]) * (acc[rt][2 * e] + bias), bfhi(za[e]) * (acc[rt][2 * e + 1] + bias));
                ub[e] = cvtpk(bflo(zb[e]) * (acc[rt][8 + 2 * e] + bias), bfhi(zb[e]) * (acc[rt][8 + 2 * e + 1] + bias)); }
            char* ud = ws + WS_U + ((size_t)tt * 56 + (fb * 4 + wid) * 2) * 1024 + lane * 16;
            st16_wt(ud, ua); st16_wt(ud + 1024, ub); }
    }
    PUBLISH(FL_U + tb * 7 + fb);
}

constexpr int F_SLOT = 8192 + 8192, F_LDS = 4 * F_SLOT;
__device__ __forceinline__ void final_role(const uint16_t* __restrict__ x, const uint16_t* __restrict__ wo, const uint16_t* __restrict__ wout, char* __restrict__ ws, uint16_t* __restrict__ out, char* lds, int g, u64 tag) {
    const int tb = 4 * (g & 7) + ((g >> 3) & 3), db = g >> 5;
    const int tid = threadIdx.x, lane = tid & 63, wid8 = __builtin_amdgcn_readfirstlane(tid >> 6), wid = wid8 & 3, wr = wid >> 1, wc = wid & 1, l31 = lane & 31, hh = lane >> 5;
    const unsigned lds0 = (unsigned)(uintptr_t)lds;
    auto wswz = [](int r) { return ((r & 2) << 1) | ((r >> 2) & 3); };
    f32x16 as = f32x16{}, ag = f32x16{};
    const int k0 = 16 * wid + (lane >> 3), k1 = k0 + 8;
    const unsigned wo0 = (k0 * DM + db * 64 + ((lane & 7) ^ wswz(k0)) * 8) * 2, wo1 = (k1 * DM + db * 64 + ((lane & 7) ^ wswz(k1)) * 8) * 2;
    const int att = tb * 2 + (wid & 1), aks = 2 * (wid >> 1);
    unsigned woff[2];
    { const int blk = (lane >> 4) & 1, q = (lane & 15) >> 2, p = lane & 3; const int kr = 4 * hh + q; const int ch = 4 * wc + 2 * blk + (p >> 1);
      woff[0] = kr * 128 + ((ch ^ wswz(kr)) * 16) + 8 * (p & 1); woff[1] = (woff[0] + 1024) ^ 32; }
    const unsigned aoff = 8192 + wr * 4096 + lane * 16;
    struct FF { bf16x8 w, a; };
    auto rd = [&](FF& f, int slot, int ks) { lds_cptr sb = (lds_cptr)lds + slot * F_SLOT;
        f.w = cat(trread(sb + woff[0] + ks * 2048), trread(sb + woff[1] + ks * 2048)); f.a = ldsfrag(sb + aoff + ks * 1024); };
    auto pass = [&](const uint16_t* wb, const char* ab, int nfr, int n, f32x16& acc, bool pace) {
        if (wid8 >= 4) {
            auto issue = [&](int it) { const unsigned so = lds0 + (it & 3) * F_SLOT; const char* w = (const char*)(wb + (size_t)it * 64 * DM);
                set_m0(so + wid * 2048); glds_s<0, false>(w, wo0);
                set_m0(so + wid * 2048 + 1024); glds_s<0, false>(w, wo1);
                const char* a = ab + ((size_t)att * nfr + it * 4 + aks) * 1024;
                set_m0(so + 8192 + (wid & 1) * 4096 + aks * 1024); glds_s<0, true>(a, lane * 16); glds_s<1024, true>(a, lane * 16); };
            issue(0); issue(1); issue(2); issue(3);
            WAIT_BAR(12);
#pragma unroll 1
            for (int i = 1; i < n - 2; ++i) { WAIT_BAR(8); if (i + 3 < n) issue(i + 3); }
            WAIT_BAR(4);
            WAIT_BAR(0);
            BAR();
        } else {
            FF fa[4], fb[4];
            BAR();
#pragma unroll
            for (int ks = 0; ks < 4; ++ks) rd(fa[ks], 0, ks);
            auto step = [&](int nsl, FF (&cur)[4], FF (&nxt)[4]) {
                LGKM_BAR();
#pragma unroll
                for (int ks = 0; ks < 4; ++ks) { acc = MFMA(cur[ks].w, cur[ks].a, acc); SBAR(); rd(nxt[ks], nsl, ks); SBAR(); if (pace) PACE(); } };
#pragma unroll 1
            for (int it = 0; it < n; it += 2) { step((it + 1) & 3, fa, fb); step((it + 2) & 3, fb, fa); }
        } };
    if (wid8 == 0) wait_flags(flagp(ws, FL_U + tb * 7 + lane), lane < 7, tag);
    __syncthreads();
    STAMP(5);
    pass(wout, ws + WS_U, 56, 14, ag, true);
    STAMP(6);
    if (wid8 == 0) wait_flags(flagp(ws, FL_O + (tb >> 2) * 32 + lane), lane < 32, tag);
    __syncthreads();
    STAMP(7);
    pass(wo, ws + WS_OF, 32, 8, as, false);
    STAMP(8);
    if (wid8 < 4) {
        const int tok = tb * 64 + wr * 32 + l31;
#pragma unroll
        for (int q = 0; q < 4; ++q) { const size_t o = (size_t)tok * DM + db * 64 + wc * 32 + 8 * q + 4 * hh;
            const u32x2 xv = *(const u32x2*)(x + o);
            const float r0 = bflo(xv[0]) + as[4 * q] * silu_f(ag[4 * q]), r1 = bfhi(xv[0]) + as[4 * q + 1] * silu_f(ag[4 * q + 1]);
            const float r2 = bflo(xv[1]) + as[4 * q + 2] * silu_f(ag[4 * q + 2]), r3 = bfhi(xv[1]) + as[4 * q + 3] * silu_f(ag[4 * q + 3]);
            *(u32x2*)(out + o) = (u32x2){cvtpk(r0, r1), cvtpk(r2, r3)}; }
    }
}

#define GDELAY 0
#define GPRIO 2
constexpr int ALL_LDS = S_LDS;
__global__ __launch_bounds__(512, 4) void k_all(const uint16_t* __restrict__ x, const uint16_t* __restrict__ wqkv, const uint16_t* __restrict__ wo, const uint16_t* __restrict__ win,
                                                const uint16_t* __restrict__ wsp, const uint16_t* __restrict__ bsp, const uint16_t* __restrict__ wout, char* __restrict__ ws, uint16_t* __restrict__ out) {
    extern __shared__ __attribute__((aligned(16))) char lds[];
    const int b = blockIdx.x;
    const u64 tag = *(const __attribute__((address_space(4))) u64*)(ws + WS_FLAG + FL_EPOCH * 128) + 1;
    STAMP(0);
    if (b < 256) {
        __builtin_amdgcn_s_setprio(3);
        if (b < 192) qkv_role(x, wqkv, ws, lds, b, tag); else stats_role(x, wqkv, win, ws, lds, b - 192, tag);
        __builtin_amdgcn_s_setprio(0);
        __syncthreads();
        attn_role(ws, lds, b, tag);
        STAMP(5);
    } else {
        const int g = b - 256;
        __builtin_amdgcn_s_setprio(GPRIO);
            if (threadIdx.x == 0) __hip_atomic_store(flagp(ws, FL_G + g), tag, RLX_AGENT);
        { const u64 t0 = __builtin_amdgcn_s_memrealtime(); while (__builtin_amdgcn_s_memrealtime() - t0 < GDELAY) __builtin_amdgcn_s_sleep(8); }
        if (g < 224) { z_role(x, win, ws, lds, g, tag); STAMP(2); __syncthreads(); spatial_role(wsp, bsp, ws, lds, g, tag); STAMP(4); __syncthreads(); }
        final_role(x, wo, wout, ws, out, lds, g, tag);
        if (g == 255 && threadIdx.x < 64) {
            const int lane = threadIdx.x;
#pragma unroll 1
            for (int c = 0; c < 4; ++c) { wait_flags(flagp(ws, FL_O + c * 64 + lane), true, tag); wait_flags(flagp(ws, FL_G + c * 64 + lane), true, tag); }
            if (lane == 0) __hip_atomic_store(flagp(ws, FL_EPOCH), tag, RLX_AGENT);
        }
        STAMP(9);
    }
}

extern "C" void kernel_launch(void* const* d_in, const int* in_sizes, int n_in, void* d_out, int out_size, void* d_ws, size_t ws_size, hipStream_t stream) {
    const uint16_t* x = (const uint16_t*)d_in[0]; const uint16_t* wqkv = (const uint16_t*)d_in[1]; const uint16_t* wo = (const uint16_t*)d_in[2];
    const uint16_t* win = (const uint16_t*)d_in[4]; const uint16_t* wsp = (const uint16_t*)d_in[5]; const uint16_t* bsp = (const uint16_t*)d_in[6]; const uint16_t* wout = (const uint16_t*)d_in[7];
    (void)hipFuncSetAttribute((const void*)k_all, hipFuncAttributeMaxDynamicSharedMemorySize, ALL_LDS);
    k_all<<<512, 512, ALL_LDS, stream>>>(x, wqkv, wo, win, wsp, bsp, wout, (char*)d_ws, (uint16_t*)d_out);
}
```

```cpp
#include <hip/hip_runtime.h>
#include <hip/hip_bf16.h>
#include <stdint.h>
#define PRINT_ST 1
#define PACE_N 2
#include <stdio.h>

typedef short bf16x8 __attribute__((ext_vector_type(8)));
typedef short s16x4 __attribute__((ext_vector_type(4)));
typedef __bf16 bf16x2_t __attribute__((ext_vector_type(2)));
typedef float f32x16 __attribute__((ext_vector_type(16)));
typedef float f32x4 __attribute__((ext_vector_type(4)));
typedef float f32x2 __attribute__((ext_vector_type(2)));
typedef unsigned u32x4 __attribute__((ext_vector_type(4)));
typedef unsigned u32x2 __attribute__((ext_vector_type(2)));
typedef unsigned long long u64;
#define GAS __attribute__((address_space(1)))
#define LAS __attribute__((address_space(3)))
typedef LAS const char* lds_cptr;
typedef short v4i16_t __attribute__((ext_vector_type(4)));
#define RLX_AGENT __ATOMIC_RELAXED, __HIP_MEMORY_SCOPE_AGENT

constexpr int SEQ = 2048, DM = 512, NQKV = 1536, NIN = 1792, FH = 896;
constexpr float QSCALE = 0.25f * 1.4426950408889634f;
constexpr float LN_EPS = 1e-5f;
constexpr size_t MB = 1024 * 1024;
constexpr size_t WS_QF = 0, WS_KV = 2 * MB, WS_Z1 = 6 * MB, WS_Z2N = 10 * MB, WS_U = 14 * MB, WS_OF = 18 * MB;
constexpr size_t WS_KM = 20 * MB, WS_ST = 20 * MB + 65536, WS_RS = 20 * MB + 196608, WS_CS = 20 * MB + 229376  , WS_FLAG = 21 * MB;
constexpr int FL_RS = 0, FL_CS = 64, FL_QKV = 128, FL_ST = 320, FL_Z = 432, FL_O = 656, FL_U = 912, FL_G = 1136, FL_EPOCH = 1392;

#define MFMA(a, b, c) __builtin_amdgcn_mfma_f32_32x32x16_bf16(a, b, c, 0, 0, 0)
#define SBAR() __builtin_amdgcn_sched_barrier(0)
__device__ __forceinline__ unsigned cvtpk(float lo, float hi) { unsigned r; asm("v_cvt_pk_bf16_f32 %0, %1, %2" : "=v"(r) : "v"(lo), "v"(hi)); return r; }
__device__ __forceinline__ float bflo(unsigned u) { return __uint_as_float(u << 16); }
__device__ __forceinline__ float bfhi(unsigned u) { return __uint_as_float(u & 0xffff0000u); }
__device__ __forceinline__ float dot2(unsigned a, unsigned b, float acc) {
    return __builtin_amdgcn_fdot2_f32_bf16(__builtin_bit_cast(bf16x2_t, a), __builtin_bit_cast(bf16x2_t, b), acc, false); }
__device__ __forceinline__ void glds16(const void* g, unsigned lds_base) {
    asm volatile("s_mov_b32 m0, %1\n\ts_nop 0\n\tglobal_load_lds_dwordx4 %0, off" :: "v"(g), "s"(lds_base) : "memory", "m0"); }
template <int IMM, bool WT> __device__ __forceinline__ void glds_s(const void* sbase, unsigned voff) {
    if (WT) asm volatile("global_load_lds_dwordx4 %0, %1 offset:%2 sc1" :: "v"(voff), "s"(sbase), "n"(IMM) : "memory");
    else asm volatile("global_load_lds_dwordx4 %0, %1 offset:%2" :: "v"(voff), "s"(sbase), "n"(IMM) : "memory"); }
__device__ __forceinline__ void set_m0(unsigned v) { asm volatile("s_mov_b32 m0, %0\n\ts_nop 0" :: "s"(v) : "memory", "m0"); }
__device__ __forceinline__ s16x4 trread(lds_cptr p) { return __builtin_bit_cast(s16x4, __builtin_amdgcn_ds_read_tr16_b64_v4i16((LAS v4i16_t*)p)); }
__device__ __forceinline__ bf16x8 ldsfrag(lds_cptr p) { return *(LAS const bf16x8*)p; }
__device__ __forceinline__ bf16x8 cat(s16x4 a, s16x4 b) { return (bf16x8){a[0], a[1], a[2], a[3], b[0], b[1], b[2], b[3]}; }
__device__ __forceinline__ float swap32_add(float v) { auto rr = __builtin_amdgcn_permlane32_swap(__float_as_uint(v), __float_as_uint(v), false, false); return __uint_as_float(rr[0]) + __uint_as_float(rr[1]); }
__device__ __forceinline__ float swap32_max(float v) { auto rr = __builtin_amdgcn_permlane32_swap(__float_as_uint(v), __float_as_uint(v), false, false); return fmaxf(__uint_as_float(rr[0]), __uint_as_float(rr[1])); }
template <int CTRL> __device__ __forceinline__ float dppf(float v) { return __int_as_float(__builtin_amdgcn_update_dpp(0, __float_as_int(v), CTRL, 0xf, 0xf, false)); }
__device__ __forceinline__ float row16_add(float v) { v += dppf<0x128>(v); v += dppf<0x124>(v); v += dppf<0x122>(v); v += dppf<0x121>(v); return v; }
__device__ __forceinline__ float row16_max(float v) { v = fmaxf(v, dppf<0x128>(v)); v = fmaxf(v, dppf<0x124>(v)); v = fmaxf(v, dppf<0x122>(v)); v = fmaxf(v, dppf<0x121>(v)); return v; }
__device__ __forceinline__ float swap16_max(float v) { auto rr = __builtin_amdgcn_permlane16_swap(__float_as_uint(v), __float_as_uint(v), false, false); return fmaxf(__uint_as_float(rr[0]), __uint_as_float(rr[1])); }
__device__ __forceinline__ float gelu_t(float x) {
    const float c1 = -2.3022082f, c2 = -0.10294324f;
    float e = __builtin_amdgcn_exp2f(x * __builtin_fmaf(x * x, c2, c1));
    return x * __builtin_amdgcn_rcpf(1.0f + e); }
__device__ __forceinline__ float silu_f(float x) { return x * __builtin_amdgcn_rcpf(1.0f + __builtin_amdgcn_exp2f(-1.4426950408889634f * x)); }
#define STAMP(slot) do { if (threadIdx.x == 0) ((unsigned*)(ws + 32 * MB))[blockIdx.x * 16 + (slot)] = (unsigned)__builtin_amdgcn_s_memrealtime(); } while (0)
#define PF_DIST 9
#define PACE() do { asm volatile("s_nop %0" :: "n"(PACE_N)); SBAR(); } while (0)
#define WAITV(n) asm volatile("s_waitcnt vmcnt(" #n ")" ::: "memory")
#define WAIT_BAR(n) asm volatile("s_waitcnt vmcnt(" #n ")\n\ts_barrier" ::: "memory")
#define BAR() asm volatile("s_barrier" ::: "memory")
#define LGKM_BAR() do { __builtin_amdgcn_s_waitcnt(0xC07F); asm volatile("" ::: "memory"); __builtin_amdgcn_s_barrier(); asm volatile("" ::: "memory"); } while (0)
__device__ __forceinline__ unsigned wsw(unsigned row) { return ((row & 3) << 2) | ((row >> 2) & 3); }
__device__ __forceinline__ void st16_wt(void* p, u32x4 v) {
    asm volatile("global_store_dwordx4 %0, %1, off sc1\n\ts_nop 1" :: "v"(p), "v"(v)); }
__device__ __forceinline__ void st8_wt(void* p, u64 v) { __hip_atomic_store((GAS u64*)p, v, RLX_AGENT); }
__device__ __forceinline__ u64 ld8_wt(const void* p) { return __hip_atomic_load((GAS u64*)p, RLX_AGENT); }
__device__ __forceinline__ u32x4 ld16_wt(const void* p) { u32x4 v; asm volatile("global_load_dwordx4 %0, %1, off sc1\n\ts_waitcnt vmcnt(0)" : "=&v"(v) : "v"(p) : "memory"); return v; }
__device__ __forceinline__ void glds16_wt(const void* g, unsigned lds_base) {
    asm volatile("s_mov_b32 m0, %1\n\ts_nop 0\n\tglobal_load_lds_dwordx4 %0, off sc1" :: "v"(g), "s"(lds_base) : "memory", "m0"); }
__device__ __forceinline__ GAS u64* flagp(char* ws, int idx) { return (GAS u64*)(ws + WS_FLAG) + idx * 16; }
__device__ __forceinline__ void wait_flags(GAS u64* f, bool want, u64 tag) {
    for (unsigned spins = 0; spins < (1u << 20); ++spins) {
        const u64 v = want ? __hip_atomic_load(f, RLX_AGENT) : tag;
        if (__all(v == tag)) return;
        __builtin_amdgcn_s_sleep(2); } }
#define ACQUIRE() do { __builtin_amdgcn_fence(__ATOMIC_ACQUIRE, "agent"); asm volatile("s_waitcnt vmcnt(0)" ::: "memory"); } while (0)
#define PUBLISH(idx) do { WAITV(0); __syncthreads(); if (threadIdx.x == 0) __hip_atomic_store(flagp(ws, idx), tag, RLX_AGENT); } while (0)

__device__ __forceinline__ void stats_role(const uint16_t* __restrict__ x, const uint16_t* __restrict__ wqkv, const uint16_t* __restrict__ win, char* __restrict__ ws, char* lds, int j, u64 tag) {
    const int tid = threadIdx.x, lane = tid & 63, wid = tid >> 6, rr = lane >> 4, cc = lane & 15;
    const unsigned ONES = 0x3f803f80u;
    u32x4 xv[4], w[8];
    const int row = 32 * j + 4 * wid + rr;
#pragma unroll
    for (int i = 0; i < 4; ++i) xv[i] = *(const u32x4*)(x + (size_t)row * DM + i * 128 + cc * 8);
    const int cbk = j >> 1;
    if (j < 52) {
        const uint16_t* W = cbk < 12 ? wqkv + cbk * 128 : win + (cbk - 12) * 128; const int ldw = cbk < 12 ? NQKV : NIN;
        const uint16_t* p = W + (size_t)((j & 1) * 256 + 32 * wid + rr) * ldw + cc * 8;
#pragma unroll
        for (int i = 0; i < 8; ++i) w[i] = *(const u32x4*)(p + (size_t)(i * 4) * ldw);
    }
    float s1 = 0.f, s2 = 0.f;
#pragma unroll
    for (int i = 0; i < 4; ++i)
#pragma unroll
        for (int e = 0; e < 4; ++e) { s1 = dot2(xv[i][e], ONES, s1); s2 = dot2(xv[i][e], xv[i][e], s2); }
    s1 = row16_add(s1); s2 = row16_add(s2);
    { const float mu = s1 * (1.0f / DM), var = fmaxf(s2 * (1.0f / DM) - mu * mu, 0.f);
      if (cc == 0) st8_wt(ws + WS_RS + (size_t)row * 8, __builtin_bit_cast(u64, (f32x2){mu, __builtin_amdgcn_rsqf(var + LN_EPS)})); }
    if (j < 52) {
        float cs[8] = {0.f, 0.f, 0.f, 0.f, 0.f, 0.f, 0.f, 0.f};
        unsigned sel_lo = 0x00003f80u, sel_hi = 0x3f800000u;
        asm volatile("" : "+v"(sel_lo), "+v"(sel_hi));
#pragma unroll
        for (int i = 0; i < 8; ++i)
#pragma unroll
            for (int e = 0; e < 4; ++e) { cs[2 * e] = dot2(w[i][e], sel_lo, cs[2 * e]); cs[2 * e + 1] = dot2(w[i][e], sel_hi, cs[2 * e + 1]); }
        float* red = (float*)lds;
        *(f32x4*)(red + (wid * 4 + rr) * 128 + cc * 8) = (f32x4){cs[0], cs[1], cs[2], cs[3]}; *(f32x4*)(red + (wid * 4 + rr) * 128 + cc * 8 + 4) = (f32x4){cs[4], cs[5], cs[6], cs[7]};
        __syncthreads();
        if (tid < 64) { f32x2 a = {0.f, 0.f};
#pragma unroll
            for (int q = 0; q < 32; ++q) a += *(const f32x2*)(red + q * 128 + 2 * tid);
            st8_wt(ws + WS_CS + (size_t)((j & 1) * 3328 + cbk * 128 + 2 * tid) * 4, __builtin_bit_cast(u64, a)); }
    }
    STAMP(11);
    WAITV(0); __syncthreads();
    STAMP(1);
    if (tid == 0) { __hip_atomic_store(flagp(ws, FL_RS + j), tag, RLX_AGENT); if (j < 52) __hip_atomic_store(flagp(ws, FL_CS + j), tag, RLX_AGENT); }
}

constexpr int P_SLOT = 16384, P_ST = 4 * P_SLOT, P_LDS = 4 * P_SLOT + 4096;
struct PFrag { bf16x8 x[2], w; };
template <bool SWAP>
__device__ __forceinline__ void proj_main(const uint16_t* __restrict__ x, const uint16_t* __restrict__ W, int ldw, int rb, int col0, int cblk, char* __restrict__ ws, char* lds, int rot, u64 tag, f32x16 (&acc)[2]) {
    const int tid = threadIdx.x, lane = tid & 63, wid = __builtin_amdgcn_readfirstlane(tid >> 6), wr = wid >> 2, wc = wid & 3, l31 = lane & 31, hh = lane >> 5;
    const unsigned lds0 = (unsigned)(uintptr_t)lds;
    const int xr = 16 * wid + (lane >> 2), wk = 4 * wid + (lane >> 4);
    const char* xb = (const char*)(x + (size_t)rb * 128 * DM); const char* wb = (const char*)(W + col0);
    const unsigned xvo = (xr * DM + ((lane & 3) ^ ((xr >> 2) & 3)) * 8) * 2, wvo = (wk * ldw + ((lane & 15) ^ wsw(wk)) * 8) * 2;
    const unsigned xd = lds0 + wid * 1024, wd = lds0 + 8192 + wid * 1024;
    auto issue1 = [&](int it, int which) { const unsigned so = (it & 3) * P_SLOT; const int kk = (it + rot) & 15;
        if (which == 0) { set_m0(xd + so); glds_s<0, false>(xb + kk * 64, xvo); } else { set_m0(wd + so); glds_s<0, false>(wb + (size_t)kk * 64 * ldw, wvo); } SBAR(); };
#pragma unroll
    for (int it = 0; it < 4; ++it) { issue1(it, 0); issue1(it, 1); }
    unsigned xoff[2][2], woff[2][2];
    const int blk = (lane >> 4) & 1, q = (lane & 15) >> 2, p = lane & 3;
#pragma unroll
    for (int ks = 0; ks < 2; ++ks) {
#pragma unroll
        for (int rt = 0; rt < 2; ++rt) { const int r = wr * 64 + rt * 32 + l31; xoff[rt][ks] = r * 64 + (((2 * ks + hh) ^ ((r >> 2) & 3)) * 16); }
        const int kr = 8 * hh + q, ch = 4 * wc + 2 * blk + (p >> 1);
        woff[0][ks] = 8192 + ks * 4096 + kr * 256 + ((ch ^ wsw(kr)) * 16) + 8 * (p & 1); woff[1][ks] = (woff[0][ks] + 1024) ^ 16; }
    auto rd = [&](PFrag& f, int slot, int j) { lds_cptr sb = (lds_cptr)lds + slot * P_SLOT;
        f.x[0] = ldsfrag(sb + xoff[0][j]); f.x[1] = ldsfrag(sb + xoff[1][j]); f.w = cat(trread(sb + woff[0][j]), trread(sb + woff[1][j])); };
    auto mm = [&](const PFrag& f) {
#pragma unroll
        for (int rt = 0; rt < 2; ++rt) acc[rt] = SWAP ? MFMA(f.x[rt], f.w, acc[rt]) : MFMA(f.w, f.x[rt], acc[rt]); };
    acc[0] = f32x16{}; acc[1] = f32x16{};
    PFrag fa[2], fb[2];
    WAIT_BAR(6);
    STAMP(10);
    rd(fa[0], 0, 0); rd(fa[1], 0, 1);
    auto mm1 = [&](const PFrag& f, int rt) { acc[rt] = SWAP ? MFMA(f.x[rt], f.w, acc[rt]) : MFMA(f.w, f.x[rt], acc[rt]); SBAR(); };
    auto step = [&](int it, int sl, PFrag (&cur)[2], PFrag (&nxt)[2]) {
        __builtin_amdgcn_s_waitcnt(0xC07F);
        if (it < 13) WAITV(4); else if (it == 13) WAITV(2); else WAITV(0);
        __builtin_amdgcn_s_barrier(); asm volatile("" ::: "memory");
        lds_cptr sb = (lds_cptr)lds + ((sl + 1) & 3) * P_SLOT;
        mm1(cur[0], 0);
        nxt[0].x[0] = ldsfrag(sb + xoff[0][0]); nxt[0].x[1] = ldsfrag(sb + xoff[1][0]); SBAR();
        mm1(cur[0], 1);
        nxt[0].w = cat(trread(sb + woff[0][0]), trread(sb + woff[1][0])); SBAR();
        if (it + 4 < 16) issue1(it + 4, 0);
        mm1(cur[1], 0);
        nxt[1].x[0] = ldsfrag(sb + xoff[0][1]); nxt[1].x[1] = ldsfrag(sb + xoff[1][1]); SBAR();
        mm1(cur[1], 1);
        nxt[1].w = cat(trread(sb + woff[0][1]), trread(sb + woff[1][1])); SBAR();
        if (it + 4 < 16) issue1(it + 4, 1); };
#pragma unroll 1
    for (int io = 0; io < 2; ++io) { step(io * 4, 0, fa, fb); step(io * 4 + 1, 1, fb, fa); step(io * 4 + 2, 2, fa, fb); step(io * 4 + 3, 3, fb, fa); }
    const char* fa_ = (const char*)(lane < 4 ? flagp(ws, FL_RS + rb * 4 + lane) : flagp(ws, FL_CS + 2 * cblk + (lane & 1)));
    const char* sa_ = tid < 128 ? ws + WS_RS + (size_t)(rb * 128 + tid) * 8 : ws + WS_CS + (size_t)(cblk * 128 + 2 * (tid & 63)) * 4;
    const char* sb_ = ws + WS_CS + (size_t)(3328 + cblk * 128 + 2 * (tid & 63)) * 4;
    u64 fl, stv, stw;
    asm volatile("global_load_dwordx2 %0, %1, off sc1" : "=v"(fl) : "v"(fa_) : "memory");
    step(8, 0, fa, fb); step(9, 1, fb, fa); step(10, 2, fa, fb); step(11, 3, fb, fa);
    asm volatile("" : "+v"(fl));
    const bool got = __all(lane >= 6 || fl == tag);
    asm volatile("global_load_dwordx2 %0, %2, off sc1\n\tglobal_load_dwordx2 %1, %3, off sc1" : "=&v"(stv), "=&v"(stw) : "v"(sa_), "v"(sb_) : "memory");
    step(12, 0, fa, fb); step(13, 1, fb, fa); step(14, 2, fa, fb); step(15, 3, fb, fa);
    asm volatile("" : "+v"(stv), "+v"(stw));
    if (!got) { wait_flags((GAS u64*)fa_, lane < 6, tag); stv = ld8_wt(sa_); stw = ld8_wt(sb_); }
    STAMP(11);
    float* st = (float*)(lds + P_ST);
    { const f32x2 v = __builtin_bit_cast(f32x2, stv);
      if (tid < 128) { st[tid] = v[0]; st[128 + tid] = v[1]; } else if (tid < 192) *(f32x2*)(st + 256 + 2 * (tid - 128)) = v + __builtin_bit_cast(f32x2, stw); }
    __syncthreads();
}

__device__ __forceinline__ void qkv_role(const uint16_t* __restrict__ x, const uint16_t* __restrict__ wqkv, char* __restrict__ ws, char* lds, int b, u64 tag) {
    const int xcd = b & 7, i = b >> 3;
    const int rb = 4 * (xcd >> 1) + (i & 3), cb = 6 * (xcd & 1) + (i >> 2);
    const int tid = threadIdx.x, lane = tid & 63, wid = __builtin_amdgcn_readfirstlane(tid >> 6), wr = wid >> 2, wc = wid & 3, l31 = lane & 31, hh = lane >> 5;
    f32x16 acc[2];
    const int rot = ((i & 3) * 4 + (i >> 2) * 3) & 15;
    if (cb >= 8) proj_main<true>(x, wqkv, NQKV, rb, cb * 128, cb, ws, lds, rot, tag, acc); else proj_main<false>(x, wqkv, NQKV, rb, cb * 128, cb, ws, lds, rot, tag, acc);
    STAMP(1);
    const float* st = (const float*)(lds + P_ST);
    if (cb >= 8) {
        const float cs = st[256 + wc * 32 + l31];
        const int head = (cb - 8) * 8 + wc * 2 + (l31 >> 4);
#pragma unroll
        for (int rt = 0; rt < 2; ++rt) { const int tr = wr * 64 + rt * 32;
            unsigned pk[8];
#pragma unroll
            for (int q = 0; q < 4; ++q) { const f32x4 m = *(const f32x4*)(st + tr + 8 * q + 4 * hh), r = *(const f32x4*)(st + 128 + tr + 8 * q + 4 * hh);
                pk[2 * q] = cvtpk((acc[rt][4 * q] - m[0] * cs) * r[0], (acc[rt][4 * q + 1] - m[1] * cs) * r[1]);
                pk[2 * q + 1] = cvtpk((acc[rt][4 * q + 2] - m[2] * cs) * r[2], (acc[rt][4 * q + 3] - m[3] * cs) * r[3]); }
            const int tile = rb * 4 + (tr >> 5);
            char* dst = ws + WS_KV + (size_t)head * 131072 + (tile >> 3) * 16384 + 8192 + (tile & 7) * 1024 + hh * 256 + (l31 & 15) * 16;
            st16_wt(dst, (u32x4){pk[0], pk[1], pk[2], pk[3]}); st16_wt(dst + 512, (u32x4){pk[4], pk[5], pk[6], pk[7]}); }
    } else {
        const bool isK = cb >= 4; const float sc = isK ? 1.0f : QSCALE;
        float kmx[2] = {0.f, 0.f};
#pragma unroll
        for (int rt = 0; rt < 2; ++rt) { const int tr = wr * 64 + rt * 32, tile = rb * 4 + (tr >> 5);
            const float mu = st[tr + l31], rs = st[128 + tr + l31] * sc;
#pragma unroll
            for (int hb = 0; hb < 2; ++hb) {
                float v[8];
#pragma unroll
                for (int q = 0; q < 2; ++q) { const f32x4 c = *(const f32x4*)(st + 256 + wc * 32 + 16 * hb + 8 * q + 4 * hh);
#pragma unroll
                    for (int e = 0; e < 4; ++e) v[4 * q + e] = (acc[rt][8 * hb + 4 * q + e] - mu * c[e]) * rs; }
                u32x4 pk;
#pragma unroll
                for (int e = 0; e < 4; ++e) pk[e] = cvtpk(v[2 * e], v[2 * e + 1]);
                const int head = (cb & 3) * 8 + wc * 2 + hb;
                char* dst = isK ? ws + WS_KV + (size_t)head * 131072 + (tile >> 3) * 16384 + (tile & 7) * 1024 + lane * 16 : ws + WS_QF + (size_t)head * 65536 + tile * 1024 + lane * 16;
                st16_wt(dst, pk);
                float n2 = 0.f;
#pragma unroll
                for (int e = 0; e < 4; ++e) n2 = dot2(pk[e], pk[e], n2);
                kmx[hb] = fmaxf(kmx[hb], swap32_add(n2));
            } }
        if (isK) {
#pragma unroll
            for (int hb = 0; hb < 2; ++hb) { const float m = swap16_max(row16_max(kmx[hb]));
                if (lane == 0) __hip_atomic_store((GAS unsigned*)(ws + WS_KM) + ((cb & 3) * 8 + wc * 2 + hb) * 32 + rb * 2 + wr, __float_as_uint(m), RLX_AGENT); } }
    }
    STAMP(12);
    PUBLISH(FL_QKV + cb * 16 + rb);
    STAMP(13);
}

constexpr int A_ONES = 3 * 16384, A_LDS = A_ONES + 8192 + 512;
__device__ __forceinline__ void attn_role(char* __restrict__ ws, char* lds, int b, u64 tag) {
    const int tid = threadIdx.x, lane = tid & 63, wid = __builtin_amdgcn_readfirstlane(tid >> 6);
    const int head = (b & 7) * 4 + ((b >> 3) & 3), jb = b >> 5;
    const unsigned lds0 = (unsigned)(uintptr_t)lds;
    const char* kvb = ws + WS_KV + (size_t)head * 131072;
    for (int i = tid; i < (8192 + 512) / 16; i += 512) { const unsigned o1 = ((i & 11) == 0) ? 0x3f803f80u : 0u; *(u32x4*)(lds + A_ONES + i * 16) = (u32x4){o1, o1, o1, o1}; }
    if (wid == 0) {
        const int hb = head >> 3;
        const int idx = lane < 16 ? (4 + hb) * 16 + lane : lane < 32 ? (8 + hb) * 16 + (lane - 16) : hb * 16 + 2 * jb + (lane & 1);
        wait_flags(flagp(ws, FL_QKV + idx), lane < 34, tag);
        STAMP(14);
        }
    __syncthreads();
    STAMP(2);
    unsigned goff = wid * 2048 + lane * 16;
#pragma unroll
    for (int c = 0; c < 2; ++c)
#pragma unroll
        for (int k = 0; k < 2; ++k) glds16_wt(kvb + c * 16384 + goff + k * 1024, lds0 + c * 16384 + wid * 2048 + k * 1024);
    const int tile0 = jb * 8 + wid;
    u32x4 qa; unsigned kmu;
    asm volatile("global_load_dwordx4 %0, %2, off sc1\n\tglobal_load_dword %1, %3, off sc1\n\ts_waitcnt vmcnt(0)" : "=&v"(qa), "=&v"(kmu)
                 : "v"(ws + WS_QF + (size_t)head * 65536 + tile0 * 1024 + lane * 16), "v"(ws + WS_KM + (size_t)(head * 32 + (lane & 31)) * 4) : "memory");
    const float km = swap16_max(row16_max(__uint_as_float(kmu)));
    float na = 0.f;
#pragma unroll
    for (int e = 0; e < 4; ++e) na = dot2(qa[e], qa[e], na);
    na = swap32_add(na);
    float ma = __builtin_amdgcn_sqrtf(na * km);
    if (__any(!(ma <= 40.0f))) {
        float xa = -3.0e38f;
        for (int t = 0; t < 64; ++t) {
            const bf16x8 kf = __builtin_bit_cast(bf16x8, ld16_wt(kvb + (t >> 3) * 16384 + (t & 7) * 1024 + lane * 16));
            const f32x16 sa = MFMA(kf, __builtin_bit_cast(bf16x8, qa), f32x16{});
#pragma unroll
            for (int r = 0; r < 16; ++r) xa = fmaxf(xa, sa[r]); }
        ma = swap32_max(xa);
    }
    const float nma = -ma;
    const unsigned kaddr = lds0 + lane * 16;
    const bool ones = (lane & 16) != 0;
    const unsigned vaddr = ones ? lds0 + A_ONES + (lane & 15) * 16 : lds0 + 8192 + (lane >> 5) * 256 + (lane & 15) * 16;
    const unsigned vsel = ones ? 0u : 0xffffffffu;
    goff += 2 * 16384;
    unsigned dbeg = lds0 + wid * 2048, dend = dbeg + 3 * 16384, dslot = dbeg + 2 * 16384, nslot = 0x4000, cnt, m0save;
    unsigned oa0, oa1, oa2, oa3;
    STAMP(3);
    asm volatile(
    "s_mov_b32 %[m0save], m0\n"
    "v_mov_b32 v96, %[qa0]\n"
    "v_mov_b32 v97, %[qa1]\n"
    "v_mov_b32 v98, %[qa2]\n"
    "v_mov_b32 v99, %[qa3]\n"
    "v_mov_b32 v72, %[nma]\n"
    "v_mov_b32 v56, 0\n"
    "v_mov_b32 v73, %[nma]\n"
    "v_mov_b32 v57, 0\n"
    "v_mov_b32 v74, %[nma]\n"
    "v_mov_b32 v58, 0\n"
    "v_mov_b32 v75, %[nma]\n"
    "v_mov_b32 v59, 0\n"
    "v_mov_b32 v76, %[nma]\n"
    "v_mov_b32 v60, 0\n"
    "v_mov_b32 v77, %[nma]\n"
    "v_mov_b32 v61, 0\n"
    "v_mov_b32 v78, %[nma]\n"
    "v_mov_b32 v62, 0\n"
    "v_mov_b32 v79, %[nma]\n"
    "v_mov_b32 v63, 0\n"
    "v_mov_b32 v80, %[nma]\n"
    "v_mov_b32 v64, 0\n"
    "v_mov_b32 v81, %[nma]\n"
    "v_mov_b32 v65, 0\n"
    "v_mov_b32 v82, %[nma]\n"
    "v_mov_b32 v66, 0\n"
    "v_mov_b32 v83, %[nma]\n"
    "v_mov_b32 v67, 0\n"
    "v_mov_b32 v84, %[nma]\n"
    "v_mov_b32 v68, 0\n"
    "v_mov_b32 v85, %[nma]\n"
    "v_mov_b32 v69, 0\n"
    "v_mov_b32 v86, %[nma]\n"
    "v_mov_b32 v70, 0\n"
    "v_mov_b32 v87, %[nma]\n"
    "v_mov_b32 v71, 0\n"
    "v_mov_b32 v120, 0\n"
    "v_mov_b32 v121, 0\n"
    "v_mov_b32 v122, 0\n"
    "v_mov_b32 v123, 0\n"
    "v_mov_b32 v88, 0\n"
    "v_mov_b32 v89, 0\n"
    "v_mov_b32 v90, 0\n"
    "v_mov_b32 v91, 0\n"
    "v_mov_b32 v92, 0\n"
    "v_mov_b32 v93, 0\n"
    "v_mov_b32 v94, 0\n"
    "v_mov_b32 v95, 0\n"
    "v_mov_b32 v124, %[kaddr]\n"
    "v_mov_b32 v125, %[vaddr]\n"
    "v_add_u32 v126, 0x4000, v124\n"
    "v_and_b32 v127, 0x4000, %[vsel]\n"
    "v_add_u32 v127, v127, v125\n"
    "s_waitcnt vmcnt(2) lgkmcnt(0)\n"
    "s_barrier\n"
    "ds_read_b128 v[100:103], v124\n"
    "ds_read_b128 v[104:107], v124 offset:1024\n"
    "ds_read_b128 v[108:111], v125\n"
    "ds_read_b128 v[112:115], v125 offset:512\n"
    "s_mov_b32 %[cnt], 0\n"
    "s_waitcnt lgkmcnt(0)\n"
    "v_mfma_f32_32x32x16_bf16 v[24:39], v[100:103], v[96:99], v[72:87]\n"
    "s_nop 15\n"
    ".Lloop%=:\n"
    "v_exp_f32 v24, v24\n"
    "v_exp_f32 v25, v25\n"
    "v_mfma_f32_32x32x16_bf16 v[56:71], v[120:123], v[92:95], v[56:71]\n"
    "v_exp_f32 v26, v26\n"
    "v_exp_f32 v27, v27\n"
    "v_cvt_pk_bf16_f32 v88, v24, v25\n"
    "v_exp_f32 v28, v28\n"
    "s_waitcnt lgkmcnt(0)\n"
    "v_mfma_f32_32x32x16_bf16 v[40:55], v[104:107], v[96:99], v[72:87]\n"
    "v_exp_f32 v29, v29\n"
    "v_cvt_pk_bf16_f32 v89, v26, v27\n"
    "ds_read_b128 v[100:103], v124 offset:2048\n"
    "v_exp_f32 v30, v30\n"
    "ds_read_b128 v[116:119], v125 offset:1024\n"
    "v_exp_f32 v31, v31\n"
    "v_cvt_pk_bf16_f32 v90, v28, v29\n"
    "ds_read_b128 v[120:123], v125 offset:1536\n"
    "v_exp_f32 v32, v32\n"
    "v_exp_f32 v33, v33\n"
    "v_cvt_pk_bf16_f32 v91, v30, v31\n"
    "v_exp_f32 v34, v34\n"
    "v_exp_f32 v35, v35\n"
    "v_mfma_f32_32x32x16_bf16 v[56:71], v[108:111], v[88:91], v[56:71]\n"
    "v_cvt_pk_bf16_f32 v92, v32, v33\n"
    "v_exp_f32 v36, v36\n"
    "v_exp_f32 v37, v37\n"
    "v_cvt_pk_bf16_f32 v93, v34, v35\n"
    "v_exp_f32 v38, v38\n"
    "v_exp_f32 v39, v39\n"
    "v_cvt_pk_bf16_f32 v94, v36, v37\n"
    "v_cvt_pk_bf16_f32 v95, v38, v39\n"
    "v_exp_f32 v40, v40\n"
    "v_exp_f32 v41, v41\n"
    "v_mfma_f32_32x32x16_bf16 v[56:71], v[112:115], v[92:95], v[56:71]\n"
    "v_exp_f32 v42, v42\n"
    "v_exp_f32 v43, v43\n"
    "v_cvt_pk_bf16_f32 v88, v40, v41\n"
    "v_exp_f32 v44, v44\n"
    "s_waitcnt lgkmcnt(0)\n"
    "v_mfma_f32_32x32x16_bf16 v[24:39], v[100:103], v[96:99], v[72:87]\n"
    "v_exp_f32 v45, v45\n"
    "v_cvt_pk_bf16_f32 v89, v42, v43\n"
    "ds_read_b128 v[104:107], v124 offset:3072\n"
    "v_exp_f32 v46, v46\n"
    "ds_read_b128 v[108:111], v125 offset:2048\n"
    "v_exp_f32 v47, v47\n"
    "v_cvt_pk_bf16_f32 v90, v44, v45\n"
    "ds_read_b128 v[112:115], v125 offset:2560\n"
    "v_exp_f32 v48, v48\n"
    "v_exp_f32 v49, v49\n"
    "v_cvt_pk_bf16_f32 v91, v46, v47\n"
    "v_exp_f32 v50, v50\n"
    "v_exp_f32 v51, v51\n"
    "v_mfma_f32_32x32x16_bf16 v[56:71], v[116:119], v[88:91], v[56:71]\n"
    "v_cvt_pk_bf16_f32 v92, v48, v49\n"
    "v_exp_f32 v52, v52\n"
    "v_exp_f32 v53, v53\n"
    "v_cvt_pk_bf16_f32 v93, v50, v51\n"
    "v_exp_f32 v54, v54\n"
    "v_exp_f32 v55, v55\n"
    "v_cvt_pk_bf16_f32 v94, v52, v53\n"
    "v_cvt_pk_bf16_f32 v95, v54, v55\n"
    "v_exp_f32 v24, v24\n"
    "v_exp_f32 v25, v25\n"
    "v_mfma_f32_32x32x16_bf16 v[56:71], v[120:123], v[92:95], v[56:71]\n"
    "v_exp_f32 v26, v26\n"
    "v_exp_f32 v27, v27\n"
    "v_cvt_pk_bf16_f32 v88, v24, v25\n"
    "v_exp_f32 v28, v28\n"
    "s_waitcnt lgkmcnt(0)\n"
    "v_mfma_f32_32x32x16_bf16 v[40:55], v[104:107], v[96:99], v[72:87]\n"
    "v_exp_f32 v29, v29\n"
    "v_cvt_pk_bf16_f32 v89, v26, v27\n"
    "ds_read_b128 v[100:103], v124 offset:4096\n"
    "v_exp_f32 v30, v30\n"
    "ds_read_b128 v[116:119], v125 offset:3072\n"
    "v_exp_f32 v31, v31\n"
    "v_cvt_pk_bf16_f32 v90, v28, v29\n"
    "ds_read_b128 v[120:123], v125 offset:3584\n"
    "v_exp_f32 v32, v32\n"
    "v_exp_f32 v33, v33\n"
    "v_cvt_pk_bf16_f32 v91, v30, v31\n"
    "v_exp_f32 v34, v34\n"
    "v_exp_f32 v35, v35\n"
    "v_mfma_f32_32x32x16_bf16 v[56:71], v[108:111], v[88:91], v[56:71]\n"
    "v_cvt_pk_bf16_f32 v92, v32, v33\n"
    "v_exp_f32 v36, v36\n"
    "v_exp_f32 v37, v37\n"
    "v_cvt_pk_bf16_f32 v93, v34, v35\n"
    "v_exp_f32 v38, v38\n"
    "v_exp_f32 v39, v39\n"
    "v_cvt_pk_bf16_f32 v94, v36, v37\n"
    "v_cvt_pk_bf16_f32 v95, v38, v39\n"
    "v_exp_f32 v40, v40\n"
    "v_exp_f32 v41, v41\n"
    "v_mfma_f32_32x32x16_bf16 v[56:71], v[112:115], v[92:95], v[56:71]\n"
    "v_exp_f32 v42, v42\n"
    "v_exp_f32 v43, v43\n"
    "v_cvt_pk_bf16_f32 v88, v40, v41\n"
    "v_exp_f32 v44, v44\n"
    "s_waitcnt lgkmcnt(0)\n"
    "v_mfma_f32_32x32x16_bf16 v[24:39], v[100:103], v[96:99], v[72:87]\n"
    "v_exp_f32 v45, v45\n"
    "v_cvt_pk_bf16_f32 v89, v42, v43\n"
    "ds_read_b128 v[104:107], v124 offset:5120\n"
    "v_exp_f32 v46, v46\n"
    "ds_read_b128 v[108:111], v125 offset:4096\n"
    "v_exp_f32 v47, v47\n"
    "v_cvt_pk_bf16_f32 v90, v44, v45\n"
    "ds_read_b128 v[112:115], v125 offset:4608\n"
    "v_exp_f32 v48, v48\n"
    "v_exp_f32 v49, v49\n"
    "v_cvt_pk_bf16_f32 v91, v46, v47\n"
    "v_exp_f32 v50, v50\n"
    "v_exp_f32 v51, v51\n"
    "v_mfma_f32_32x32x16_bf16 v[56:71], v[116:119], v[88:91], v[56:71]\n"
    "v_cvt_pk_bf16_f32 v92, v48, v49\n"
    "v_exp_f32 v52, v52\n"
    "v_exp_f32 v53, v53\n"
    "v_cvt_pk_bf16_f32 v93, v50, v51\n"
    "v_exp_f32 v54, v54\n"
    "v_exp_f32 v55, v55\n"
    "v_cvt_pk_bf16_f32 v94, v52, v53\n"
    "v_cvt_pk_bf16_f32 v95, v54, v55\n"
    "v_exp_f32 v24, v24\n"
    "v_exp_f32 v25, v25\n"
    "v_mfma_f32_32x32x16_bf16 v[56:71], v[120:123], v[92:95], v[56:71]\n"
    "v_exp_f32 v26, v26\n"
    "v_exp_f32 v27, v27\n"
    "v_cvt_pk_bf16_f32 v88, v24, v25\n"
    "v_exp_f32 v28, v28\n"
    "s_waitcnt lgkmcnt(0)\n"
    "v_mfma_f32_32x32x16_bf16 v[40:55], v[104:107], v[96:99], v[72:87]\n"
    "v_exp_f32 v29, v29\n"
    "v_cvt_pk_bf16_f32 v89, v26, v27\n"
    "ds_read_b128 v[100:103], v124 offset:6144\n"
    "v_exp_f32 v30, v30\n"
    "ds_read_b128 v[116:119], v125 offset:5120\n"
    "v_exp_f32 v31, v31\n"
    "v_cvt_pk_bf16_f32 v90, v28, v29\n"
    "ds_read_b128 v[120:123], v125 offset:5632\n"
    "v_exp_f32 v32, v32\n"
    "v_exp_f32 v33, v33\n"
    "v_cvt_pk_bf16_f32 v91, v30, v31\n"
    "v_exp_f32 v34, v34\n"
    "v_exp_f32 v35, v35\n"
    "v_mfma_f32_32x32x16_bf16 v[56:71], v[108:111], v[88:91], v[56:71]\n"
    "v_cvt_pk_bf16_f32 v92, v32, v33\n"
    "v_exp_f32 v36, v36\n"
    "v_exp_f32 v37, v37\n"
    "v_cvt_pk_bf16_f32 v93, v34, v35\n"
    "v_exp_f32 v38, v38\n"
    "v_exp_f32 v39, v39\n"
    "v_cvt_pk_bf16_f32 v94, v36, v37\n"
    "v_cvt_pk_bf16_f32 v95, v38, v39\n"
    "v_exp_f32 v40, v40\n"
    "v_exp_f32 v41, v41\n"
    "v_mfma_f32_32x32x16_bf16 v[56:71], v[112:115], v[92:95], v[56:71]\n"
    "v_exp_f32 v42, v42\n"
    "v_exp_f32 v43, v43\n"
    "v_cvt_pk_bf16_f32 v88, v40, v41\n"
    "v_exp_f32 v44, v44\n"
    "s_waitcnt lgkmcnt(0)\n"
    "v_mfma_f32_32x32x16_bf16 v[24:39], v[100:103], v[96:99], v[72:87]\n"
    "v_exp_f32 v45, v45\n"
    "v_cvt_pk_bf16_f32 v89, v42, v43\n"
    "ds_read_b128 v[104:107], v124 offset:7168\n"
    "v_exp_f32 v46, v46\n"
    "ds_read_b128 v[108:111], v125 offset:6144\n"
    "v_exp_f32 v47, v47\n"
    "v_cvt_pk_bf16_f32 v90, v44, v45\n"
    "ds_read_b128 v[112:115], v125 offset:6656\n"
    "v_exp_f32 v48, v48\n"
    "v_exp_f32 v49, v49\n"
    "v_cvt_pk_bf16_f32 v91, v46, v47\n"
    "v_exp_f32 v50, v50\n"
    "v_exp_f32 v51, v51\n"
    "v_mfma_f32_32x32x16_bf16 v[56:71], v[116:119], v[88:91], v[56:71]\n"
    "v_cvt_pk_bf16_f32 v92, v48, v49\n"
    "v_exp_f32 v52, v52\n"
    "v_exp_f32 v53, v53\n"
    "v_cvt_pk_bf16_f32 v93, v50, v51\n"
    "v_exp_f32 v54, v54\n"
    "v_exp_f32 v55, v55\n"
    "v_cvt_pk_bf16_f32 v94, v52, v53\n"
    "v_cvt_pk_bf16_f32 v95, v54, v55\n"
    "v_exp_f32 v24, v24\n"
    "v_exp_f32 v25, v25\n"
    "v_mfma_f32_32x32x16_bf16 v[56:71], v[120:123], v[92:95], v[56:71]\n"
    "v_exp_f32 v26, v26\n"
    "v_exp_f32 v27, v27\n"
    "v_cvt_pk_bf16_f32 v88, v24, v25\n"
    "v_exp_f32 v28, v28\n"
    "s_waitcnt lgkmcnt(0)\n"
    "v_mfma_f32_32x32x16_bf16 v[40:55], v[104:107], v[96:99], v[72:87]\n"
    "v_exp_f32 v29, v29\n"
    "v_cvt_pk_bf16_f32 v89, v26, v27\n"
    "s_waitcnt vmcnt(0)\n"
    "s_barrier\n"
    "ds_read_b128 v[100:103], v126 offset:0\n"
    "v_exp_f32 v30, v30\n"
    "ds_read_b128 v[116:119], v125 offset:7168\n"
    "v_exp_f32 v31, v31\n"
    "v_cvt_pk_bf16_f32 v90, v28, v29\n"
    "ds_read_b128 v[120:123], v125 offset:7680\n"
    "v_exp_f32 v32, v32\n"
    "v_exp_f32 v33, v33\n"
    "v_cvt_pk_bf16_f32 v91, v30, v31\n"
    "v_exp_f32 v34, v34\n"
    "v_exp_f32 v35, v35\n"
    "v_mfma_f32_32x32x16_bf16 v[56:71], v[108:111], v[88:91], v[56:71]\n"
    "s_cmp_ge_u32 %[cnt], 6\n"
    "s_cbranch_scc1 .Lnodma%=\n"
    "s_mov_b32 m0, %[dslot]\n"
    "s_nop 0\n"
    "global_load_lds_dwordx4 %[goff], %[gbase] offset:0\n"
    "global_load_lds_dwordx4 %[goff], %[gbase] offset:1024\n"
    ".Lnodma%=:\n"
    "v_cvt_pk_bf16_f32 v92, v32, v33\n"
    "v_exp_f32 v36, v36\n"
    "v_exp_f32 v37, v37\n"
    "v_cvt_pk_bf16_f32 v93, v34, v35\n"
    "v_exp_f32 v38, v38\n"
    "v_exp_f32 v39, v39\n"
    "v_cvt_pk_bf16_f32 v94, v36, v37\n"
    "v_cvt_pk_bf16_f32 v95, v38, v39\n"
    "v_exp_f32 v40, v40\n"
    "v_exp_f32 v41, v41\n"
    "v_mfma_f32_32x32x16_bf16 v[56:71], v[112:115], v[92:95], v[56:71]\n"
    "v_exp_f32 v42, v42\n"
    "v_exp_f32 v43, v43\n"
    "v_cvt_pk_bf16_f32 v88, v40, v41\n"
    "v_exp_f32 v44, v44\n"
    "s_waitcnt lgkmcnt(0)\n"
    "v_mfma_f32_32x32x16_bf16 v[24:39], v[100:103], v[96:99], v[72:87]\n"
    "v_exp_f32 v45, v45\n"
    "v_cvt_pk_bf16_f32 v89, v42, v43\n"
    "ds_read_b128 v[104:107], v126 offset:1024\n"
    "v_exp_f32 v46, v46\n"
    "ds_read_b128 v[108:111], v127 offset:0\n"
    "v_exp_f32 v47, v47\n"
    "v_cvt_pk_bf16_f32 v90, v44, v45\n"
    "ds_read_b128 v[112:115], v127 offset:512\n"
    "v_exp_f32 v48, v48\n"
    "v_exp_f32 v49, v49\n"
    "v_cvt_pk_bf16_f32 v91, v46, v47\n"
    "v_exp_f32 v50, v50\n"
    "v_exp_f32 v51, v51\n"
    "v_mfma_f32_32x32x16_bf16 v[56:71], v[116:119], v[88:91], v[56:71]\n"
    "v_cvt_pk_bf16_f32 v92, v48, v49\n"
    "v_exp_f32 v52, v52\n"
    "v_exp_f32 v53, v53\n"
    "v_cvt_pk_bf16_f32 v93, v50, v51\n"
    "v_exp_f32 v54, v54\n"
    "v_exp_f32 v55, v55\n"
    "v_cvt_pk_bf16_f32 v94, v52, v53\n"
    "v_cvt_pk_bf16_f32 v95, v54, v55\n"
    "v_mov_b32 v124, v126\n"
    "v_mov_b32 v125, v127\n"
    "s_add_u32 %[nslot], %[nslot], 0x4000\n"
    "s_cmp_eq_u32 %[nslot], 0xc000\n"
    "s_cselect_b32 %[nslot], 0, %[nslot]\n"
    "v_add_u32 v126, %[nslot], %[kaddr]\n"
    "v_and_b32 v127, %[nslot], %[vsel]\n"
    "v_add_u32 v127, v127, %[vaddr]\n"
    "s_add_u32 %[dslot], %[dslot], 0x4000\n"
    "s_cmp_eq_u32 %[dslot], %[dend]\n"
    "s_cselect_b32 %[dslot], %[dbeg], %[dslot]\n"
    "v_add_u32 %[goff], 0x4000, %[goff]\n"
    "s_add_u32 %[cnt], %[cnt], 1\n"
    "s_cmp_lt_u32 %[cnt], 8\n"
    "s_cbranch_scc1 .Lloop%=\n"
    "s_nop 1\n"
    "v_mfma_f32_32x32x16_bf16 v[56:71], v[120:123], v[92:95], v[56:71]\n"
    "s_nop 15\n"
    "s_nop 3\n"
    "v_rcp_f32 v24, v64\n"
    "s_nop 0\n"
    "v_mul_f32 v56, v56, v24\n"
    "v_mul_f32 v57, v57, v24\n"
    "v_mul_f32 v58, v58, v24\n"
    "v_mul_f32 v59, v59, v24\n"
    "v_mul_f32 v60, v60, v24\n"
    "v_mul_f32 v61, v61, v24\n"
    "v_mul_f32 v62, v62, v24\n"
    "v_mul_f32 v63, v63, v24\n"
    "v_cvt_pk_bf16_f32 %[oa0], v56, v57\n"
    "v_cvt_pk_bf16_f32 %[oa1], v58, v59\n"
    "v_cvt_pk_bf16_f32 %[oa2], v60, v61\n"
    "v_cvt_pk_bf16_f32 %[oa3], v62, v63\n"
    "s_waitcnt lgkmcnt(0)\n"
    "s_mov_b32 m0, %[m0save]\n"
        : [oa0] "=&v"(oa0), [oa1] "=&v"(oa1), [oa2] "=&v"(oa2), [oa3] "=&v"(oa3),
          [goff] "+v"(goff), [dslot] "+s"(dslot), [nslot] "+s"(nslot), [cnt] "=&s"(cnt), [m0save] "=&s"(m0save)
        : [qa0] "v"(qa[0]), [qa1] "v"(qa[1]), [qa2] "v"(qa[2]), [qa3] "v"(qa[3]),
          [nma] "v"(nma), [kaddr] "v"(kaddr), [vaddr] "v"(vaddr), [vsel] "v"(vsel), [gbase] "s"(kvb), [dbeg] "s"(dbeg), [dend] "s"(dend), [psel] "s"(0x07060302u), [dbgaddr] "s"(lds0 + A_LDS + wid * 64)
        : "memory", "scc", "vcc",
          "v24", "v25", "v26", "v27", "v28", "v29", "v30", "v31", "v32", "v33", "v34", "v35", "v36", "v37", "v38", "v39", "v40", "v41", "v42", "v43", "v44", "v45", "v46", "v47", "v48", "v49", "v50", "v51", "v52", "v53", "v54", "v55", "v56", "v57", "v58", "v59", "v60", "v61", "v62", "v63", "v64", "v65", "v66", "v67", "v68", "v69", "v70", "v71", "v72", "v73", "v74", "v75", "v76", "v77", "v78", "v79", "v80", "v81", "v82", "v83", "v84", "v85", "v86", "v87", "v88", "v89", "v90", "v91", "v92", "v93", "v94", "v95", "v96", "v97", "v98", "v99", "v100", "v101", "v102", "v103", "v104", "v105", "v106", "v107", "v108", "v109", "v110", "v111", "v112", "v113", "v114", "v115", "v116", "v117", "v118", "v119", "v120", "v121", "v122", "v123", "v124", "v125", "v126", "v127"
    );
    STAMP(4);
    st16_wt(ws + WS_OF + ((size_t)tile0 * 32 + head) * 1024 + lane * 16, (u32x4){oa0, oa1, oa2, oa3});
    PUBLISH(FL_O + jb * 32 + head);
}

__device__ __forceinline__ void z_role(const uint16_t* __restrict__ x, const uint16_t* __restrict__ win, char* __restrict__ ws, char* lds, int g, u64 tag) {
    const int tid = threadIdx.x, lane = tid & 63, wid = __builtin_amdgcn_readfirstlane(tid >> 6), wr = wid >> 2, wc = wid & 3, l31 = lane & 31, hh = lane >> 5;
    const int xcd = g & 7, i = g >> 3;
    const int rb = 4 * (xcd >> 1) + (i & 3), cbz = 7 * ((xcd & 1) ^ 1) + (i >> 2);
    f32x16 acc[2];
    proj_main<false>(x, win, NIN, rb, cbz * 128, 12 + cbz, ws, lds, ((i & 3) * 4 + (i >> 2) * 3) & 15, tag, acc);
    STAMP(1);
    const float* st = (const float*)(lds + P_ST);
    unsigned zp[2][8];
#pragma unroll
    for (int rt = 0; rt < 2; ++rt) { const int tr = wr * 64 + rt * 32; const float mu = st[tr + l31], rs = st[128 + tr + l31];
#pragma unroll
        for (int q = 0; q < 4; ++q) { const f32x4 c = *(const f32x4*)(st + 256 + wc * 32 + 8 * q + 4 * hh);
            zp[rt][2 * q] = cvtpk(gelu_t((acc[rt][4 * q] - mu * c[0]) * rs), gelu_t((acc[rt][4 * q + 1] - mu * c[1]) * rs));
            zp[rt][2 * q + 1] = cvtpk(gelu_t((acc[rt][4 * q + 2] - mu * c[2]) * rs), gelu_t((acc[rt][4 * q + 3] - mu * c[3]) * rs)); } }
    if (cbz < 7) {
#pragma unroll
        for (int rt = 0; rt < 2; ++rt) { const int tt = rb * 4 + wr * 2 + rt;
            char* dst = ws + WS_Z1 + ((size_t)tt * 28 + cbz * 4 + wc) * 2048 + lane * 32;
            st16_wt(dst, (u32x4){zp[rt][0], zp[rt][1], zp[rt][2], zp[rt][3]}); st16_wt(dst + 16, (u32x4){zp[rt][4], zp[rt][5], zp[rt][6], zp[rt][7]}); }
    } else {
        const int cj = cbz - 7;
        float* pst = (float*)lds;
        const unsigned ONES = 0x3f803f80u;
#pragma unroll
        for (int rt = 0; rt < 2; ++rt) { float s1 = 0.f, s2 = 0.f;
#pragma unroll
            for (int e = 0; e < 8; ++e) { s1 = dot2(zp[rt][e], ONES, s1); s2 = dot2(zp[rt][e], zp[rt][e], s2); }
            s1 = swap32_add(s1); s2 = swap32_add(s2);
            if (hh == 0) *(f32x2*)(pst + (wc * 128 + wr * 64 + rt * 32 + l31) * 2) = (f32x2){s1, s2}; }
        __syncthreads();
        char* gst = ws + WS_ST + (size_t)rb * 7 * 1024;
        if (tid < 128) { f32x2 s = *(const f32x2*)(pst + tid * 2);
#pragma unroll
            for (int j = 1; j < 4; ++j) s += *(const f32x2*)(pst + (j * 128 + tid) * 2);
            st8_wt(gst + (cj * 128 + tid) * 8, __builtin_bit_cast(u64, s)); }
        PUBLISH(FL_ST + rb * 7 + cj);
        if (wid == 0) wait_flags(flagp(ws, FL_ST + rb * 7 + lane), lane < 7, tag);
        __syncthreads();
        float* fin = (float*)(lds + 4096);
        if (tid < 128) { f32x2 s = {0.f, 0.f};
#pragma unroll
            for (int j = 0; j < 7; ++j) s += __builtin_bit_cast(f32x2, ld8_wt(gst + (j * 128 + tid) * 8));
            const float mu = s[0] * (1.0f / FH); const float var = fmaxf(s[1] * (1.0f / FH) - mu * mu, 0.f);
            fin[tid] = mu; fin[128 + tid] = __builtin_amdgcn_rsqf(var + LN_EPS); }
        __syncthreads();
        char* img = lds + 8192;
#pragma unroll
        for (int rt = 0; rt < 2; ++rt) { const int tok = wr * 64 + rt * 32 + l31; const float mu = fin[tok], rs = fin[128 + tok];
#pragma unroll
            for (int q = 0; q < 4; ++q) { const unsigned a = zp[rt][2 * q], c = zp[rt][2 * q + 1];
                const u32x2 o = {cvtpk((bflo(a) - mu) * rs, (bfhi(a) - mu) * rs), cvtpk((bflo(c) - mu) * rs, (bfhi(c) - mu) * rs)};
                const int f = wc * 32 + 8 * q + 4 * hh;
                *(u32x2*)(img + tok * 256 + (((f >> 3) ^ wsw(tok)) * 16) + (f & 4) * 2) = o; } }
        __syncthreads();
        { const int blk = (lane >> 4) & 1, q = (lane & 15) >> 2, p = lane & 3;
#pragma unroll
          for (int kq = 0; kq < 4; ++kq) { const int ks = wr * 4 + kq; s16x4 t2[2];
#pragma unroll
              for (int t = 0; t < 2; ++t) { const int kr = 16 * ks + 8 * hh + 4 * t + q; const int ch = 4 * wc + 2 * blk + (p >> 1);
                  t2[t] = trread((lds_cptr)img + kr * 256 + ((ch ^ wsw(kr)) * 16) + 8 * (p & 1)); }
              st16_wt(ws + WS_Z2N + ((size_t)(cj * 4 + wc) * 128 + rb * 8 + ks) * 1024 + lane * 16, __builtin_bit_cast(u32x4, cat(t2[0], t2[1]))); } }
    }
    PUBLISH(FL_Z + rb * 14 + cbz);
}

constexpr int S_SLOT = 24576, S_LDS = 3 * S_SLOT;
__device__ __forceinline__ void spatial_role(const uint16_t* __restrict__ wsp, const uint16_t* __restrict__ bsp, char* __restrict__ ws, char* lds, int g, u64 tag) {
    const int tid = threadIdx.x, lane = tid & 63, wid8 = __builtin_amdgcn_readfirstlane(tid >> 6), wid = wid8 & 3, l31 = lane & 31, hh = lane >> 5;
    const int i = g >> 3, tb = 4 * (g & 7) + (i & 3), fb = i >> 2;
    const unsigned lds0 = (unsigned)(uintptr_t)lds;
    if (wid8 == 0) {
        wait_flags(flagp(ws, lane < 16 ? FL_Z + lane * 14 + 7 + fb : FL_Z + (tb >> 1) * 14 + fb), lane < 17, tag); }
    __syncthreads();
    STAMP(3);
    if (wid8 >= 4) {
        const int r0 = 16 * wid + (lane >> 3), r1 = r0 + 8;
        const char* sb_ = (const char*)(wsp + (size_t)tb * 64 * SEQ);
        const unsigned vo0 = (r0 * SEQ + ((lane & 7) ^ ((r0 >> 1) & 7)) * 8) * 2, vo1 = (r1 * SEQ + ((lane & 7) ^ ((r1 >> 1) & 7)) * 8) * 2;
        const char* zs = ws + WS_Z2N + (size_t)(fb * 4 + wid) * 131072; const unsigned zvo = lane * 16;
        auto issue = [&](int it, int slot) { const unsigned so = lds0 + slot * S_SLOT;
            set_m0(so + wid * 2048); glds_s<0, false>(sb_ + it * 128, vo0);
            set_m0(so + wid * 2048 + 1024); glds_s<0, false>(sb_ + it * 128, vo1);
            set_m0(so + 8192 + wid * 4096);
            glds_s<0, true>(zs + it * 4096, zvo); glds_s<1024, true>(zs + it * 4096, zvo); glds_s<2048, true>(zs + it * 4096, zvo); glds_s<3072, true>(zs + it * 4096, zvo); };
        issue(0, 0); issue(1, 1); issue(2, 2);
        WAIT_BAR(12);
        int slot = 0;
#pragma unroll 1
        for (int it = 1; it < 31; ++it) { WAIT_BAR(6); if (it + 2 < 32) issue(it + 2, slot); slot = slot == 2 ? 0 : slot + 1; }
        WAIT_BAR(0);
        BAR();
    } else {
        unsigned boff[2][4];
#pragma unroll
        for (int rt = 0; rt < 2; ++rt)
#pragma unroll
            for (int ks = 0; ks < 4; ++ks) { const int r = rt * 32 + l31; boff[rt][ks] = (r * 128 + ((hh ^ ((r >> 1) & 7)) * 16)) ^ (ks * 32); }
        const unsigned aoff = 8192 + wid * 4096 + lane * 16;
        struct SF { bf16x8 a, b[2]; };
        auto rd = [&](SF& f, int slot, int ks) { lds_cptr sb = (lds_cptr)lds + slot * S_SLOT;
            f.a = ldsfrag(sb + aoff + ks * 1024); f.b[0] = ldsfrag(sb + boff[0][ks]); f.b[1] = ldsfrag(sb + boff[1][ks]); };
        f32x16 acc[2] = {f32x16{}, f32x16{}};
        auto mm = [&](const SF& f) { acc[0] = MFMA(f.a, f.b[0], acc[0]); acc[1] = MFMA(f.a, f.b[1], acc[1]); };
        SF f[4];
        BAR();
#pragma unroll
        for (int ks = 0; ks < 4; ++ks) rd(f[ks], 0, ks);
        auto step = [&](int nsl) {
            LGKM_BAR();
#pragma unroll
            for (int ks = 0; ks < 4; ++ks) { lds_cptr sb = (lds_cptr)lds + nsl * S_SLOT;
                acc[0] = MFMA(f[ks].a, f[ks].b[0], acc[0]); SBAR();
                f[ks].b[0] = ldsfrag(sb + boff[0][ks]); SBAR();
                PACE();
                acc[1] = MFMA(f[ks].a, f[ks].b[1], acc[1]); SBAR();
                f[ks].b[1] = ldsfrag(sb + boff[1][ks]); f[ks].a = ldsfrag(sb + aoff + ks * 1024); SBAR();
                PACE(); } };
#pragma unroll 1
        for (int io = 0; io < 10; ++io) { step(1); step(2); step(0); }
        step(1); step(2);
#pragma unroll
        for (int rt = 0; rt < 2; ++rt) { const int tt = tb * 2 + rt; const float bias = __uint_as_float((unsigned)bsp[tt * 32 + l31] << 16);
            const char* z1p = ws + WS_Z1 + ((size_t)tt * 28 + fb * 4 + wid) * 2048 + lane * 32;
            const u32x4 za = ld16_wt(z1p), zb = ld16_wt(z1p + 16);
            u32x4 ua, ub;
#pragma unroll
            for (int e = 0; e < 4; ++e) {
                ua[e] = cvtpk(bflo(za[e]) * (acc[rt][2 * e] + bias), bfhi(za[e]) * (acc[rt][2 * e + 1] + bias));
                ub[e] = cvtpk(bflo(zb[e]) * (acc[rt][8 + 2 * e] + bias), bfhi(zb[e]) * (acc[rt][8 + 2 * e + 1] + bias)); }
            char* ud = ws + WS_U + ((size_t)tt * 56 + (fb * 4 + wid) * 2) * 1024 + lane * 16;
            st16_wt(ud, ua); st16_wt(ud + 1024, ub); }
    }
    PUBLISH(FL_U + tb * 7 + fb);
}

constexpr int F_SLOT = 8192 + 8192, F_LDS = 4 * F_SLOT;
__device__ __forceinline__ void final_role(const uint16_t* __restrict__ x, const uint16_t* __restrict__ wo, const uint16_t* __restrict__ wout, char* __restrict__ ws, uint16_t* __restrict__ out, char* lds, int g, u64 tag) {
    const int tb = 4 * (g & 7) + ((g >> 3) & 3), db = g >> 5;
    const int tid = threadIdx.x, lane = tid & 63, wid8 = __builtin_amdgcn_readfirstlane(tid >> 6), wid = wid8 & 3, wr = wid >> 1, wc = wid & 1, l31 = lane & 31, hh = lane >> 5;
    const unsigned lds0 = (unsigned)(uintptr_t)lds;
    auto wswz = [](int r) { return ((r & 2) << 1) | ((r >> 2) & 3); };
    f32x16 as = f32x16{}, ag = f32x16{};
    const int k0 = 16 * wid + (lane >> 3), k1 = k0 + 8;
    const unsigned wo0 = (k0 * DM + db * 64 + ((lane & 7) ^ wswz(k0)) * 8) * 2, wo1 = (k1 * DM + db * 64 + ((lane & 7) ^ wswz(k1)) * 8) * 2;
    const int att = tb * 2 + (wid & 1), aks = 2 * (wid >> 1);
    unsigned woff[2];
    { const int blk = (lane >> 4) & 1, q = (lane & 15) >> 2, p = lane & 3; const int kr = 4 * hh + q; const int ch = 4 * wc + 2 * blk + (p >> 1);
      woff[0] = kr * 128 + ((ch ^ wswz(kr)) * 16) + 8 * (p & 1); woff[1] = (woff[0] + 1024) ^ 32; }
    const unsigned aoff = 8192 + wr * 4096 + lane * 16;
    struct FF { bf16x8 w, a; };
    auto rd = [&](FF& f, int slot, int ks) { lds_cptr sb = (lds_cptr)lds + slot * F_SLOT;
        f.w = cat(trread(sb + woff[0] + ks * 2048), trread(sb + woff[1] + ks * 2048)); f.a = ldsfrag(sb + aoff + ks * 1024); };
    auto pass = [&](const uint16_t* wb, const char* ab, int nfr, int n, f32x16& acc, bool pace) {
        if (wid8 >= 4) {
            auto issue = [&](int it) { const unsigned so = lds0 + (it & 3) * F_SLOT; const char* w = (const char*)(wb + (size_t)it * 64 * DM);
                set_m0(so + wid * 2048); glds_s<0, false>(w, wo0);
                set_m0(so + wid * 2048 + 1024); glds_s<0, false>(w, wo1);
                const char* a = ab + ((size_t)att * nfr + it * 4 + aks) * 1024;
                set_m0(so + 8192 + (wid & 1) * 4096 + aks * 1024); glds_s<0, true>(a, lane * 16); glds_s<1024, true>(a, lane * 16); };
            issue(0); issue(1); issue(2); issue(3);
            WAIT_BAR(12);
#pragma unroll 1
            for (int i = 1; i < n - 2; ++i) { WAIT_BAR(8); if (i + 3 < n) issue(i + 3); }
            WAIT_BAR(4);
            WAIT_BAR(0);
            BAR();
        } else {
            FF fa[4], fb[4];
            BAR();
#pragma unroll
            for (int ks = 0; ks < 4; ++ks) rd(fa[ks], 0, ks);
            auto step = [&](int nsl, FF (&cur)[4], FF (&nxt)[4]) {
                LGKM_BAR();
#pragma unroll
                for (int ks = 0; ks < 4; ++ks) { acc = MFMA(cur[ks].w, cur[ks].a, acc); SBAR(); rd(nxt[ks], nsl, ks); SBAR(); if (pace) PACE(); } };
#pragma unroll 1
            for (int it = 0; it < n; it += 2) { step((it + 1) & 3, fa, fb); step((it + 2) & 3, fb, fa); }
        } };
    if (wid8 == 0) wait_flags(flagp(ws, FL_U + tb * 7 + lane), lane < 7, tag);
    __syncthreads();
    STAMP(5);
    pass(wout, ws + WS_U, 56, 14, ag, true);
    STAMP(6);
    if (wid8 == 0) wait_flags(flagp(ws, FL_O + (tb >> 2) * 32 + lane), lane < 32, tag);
    __syncthreads();
    STAMP(7);
    pass(wo, ws + WS_OF, 32, 8, as, false);
    STAMP(8);
    if (wid8 < 4) {
        const int tok = tb * 64 + wr * 32 + l31;
#pragma unroll
        for (int q = 0; q < 4; ++q) { const size_t o = (size_t)tok * DM + db * 64 + wc * 32 + 8 * q + 4 * hh;
            const u32x2 xv = *(const u32x2*)(x + o);
            const float r0 = bflo(xv[0]) + as[4 * q] * silu_f(ag[4 * q]), r1 = bfhi(xv[0]) + as[4 * q + 1] * silu_f(ag[4 * q + 1]);
            const float r2 = bflo(xv[1]) + as[4 * q + 2] * silu_f(ag[4 * q + 2]), r3 = bfhi(xv[1]) + as[4 * q + 3] * silu_f(ag[4 * q + 3]);
            *(u32x2*)(out + o) = (u32x2){cvtpk(r0, r1), cvtpk(r2, r3)}; }
    }
}

#define GDELAY 0
#define GPRIO 2
constexpr int ALL_LDS = S_LDS;
__global__ __launch_bounds__(512, 4) void k_all(const uint16_t* __restrict__ x, const uint16_t* __restrict__ wqkv, const uint16_t* __restrict__ wo, const uint16_t* __restrict__ win,
                                                const uint16_t* __restrict__ wsp, const uint16_t* __restrict__ bsp, const uint16_t* __restrict__ wout, char* __restrict__ ws, uint16_t* __restrict__ out) {
    extern __shared__ __attribute__((aligned(16))) char lds[];
    const int b = blockIdx.x;
    const u64 tag = *(const __attribute__((address_space(4))) u64*)(ws + WS_FLAG + FL_EPOCH * 128) + 1;
    STAMP(0);
    if (b < 256) {
        __builtin_amdgcn_s_setprio(3);
        if (b < 192) qkv_role(x, wqkv, ws, lds, b, tag); else stats_role(x, wqkv, win, ws, lds, b - 192, tag);
        __builtin_amdgcn_s_setprio(0);
        __syncthreads();
        attn_role(ws, lds, b, tag);
        STAMP(5);
    } else {
        const int g = b - 256;
        __builtin_amdgcn_s_setprio(GPRIO);
            if (threadIdx.x == 0) __hip_atomic_store(flagp(ws, FL_G + g), tag, RLX_AGENT);
        { const u64 t0 = __builtin_amdgcn_s_memrealtime(); while (__builtin_amdgcn_s_memrealtime() - t0 < GDELAY) __builtin_amdgcn_s_sleep(8); }
        if (g < 224) { z_role(x, win, ws, lds, g, tag); STAMP(2); __syncthreads(); spatial_role(wsp, bsp, ws, lds, g, tag); STAMP(4); __syncthreads(); }
        final_role(x, wo, wout, ws, out, lds, g, tag);
        if (g == 255 && threadIdx.x < 64) {
            const int lane = threadIdx.x;
#pragma unroll 1
            for (int c = 0; c < 4; ++c) { wait_flags(flagp(ws, FL_O + c * 64 + lane), true, tag); wait_flags(flagp(ws, FL_G + c * 64 + lane), true, tag); }
            if (lane == 0) __hip_atomic_store(flagp(ws, FL_EPOCH), tag, RLX_AGENT);
            __builtin_amdgcn_s_sleep(127); __builtin_amdgcn_s_sleep(127); __builtin_amdgcn_s_sleep(127);
            { const GAS unsigned* sp = (const GAS unsigned*)(ws + 32 * MB); const unsigned t0 = __hip_atomic_load(sp, RLX_AGENT);
              const int blk = (lane >> 4) == 0 ? 0 : (lane >> 4) == 1 ? 200 : (lane >> 4) == 2 ? 256 : 470;
              const unsigned v = __hip_atomic_load(sp + blk * 16 + (lane & 15), RLX_AGENT);
              printf("b%d s%d %d\n", blk, lane & 15, (int)(v - t0)); }
        }
        STAMP(9);
    }
}

extern "C" void kernel_launch(void* const* d_in, const int* in_sizes, int n_in, void* d_out, int out_size, void* d_ws, size_t ws_size, hipStream_t stream) {
    const uint16_t* x = (const uint16_t*)d_in[0]; const uint16_t* wqkv = (const uint16_t*)d_in[1]; const uint16_t* wo = (const uint16_t*)d_in[2];
    const uint16_t* win = (const uint16_t*)d_in[4]; const uint16_t* wsp = (const uint16_t*)d_in[5]; const uint16_t* bsp = (const uint16_t*)d_in[6]; const uint16_t* wout = (const uint16_t*)d_in[7];
    (void)hipFuncSetAttribute((const void*)k_all, hipFuncAttributeMaxDynamicSharedMemorySize, ALL_LDS);
    k_all<<<512, 512, ALL_LDS, stream>>>(x, wqkv, wo, win, wsp, bsp, wout, (char*)d_ws, (uint16_t*)d_out);
}
```

```cpp
#include <hip/hip_runtime.h>
#include <hip/hip_bf16.h>
#include <stdint.h>
#define PACE_N 2
#include <stdio.h>

typedef short bf16x8 __attribute__((ext_vector_type(8)));
typedef short s16x4 __attribute__((ext_vector_type(4)));
typedef __bf16 bf16x2_t __attribute__((ext_vector_type(2)));
typedef float f32x16 __attribute__((ext_vector_type(16)));
typedef float f32x4 __attribute__((ext_vector_type(4)));
typedef float f32x2 __attribute__((ext_vector_type(2)));
typedef unsigned u32x4 __attribute__((ext_vector_type(4)));
typedef unsigned u32x2 __attribute__((ext_vector_type(2)));
typedef unsigned long long u64;
#define GAS __attribute__((address_space(1)))
#define LAS __attribute__((address_space(3)))
typedef LAS const char* lds_cptr;
typedef short v4i16_t __attribute__((ext_vector_type(4)));
#define RLX_AGENT __ATOMIC_RELAXED, __HIP_MEMORY_SCOPE_AGENT

constexpr int SEQ = 2048, DM = 512, NQKV = 1536, NIN = 1792, FH = 896;
constexpr float QSCALE = 0.25f * 1.4426950408889634f;
constexpr float LN_EPS = 1e-5f;
constexpr size_t MB = 1024 * 1024;
constexpr size_t WS_QF = 0, WS_KV = 2 * MB, WS_Z1 = 6 * MB, WS_Z2N = 10 * MB, WS_U = 14 * MB, WS_OF = 18 * MB;
constexpr size_t WS_KM = 20 * MB, WS_ST = 20 * MB + 65536, WS_RS = 20 * MB + 196608, WS_CS = 20 * MB + 229376  , WS_FLAG = 21 * MB;
constexpr int FL_RS = 0, FL_CS = 64, FL_QKV = 128, FL_ST = 320, FL_Z = 432, FL_O = 656, FL_U = 912, FL_G = 1136, FL_EPOCH = 1392;

#define MFMA(a, b, c) __builtin_amdgcn_mfma_f32_32x32x16_bf16(a, b, c, 0, 0, 0)
#define SBAR() __builtin_amdgcn_sched_barrier(0)
__device__ __forceinline__ unsigned cvtpk(float lo, float hi) { unsigned r; asm("v_cvt_pk_bf16_f32 %0, %1, %2" : "=v"(r) : "v"(lo), "v"(hi)); return r; }
__device__ __forceinline__ float bflo(unsigned u) { return __uint_as_float(u << 16); }
__device__ __forceinline__ float bfhi(unsigned u) { return __uint_as_float(u & 0xffff0000u); }
__device__ __forceinline__ float dot2(unsigned a, unsigned b, float acc) {
    return __builtin_amdgcn_fdot2_f32_bf16(__builtin_bit_cast(bf16x2_t, a), __builtin_bit_cast(bf16x2_t, b), acc, false); }
__device__ __forceinline__ void glds16(const void* g, unsigned lds_base) {
    asm volatile("s_mov_b32 m0, %1\n\ts_nop 0\n\tglobal_load_lds_dwordx4 %0, off" :: "v"(g), "s"(lds_base) : "memory", "m0"); }
template <int IMM, bool WT> __device__ __forceinline__ void glds_s(const void* sbase, unsigned voff) {
    if (WT) asm volatile("global_load_lds_dwordx4 %0, %1 offset:%2 sc1" :: "v"(voff), "s"(sbase), "n"(IMM) : "memory");
    else asm volatile("global_load_lds_dwordx4 %0, %1 offset:%2" :: "v"(voff), "s"(sbase), "n"(IMM) : "memory"); }
__device__ __forceinline__ void set_m0(unsigned v) { asm volatile("s_mov_b32 m0, %0\n\ts_nop 0" :: "s"(v) : "memory", "m0"); }
__device__ __forceinline__ s16x4 trread(lds_cptr p) { return __builtin_bit_cast(s16x4, __builtin_amdgcn_ds_read_tr16_b64_v4i16((LAS v4i16_t*)p)); }
__device__ __forceinline__ bf16x8 ldsfrag(lds_cptr p) { return *(LAS const bf16x8*)p; }
__device__ __forceinline__ bf16x8 cat(s16x4 a, s16x4 b) { return (bf16x8){a[0], a[1], a[2], a[3], b[0], b[1], b[2], b[3]}; }
__device__ __forceinline__ float swap32_add(float v) { auto rr = __builtin_amdgcn_permlane32_swap(__float_as_uint(v), __float_as_uint(v), false, false); return __uint_as_float(rr[0]) + __uint_as_float(rr[1]); }
__device__ __forceinline__ float swap32_max(float v) { auto rr = __builtin_amdgcn_permlane32_swap(__float_as_uint(v), __float_as_uint(v), false, false); return fmaxf(__uint_as_float(rr[0]), __uint_as_float(rr[1])); }
template <int CTRL> __device__ __forceinline__ float dppf(float v) { return __int_as_float(__builtin_amdgcn_update_dpp(0, __float_as_int(v), CTRL, 0xf, 0xf, false)); }
__device__ __forceinline__ float row16_add(float v) { v += dppf<0x128>(v); v += dppf<0x124>(v); v += dppf<0x122>(v); v += dppf<0x121>(v); return v; }
__device__ __forceinline__ float row16_max(float v) { v = fmaxf(v, dppf<0x128>(v)); v = fmaxf(v, dppf<0x124>(v)); v = fmaxf(v, dppf<0x122>(v)); v = fmaxf(v, dppf<0x121>(v)); return v; }
__device__ __forceinline__ float swap16_max(float v) { auto rr = __builtin_amdgcn_permlane16_swap(__float_as_uint(v), __float_as_uint(v), false, false); return fmaxf(__uint_as_float(rr[0]), __uint_as_float(rr[1])); }
__device__ __forceinline__ float gelu_t(float x) {
    const float c1 = -2.3022082f, c2 = -0.10294324f;
    float e = __builtin_amdgcn_exp2f(x * __builtin_fmaf(x * x, c2, c1));
    return x * __builtin_amdgcn_rcpf(1.0f + e); }
__device__ __forceinline__ float silu_f(float x) { return x * __builtin_amdgcn_rcpf(1.0f + __builtin_amdgcn_exp2f(-1.4426950408889634f * x)); }
#define STAMP(slot) do {} while (0)
#define PF_DIST 9
#define PACE() do { asm volatile("s_nop %0" :: "n"(PACE_N)); SBAR(); } while (0)
#define WAITV(n) asm volatile("s_waitcnt vmcnt(" #n ")" ::: "memory")
#define WAIT_BAR(n) asm volatile("s_waitcnt vmcnt(" #n ")\n\ts_barrier" ::: "memory")
#define BAR() asm volatile("s_barrier" ::: "memory")
#define LGKM_BAR() do { __builtin_amdgcn_s_waitcnt(0xC07F); asm volatile("" ::: "memory"); __builtin_amdgcn_s_barrier(); asm volatile("" ::: "memory"); } while (0)
__device__ __forceinline__ unsigned wsw(unsigned row) { return ((row & 3) << 2) | ((row >> 2) & 3); }
__device__ __forceinline__ void st16_wt(void* p, u32x4 v) {
    asm volatile("global_store_dwordx4 %0, %1, off sc1\n\ts_nop 1" :: "v"(p), "v"(v)); }
__device__ __forceinline__ void st8_wt(void* p, u64 v) { __hip_atomic_store((GAS u64*)p, v, RLX_AGENT); }
__device__ __forceinline__ u64 ld8_wt(const void* p) { return __hip_atomic_load((GAS u64*)p, RLX_AGENT); }
__device__ __forceinline__ u32x4 ld16_wt(const void* p) { u32x4 v; asm volatile("global_load_dwordx4 %0, %1, off sc1\n\ts_waitcnt vmcnt(0)" : "=&v"(v) : "v"(p) : "memory"); return v; }
__device__ __forceinline__ void glds16_wt(const void* g, unsigned lds_base) {
    asm volatile("s_mov_b32 m0, %1\n\ts_nop 0\n\tglobal_load_lds_dwordx4 %0, off sc1" :: "v"(g), "s"(lds_base) : "memory", "m0"); }
__device__ __forceinline__ GAS u64* flagp(char* ws, int idx) { return (GAS u64*)(ws + WS_FLAG) + idx * 16; }
__device__ __forceinline__ void wait_flags(GAS u64* f, bool want, u64 tag) {
    for (unsigned spins = 0; spins < (1u << 20); ++spins) {
        const u64 v = want ? __hip_atomic_load(f, RLX_AGENT) : tag;
        if (__all(v == tag)) return;
        __builtin_amdgcn_s_sleep(2); } }
#define ACQUIRE() do { __builtin_amdgcn_fence(__ATOMIC_ACQUIRE, "agent"); asm volatile("s_waitcnt vmcnt(0)" ::: "memory"); } while (0)
#define PUBLISH(idx) do { WAITV(0); __syncthreads(); if (threadIdx.x == 0) __hip_atomic_store(flagp(ws, idx), tag, RLX_AGENT); } while (0)

__device__ __forceinline__ void stats_role(const uint16_t* __restrict__ x, const uint16_t* __restrict__ wqkv, const uint16_t* __restrict__ win, char* __restrict__ ws, char* lds, int j, u64 tag) {
    const int tid = threadIdx.x, lane = tid & 63, wid = tid >> 6, rr = lane >> 4, cc = lane & 15;
    const unsigned ONES = 0x3f803f80u;
    u32x4 xv[4], w[8];
    const int row = 32 * j + 4 * wid + rr;
#pragma unroll
    for (int i = 0; i < 4; ++i) xv[i] = *(const u32x4*)(x + (size_t)row * DM + i * 128 + cc * 8);
    const int cbk = j >> 1;
    if (j < 52) {
        const uint16_t* W = cbk < 12 ? wqkv + cbk * 128 : win + (cbk - 12) * 128; const int ldw = cbk < 12 ? NQKV : NIN;
        const uint16_t* p = W + (size_t)((j & 1) * 256 + 32 * wid + rr) * ldw + cc * 8;
#pragma unroll
        for (int i = 0; i < 8; ++i) w[i] = *(const u32x4*)(p + (size_t)(i * 4) * ldw);
    }
    float s1 = 0.f, s2 = 0.f;
#pragma unroll
    for (int i = 0; i < 4; ++i)
#pragma unroll
        for (int e = 0; e < 4; ++e) { s1 = dot2(xv[i][e], ONES, s1); s2 = dot2(xv[i][e], xv[i][e], s2); }
    s1 = row16_add(s1); s2 = row16_add(s2);
    { const float mu = s1 * (1.0f / DM), var = fmaxf(s2 * (1.0f / DM) - mu * mu, 0.f);
      if (cc == 0) st8_wt(ws + WS_RS + (size_t)row * 8, __builtin_bit_cast(u64, (f32x2){mu, __builtin_amdgcn_rsqf(var + LN_EPS)})); }
    if (j < 52) {
        float cs[8] = {0.f, 0.f, 0.f, 0.f, 0.f, 0.f, 0.f, 0.f};
        unsigned sel_lo = 0x00003f80u, sel_hi = 0x3f800000u;
        asm volatile("" : "+v"(sel_lo), "+v"(sel_hi));
#pragma unroll
        for (int i = 0; i < 8; ++i)
#pragma unroll
            for (int e = 0; e < 4; ++e) { cs[2 * e] = dot2(w[i][e], sel_lo, cs[2 * e]); cs[2 * e + 1] = dot2(w[i][e], sel_hi, cs[2 * e + 1]); }
        float* red = (float*)lds;
        *(f32x4*)(red + (wid * 4 + rr) * 128 + cc * 8) = (f32x4){cs[0], cs[1], cs[2], cs[3]}; *(f32x4*)(red + (wid * 4 + rr) * 128 + cc * 8 + 4) = (f32x4){cs[4], cs[5], cs[6], cs[7]};
        __syncthreads();
        if (tid < 64) { f32x2 a = {0.f, 0.f};
#pragma unroll
            for (int q = 0; q < 32; ++q) a += *(const f32x2*)(red + q * 128 + 2 * tid);
            st8_wt(ws + WS_CS + (size_t)((j & 1) * 3328 + cbk * 128 + 2 * tid) * 4, __builtin_bit_cast(u64, a)); }
    }
    STAMP(11);
    WAITV(0); __syncthreads();
    STAMP(1);
    if (tid == 0) { __hip_atomic_store(flagp(ws, FL_RS + j), tag, RLX_AGENT); if (j < 52) __hip_atomic_store(flagp(ws, FL_CS + j), tag, RLX_AGENT); }
}

constexpr int P_SLOT = 16384, P_ST = 4 * P_SLOT, P_LDS = 4 * P_SLOT + 4096;
struct PFrag { bf16x8 x[2], w; };
template <bool SWAP>
__device__ __forceinline__ void proj_main(const uint16_t* __restrict__ x, const uint16_t* __restrict__ W, int ldw, int rb, int col0, int cblk, char* __restrict__ ws, char* lds, int rot, u64 tag, f32x16 (&acc)[2]) {
    const int tid = threadIdx.x, lane = tid & 63, wid = __builtin_amdgcn_readfirstlane(tid >> 6), wr = wid >> 2, wc = wid & 3, l31 = lane & 31, hh = lane >> 5;
    const unsigned lds0 = (unsigned)(uintptr_t)lds;
    const int xr = 16 * wid + (lane >> 2), wk = 4 * wid + (lane >> 4);
    const char* xb = (const char*)(x + (size_t)rb * 128 * DM); const char* wb = (const char*)(W + col0);
    const unsigned xvo = (xr * DM + ((lane & 3) ^ ((xr >> 2) & 3)) * 8) * 2, wvo = (wk * ldw + ((lane & 15) ^ wsw(wk)) * 8) * 2;
    const unsigned xd = lds0 + wid * 1024, wd = lds0 + 8192 + wid * 1024;
    auto issue1 = [&](int it, int which) { const unsigned so = (it & 3) * P_SLOT; const int kk = (it + rot) & 15;
        if (which == 0) { set_m0(xd + so); glds_s<0, false>(xb + kk * 64, xvo); } else { set_m0(wd + so); glds_s<0, false>(wb + (size_t)kk * 64 * ldw, wvo); } SBAR(); };
#pragma unroll
    for (int it = 0; it < 4; ++it) { issue1(it, 0); issue1(it, 1); }
    unsigned xoff[2][2], woff[2][2];
    const int blk = (lane >> 4) & 1, q = (lane & 15) >> 2, p = lane & 3;
#pragma unroll
    for (int ks = 0; ks < 2; ++ks) {
#pragma unroll
        for (int rt = 0; rt < 2; ++rt) { const int r = wr * 64 + rt * 32 + l31; xoff[rt][ks] = r * 64 + (((2 * ks + hh) ^ ((r >> 2) & 3)) * 16); }
        const int kr = 8 * hh + q, ch = 4 * wc + 2 * blk + (p >> 1);
        woff[0][ks] = 8192 + ks * 4096 + kr * 256 + ((ch ^ wsw(kr)) * 16) + 8 * (p & 1); woff[1][ks] = (woff[0][ks] + 1024) ^ 16; }
    auto rd = [&](PFrag& f, int slot, int j) { lds_cptr sb = (lds_cptr)lds + slot * P_SLOT;
        f.x[0] = ldsfrag(sb + xoff[0][j]); f.x[1] = ldsfrag(sb + xoff[1][j]); f.w = cat(trread(sb + woff[0][j]), trread(sb + woff[1][j])); };
    auto mm = [&](const PFrag& f) {
#pragma unroll
        for (int rt = 0; rt < 2; ++rt) acc[rt] = SWAP ? MFMA(f.x[rt], f.w, acc[rt]) : MFMA(f.w, f.x[rt], acc[rt]); };
    acc[0] = f32x16{}; acc[1] = f32x16{};
    PFrag fa[2], fb[2];
    WAIT_BAR(6);
    STAMP(10);
    rd(fa[0], 0, 0); rd(fa[1], 0, 1);
    auto mm1 = [&](const PFrag& f, int rt) { acc[rt] = SWAP ? MFMA(f.x[rt], f.w, acc[rt]) : MFMA(f.w, f.x[rt], acc[rt]); SBAR(); };
    auto step = [&](int it, int sl, PFrag (&cur)[2], PFrag (&nxt)[2]) {
        __builtin_amdgcn_s_waitcnt(0xC07F);
        if (it < 13) WAITV(4); else if (it == 13) WAITV(2); else WAITV(0);
        __builtin_amdgcn_s_barrier(); asm volatile("" ::: "memory");
        lds_cptr sb = (lds_cptr)lds + ((sl + 1) & 3) * P_SLOT;
        mm1(cur[0], 0);
        nxt[0].x[0] = ldsfrag(sb + xoff[0][0]); nxt[0].x[1] = ldsfrag(sb + xoff[1][0]); SBAR();
        mm1(cur[0], 1);
        nxt[0].w = cat(trread(sb + woff[0][0]), trread(sb + woff[1][0])); SBAR();
        if (it + 4 < 16) issue1(it + 4, 0);
        mm1(cur[1], 0);
        nxt[1].x[0] = ldsfrag(sb + xoff[0][1]); nxt[1].x[1] = ldsfrag(sb + xoff[1][1]); SBAR();
        mm1(cur[1], 1);
        nxt[1].w = cat(trread(sb + woff[0][1]), trread(sb + woff[1][1])); SBAR();
        if (it + 4 < 16) issue1(it + 4, 1); };
#pragma unroll 1
    for (int io = 0; io < 2; ++io) { step(io * 4, 0, fa, fb); step(io * 4 + 1, 1, fb, fa); step(io * 4 + 2, 2, fa, fb); step(io * 4 + 3, 3, fb, fa); }
    const char* fa_ = (const char*)(lane < 4 ? flagp(ws, FL_RS + rb * 4 + lane) : flagp(ws, FL_CS + 2 * cblk + (lane & 1)));
    const char* sa_ = tid < 128 ? ws + WS_RS + (size_t)(rb * 128 + tid) * 8 : ws + WS_CS + (size_t)(cblk * 128 + 2 * (tid & 63)) * 4;
    const char* sb_ = ws + WS_CS + (size_t)(3328 + cblk * 128 + 2 * (tid & 63)) * 4;
    u64 fl, stv, stw;
    asm volatile("global_load_dwordx2 %0, %1, off sc1" : "=v"(fl) : "v"(fa_) : "memory");
    step(8, 0, fa, fb); step(9, 1, fb, fa); step(10, 2, fa, fb); step(11, 3, fb, fa);
    asm volatile("" : "+v"(fl));
    const bool got = __all(lane >= 6 || fl == tag);
    asm volatile("global_load_dwordx2 %0, %2, off sc1\n\tglobal_load_dwordx2 %1, %3, off sc1" : "=&v"(stv), "=&v"(stw) : "v"(sa_), "v"(sb_) : "memory");
    step(12, 0, fa, fb); step(13, 1, fb, fa); step(14, 2, fa, fb); step(15, 3, fb, fa);
    asm volatile("" : "+v"(stv), "+v"(stw));
    if (!got) { wait_flags((GAS u64*)fa_, lane < 6, tag); stv = ld8_wt(sa_); stw = ld8_wt(sb_); }
    STAMP(11);
    float* st = (float*)(lds + P_ST);
    { const f32x2 v = __builtin_bit_cast(f32x2, stv);
      if (tid < 128) { st[tid] = v[0]; st[128 + tid] = v[1]; } else if (tid < 192) *(f32x2*)(st + 256 + 2 * (tid - 128)) = v + __builtin_bit_cast(f32x2, stw); }
    __syncthreads();
}

__device__ __forceinline__ void qkv_role(const uint16_t* __restrict__ x, const uint16_t* __restrict__ wqkv, char* __restrict__ ws, char* lds, int b, u64 tag) {
    const int xcd = b & 7, i = b >> 3;
    const int rb = 4 * (xcd >> 1) + (i & 3), cb = 6 * (xcd & 1) + (i >> 2);
    const int tid = threadIdx.x, lane = tid & 63, wid = __builtin_amdgcn_readfirstlane(tid >> 6), wr = wid >> 2, wc = wid & 3, l31 = lane & 31, hh = lane >> 5;
    f32x16 acc[2];
    const int rot = ((i & 3) * 4 + (i >> 2) * 3) & 15;
    if (cb >= 8) proj_main<true>(x, wqkv, NQKV, rb, cb * 128, cb, ws, lds, rot, tag, acc); else proj_main<false>(x, wqkv, NQKV, rb, cb * 128, cb, ws, lds, rot, tag, acc);
    STAMP(1);
    const float* st = (const float*)(lds + P_ST);
    if (cb >= 8) {
        const float cs = st[256 + wc * 32 + l31];
        const int head = (cb - 8) * 8 + wc * 2 + (l31 >> 4);
#pragma unroll
        for (int rt = 0; rt < 2; ++rt) { const int tr = wr * 64 + rt * 32;
            unsigned pk[8];
#pragma unroll
            for (int q = 0; q < 4; ++q) { const f32x4 m = *(const f32x4*)(st + tr + 8 * q + 4 * hh), r = *(const f32x4*)(st + 128 + tr + 8 * q + 4 * hh);
                pk[2 * q] = cvtpk((acc[rt][4 * q] - m[0] * cs) * r[0], (acc[rt][4 * q + 1] - m[1] * cs) * r[1]);
                pk[2 * q + 1] = cvtpk((acc[rt][4 * q + 2] - m[2] * cs) * r[2], (acc[rt][4 * q + 3] - m[3] * cs) * r[3]); }
            const int tile = rb * 4 + (tr >> 5);
            char* dst = ws + WS_KV + (size_t)head * 131072 + (tile >> 3) * 16384 + 8192 + (tile & 7) * 1024 + hh * 256 + (l31 & 15) * 16;
            st16_wt(dst, (u32x4){pk[0], pk[1], pk[2], pk[3]}); st16_wt(dst + 512, (u32x4){pk[4], pk[5], pk[6], pk[7]}); }
    } else {
        const bool isK = cb >= 4; const float sc = isK ? 1.0f : QSCALE;
        float kmx[2] = {0.f, 0.f};
#pragma unroll
        for (int rt = 0; rt < 2; ++rt) { const int tr = wr * 64 + rt * 32, tile = rb * 4 + (tr >> 5);
            const float mu = st[tr + l31], rs = st[128 + tr + l31] * sc;
#pragma unroll
            for (int hb = 0; hb < 2; ++hb) {
                float v[8];
#pragma unroll
                for (int q = 0; q < 2; ++q) { const f32x4 c = *(const f32x4*)(st + 256 + wc * 32 + 16 * hb + 8 * q + 4 * hh);
#pragma unroll
                    for (int e = 0; e < 4; ++e) v[4 * q + e] = (acc[rt][8 * hb + 4 * q + e] - mu * c[e]) * rs; }
                u32x4 pk;
#pragma unroll
                for (int e = 0; e < 4; ++e) pk[e] = cvtpk(v[2 * e], v[2 * e + 1]);
                const int head = (cb & 3) * 8 + wc * 2 + hb;
                char* dst = isK ? ws + WS_KV + (size_t)head * 131072 + (tile >> 3) * 16384 + (tile & 7) * 1024 + lane * 16 : ws + WS_QF + (size_t)head * 65536 + tile * 1024 + lane * 16;
                st16_wt(dst, pk);
                float n2 = 0.f;
#pragma unroll
                for (int e = 0; e < 4; ++e) n2 = dot2(pk[e], pk[e], n2);
                kmx[hb] = fmaxf(kmx[hb], swap32_add(n2));
            } }
        if (isK) {
#pragma unroll
            for (int hb = 0; hb < 2; ++hb) { const float m = swap16_max(row16_max(kmx[hb]));
                if (lane == 0) __hip_atomic_store((GAS unsigned*)(ws + WS_KM) + ((cb & 3) * 8 + wc * 2 + hb) * 32 + rb * 2 + wr, __float_as_uint(m), RLX_AGENT); } }
    }
    STAMP(12);
    PUBLISH(FL_QKV + cb * 16 + rb);
    STAMP(13);
}

constexpr int A_ONES = 3 * 16384, A_LDS = A_ONES + 8192 + 512;
__device__ __forceinline__ void attn_role(char* __restrict__ ws, char* lds, int b, u64 tag) {
    const int tid = threadIdx.x, lane = tid & 63, wid = __builtin_amdgcn_readfirstlane(tid >> 6);
    const int head = (b & 7) * 4 + ((b >> 3) & 3), jb = b >> 5;
    const unsigned lds0 = (unsigned)(uintptr_t)lds;
    const char* kvb = ws + WS_KV + (size_t)head * 131072;
    for (int i = tid; i < (8192 + 512) / 16; i += 512) { const unsigned o1 = ((i & 11) == 0) ? 0x3f803f80u : 0u; *(u32x4*)(lds + A_ONES + i * 16) = (u32x4){o1, o1, o1, o1}; }
    if (wid == 0) {
        const int hb = head >> 3;
        const int idx = lane < 16 ? (4 + hb) * 16 + lane : lane < 32 ? (8 + hb) * 16 + (lane - 16) : hb * 16 + 2 * jb + (lane & 1);
        wait_flags(flagp(ws, FL_QKV + idx), lane < 34, tag);
        STAMP(14);
        }
    __syncthreads();
    STAMP(2);
    unsigned goff = wid * 2048 + lane * 16;
#pragma unroll
    for (int c = 0; c < 2; ++c)
#pragma unroll
        for (int k = 0; k < 2; ++k) glds16_wt(kvb + c * 16384 + goff + k * 1024, lds0 + c * 16384 + wid * 2048 + k * 1024);
    const int tile0 = jb * 8 + wid;
    u32x4 qa; unsigned kmu;
    asm volatile("global_load_dwordx4 %0, %2, off sc1\n\tglobal_load_dword %1, %3, off sc1\n\ts_waitcnt vmcnt(0)" : "=&v"(qa), "=&v"(kmu)
                 : "v"(ws + WS_QF + (size_t)head * 65536 + tile0 * 1024 + lane * 16), "v"(ws + WS_KM + (size_t)(head * 32 + (lane & 31)) * 4) : "memory");
    const float km = swap16_max(row16_max(__uint_as_float(kmu)));
    float na = 0.f;
#pragma unroll
    for (int e = 0; e < 4; ++e) na = dot2(qa[e], qa[e], na);
    na = swap32_add(na);
    float ma = __builtin_amdgcn_sqrtf(na * km);
    if (__any(!(ma <= 40.0f))) {
        float xa = -3.0e38f;
        for (int t = 0; t < 64; ++t) {
            const bf16x8 kf = __builtin_bit_cast(bf16x8, ld16_wt(kvb + (t >> 3) * 16384 + (t & 7) * 1024 + lane * 16));
            const f32x16 sa = MFMA(kf, __builtin_bit_cast(bf16x8, qa), f32x16{});
#pragma unroll
            for (int r = 0; r < 16; ++r) xa = fmaxf(xa, sa[r]); }
        ma = swap32_max(xa);
    }
    const float nma = -ma;
    const unsigned kaddr = lds0 + lane * 16;
    const bool ones = (lane & 16) != 0;
    const unsigned vaddr = ones ? lds0 + A_ONES + (lane & 15) * 16 : lds0 + 8192 + (lane >> 5) * 256 + (lane & 15) * 16;
    const unsigned vsel = ones ? 0u : 0xffffffffu;
    goff += 2 * 16384;
    unsigned dbeg = lds0 + wid * 2048, dend = dbeg + 3 * 16384, dslot = dbeg + 2 * 16384, nslot = 0x4000, cnt, m0save;
    unsigned oa0, oa1, oa2, oa3;
    STAMP(3);
    asm volatile(
    "s_mov_b32 %[m0save], m0\n"
    "v_mov_b32 v96, %[qa0]\n"
    "v_mov_b32 v97, %[qa1]\n"
    "v_mov_b32 v98, %[qa2]\n"
    "v_mov_b32 v99, %[qa3]\n"
    "v_mov_b32 v72, %[nma]\n"
    "v_mov_b32 v56, 0\n"
    "v_mov_b32 v73, %[nma]\n"
    "v_mov_b32 v57, 0\n"
    "v_mov_b32 v74, %[nma]\n"
    "v_mov_b32 v58, 0\n"
    "v_mov_b32 v75, %[nma]\n"
    "v_mov_b32 v59, 0\n"
    "v_mov_b32 v76, %[nma]\n"
    "v_mov_b32 v60, 0\n"
    "v_mov_b32 v77, %[nma]\n"
    "v_mov_b32 v61, 0\n"
    "v_mov_b32 v78, %[nma]\n"
    "v_mov_b32 v62, 0\n"
    "v_mov_b32 v79, %[nma]\n"
    "v_mov_b32 v63, 0\n"
    "v_mov_b32 v80, %[nma]\n"
    "v_mov_b32 v64, 0\n"
    "v_mov_b32 v81, %[nma]\n"
    "v_mov_b32 v65, 0\n"
    "v_mov_b32 v82, %[nma]\n"
    "v_mov_b32 v66, 0\n"
    "v_mov_b32 v83, %[nma]\n"
    "v_mov_b32 v67, 0\n"
    "v_mov_b32 v84, %[nma]\n"
    "v_mov_b32 v68, 0\n"
    "v_mov_b32 v85, %[nma]\n"
    "v_mov_b32 v69, 0\n"
    "v_mov_b32 v86, %[nma]\n"
    "v_mov_b32 v70, 0\n"
    "v_mov_b32 v87, %[nma]\n"
    "v_mov_b32 v71, 0\n"
    "v_mov_b32 v120, 0\n"
    "v_mov_b32 v121, 0\n"
    "v_mov_b32 v122, 0\n"
    "v_mov_b32 v123, 0\n"
    "v_mov_b32 v88, 0\n"
    "v_mov_b32 v89, 0\n"
    "v_mov_b32 v90, 0\n"
    "v_mov_b32 v91, 0\n"
    "v_mov_b32 v92, 0\n"
    "v_mov_b32 v93, 0\n"
    "v_mov_b32 v94, 0\n"
    "v_mov_b32 v95, 0\n"
    "v_mov_b32 v124, %[kaddr]\n"
    "v_mov_b32 v125, %[vaddr]\n"
    "v_add_u32 v126, 0x4000, v124\n"
    "v_and_b32 v127, 0x4000, %[vsel]\n"
    "v_add_u32 v127, v127, v125\n"
    "s_waitcnt vmcnt(2) lgkmcnt(0)\n"
    "s_barrier\n"
    "ds_read_b128 v[100:103], v124\n"
    "ds_read_b128 v[104:107], v124 offset:1024\n"
    "ds_read_b128 v[108:111], v125\n"
    "ds_read_b128 v[112:115], v125 offset:512\n"
    "s_mov_b32 %[cnt], 0\n"
    "s_waitcnt lgkmcnt(0)\n"
    "v_mfma_f32_32x32x16_bf16 v[24:39], v[100:103], v[96:99], v[72:87]\n"
    "s_nop 15\n"
    ".Lloop%=:\n"
    "v_exp_f32 v24, v24\n"
    "v_exp_f32 v25, v25\n"
    "v_mfma_f32_32x32x16_bf16 v[56:71], v[120:123], v[92:95], v[56:71]\n"
    "v_exp_f32 v26, v26\n"
    "v_exp_f32 v27, v27\n"
    "v_cvt_pk_bf16_f32 v88, v24, v25\n"
    "v_exp_f32 v28, v28\n"
    "s_waitcnt lgkmcnt(0)\n"
    "v_mfma_f32_32x32x16_bf16 v[40:55], v[104:107], v[96:99], v[72:87]\n"
    "v_exp_f32 v29, v29\n"
    "v_cvt_pk_bf16_f32 v89, v26, v27\n"
    "ds_read_b128 v[100:103], v124 offset:2048\n"
    "v_exp_f32 v30, v30\n"
    "ds_read_b128 v[116:119], v125 offset:1024\n"
    "v_exp_f32 v31, v31\n"
    "v_cvt_pk_bf16_f32 v90, v28, v29\n"
    "ds_read_b128 v[120:123], v125 offset:1536\n"
    "v_exp_f32 v32, v32\n"
    "v_exp_f32 v33, v33\n"
    "v_cvt_pk_bf16_f32 v91, v30, v31\n"
    "v_exp_f32 v34, v34\n"
    "v_exp_f32 v35, v35\n"
    "v_mfma_f32_32x32x16_bf16 v[56:71], v[108:111], v[88:91], v[56:71]\n"
    "v_cvt_pk_bf16_f32 v92, v32, v33\n"
    "v_exp_f32 v36, v36\n"
    "v_exp_f32 v37, v37\n"
    "v_cvt_pk_bf16_f32 v93, v34, v35\n"
    "v_exp_f32 v38, v38\n"
    "v_exp_f32 v39, v39\n"
    "v_cvt_pk_bf16_f32 v94, v36, v37\n"
    "v_cvt_pk_bf16_f32 v95, v38, v39\n"
    "v_exp_f32 v40, v40\n"
    "v_exp_f32 v41, v41\n"
    "v_mfma_f32_32x32x16_bf16 v[56:71], v[112:115], v[92:95], v[56:71]\n"
    "v_exp_f32 v42, v42\n"
    "v_exp_f32 v43, v43\n"
    "v_cvt_pk_bf16_f32 v88, v40, v41\n"
    "v_exp_f32 v44, v44\n"
    "s_waitcnt lgkmcnt(0)\n"
    "v_mfma_f32_32x32x16_bf16 v[24:39], v[100:103], v[96:99], v[72:87]\n"
    "v_exp_f32 v45, v45\n"
    "v_cvt_pk_bf16_f32 v89, v42, v43\n"
    "ds_read_b128 v[104:107], v124 offset:3072\n"
    "v_exp_f32 v46, v46\n"
    "ds_read_b128 v[108:111], v125 offset:2048\n"
    "v_exp_f32 v47, v47\n"
    "v_cvt_pk_bf16_f32 v90, v44, v45\n"
    "ds_read_b128 v[112:115], v125 offset:2560\n"
    "v_exp_f32 v48, v48\n"
    "v_exp_f32 v49, v49\n"
    "v_cvt_pk_bf16_f32 v91, v46, v47\n"
    "v_exp_f32 v50, v50\n"
    "v_exp_f32 v51, v51\n"
    "v_mfma_f32_32x32x16_bf16 v[56:71], v[116:119], v[88:91], v[56:71]\n"
    "v_cvt_pk_bf16_f32 v92, v48, v49\n"
    "v_exp_f32 v52, v52\n"
    "v_exp_f32 v53, v53\n"
    "v_cvt_pk_bf16_f32 v93, v50, v51\n"
    "v_exp_f32 v54, v54\n"
    "v_exp_f32 v55, v55\n"
    "v_cvt_pk_bf16_f32 v94, v52, v53\n"
    "v_cvt_pk_bf16_f32 v95, v54, v55\n"
    "v_exp_f32 v24, v24\n"
    "v_exp_f32 v25, v25\n"
    "v_mfma_f32_32x32x16_bf16 v[56:71], v[120:123], v[92:95], v[56:71]\n"
    "v_exp_f32 v26, v26\n"
    "v_exp_f32 v27, v27\n"
    "v_cvt_pk_bf16_f32 v88, v24, v25\n"
    "v_exp_f32 v28, v28\n"
    "s_waitcnt lgkmcnt(0)\n"
    "v_mfma_f32_32x32x16_bf16 v[40:55], v[104:107], v[96:99], v[72:87]\n"
    "v_exp_f32 v29, v29\n"
    "v_cvt_pk_bf16_f32 v89, v26, v27\n"
    "ds_read_b128 v[100:103], v124 offset:4096\n"
    "v_exp_f32 v30, v30\n"
    "ds_read_b128 v[116:119], v125 offset:3072\n"
    "v_exp_f32 v31, v31\n"
    "v_cvt_pk_bf16_f32 v90, v28, v29\n"
    "ds_read_b128 v[120:123], v125 offset:3584\n"
    "v_exp_f32 v32, v32\n"
    "v_exp_f32 v33, v33\n"
    "v_cvt_pk_bf16_f32 v91, v30, v31\n"
    "v_exp_f32 v34, v34\n"
    "v_exp_f32 v35, v35\n"
    "v_mfma_f32_32x32x16_bf16 v[56:71], v[108:111], v[88:91], v[56:71]\n"
    "v_cvt_pk_bf16_f32 v92, v32, v33\n"
    "v_exp_f32 v36, v36\n"
    "v_exp_f32 v37, v37\n"
    "v_cvt_pk_bf16_f32 v93, v34, v35\n"
    "v_exp_f32 v38, v38\n"
    "v_exp_f32 v39, v39\n"
    "v_cvt_pk_bf16_f32 v94, v36, v37\n"
    "v_cvt_pk_bf16_f32 v95, v38, v39\n"
    "v_exp_f32 v40, v40\n"
    "v_exp_f32 v41, v41\n"
    "v_mfma_f32_32x32x16_bf16 v[56:71], v[112:115], v[92:95], v[56:71]\n"
    "v_exp_f32 v42, v42\n"
    "v_exp_f32 v43, v43\n"
    "v_cvt_pk_bf16_f32 v88, v40, v41\n"
    "v_exp_f32 v44, v44\n"
    "s_waitcnt lgkmcnt(0)\n"
    "v_mfma_f32_32x32x16_bf16 v[24:39], v[100:103], v[96:99], v[72:87]\n"
    "v_exp_f32 v45, v45\n"
    "v_cvt_pk_bf16_f32 v89, v42, v43\n"
    "ds_read_b128 v[104:107], v124 offset:5120\n"
    "v_exp_f32 v46, v46\n"
    "ds_read_b128 v[108:111], v125 offset:4096\n"
    "v_exp_f32 v47, v47\n"
    "v_cvt_pk_bf16_f32 v90, v44, v45\n"
    "ds_read_b128 v[112:115], v125 offset:4608\n"
    "v_exp_f32 v48, v48\n"
    "v_exp_f32 v49, v49\n"
    "v_cvt_pk_bf16_f32 v91, v46, v47\n"
    "v_exp_f32 v50, v50\n"
    "v_exp_f32 v51, v51\n"
    "v_mfma_f32_32x32x16_bf16 v[56:71], v[116:119], v[88:91], v[56:71]\n"
    "v_cvt_pk_bf16_f32 v92, v48, v49\n"
    "v_exp_f32 v52, v52\n"
    "v_exp_f32 v53, v53\n"
    "v_cvt_pk_bf16_f32 v93, v50, v51\n"
    "v_exp_f32 v54, v54\n"
    "v_exp_f32 v55, v55\n"
    "v_cvt_pk_bf16_f32 v94, v52, v53\n"
    "v_cvt_pk_bf16_f32 v95, v54, v55\n"
    "v_exp_f32 v24, v24\n"
    "v_exp_f32 v25, v25\n"
    "v_mfma_f32_32x32x16_bf16 v[56:71], v[120:123], v[92:95], v[56:71]\n"
    "v_exp_f32 v26, v26\n"
    "v_exp_f32 v27, v27\n"
    "v_cvt_pk_bf16_f32 v88, v24, v25\n"
    "v_exp_f32 v28, v28\n"
    "s_waitcnt lgkmcnt(0)\n"
    "v_mfma_f32_32x32x16_bf16 v[40:55], v[104:107], v[96:99], v[72:87]\n"
    "v_exp_f32 v29, v29\n"
    "v_cvt_pk_bf16_f32 v89, v26, v27\n"
    "ds_read_b128 v[100:103], v124 offset:6144\n"
    "v_exp_f32 v30, v30\n"
    "ds_read_b128 v[116:119], v125 offset:5120\n"
    "v_exp_f32 v31, v31\n"
    "v_cvt_pk_bf16_f32 v90, v28, v29\n"
    "ds_read_b128 v[120:123], v125 offset:5632\n"
    "v_exp_f32 v32, v32\n"
    "v_exp_f32 v33, v33\n"
    "v_cvt_pk_bf16_f32 v91, v30, v31\n"
    "v_exp_f32 v34, v34\n"
    "v_exp_f32 v35, v35\n"
    "v_mfma_f32_32x32x16_bf16 v[56:71], v[108:111], v[88:91], v[56:71]\n"
    "v_cvt_pk_bf16_f32 v92, v32, v33\n"
    "v_exp_f32 v36, v36\n"
    "v_exp_f32 v37, v37\n"
    "v_cvt_pk_bf16_f32 v93, v34, v35\n"
    "v_exp_f32 v38, v38\n"
    "v_exp_f32 v39, v39\n"
    "v_cvt_pk_bf16_f32 v94, v36, v37\n"
    "v_cvt_pk_bf16_f32 v95, v38, v39\n"
    "v_exp_f32 v40, v40\n"
    "v_exp_f32 v41, v41\n"
    "v_mfma_f32_32x32x16_bf16 v[56:71], v[112:115], v[92:95], v[56:71]\n"
    "v_exp_f32 v42, v42\n"
    "v_exp_f32 v43, v43\n"
    "v_cvt_pk_bf16_f32 v88, v40, v41\n"
    "v_exp_f32 v44, v44\n"
    "s_waitcnt lgkmcnt(0)\n"
    "v_mfma_f32_32x32x16_bf16 v[24:39], v[100:103], v[96:99], v[72:87]\n"
    "v_exp_f32 v45, v45\n"
    "v_cvt_pk_bf16_f32 v89, v42, v43\n"
    "ds_read_b128 v[104:107], v124 offset:7168\n"
    "v_exp_f32 v46, v46\n"
    "ds_read_b128 v[108:111], v125 offset:6144\n"
    "v_exp_f32 v47, v47\n"
    "v_cvt_pk_bf16_f32 v90, v44, v45\n"
    "ds_read_b128 v[112:115], v125 offset:6656\n"
    "v_exp_f32 v48, v48\n"
    "v_exp_f32 v49, v49\n"
    "v_cvt_pk_bf16_f32 v91, v46, v47\n"
    "v_exp_f32 v50, v50\n"
    "v_exp_f32 v51, v51\n"
    "v_mfma_f32_32x32x16_bf16 v[56:71], v[116:119], v[88:91], v[56:71]\n"
    "v_cvt_pk_bf16_f32 v92, v48, v49\n"
    "v_exp_f32 v52, v52\n"
    "v_exp_f32 v53, v53\n"
    "v_cvt_pk_bf16_f32 v93, v50, v51\n"
    "v_exp_f32 v54, v54\n"
    "v_exp_f32 v55, v55\n"
    "v_cvt_pk_bf16_f32 v94, v52, v53\n"
    "v_cvt_pk_bf16_f32 v95, v54, v55\n"
    "v_exp_f32 v24, v24\n"
    "v_exp_f32 v25, v25\n"
    "v_mfma_f32_32x32x16_bf16 v[56:71], v[120:123], v[92:95], v[56:71]\n"
    "v_exp_f32 v26, v26\n"
    "v_exp_f32 v27, v27\n"
    "v_cvt_pk_bf16_f32 v88, v24, v25\n"
    "v_exp_f32 v28, v28\n"
    "s_waitcnt lgkmcnt(0)\n"
    "v_mfma_f32_32x32x16_bf16 v[40:55], v[104:107], v[96:99], v[72:87]\n"
    "v_exp_f32 v29, v29\n"
    "v_cvt_pk_bf16_f32 v89, v26, v27\n"
    "s_waitcnt vmcnt(0)\n"
    "s_barrier\n"
    "ds_read_b128 v[100:103], v126 offset:0\n"
    "v_exp_f32 v30, v30\n"
    "ds_read_b128 v[116:119], v125 offset:7168\n"
    "v_exp_f32 v31, v31\n"
    "v_cvt_pk_bf16_f32 v90, v28, v29\n"
    "ds_read_b128 v[120:123], v125 offset:7680\n"
    "v_exp_f32 v32, v32\n"
    "v_exp_f32 v33, v33\n"
    "v_cvt_pk_bf16_f32 v91, v30, v31\n"
    "v_exp_f32 v34, v34\n"
    "v_exp_f32 v35, v35\n"
    "v_mfma_f32_32x32x16_bf16 v[56:71], v[108:111], v[88:91], v[56:71]\n"
    "s_cmp_ge_u32 %[cnt], 6\n"
    "s_cbranch_scc1 .Lnodma%=\n"
    "s_mov_b32 m0, %[dslot]\n"
    "s_nop 0\n"
    "global_load_lds_dwordx4 %[goff], %[gbase] offset:0\n"
    "global_load_lds_dwordx4 %[goff], %[gbase] offset:1024\n"
    ".Lnodma%=:\n"
    "v_cvt_pk_bf16_f32 v92, v32, v33\n"
    "v_exp_f32 v36, v36\n"
    "v_exp_f32 v37, v37\n"
    "v_cvt_pk_bf16_f32 v93, v34, v35\n"
    "v_exp_f32 v38, v38\n"
    "v_exp_f32 v39, v39\n"
    "v_cvt_pk_bf16_f32 v94, v36, v37\n"
    "v_cvt_pk_bf16_f32 v95, v38, v39\n"
    "v_exp_f32 v40, v40\n"
    "v_exp_f32 v41, v41\n"
    "v_mfma_f32_32x32x16_bf16 v[56:71], v[112:115], v[92:95], v[56:71]\n"
    "v_exp_f32 v42, v42\n"
    "v_exp_f32 v43, v43\n"
    "v_cvt_pk_bf16_f32 v88, v40, v41\n"
    "v_exp_f32 v44, v44\n"
    "s_waitcnt lgkmcnt(0)\n"
    "v_mfma_f32_32x32x16_bf16 v[24:39], v[100:103], v[96:99], v[72:87]\n"
    "v_exp_f32 v45, v45\n"
    "v_cvt_pk_bf16_f32 v89, v42, v43\n"
    "ds_read_b128 v[104:107], v126 offset:1024\n"
    "v_exp_f32 v46, v46\n"
    "ds_read_b128 v[108:111], v127 offset:0\n"
    "v_exp_f32 v47, v47\n"
    "v_cvt_pk_bf16_f32 v90, v44, v45\n"
    "ds_read_b128 v[112:115], v127 offset:512\n"
    "v_exp_f32 v48, v48\n"
    "v_exp_f32 v49, v49\n"
    "v_cvt_pk_bf16_f32 v91, v46, v47\n"
    "v_exp_f32 v50, v50\n"
    "v_exp_f32 v51, v51\n"
    "v_mfma_f32_32x32x16_bf16 v[56:71], v[116:119], v[88:91], v[56:71]\n"
    "v_cvt_pk_bf16_f32 v92, v48, v49\n"
    "v_exp_f32 v52, v52\n"
    "v_exp_f32 v53, v53\n"
    "v_cvt_pk_bf16_f32 v93, v50, v51\n"
    "v_exp_f32 v54, v54\n"
    "v_exp_f32 v55, v55\n"
    "v_cvt_pk_bf16_f32 v94, v52, v53\n"
    "v_cvt_pk_bf16_f32 v95, v54, v55\n"
    "v_mov_b32 v124, v126\n"
    "v_mov_b32 v125, v127\n"
    "s_add_u32 %[nslot], %[nslot], 0x4000\n"
    "s_cmp_eq_u32 %[nslot], 0xc000\n"
    "s_cselect_b32 %[nslot], 0, %[nslot]\n"
    "v_add_u32 v126, %[nslot], %[kaddr]\n"
    "v_and_b32 v127, %[nslot], %[vsel]\n"
    "v_add_u32 v127, v127, %[vaddr]\n"
    "s_add_u32 %[dslot], %[dslot], 0x4000\n"
    "s_cmp_eq_u32 %[dslot], %[dend]\n"
    "s_cselect_b32 %[dslot], %[dbeg], %[dslot]\n"
    "v_add_u32 %[goff], 0x4000, %[goff]\n"
    "s_add_u32 %[cnt], %[cnt], 1\n"
    "s_cmp_lt_u32 %[cnt], 8\n"
    "s_cbranch_scc1 .Lloop%=\n"
    "s_nop 1\n"
    "v_mfma_f32_32x32x16_bf16 v[56:71], v[120:123], v[92:95], v[56:71]\n"
    "s_nop 15\n"
    "s_nop 3\n"
    "v_rcp_f32 v24, v64\n"
    "s_nop 0\n"
    "v_mul_f32 v56, v56, v24\n"
    "v_mul_f32 v57, v57, v24\n"
    "v_mul_f32 v58, v58, v24\n"
    "v_mul_f32 v59, v59, v24\n"
    "v_mul_f32 v60, v60, v24\n"
    "v_mul_f32 v61, v61, v24\n"
    "v_mul_f32 v62, v62, v24\n"
    "v_mul_f32 v63, v63, v24\n"
    "v_cvt_pk_bf16_f32 %[oa0], v56, v57\n"
    "v_cvt_pk_bf16_f32 %[oa1], v58, v59\n"
    "v_cvt_pk_bf16_f32 %[oa2], v60, v61\n"
    "v_cvt_pk_bf16_f32 %[oa3], v62, v63\n"
    "s_waitcnt lgkmcnt(0)\n"
    "s_mov_b32 m0, %[m0save]\n"
        : [oa0] "=&v"(oa0), [oa1] "=&v"(oa1), [oa2] "=&v"(oa2), [oa3] "=&v"(oa3),
          [goff] "+v"(goff), [dslot] "+s"(dslot), [nslot] "+s"(nslot), [cnt] "=&s"(cnt), [m0save] "=&s"(m0save)
        : [qa0] "v"(qa[0]), [qa1] "v"(qa[1]), [qa2] "v"(qa[2]), [qa3] "v"(qa[3]),
          [nma] "v"(nma), [kaddr] "v"(kaddr), [vaddr] "v"(vaddr), [vsel] "v"(vsel), [gbase] "s"(kvb), [dbeg] "s"(dbeg), [dend] "s"(dend), [psel] "s"(0x07060302u), [dbgaddr] "s"(lds0 + A_LDS + wid * 64)
        : "memory", "scc", "vcc",
          "v24", "v25", "v26", "v27", "v28", "v29", "v30", "v31", "v32", "v33", "v34", "v35", "v36", "v37", "v38", "v39", "v40", "v41", "v42", "v43", "v44", "v45", "v46", "v47", "v48", "v49", "v50", "v51", "v52", "v53", "v54", "v55", "v56", "v57", "v58", "v59", "v60", "v61", "v62", "v63", "v64", "v65", "v66", "v67", "v68", "v69", "v70", "v71", "v72", "v73", "v74", "v75", "v76", "v77", "v78", "v79", "v80", "v81", "v82", "v83", "v84", "v85", "v86", "v87", "v88", "v89", "v90", "v91", "v92", "v93", "v94", "v95", "v96", "v97", "v98", "v99", "v100", "v101", "v102", "v103", "v104", "v105", "v106", "v107", "v108", "v109", "v110", "v111", "v112", "v113", "v114", "v115", "v116", "v117", "v118", "v119", "v120", "v121", "v122", "v123", "v124", "v125", "v126", "v127"
    );
    STAMP(4);
    st16_wt(ws + WS_OF + ((size_t)tile0 * 32 + head) * 1024 + lane * 16, (u32x4){oa0, oa1, oa2, oa3});
    PUBLISH(FL_O + jb * 32 + head);
}

__device__ __forceinline__ void z_role(const uint16_t* __restrict__ x, const uint16_t* __restrict__ win, char* __restrict__ ws, char* lds, int g, u64 tag) {
    const int tid = threadIdx.x, lane = tid & 63, wid = __builtin_amdgcn_readfirstlane(tid >> 6), wr = wid >> 2, wc = wid & 3, l31 = lane & 31, hh = lane >> 5;
    const int xcd = g & 7, i = g >> 3;
    const int rb = 4 * (xcd >> 1) + (i & 3), cbz = 7 * ((xcd & 1) ^ 1) + (i >> 2);
    f32x16 acc[2];
    proj_main<false>(x, win, NIN, rb, cbz * 128, 12 + cbz, ws, lds, ((i & 3) * 4 + (i >> 2) * 3) & 15, tag, acc);
    STAMP(1);
    const float* st = (const float*)(lds + P_ST);
    unsigned zp[2][8];
#pragma unroll
    for (int rt = 0; rt < 2; ++rt) { const int tr = wr * 64 + rt * 32; const float mu = st[tr + l31], rs = st[128 + tr + l31];
#pragma unroll
        for (int q = 0; q < 4; ++q) { const f32x4 c = *(const f32x4*)(st + 256 + wc * 32 + 8 * q + 4 * hh);
            zp[rt][2 * q] = cvtpk(gelu_t((acc[rt][4 * q] - mu * c[0]) * rs), gelu_t((acc[rt][4 * q + 1] - mu * c[1]) * rs));
            zp[rt][2 * q + 1] = cvtpk(gelu_t((acc[rt][4 * q + 2] - mu * c[2]) * rs), gelu_t((acc[rt][4 * q + 3] - mu * c[3]) * rs)); } }
    if (cbz < 7) {
#pragma unroll
        for (int rt = 0; rt < 2; ++rt) { const int tt = rb * 4 + wr * 2 + rt;
            char* dst = ws + WS_Z1 + ((size_t)tt * 28 + cbz * 4 + wc) * 2048 + lane * 32;
            st16_wt(dst, (u32x4){zp[rt][0], zp[rt][1], zp[rt][2], zp[rt][3]}); st16_wt(dst + 16, (u32x4){zp[rt][4], zp[rt][5], zp[rt][6], zp[rt][7]}); }
    } else {
        const int cj = cbz - 7;
        float* pst = (float*)lds;
        const unsigned ONES = 0x3f803f80u;
#pragma unroll
        for (int rt = 0; rt < 2; ++rt) { float s1 = 0.f, s2 = 0.f;
#pragma unroll
            for (int e = 0; e < 8; ++e) { s1 = dot2(zp[rt][e], ONES, s1); s2 = dot2(zp[rt][e], zp[rt][e], s2); }
            s1 = swap32_add(s1); s2 = swap32_add(s2);
            if (hh == 0) *(f32x2*)(pst + (wc * 128 + wr * 64 + rt * 32 + l31) * 2) = (f32x2){s1, s2}; }
        __syncthreads();
        char* gst = ws + WS_ST + (size_t)rb * 7 * 1024;
        if (tid < 128) { f32x2 s = *(const f32x2*)(pst + tid * 2);
#pragma unroll
            for (int j = 1; j < 4; ++j) s += *(const f32x2*)(pst + (j * 128 + tid) * 2);
            st8_wt(gst + (cj * 128 + tid) * 8, __builtin_bit_cast(u64, s)); }
        PUBLISH(FL_ST + rb * 7 + cj);
        if (wid == 0) wait_flags(flagp(ws, FL_ST + rb * 7 + lane), lane < 7, tag);
        __syncthreads();
        float* fin = (float*)(lds + 4096);
        if (tid < 128) { f32x2 s = {0.f, 0.f};
#pragma unroll
            for (int j = 0; j < 7; ++j) s += __builtin_bit_cast(f32x2, ld8_wt(gst + (j * 128 + tid) * 8));
            const float mu = s[0] * (1.0f / FH); const float var = fmaxf(s[1] * (1.0f / FH) - mu * mu, 0.f);
            fin[tid] = mu; fin[128 + tid] = __builtin_amdgcn_rsqf(var + LN_EPS); }
        __syncthreads();
        char* img = lds + 8192;
#pragma unroll
        for (int rt = 0; rt < 2; ++rt) { const int tok = wr * 64 + rt * 32 + l31; const float mu = fin[tok], rs = fin[128 + tok];
#pragma unroll
            for (int q = 0; q < 4; ++q) { const unsigned a = zp[rt][2 * q], c = zp[rt][2 * q + 1];
                const u32x2 o = {cvtpk((bflo(a) - mu) * rs, (bfhi(a) - mu) * rs), cvtpk((bflo(c) - mu) * rs, (bfhi(c) - mu) * rs)};
                const int f = wc * 32 + 8 * q + 4 * hh;
                *(u32x2*)(img + tok * 256 + (((f >> 3) ^ wsw(tok)) * 16) + (f & 4) * 2) = o; } }
        __syncthreads();
        { const int blk = (lane >> 4) & 1, q = (lane & 15) >> 2, p = lane & 3;
#pragma unroll
          for (int kq = 0; kq < 4; ++kq) { const int ks = wr * 4 + kq; s16x4 t2[2];
#pragma unroll
              for (int t = 0; t < 2; ++t) { const int kr = 16 * ks + 8 * hh + 4 * t + q; const int ch = 4 * wc + 2 * blk + (p >> 1);
                  t2[t] = trread((lds_cptr)img + kr * 256 + ((ch ^ wsw(kr)) * 16) + 8 * (p & 1)); }
              st16_wt(ws + WS_Z2N + ((size_t)(cj * 4 + wc) * 128 + rb * 8 + ks) * 1024 + lane * 16, __builtin_bit_cast(u32x4, cat(t2[0], t2[1]))); } }
    }
    PUBLISH(FL_Z + rb * 14 + cbz);
}

constexpr int S_SLOT = 24576, S_LDS = 3 * S_SLOT;
__device__ __forceinline__ void spatial_role(const uint16_t* __restrict__ wsp, const uint16_t* __restrict__ bsp, char* __restrict__ ws, char* lds, int g, u64 tag) {
    const int tid = threadIdx.x, lane = tid & 63, wid8 = __builtin_amdgcn_readfirstlane(tid >> 6), wid = wid8 & 3, l31 = lane & 31, hh = lane >> 5;
    const int i = g >> 3, tb = 4 * (g & 7) + (i & 3), fb = i >> 2;
    const unsigned lds0 = (unsigned)(uintptr_t)lds;
    if (wid8 == 0) {
        wait_flags(flagp(ws, lane < 16 ? FL_Z + lane * 14 + 7 + fb : FL_Z + (tb >> 1) * 14 + fb), lane < 17, tag); }
    __syncthreads();
    STAMP(3);
    if (wid8 >= 4) {
        const int r0 = 16 * wid + (lane >> 3), r1 = r0 + 8;
        const char* sb_ = (const char*)(wsp + (size_t)tb * 64 * SEQ);
        const unsigned vo0 = (r0 * SEQ + ((lane & 7) ^ ((r0 >> 1) & 7)) * 8) * 2, vo1 = (r1 * SEQ + ((lane & 7) ^ ((r1 >> 1) & 7)) * 8) * 2;
        const char* zs = ws + WS_Z2N + (size_t)(fb * 4 + wid) * 131072; const unsigned zvo = lane * 16;
        auto issue = [&](int it, int slot) { const unsigned so = lds0 + slot * S_SLOT;
            set_m0(so + wid * 2048); glds_s<0, false>(sb_ + it * 128, vo0);
            set_m0(so + wid * 2048 + 1024); glds_s<0, false>(sb_ + it * 128, vo1);
            set_m0(so + 8192 + wid * 4096);
            glds_s<0, true>(zs + it * 4096, zvo); glds_s<1024, true>(zs + it * 4096, zvo); glds_s<2048, true>(zs + it * 4096, zvo); glds_s<3072, true>(zs + it * 4096, zvo); };
        issue(0, 0); issue(1, 1); issue(2, 2);
        WAIT_BAR(12);
        int slot = 0;
#pragma unroll 1
        for (int it = 1; it < 31; ++it) { WAIT_BAR(6); if (it + 2 < 32) issue(it + 2, slot); slot = slot == 2 ? 0 : slot + 1; }
        WAIT_BAR(0);
        BAR();
    } else {
        unsigned boff[2][4];
#pragma unroll
        for (int rt = 0; rt < 2; ++rt)
#pragma unroll
            for (int ks = 0; ks < 4; ++ks) { const int r = rt * 32 + l31; boff[rt][ks] = (r * 128 + ((hh ^ ((r >> 1) & 7)) * 16)) ^ (ks * 32); }
        const unsigned aoff = 8192 + wid * 4096 + lane * 16;
        struct SF { bf16x8 a, b[2]; };
        auto rd = [&](SF& f, int slot, int ks) { lds_cptr sb = (lds_cptr)lds + slot * S_SLOT;
            f.a = ldsfrag(sb + aoff + ks * 1024); f.b[0] = ldsfrag(sb + boff[0][ks]); f.b[1] = ldsfrag(sb + boff[1][ks]); };
        f32x16 acc[2] = {f32x16{}, f32x16{}};
        auto mm = [&](const SF& f) { acc[0] = MFMA(f.a, f.b[0], acc[0]); acc[1] = MFMA(f.a, f.b[1], acc[1]); };
        SF f[4];
        BAR();
#pragma unroll
        for (int ks = 0; ks < 4; ++ks) rd(f[ks], 0, ks);
        auto step = [&](int nsl) {
            LGKM_BAR();
#pragma unroll
            for (int ks = 0; ks < 4; ++ks) { lds_cptr sb = (lds_cptr)lds + nsl * S_SLOT;
                acc[0] = MFMA(f[ks].a, f[ks].b[0], acc[0]); SBAR();
                f[ks].b[0] = ldsfrag(sb + boff[0][ks]); SBAR();
                PACE();
                acc[1] = MFMA(f[ks].a, f[ks].b[1], acc[1]); SBAR();
                f[ks].b[1] = ldsfrag(sb + boff[1][ks]); f[ks].a = ldsfrag(sb + aoff + ks * 1024); SBAR();
                PACE(); } };
#pragma unroll 1
        for (int io = 0; io < 10; ++io) { step(1); step(2); step(0); }
        step(1); step(2);
#pragma unroll
        for (int rt = 0; rt < 2; ++rt) { const int tt = tb * 2 + rt; const float bias = __uint_as_float((unsigned)bsp[tt * 32 + l31] << 16);
            const char* z1p = ws + WS_Z1 + ((size_t)tt * 28 + fb * 4 + wid) * 2048 + lane * 32;
            const u32x4 za = ld16_wt(z1p), zb = ld16_wt(z1p + 16);
            u32x4 ua, ub;
#pragma unroll
            for (int e = 0; e < 4; ++e) {
                ua[e] = cvtpk(bflo(za[e]) * (acc[rt][2 * e] + bias), bfhi(za[e]) * (acc[rt][2 * e + 1] + bias));
                ub[e] = cvtpk(bflo(zb[e]) * (acc[rt][8 + 2 * e] + bias), bfhi(zb[e]) * (acc[rt][8 + 2 * e + 1] + bias)); }
            char* ud = ws + WS_U + ((size_t)tt * 56 + (fb * 4 + wid) * 2) * 1024 + lane * 16;
            st16_wt(ud, ua); st16_wt(ud + 1024, ub); }
    }
    PUBLISH(FL_U + tb * 7 + fb);
}

constexpr int F_SLOT = 8192 + 8192, F_LDS = 4 * F_SLOT;
__device__ __forceinline__ void final_role(const uint16_t* __restrict__ x, const uint16_t* __restrict__ wo, const uint16_t* __restrict__ wout, char* __restrict__ ws, uint16_t* __restrict__ out, char* lds, int g, u64 tag) {
    const int tb = 4 * (g & 7) + ((g >> 3) & 3), db = g >> 5;
    const int tid = threadIdx.x, lane = tid & 63, wid8 = __builtin_amdgcn_readfirstlane(tid >> 6), wid = wid8 & 3, wr = wid >> 1, wc = wid & 1, l31 = lane & 31, hh = lane >> 5;
    const unsigned lds0 = (unsigned)(uintptr_t)lds;
    auto wswz = [](int r) { return ((r & 2) << 1) | ((r >> 2) & 3); };
    f32x16 as = f32x16{}, ag = f32x16{};
    const int k0 = 16 * wid + (lane >> 3), k1 = k0 + 8;
    const unsigned wo0 = (k0 * DM + db * 64 + ((lane & 7) ^ wswz(k0)) * 8) * 2, wo1 = (k1 * DM + db * 64 + ((lane & 7) ^ wswz(k1)) * 8) * 2;
    const int att = tb * 2 + (wid & 1), aks = 2 * (wid >> 1);
    unsigned woff[2];
    { const int blk = (lane >> 4) & 1, q = (lane & 15) >> 2, p = lane & 3; const int kr = 4 * hh + q; const int ch = 4 * wc + 2 * blk + (p >> 1);
      woff[0] = kr * 128 + ((ch ^ wswz(kr)) * 16) + 8 * (p & 1); woff[1] = (woff[0] + 1024) ^ 32; }
    const unsigned aoff = 8192 + wr * 4096 + lane * 16;
    struct FF { bf16x8 w, a; };
    auto rd = [&](FF& f, int slot, int ks) { lds_cptr sb = (lds_cptr)lds + slot * F_SLOT;
        f.w = cat(trread(sb + woff[0] + ks * 2048), trread(sb + woff[1] + ks * 2048)); f.a = ldsfrag(sb + aoff + ks * 1024); };
    auto pass = [&](const uint16_t* wb, const char* ab, int nfr, int n, f32x16& acc, bool pace) {
        if (wid8 >= 4) {
            auto issue = [&](int it) { const unsigned so = lds0 + (it & 3) * F_SLOT; const char* w = (const char*)(wb + (size_t)it * 64 * DM);
                set_m0(so + wid * 2048); glds_s<0, false>(w, wo0);
                set_m0(so + wid * 2048 + 1024); glds_s<0, false>(w, wo1);
                const char* a = ab + ((size_t)att * nfr + it * 4 + aks) * 1024;
                set_m0(so + 8192 + (wid & 1) * 4096 + aks * 1024); glds_s<0, true>(a, lane * 16); glds_s<1024, true>(a, lane * 16); };
            issue(0); issue(1); issue(2); issue(3);
            WAIT_BAR(12);
#pragma unroll 1
            for (int i = 1; i < n - 2; ++i) { WAIT_BAR(8); if (i + 3 < n) issue(i + 3); }
            WAIT_BAR(4);
            WAIT_BAR(0);
            BAR();
        } else {
            FF fa[4], fb[4];
            BAR();
#pragma unroll
            for (int ks = 0; ks < 4; ++ks) rd(fa[ks], 0, ks);
            auto step = [&](int nsl, FF (&cur)[4], FF (&nxt)[4]) {
                LGKM_BAR();
#pragma unroll
                for (int ks = 0; ks < 4; ++ks) { acc = MFMA(cur[ks].w, cur[ks].a, acc); SBAR(); rd(nxt[ks], nsl, ks); SBAR(); if (pace) PACE(); } };
#pragma unroll 1
            for (int it = 0; it < n; it += 2) { step((it + 1) & 3, fa, fb); step((it + 2) & 3, fb, fa); }
        } };
    if (wid8 == 0) wait_flags(flagp(ws, FL_U + tb * 7 + lane), lane < 7, tag);
    __syncthreads();
    STAMP(5);
    pass(wout, ws + WS_U, 56, 14, ag, true);
    STAMP(6);
    if (wid8 == 0) wait_flags(flagp(ws, FL_O + (tb >> 2) * 32 + lane), lane < 32, tag);
    __syncthreads();
    STAMP(7);
    pass(wo, ws + WS_OF, 32, 8, as, false);
    STAMP(8);
    if (wid8 < 4) {
        const int tok = tb * 64 + wr * 32 + l31;
#pragma unroll
        for (int q = 0; q < 4; ++q) { const size_t o = (size_t)tok * DM + db * 64 + wc * 32 + 8 * q + 4 * hh;
            const u32x2 xv = *(const u32x2*)(x + o);
            const float r0 = bflo(xv[0]) + as[4 * q] * silu_f(ag[4 * q]), r1 = bfhi(xv[0]) + as[4 * q + 1] * silu_f(ag[4 * q + 1]);
            const float r2 = bflo(xv[1]) + as[4 * q + 2] * silu_f(ag[4 * q + 2]), r3 = bfhi(xv[1]) + as[4 * q + 3] * silu_f(ag[4 * q + 3]);
            *(u32x2*)(out + o) = (u32x2){cvtpk(r0, r1), cvtpk(r2, r3)}; }
    }
}

#define GDELAY 0
#define GPRIO 2
constexpr int ALL_LDS = S_LDS;
__global__ __launch_bounds__(512, 4) void k_all(const uint16_t* __restrict__ x, const uint16_t* __restrict__ wqkv, const uint16_t* __restrict__ wo, const uint16_t* __restrict__ win,
                                                const uint16_t* __restrict__ wsp, const uint16_t* __restrict__ bsp, const uint16_t* __restrict__ wout, char* __restrict__ ws, uint16_t* __restrict__ out) {
    extern __shared__ __attribute__((aligned(16))) char lds[];
    const int b = blockIdx.x;
    const u64 tag = *(const __attribute__((address_space(4))) u64*)(ws + WS_FLAG + FL_EPOCH * 128) + 1;
    STAMP(0);
    if (b < 256) {
        __builtin_amdgcn_s_setprio(3);
        if (b < 192) qkv_role(x, wqkv, ws, lds, b, tag); else stats_role(x, wqkv, win, ws, lds, b - 192, tag);
        __builtin_amdgcn_s_setprio(0);
        __syncthreads();
        attn_role(ws, lds, b, tag);
        STAMP(5);
    } else {
        const int g = b - 256;
        __builtin_amdgcn_s_setprio(GPRIO);
            if (threadIdx.x == 0) __hip_atomic_store(flagp(ws, FL_G + g), tag, RLX_AGENT);
        { const u64 t0 = __builtin_amdgcn_s_memrealtime(); while (__builtin_amdgcn_s_memrealtime() - t0 < GDELAY) __builtin_amdgcn_s_sleep(8); }
        if (g < 224) { z_role(x, win, ws, lds, g, tag); STAMP(2); __syncthreads(); spatial_role(wsp, bsp, ws, lds, g, tag); STAMP(4); __syncthreads(); }
        final_role(x, wo, wout, ws, out, lds, g, tag);
        if (g == 255 && threadIdx.x < 64) {
            const int lane = threadIdx.x;
#pragma unroll 1
            for (int c = 0; c < 4; ++c) { wait_flags(flagp(ws, FL_O + c * 64 + lane), true, tag); wait_flags(flagp(ws, FL_G + c * 64 + lane), true, tag); }
            if (lane == 0) __hip_atomic_store(flagp(ws, FL_EPOCH), tag, RLX_AGENT);
        }
        STAMP(9);
    }
}

extern "C" void kernel_launch(void* const* d_in, const int* in_sizes, int n_in, void* d_out, int out_size, void* d_ws, size_t ws_size, hipStream_t stream) {
    const uint16_t* x = (const uint16_t*)d_in[0]; const uint16_t* wqkv = (const uint16_t*)d_in[1]; const uint16_t* wo = (const uint16_t*)d_in[2];
    const uint16_t* win = (const uint16_t*)d_in[4]; const uint16_t* wsp = (const uint16_t*)d_in[5]; const uint16_t* bsp = (const uint16_t*)d_in[6]; const uint16_t* wout = (const uint16_t*)d_in[7];
    (void)hipFuncSetAttribute((const void*)k_all, hipFuncAttributeMaxDynamicSharedMemorySize, ALL_LDS);
    k_all<<<512, 512, ALL_LDS, stream>>>(x, wqkv, wo, win, wsp, bsp, wout, (char*)d_ws, (uint16_t*)d_out);
}
```

```cpp
#include <hip/hip_runtime.h>
#include <hip/hip_bf16.h>
#include <stdint.h>
#define ADELAY 300
#include <stdio.h>

typedef short bf16x8 __attribute__((ext_vector_type(8)));
typedef short s16x4 __attribute__((ext_vector_type(4)));
typedef __bf16 bf16x2_t __attribute__((ext_vector_type(2)));
typedef float f32x16 __attribute__((ext_vector_type(16)));
typedef float f32x4 __attribute__((ext_vector_type(4)));
typedef float f32x2 __attribute__((ext_vector_type(2)));
typedef unsigned u32x4 __attribute__((ext_vector_type(4)));
typedef unsigned u32x2 __attribute__((ext_vector_type(2)));
typedef unsigned long long u64;
#define GAS __attribute__((address_space(1)))
#define LAS __attribute__((address_space(3)))
typedef LAS const char* lds_cptr;
typedef short v4i16_t __attribute__((ext_vector_type(4)));
#define RLX_AGENT __ATOMIC_RELAXED, __HIP_MEMORY_SCOPE_AGENT

constexpr int SEQ = 2048, DM = 512, NQKV = 1536, NIN = 1792, FH = 896;
constexpr float QSCALE = 0.25f * 1.4426950408889634f;
constexpr float LN_EPS = 1e-5f;
constexpr size_t MB = 1024 * 1024;
constexpr size_t WS_QF = 0, WS_KV = 2 * MB, WS_Z1 = 6 * MB, WS_Z2N = 10 * MB, WS_U = 14 * MB, WS_OF = 18 * MB;
constexpr size_t WS_KM = 20 * MB, WS_ST = 20 * MB + 65536, WS_RS = 20 * MB + 65536 + 229376, WS_CS = 20 * MB + 65536 + 245760  , WS_FLAG = 21 * MB;
constexpr int FL_RS = 0, FL_CS = 64, FL_QKV = 128, FL_ST = 320, FL_Z = 432, FL_O = 656, FL_U = 912, FL_G = 1136, FL_EPOCH = 1392;

#define MFMA(a, b, c) __builtin_amdgcn_mfma_f32_32x32x16_bf16(a, b, c, 0, 0, 0)
#define SBAR() __builtin_amdgcn_sched_barrier(0)
__device__ __forceinline__ unsigned cvtpk(float lo, float hi) { unsigned r; asm("v_cvt_pk_bf16_f32 %0, %1, %2" : "=v"(r) : "v"(lo), "v"(hi)); return r; }
__device__ __forceinline__ float bflo(unsigned u) { return __uint_as_float(u << 16); }
__device__ __forceinline__ float bfhi(unsigned u) { return __uint_as_float(u & 0xffff0000u); }
__device__ __forceinline__ float dot2(unsigned a, unsigned b, float acc) {
    return __builtin_amdgcn_fdot2_f32_bf16(__builtin_bit_cast(bf16x2_t, a), __builtin_bit_cast(bf16x2_t, b), acc, false); }
__device__ __forceinline__ void glds16(const void* g, unsigned lds_base) {
    asm volatile("s_mov_b32 m0, %1\n\ts_nop 0\n\tglobal_load_lds_dwordx4 %0, off" :: "v"(g), "s"(lds_base) : "memory", "m0"); }
template <int IMM, bool WT> __device__ __forceinline__ void glds_s(const void* sbase, unsigned voff) {
    if (WT) asm volatile("global_load_lds_dwordx4 %0, %1 offset:%2 sc1" :: "v"(voff), "s"(sbase), "n"(IMM) : "memory");
    else asm volatile("global_load_lds_dwordx4 %0, %1 offset:%2" :: "v"(voff), "s"(sbase), "n"(IMM) : "memory"); }
__device__ __forceinline__ void set_m0(unsigned v) { asm volatile("s_mov_b32 m0, %0\n\ts_nop 0" :: "s"(v) : "memory", "m0"); }
__device__ __forceinline__ s16x4 trread(lds_cptr p) { return __builtin_bit_cast(s16x4, __builtin_amdgcn_ds_read_tr16_b64_v4i16((LAS v4i16_t*)p)); }
__device__ __forceinline__ bf16x8 ldsfrag(lds_cptr p) { return *(LAS const bf16x8*)p; }
__device__ __forceinline__ bf16x8 cat(s16x4 a, s16x4 b) { return (bf16x8){a[0], a[1], a[2], a[3], b[0], b[1], b[2], b[3]}; }
__device__ __forceinline__ float swap32_add(float v) { auto rr = __builtin_amdgcn_permlane32_swap(__float_as_uint(v), __float_as_uint(v), false, false); return __uint_as_float(rr[0]) + __uint_as_float(rr[1]); }
__device__ __forceinline__ float swap32_max(float v) { auto rr = __builtin_amdgcn_permlane32_swap(__float_as_uint(v), __float_as_uint(v), false, false); return fmaxf(__uint_as_float(rr[0]), __uint_as_float(rr[1])); }
template <int CTRL> __device__ __forceinline__ float dppf(float v) { return __int_as_float(__builtin_amdgcn_update_dpp(0, __float_as_int(v), CTRL, 0xf, 0xf, false)); }
__device__ __forceinline__ float row16_add(float v) { v += dppf<0x128>(v); v += dppf<0x124>(v); v += dppf<0x122>(v); v += dppf<0x121>(v); return v; }
__device__ __forceinline__ float row16_max(float v) { v = fmaxf(v, dppf<0x128>(v)); v = fmaxf(v, dppf<0x124>(v)); v = fmaxf(v, dppf<0x122>(v)); v = fmaxf(v, dppf<0x121>(v)); return v; }
__device__ __forceinline__ float swap16_max(float v) { auto rr = __builtin_amdgcn_permlane16_swap(__float_as_uint(v), __float_as_uint(v), false, false); return fmaxf(__uint_as_float(rr[0]), __uint_as_float(rr[1])); }
__device__ __forceinline__ float gelu_t(float x) {
    const float c1 = -2.3022082f, c2 = -0.10294324f;
    float e = __builtin_amdgcn_exp2f(x * __builtin_fmaf(x * x, c2, c1));
    return x * __builtin_amdgcn_rcpf(1.0f + e); }
__device__ __forceinline__ float silu_f(float x) { return x * __builtin_amdgcn_rcpf(1.0f + __builtin_amdgcn_exp2f(-1.4426950408889634f * x)); }
#define STAMP(slot) do {} while (0)
#define PF_DIST 9
#define PACE_N 2
#define PACE() do { asm volatile("s_nop %0" :: "n"(PACE_N)); SBAR(); } while (0)
#define POLL_SLEEP 2
#define WAITV(n) asm volatile("s_waitcnt vmcnt(" #n ")" ::: "memory")
#define WAIT_BAR(n) asm volatile("s_waitcnt vmcnt(" #n ")\n\ts_barrier" ::: "memory")
#define BAR() asm volatile("s_barrier" ::: "memory")
#define LGKM_BAR() do { __builtin_amdgcn_s_waitcnt(0xC07F); asm volatile("" ::: "memory"); __builtin_amdgcn_s_barrier(); asm volatile("" ::: "memory"); } while (0)
__device__ __forceinline__ unsigned wsw(unsigned row) { return ((row & 3) << 2) | ((row >> 2) & 3); }
__device__ __forceinline__ void st16_wt(void* p, u32x4 v) {
    asm volatile("global_store_dwordx4 %0, %1, off sc1\n\ts_nop 1" :: "v"(p), "v"(v)); }
__device__ __forceinline__ void st8_wt(void* p, u64 v) { __hip_atomic_store((GAS u64*)p, v, RLX_AGENT); }
__device__ __forceinline__ u64 ld8_wt(const void* p) { return __hip_atomic_load((GAS u64*)p, RLX_AGENT); }
__device__ __forceinline__ u32x4 ld16_wt(const void* p) { u32x4 v; asm volatile("global_load_dwordx4 %0, %1, off sc1\n\ts_waitcnt vmcnt(0)" : "=&v"(v) : "v"(p) : "memory"); return v; }
__device__ __forceinline__ void glds16_wt(const void* g, unsigned lds_base) {
    asm volatile("s_mov_b32 m0, %1\n\ts_nop 0\n\tglobal_load_lds_dwordx4 %0, off sc1" :: "v"(g), "s"(lds_base) : "memory", "m0"); }
__device__ __forceinline__ GAS u64* flagp(char* ws, int idx) { return (GAS u64*)(ws + WS_FLAG) + idx * 16; }
__device__ __forceinline__ void wait_flags(GAS u64* f, bool want, u64 tag) {
    for (unsigned spins = 0; spins < (1u << 20); ++spins) {
        const u64 v = want ? __hip_atomic_load(f, RLX_AGENT) : tag;
        if (__all(v == tag)) return;
        __builtin_amdgcn_s_sleep(POLL_SLEEP); } }
#define ACQUIRE() do { __builtin_amdgcn_fence(__ATOMIC_ACQUIRE, "agent"); asm volatile("s_waitcnt vmcnt(0)" ::: "memory"); } while (0)
#define EPOCH_REQ(ep) asm volatile("global_load_dwordx2 %0, %1, off sc1" : "=v"(ep) : "v"(flagp(ws, FL_EPOCH)) : "memory")
#define EPOCH_TAG(ep) ((((u64)(unsigned)__builtin_amdgcn_readfirstlane((unsigned)((ep) >> 32)) << 32) | (unsigned)__builtin_amdgcn_readfirstlane((unsigned)(ep))) + 1)
#define PUBLISH(idx) do { WAITV(0); __syncthreads(); if (threadIdx.x == 0) __hip_atomic_store(flagp(ws, idx), tag, RLX_AGENT); } while (0)

__device__ __forceinline__ void stats_role(const uint16_t* __restrict__ x, const uint16_t* __restrict__ wqkv, const uint16_t* __restrict__ win, char* __restrict__ ws, char* lds, int j, u64& tag) {
    u64 ep; EPOCH_REQ(ep);
    const int tid = threadIdx.x, lane = tid & 63, wid = tid >> 6, rr = lane >> 4, cc = lane & 15;
    const unsigned ONES = 0x3f803f80u;
    u32x4 xv[4], w[8];
    const int row = 32 * j + 4 * wid + rr;
#pragma unroll
    for (int i = 0; i < 4; ++i) xv[i] = *(const u32x4*)(x + (size_t)row * DM + i * 128 + cc * 8);
    const int cbk = j >> 1;
    if (j < 52) {
        const uint16_t* W = cbk < 12 ? wqkv + cbk * 128 : win + (cbk - 12) * 128; const int ldw = cbk < 12 ? NQKV : NIN;
        const uint16_t* p = W + (size_t)((j & 1) * 256 + 32 * wid + rr) * ldw + cc * 8;
#pragma unroll
        for (int i = 0; i < 8; ++i) w[i] = *(const u32x4*)(p + (size_t)(i * 4) * ldw);
    }
    asm volatile("s_waitcnt vmcnt(0)" : "+v"(ep) :: "memory");
    tag = EPOCH_TAG(ep);
    float s1 = 0.f, s2 = 0.f;
#pragma unroll
    for (int i = 0; i < 4; ++i)
#pragma unroll
        for (int e = 0; e < 4; ++e) { s1 = dot2(xv[i][e], ONES, s1); s2 = dot2(xv[i][e], xv[i][e], s2); }
    s1 = row16_add(s1); s2 = row16_add(s2);
    { const float mu = s1 * (1.0f / DM), var = fmaxf(s2 * (1.0f / DM) - mu * mu, 0.f);
      if (cc == 0) st8_wt(ws + WS_RS + (size_t)row * 8, __builtin_bit_cast(u64, (f32x2){mu, __builtin_amdgcn_rsqf(var + LN_EPS)})); }
    if (j < 52) {
        float cs[8] = {0.f, 0.f, 0.f, 0.f, 0.f, 0.f, 0.f, 0.f};
        unsigned sel_lo = 0x00003f80u, sel_hi = 0x3f800000u;
        asm volatile("" : "+v"(sel_lo), "+v"(sel_hi));
#pragma unroll
        for (int i = 0; i < 8; ++i)
#pragma unroll
            for (int e = 0; e < 4; ++e) { cs[2 * e] = dot2(w[i][e], sel_lo, cs[2 * e]); cs[2 * e + 1] = dot2(w[i][e], sel_hi, cs[2 * e + 1]); }
        float* red = (float*)lds;
        *(f32x4*)(red + (wid * 4 + rr) * 128 + cc * 8) = (f32x4){cs[0], cs[1], cs[2], cs[3]}; *(f32x4*)(red + (wid * 4 + rr) * 128 + cc * 8 + 4) = (f32x4){cs[4], cs[5], cs[6], cs[7]};
        __syncthreads();
        if (tid < 64) { f32x2 a = {0.f, 0.f};
#pragma unroll
            for (int q = 0; q < 32; ++q) a += *(const f32x2*)(red + q * 128 + 2 * tid);
            st8_wt(ws + WS_CS + (size_t)((j & 1) * 3328 + cbk * 128 + 2 * tid) * 4, __builtin_bit_cast(u64, a)); }
    }
    STAMP(11);
    WAITV(0); __syncthreads();
    STAMP(1);
    if (tid == 0) { __hip_atomic_store(flagp(ws, FL_RS + j), tag, RLX_AGENT); if (j < 52) __hip_atomic_store(flagp(ws, FL_CS + j), tag, RLX_AGENT); }
}

constexpr int P_SLOT = 16384, P_ST = 4 * P_SLOT, P_LDS = 4 * P_SLOT + 4096;
struct PFrag { bf16x8 x[2], w; };
template <bool SWAP>
__device__ __forceinline__ void proj_main(const uint16_t* __restrict__ x, const uint16_t* __restrict__ W, int ldw, int rb, int col0, int cblk, char* __restrict__ ws, char* lds, int rot, u64& tag, f32x16 (&acc)[2]) {
    u64 ep; EPOCH_REQ(ep);
    const int tid = threadIdx.x, lane = tid & 63, wid = __builtin_amdgcn_readfirstlane(tid >> 6), wr = wid >> 2, wc = wid & 3, l31 = lane & 31, hh = lane >> 5;
    const unsigned lds0 = (unsigned)(uintptr_t)lds;
    const int xr = 16 * wid + (lane >> 2), wk = 4 * wid + (lane >> 4);
    const char* xb = (const char*)(x + (size_t)rb * 128 * DM); const char* wb = (const char*)(W + col0);
    const unsigned xvo = (xr * DM + ((lane & 3) ^ ((xr >> 2) & 3)) * 8) * 2, wvo = (wk * ldw + ((lane & 15) ^ wsw(wk)) * 8) * 2;
    const unsigned xd = lds0 + wid * 1024, wd = lds0 + 8192 + wid * 1024;
    auto issue1 = [&](int it, int which) { const unsigned so = (it & 3) * P_SLOT; const int kk = (it + rot) & 15;
        if (which == 0) { set_m0(xd + so); glds_s<0, false>(xb + kk * 64, xvo); } else { set_m0(wd + so); glds_s<0, false>(wb + (size_t)kk * 64 * ldw, wvo); } SBAR(); };
#pragma unroll
    for (int it = 0; it < 4; ++it) { issue1(it, 0); issue1(it, 1); }
    unsigned xoff[2][2], woff[2][2];
    const int blk = (lane >> 4) & 1, q = (lane & 15) >> 2, p = lane & 3;
#pragma unroll
    for (int ks = 0; ks < 2; ++ks) {
#pragma unroll
        for (int rt = 0; rt < 2; ++rt) { const int r = wr * 64 + rt * 32 + l31; xoff[rt][ks] = r * 64 + (((2 * ks + hh) ^ ((r >> 2) & 3)) * 16); }
        const int kr = 8 * hh + q, ch = 4 * wc + 2 * blk + (p >> 1);
        woff[0][ks] = 8192 + ks * 4096 + kr * 256 + ((ch ^ wsw(kr)) * 16) + 8 * (p & 1); woff[1][ks] = (woff[0][ks] + 1024) ^ 16; }
    auto rd = [&](PFrag& f, int slot, int j) { lds_cptr sb = (lds_cptr)lds + slot * P_SLOT;
        f.x[0] = ldsfrag(sb + xoff[0][j]); f.x[1] = ldsfrag(sb + xoff[1][j]); f.w = cat(trread(sb + woff[0][j]), trread(sb + woff[1][j])); };
    auto mm = [&](const PFrag& f) {
#pragma unroll
        for (int rt = 0; rt < 2; ++rt) acc[rt] = SWAP ? MFMA(f.x[rt], f.w, acc[rt]) : MFMA(f.w, f.x[rt], acc[rt]); };
    acc[0] = f32x16{}; acc[1] = f32x16{};
    PFrag fa[2], fb[2];
    asm volatile("s_waitcnt vmcnt(6)\n\ts_barrier" : "+v"(ep) :: "memory");
    tag = EPOCH_TAG(ep);
    STAMP(10);
    rd(fa[0], 0, 0); rd(fa[1], 0, 1);
    auto mm1 = [&](const PFrag& f, int rt) { acc[rt] = SWAP ? MFMA(f.x[rt], f.w, acc[rt]) : MFMA(f.w, f.x[rt], acc[rt]); SBAR(); };
    auto step = [&](int it, int sl, PFrag (&cur)[2], PFrag (&nxt)[2]) {
        __builtin_amdgcn_s_waitcnt(0xC07F);
        if (it < 13) WAITV(4); else if (it == 13) WAITV(2); else WAITV(0);
        __builtin_amdgcn_s_barrier(); asm volatile("" ::: "memory");
        lds_cptr sb = (lds_cptr)lds + ((sl + 1) & 3) * P_SLOT;
        mm1(cur[0], 0);
        nxt[0].x[0] = ldsfrag(sb + xoff[0][0]); nxt[0].x[1] = ldsfrag(sb + xoff[1][0]); SBAR();
        mm1(cur[0], 1);
        nxt[0].w = cat(trread(sb + woff[0][0]), trread(sb + woff[1][0])); SBAR();
        if (it + 4 < 16) issue1(it + 4, 0);
        mm1(cur[1], 0);
        nxt[1].x[0] = ldsfrag(sb + xoff[0][1]); nxt[1].x[1] = ldsfrag(sb + xoff[1][1]); SBAR();
        mm1(cur[1], 1);
        nxt[1].w = cat(trread(sb + woff[0][1]), trread(sb + woff[1][1])); SBAR();
        if (it + 4 < 16) issue1(it + 4, 1); };
#pragma unroll 1
    for (int io = 0; io < 2; ++io) { step(io * 4, 0, fa, fb); step(io * 4 + 1, 1, fb, fa); step(io * 4 + 2, 2, fa, fb); step(io * 4 + 3, 3, fb, fa); }
    const char* fa_ = (const char*)(lane < 4 ? flagp(ws, FL_RS + rb * 4 + lane) : flagp(ws, FL_CS + 2 * cblk + (lane & 1)));
    const char* sa_ = tid < 128 ? ws + WS_RS + (size_t)(rb * 128 + tid) * 8 : ws + WS_CS + (size_t)(cblk * 128 + 2 * (tid & 63)) * 4;
    const char* sb_ = ws + WS_CS + (size_t)(3328 + cblk * 128 + 2 * (tid & 63)) * 4;
    u64 fl, stv, stw;
    asm volatile("global_load_dwordx2 %0, %1, off sc1" : "=v"(fl) : "v"(fa_) : "memory");
    step(8, 0, fa, fb); step(9, 1, fb, fa); step(10, 2, fa, fb); step(11, 3, fb, fa);
    asm volatile("" : "+v"(fl));
    const bool got = __all(lane >= 6 || fl == tag);
    asm volatile("global_load_dwordx2 %0, %2, off sc1\n\tglobal_load_dwordx2 %1, %3, off sc1" : "=&v"(stv), "=&v"(stw) : "v"(sa_), "v"(sb_) : "memory");
    step(12, 0, fa, fb); step(13, 1, fb, fa); step(14, 2, fa, fb); step(15, 3, fb, fa);
    asm volatile("" : "+v"(stv), "+v"(stw));
    if (!got) { wait_flags((GAS u64*)fa_, lane < 6, tag); stv = ld8_wt(sa_); stw = ld8_wt(sb_); }
    STAMP(11);
    float* st = (float*)(lds + P_ST);
    { const f32x2 v = __builtin_bit_cast(f32x2, stv);
      if (tid < 128) { st[tid] = v[0]; st[128 + tid] = v[1]; } else if (tid < 192) *(f32x2*)(st + 256 + 2 * (tid - 128)) = v + __builtin_bit_cast(f32x2, stw); }
    __syncthreads();
}

__device__ __forceinline__ void qkv_role(const uint16_t* __restrict__ x, const uint16_t* __restrict__ wqkv, char* __restrict__ ws, char* lds, int b, u64& tag) {
    const int xcd = b & 7, i = b >> 3;
    const int rb = 4 * (xcd >> 1) + (i & 3), cb = 6 * (xcd & 1) + (i >> 2);
    const int tid = threadIdx.x, lane = tid & 63, wid = __builtin_amdgcn_readfirstlane(tid >> 6), wr = wid >> 2, wc = wid & 3, l31 = lane & 31, hh = lane >> 5;
    f32x16 acc[2];
    const int rot = ((i & 3) * 4 + (i >> 2) * 3) & 15;
    if (cb >= 8) proj_main<true>(x, wqkv, NQKV, rb, cb * 128, cb, ws, lds, rot, tag, acc); else proj_main<false>(x, wqkv, NQKV, rb, cb * 128, cb, ws, lds, rot, tag, acc);
    STAMP(1);
    const float* st = (const float*)(lds + P_ST);
    if (cb >= 8) {
        const float cs = st[256 + wc * 32 + l31];
        const int head = (cb - 8) * 8 + wc * 2 + (l31 >> 4);
#pragma unroll
        for (int rt = 0; rt < 2; ++rt) { const int tr = wr * 64 + rt * 32;
            unsigned pk[8];
#pragma unroll
            for (int q = 0; q < 4; ++q) { const f32x4 m = *(const f32x4*)(st + tr + 8 * q + 4 * hh), r = *(const f32x4*)(st + 128 + tr + 8 * q + 4 * hh);
                pk[2 * q] = cvtpk((acc[rt][4 * q] - m[0] * cs) * r[0], (acc[rt][4 * q + 1] - m[1] * cs) * r[1]);
                pk[2 * q + 1] = cvtpk((acc[rt][4 * q + 2] - m[2] * cs) * r[2], (acc[rt][4 * q + 3] - m[3] * cs) * r[3]); }
            const int tile = rb * 4 + (tr >> 5);
            char* dst = ws + WS_KV + (size_t)head * 131072 + (tile >> 3) * 16384 + 8192 + (tile & 7) * 1024 + hh * 256 + (l31 & 15) * 16;
            st16_wt(dst, (u32x4){pk[0], pk[1], pk[2], pk[3]}); st16_wt(dst + 512, (u32x4){pk[4], pk[5], pk[6], pk[7]}); }
    } else {
        const bool isK = cb >= 4; const float sc = isK ? 1.0f : QSCALE;
        float kmx[2] = {0.f, 0.f};
#pragma unroll
        for (int rt = 0; rt < 2; ++rt) { const int tr = wr * 64 + rt * 32, tile = rb * 4 + (tr >> 5);
            const float mu = st[tr + l31], rs = st[128 + tr + l31] * sc;
#pragma unroll
            for (int hb = 0; hb < 2; ++hb) {
                float v[8];
#pragma unroll
                for (int q = 0; q < 2; ++q) { const f32x4 c = *(const f32x4*)(st + 256 + wc * 32 + 16 * hb + 8 * q + 4 * hh);
#pragma unroll
                    for (int e = 0; e < 4; ++e) v[4 * q + e] = (acc[rt][8 * hb + 4 * q + e] - mu * c[e]) * rs; }
                u32x4 pk;
#pragma unroll
                for (int e = 0; e < 4; ++e) pk[e] = cvtpk(v[2 * e], v[2 * e + 1]);
                const int head = (cb & 3) * 8 + wc * 2 + hb;
                char* dst = isK ? ws + WS_KV + (size_t)head * 131072 + (tile >> 3) * 16384 + (tile & 7) * 1024 + lane * 16 : ws + WS_QF + (size_t)head * 65536 + tile * 1024 + lane * 16;
                st16_wt(dst, pk);
                float n2 = 0.f;
#pragma unroll
                for (int e = 0; e < 4; ++e) n2 = dot2(pk[e], pk[e], n2);
                kmx[hb] = fmaxf(kmx[hb], swap32_add(n2));
            } }
        if (isK) {
#pragma unroll
            for (int hb = 0; hb < 2; ++hb) { const float m = swap16_max(row16_max(kmx[hb]));
                if (lane == 0) __hip_atomic_store((GAS unsigned*)(ws + WS_KM) + ((cb & 3) * 8 + wc * 2 + hb) * 32 + rb * 2 + wr, __float_as_uint(m), RLX_AGENT); } }
    }
    STAMP(12);
    PUBLISH(FL_QKV + cb * 16 + rb);
    STAMP(13);
}

constexpr int A_ONES = 3 * 16384, A_LDS = A_ONES + 8192 + 512;
__device__ __forceinline__ void attn_role(char* __restrict__ ws, char* lds, int b, u64 tag) {
    const int tid = threadIdx.x, lane = tid & 63, wid = __builtin_amdgcn_readfirstlane(tid >> 6);
    const int head = (b & 7) * 4 + ((b >> 3) & 3), jb = b >> 5;
    const unsigned lds0 = (unsigned)(uintptr_t)lds;
    const char* kvb = ws + WS_KV + (size_t)head * 131072;
    for (int i = tid; i < (8192 + 512) / 16; i += 512) { const unsigned o1 = ((i & 11) == 0) ? 0x3f803f80u : 0u; *(u32x4*)(lds + A_ONES + i * 16) = (u32x4){o1, o1, o1, o1}; }
    if (wid == 0) {
        const int hb = head >> 3;
        const int idx = lane < 16 ? (4 + hb) * 16 + lane : lane < 32 ? (8 + hb) * 16 + (lane - 16) : hb * 16 + 2 * jb + (lane & 1);
        wait_flags(flagp(ws, FL_QKV + idx), lane < 34, tag);
        STAMP(14);
        }
    __syncthreads();
    STAMP(2);
    unsigned goff = wid * 2048 + lane * 16;
    const int tile0 = jb * 8 + wid;
    u32x4 qa; unsigned kmu;
    asm volatile("global_load_dwordx4 %0, %2, off sc1\n\tglobal_load_dword %1, %3, off sc1" : "=&v"(qa), "=&v"(kmu)
                 : "v"(ws + WS_QF + (size_t)head * 65536 + tile0 * 1024 + lane * 16), "v"(ws + WS_KM + (size_t)(head * 32 + (lane & 31)) * 4) : "memory");
#pragma unroll
    for (int c = 0; c < 2; ++c) { set_m0(lds0 + c * 16384 + wid * 2048); glds_s<0, true>(kvb + c * 16384, goff); glds_s<1024, true>(kvb + c * 16384, goff); }
    asm volatile("s_waitcnt vmcnt(4)" : "+v"(qa), "+v"(kmu) :: "memory");
    const float km = swap16_max(row16_max(__uint_as_float(kmu)));
    float na = 0.f;
#pragma unroll
    for (int e = 0; e < 4; ++e) na = dot2(qa[e], qa[e], na);
    na = swap32_add(na);
    float ma = __builtin_amdgcn_sqrtf(na * km);
    if (__any(!(ma <= 40.0f))) {
        float xa = -3.0e38f;
        for (int t = 0; t < 64; ++t) {
            const bf16x8 kf = __builtin_bit_cast(bf16x8, ld16_wt(kvb + (t >> 3) * 16384 + (t & 7) * 1024 + lane * 16));
            const f32x16 sa = MFMA(kf, __builtin_bit_cast(bf16x8, qa), f32x16{});
#pragma unroll
            for (int r = 0; r < 16; ++r) xa = fmaxf(xa, sa[r]); }
        ma = swap32_max(xa);
    }
    const float nma = -ma;
    const unsigned kaddr = lds0 + lane * 16;
    const bool ones = (lane & 16) != 0;
    const unsigned vaddr = ones ? lds0 + A_ONES + (lane & 15) * 16 : lds0 + 8192 + (lane >> 5) * 256 + (lane & 15) * 16;
    const unsigned vsel = ones ? 0u : 0xffffffffu;
    goff += 2 * 16384;
    unsigned dbeg = lds0 + wid * 2048, dend = dbeg + 3 * 16384, dslot = dbeg + 2 * 16384, nslot = 0x4000, cnt, m0save;
    unsigned oa0, oa1, oa2, oa3;
    STAMP(3);
    asm volatile(
    "s_mov_b32 %[m0save], m0\n"
    "v_mov_b32 v96, %[qa0]\n"
    "v_mov_b32 v97, %[qa1]\n"
    "v_mov_b32 v98, %[qa2]\n"
    "v_mov_b32 v99, %[qa3]\n"
    "v_mov_b32 v72, %[nma]\n"
    "v_mov_b32 v56, 0\n"
    "v_mov_b32 v73, %[nma]\n"
    "v_mov_b32 v57, 0\n"
    "v_mov_b32 v74, %[nma]\n"
    "v_mov_b32 v58, 0\n"
    "v_mov_b32 v75, %[nma]\n"
    "v_mov_b32 v59, 0\n"
    "v_mov_b32 v76, %[nma]\n"
    "v_mov_b32 v60, 0\n"
    "v_mov_b32 v77, %[nma]\n"
    "v_mov_b32 v61, 0\n"
    "v_mov_b32 v78, %[nma]\n"
    "v_mov_b32 v62, 0\n"
    "v_mov_b32 v79, %[nma]\n"
    "v_mov_b32 v63, 0\n"
    "v_mov_b32 v80, %[nma]\n"
    "v_mov_b32 v64, 0\n"
    "v_mov_b32 v81, %[nma]\n"
    "v_mov_b32 v65, 0\n"
    "v_mov_b32 v82, %[nma]\n"
    "v_mov_b32 v66, 0\n"
    "v_mov_b32 v83, %[nma]\n"
    "v_mov_b32 v67, 0\n"
    "v_mov_b32 v84, %[nma]\n"
    "v_mov_b32 v68, 0\n"
    "v_mov_b32 v85, %[nma]\n"
    "v_mov_b32 v69, 0\n"
    "v_mov_b32 v86, %[nma]\n"
    "v_mov_b32 v70, 0\n"
    "v_mov_b32 v87, %[nma]\n"
    "v_mov_b32 v71, 0\n"
    "v_mov_b32 v120, 0\n"
    "v_mov_b32 v121, 0\n"
    "v_mov_b32 v122, 0\n"
    "v_mov_b32 v123, 0\n"
    "v_mov_b32 v88, 0\n"
    "v_mov_b32 v89, 0\n"
    "v_mov_b32 v90, 0\n"
    "v_mov_b32 v91, 0\n"
    "v_mov_b32 v92, 0\n"
    "v_mov_b32 v93, 0\n"
    "v_mov_b32 v94, 0\n"
    "v_mov_b32 v95, 0\n"
    "v_mov_b32 v124, %[kaddr]\n"
    "v_mov_b32 v125, %[vaddr]\n"
    "v_add_u32 v126, 0x4000, v124\n"
    "v_and_b32 v127, 0x4000, %[vsel]\n"
    "v_add_u32 v127, v127, v125\n"
    "s_waitcnt vmcnt(2) lgkmcnt(0)\n"
    "s_barrier\n"
    "ds_read_b128 v[100:103], v124\n"
    "ds_read_b128 v[104:107], v124 offset:1024\n"
    "ds_read_b128 v[108:111], v125\n"
    "ds_read_b128 v[112:115], v125 offset:512\n"
    "s_mov_b32 %[cnt], 0\n"
    "s_waitcnt lgkmcnt(0)\n"
    "v_mfma_f32_32x32x16_bf16 v[24:39], v[100:103], v[96:99], v[72:87]\n"
    "s_nop 15\n"
    ".Lloop%=:\n"
    "v_exp_f32 v24, v24\n"
    "v_exp_f32 v25, v25\n"
    "v_mfma_f32_32x32x16_bf16 v[56:71], v[120:123], v[92:95], v[56:71]\n"
    "v_exp_f32 v26, v26\n"
    "v_exp_f32 v27, v27\n"
    "v_cvt_pk_bf16_f32 v88, v24, v25\n"
    "v_exp_f32 v28, v28\n"
    "s_waitcnt lgkmcnt(0)\n"
    "v_mfma_f32_32x32x16_bf16 v[40:55], v[104:107], v[96:99], v[72:87]\n"
    "v_exp_f32 v29, v29\n"
    "v_cvt_pk_bf16_f32 v89, v26, v27\n"
    "ds_read_b128 v[100:103], v124 offset:2048\n"
    "v_exp_f32 v30, v30\n"
    "ds_read_b128 v[116:119], v125 offset:1024\n"
    "v_exp_f32 v31, v31\n"
    "v_cvt_pk_bf16_f32 v90, v28, v29\n"
    "ds_read_b128 v[120:123], v125 offset:1536\n"
    "v_exp_f32 v32, v32\n"
    "v_exp_f32 v33, v33\n"
    "v_cvt_pk_bf16_f32 v91, v30, v31\n"
    "v_exp_f32 v34, v34\n"
    "v_exp_f32 v35, v35\n"
    "v_mfma_f32_32x32x16_bf16 v[56:71], v[108:111], v[88:91], v[56:71]\n"
    "v_cvt_pk_bf16_f32 v92, v32, v33\n"
    "v_exp_f32 v36, v36\n"
    "v_exp_f32 v37, v37\n"
    "v_cvt_pk_bf16_f32 v93, v34, v35\n"
    "v_exp_f32 v38, v38\n"
    "v_exp_f32 v39, v39\n"
    "v_cvt_pk_bf16_f32 v94, v36, v37\n"
    "v_cvt_pk_bf16_f32 v95, v38, v39\n"
    "v_exp_f32 v40, v40\n"
    "v_exp_f32 v41, v41\n"
    "v_mfma_f32_32x32x16_bf16 v[56:71], v[112:115], v[92:95], v[56:71]\n"
    "v_exp_f32 v42, v42\n"
    "v_exp_f32 v43, v43\n"
    "v_cvt_pk_bf16_f32 v88, v40, v41\n"
    "v_exp_f32 v44, v44\n"
    "s_waitcnt lgkmcnt(0)\n"
    "v_mfma_f32_32x32x16_bf16 v[24:39], v[100:103], v[96:99], v[72:87]\n"
    "v_exp_f32 v45, v45\n"
    "v_cvt_pk_bf16_f32 v89, v42, v43\n"
    "ds_read_b128 v[104:107], v124 offset:3072\n"
    "v_exp_f32 v46, v46\n"
    "ds_read_b128 v[108:111], v125 offset:2048\n"
    "v_exp_f32 v47, v47\n"
    "v_cvt_pk_bf16_f32 v90, v44, v45\n"
    "ds_read_b128 v[112:115], v125 offset:2560\n"
    "v_exp_f32 v48, v48\n"
    "v_exp_f32 v49, v49\n"
    "v_cvt_pk_bf16_f32 v91, v46, v47\n"
    "v_exp_f32 v50, v50\n"
    "v_exp_f32 v51, v51\n"
    "v_mfma_f32_32x32x16_bf16 v[56:71], v[116:119], v[88:91], v[56:71]\n"
    "v_cvt_pk_bf16_f32 v92, v48, v49\n"
    "v_exp_f32 v52, v52\n"
    "v_exp_f32 v53, v53\n"
    "v_cvt_pk_bf16_f32 v93, v50, v51\n"
    "v_exp_f32 v54, v54\n"
    "v_exp_f32 v55, v55\n"
    "v_cvt_pk_bf16_f32 v94, v52, v53\n"
    "v_cvt_pk_bf16_f32 v95, v54, v55\n"
    "v_exp_f32 v24, v24\n"
    "v_exp_f32 v25, v25\n"
    "v_mfma_f32_32x32x16_bf16 v[56:71], v[120:123], v[92:95], v[56:71]\n"
    "v_exp_f32 v26, v26\n"
    "v_exp_f32 v27, v27\n"
    "v_cvt_pk_bf16_f32 v88, v24, v25\n"
    "v_exp_f32 v28, v28\n"
    "s_waitcnt lgkmcnt(0)\n"
    "v_mfma_f32_32x32x16_bf16 v[40:55], v[104:107], v[96:99], v[72:87]\n"
    "v_exp_f32 v29, v29\n"
    "v_cvt_pk_bf16_f32 v89, v26, v27\n"
    "ds_read_b128 v[100:103], v124 offset:4096\n"
    "v_exp_f32 v30, v30\n"
    "ds_read_b128 v[116:119], v125 offset:3072\n"
    "v_exp_f32 v31, v31\n"
    "v_cvt_pk_bf16_f32 v90, v28, v29\n"
    "ds_read_b128 v[120:123], v125 offset:3584\n"
    "v_exp_f32 v32, v32\n"
    "v_exp_f32 v33, v33\n"
    "v_cvt_pk_bf16_f32 v91, v30, v31\n"
    "v_exp_f32 v34, v34\n"
    "v_exp_f32 v35, v35\n"
    "v_mfma_f32_32x32x16_bf16 v[56:71], v[108:111], v[88:91], v[56:71]\n"
    "v_cvt_pk_bf16_f32 v92, v32, v33\n"
    "v_exp_f32 v36, v36\n"
    "v_exp_f32 v37, v37\n"
    "v_cvt_pk_bf16_f32 v93, v34, v35\n"
    "v_exp_f32 v38, v38\n"
    "v_exp_f32 v39, v39\n"
    "v_cvt_pk_bf16_f32 v94, v36, v37\n"
    "v_cvt_pk_bf16_f32 v95, v38, v39\n"
    "v_exp_f32 v40, v40\n"
    "v_exp_f32 v41, v41\n"
    "v_mfma_f32_32x32x16_bf16 v[56:71], v[112:115], v[92:95], v[56:71]\n"
    "v_exp_f32 v42, v42\n"
    "v_exp_f32 v43, v43\n"
    "v_cvt_pk_bf16_f32 v88, v40, v41\n"
    "v_exp_f32 v44, v44\n"
    "s_waitcnt lgkmcnt(0)\n"
    "v_mfma_f32_32x32x16_bf16 v[24:39], v[100:103], v[96:99], v[72:87]\n"
    "v_exp_f32 v45, v45\n"
    "v_cvt_pk_bf16_f32 v89, v42, v43\n"
    "ds_read_b128 v[104:107], v124 offset:5120\n"
    "v_exp_f32 v46, v46\n"
    "ds_read_b128 v[108:111], v125 offset:4096\n"
    "v_exp_f32 v47, v47\n"
    "v_cvt_pk_bf16_f32 v90, v44, v45\n"
    "ds_read_b128 v[112:115], v125 offset:4608\n"
    "v_exp_f32 v48, v48\n"
    "v_exp_f32 v49, v49\n"
    "v_cvt_pk_bf16_f32 v91, v46, v47\n"
    "v_exp_f32 v50, v50\n"
    "v_exp_f32 v51, v51\n"
    "v_mfma_f32_32x32x16_bf16 v[56:71], v[116:119], v[88:91], v[56:71]\n"
    "v_cvt_pk_bf16_f32 v92, v48, v49\n"
    "v_exp_f32 v52, v52\n"
    "v_exp_f32 v53, v53\n"
    "v_cvt_pk_bf16_f32 v93, v50, v51\n"
    "v_exp_f32 v54, v54\n"
    "v_exp_f32 v55, v55\n"
    "v_cvt_pk_bf16_f32 v94, v52, v53\n"
    "v_cvt_pk_bf16_f32 v95, v54, v55\n"
    "v_exp_f32 v24, v24\n"
    "v_exp_f32 v25, v25\n"
    "v_mfma_f32_32x32x16_bf16 v[56:71], v[120:123], v[92:95], v[56:71]\n"
    "v_exp_f32 v26, v26\n"
    "v_exp_f32 v27, v27\n"
    "v_cvt_pk_bf16_f32 v88, v24, v25\n"
    "v_exp_f32 v28, v28\n"
    "s_waitcnt lgkmcnt(0)\n"
    "v_mfma_f32_32x32x16_bf16 v[40:55], v[104:107], v[96:99], v[72:87]\n"
    "v_exp_f32 v29, v29\n"
    "v_cvt_pk_bf16_f32 v89, v26, v27\n"
    "ds_read_b128 v[100:103], v124 offset:6144\n"
    "v_exp_f32 v30, v30\n"
    "ds_read_b128 v[116:119], v125 offset:5120\n"
    "v_exp_f32 v31, v31\n"
    "v_cvt_pk_bf16_f32 v90, v28, v29\n"
    "ds_read_b128 v[120:123], v125 offset:5632\n"
    "v_exp_f32 v32, v32\n"
    "v_exp_f32 v33, v33\n"
    "v_cvt_pk_bf16_f32 v91, v30, v31\n"
    "v_exp_f32 v34, v34\n"
    "v_exp_f32 v35, v35\n"
    "v_mfma_f32_32x32x16_bf16 v[56:71], v[108:111], v[88:91], v[56:71]\n"
    "v_cvt_pk_bf16_f32 v92, v32, v33\n"
    "v_exp_f32 v36, v36\n"
    "v_exp_f32 v37, v37\n"
    "v_cvt_pk_bf16_f32 v93, v34, v35\n"
    "v_exp_f32 v38, v38\n"
    "v_exp_f32 v39, v39\n"
    "v_cvt_pk_bf16_f32 v94, v36, v37\n"
    "v_cvt_pk_bf16_f32 v95, v38, v39\n"
    "v_exp_f32 v40, v40\n"
    "v_exp_f32 v41, v41\n"
    "v_mfma_f32_32x32x16_bf16 v[56:71], v[112:115], v[92:95], v[56:71]\n"
    "v_exp_f32 v42, v42\n"
    "v_exp_f32 v43, v43\n"
    "v_cvt_pk_bf16_f32 v88, v40, v41\n"
    "v_exp_f32 v44, v44\n"
    "s_waitcnt lgkmcnt(0)\n"
    "v_mfma_f32_32x32x16_bf16 v[24:39], v[100:103], v[96:99], v[72:87]\n"
    "v_exp_f32 v45, v45\n"
    "v_cvt_pk_bf16_f32 v89, v42, v43\n"
    "ds_read_b128 v[104:107], v124 offset:7168\n"
    "v_exp_f32 v46, v46\n"
    "ds_read_b128 v[108:111], v125 offset:6144\n"
    "v_exp_f32 v47, v47\n"
    "v_cvt_pk_bf16_f32 v90, v44, v45\n"
    "ds_read_b128 v[112:115], v125 offset:6656\n"
    "v_exp_f32 v48, v48\n"
    "v_exp_f32 v49, v49\n"
    "v_cvt_pk_bf16_f32 v91, v46, v47\n"
    "v_exp_f32 v50, v50\n"
    "v_exp_f32 v51, v51\n"
    "v_mfma_f32_32x32x16_bf16 v[56:71], v[116:119], v[88:91], v[56:71]\n"
    "v_cvt_pk_bf16_f32 v92, v48, v49\n"
    "v_exp_f32 v52, v52\n"
    "v_exp_f32 v53, v53\n"
    "v_cvt_pk_bf16_f32 v93, v50, v51\n"
    "v_exp_f32 v54, v54\n"
    "v_exp_f32 v55, v55\n"
    "v_cvt_pk_bf16_f32 v94, v52, v53\n"
    "v_cvt_pk_bf16_f32 v95, v54, v55\n"
    "v_exp_f32 v24, v24\n"
    "v_exp_f32 v25, v25\n"
    "v_mfma_f32_32x32x16_bf16 v[56:71], v[120:123], v[92:95], v[56:71]\n"
    "v_exp_f32 v26, v26\n"
    "v_exp_f32 v27, v27\n"
    "v_cvt_pk_bf16_f32 v88, v24, v25\n"
    "v_exp_f32 v28, v28\n"
    "s_waitcnt lgkmcnt(0)\n"
    "v_mfma_f32_32x32x16_bf16 v[40:55], v[104:107], v[96:99], v[72:87]\n"
    "v_exp_f32 v29, v29\n"
    "v_cvt_pk_bf16_f32 v89, v26, v27\n"
    "s_waitcnt vmcnt(0)\n"
    "s_barrier\n"
    "ds_read_b128 v[100:103], v126 offset:0\n"
    "v_exp_f32 v30, v30\n"
    "ds_read_b128 v[116:119], v125 offset:7168\n"
    "v_exp_f32 v31, v31\n"
    "v_cvt_pk_bf16_f32 v90, v28, v29\n"
    "ds_read_b128 v[120:123], v125 offset:7680\n"
    "v_exp_f32 v32, v32\n"
    "v_exp_f32 v33, v33\n"
    "v_cvt_pk_bf16_f32 v91, v30, v31\n"
    "v_exp_f32 v34, v34\n"
    "v_exp_f32 v35, v35\n"
    "v_mfma_f32_32x32x16_bf16 v[56:71], v[108:111], v[88:91], v[56:71]\n"
    "s_cmp_ge_u32 %[cnt], 6\n"
    "s_cbranch_scc1 .Lnodma%=\n"
    "s_mov_b32 m0, %[dslot]\n"
    "s_nop 0\n"
    "global_load_lds_dwordx4 %[goff], %[gbase] offset:0\n"
    "global_load_lds_dwordx4 %[goff], %[gbase] offset:1024\n"
    ".Lnodma%=:\n"
    "v_cvt_pk_bf16_f32 v92, v32, v33\n"
    "v_exp_f32 v36, v36\n"
    "v_exp_f32 v37, v37\n"
    "v_cvt_pk_bf16_f32 v93, v34, v35\n"
    "v_exp_f32 v38, v38\n"
    "v_exp_f32 v39, v39\n"
    "v_cvt_pk_bf16_f32 v94, v36, v37\n"
    "v_cvt_pk_bf16_f32 v95, v38, v39\n"
    "v_exp_f32 v40, v40\n"
    "v_exp_f32 v41, v41\n"
    "v_mfma_f32_32x32x16_bf16 v[56:71], v[112:115], v[92:95], v[56:71]\n"
    "v_exp_f32 v42, v42\n"
    "v_exp_f32 v43, v43\n"
    "v_cvt_pk_bf16_f32 v88, v40, v41\n"
    "v_exp_f32 v44, v44\n"
    "s_waitcnt lgkmcnt(0)\n"
    "v_mfma_f32_32x32x16_bf16 v[24:39], v[100:103], v[96:99], v[72:87]\n"
    "v_exp_f32 v45, v45\n"
    "v_cvt_pk_bf16_f32 v89, v42, v43\n"
    "ds_read_b128 v[104:107], v126 offset:1024\n"
    "v_exp_f32 v46, v46\n"
    "ds_read_b128 v[108:111], v127 offset:0\n"
    "v_exp_f32 v47, v47\n"
    "v_cvt_pk_bf16_f32 v90, v44, v45\n"
    "ds_read_b128 v[112:115], v127 offset:512\n"
    "v_exp_f32 v48, v48\n"
    "v_exp_f32 v49, v49\n"
    "v_cvt_pk_bf16_f32 v91, v46, v47\n"
    "v_exp_f32 v50, v50\n"
    "v_exp_f32 v51, v51\n"
    "v_mfma_f32_32x32x16_bf16 v[56:71], v[116:119], v[88:91], v[56:71]\n"
    "v_cvt_pk_bf16_f32 v92, v48, v49\n"
    "v_exp_f32 v52, v52\n"
    "v_exp_f32 v53, v53\n"
    "v_cvt_pk_bf16_f32 v93, v50, v51\n"
    "v_exp_f32 v54, v54\n"
    "v_exp_f32 v55, v55\n"
    "v_cvt_pk_bf16_f32 v94, v52, v53\n"
    "v_cvt_pk_bf16_f32 v95, v54, v55\n"
    "v_mov_b32 v124, v126\n"
    "v_mov_b32 v125, v127\n"
    "s_add_u32 %[nslot], %[nslot], 0x4000\n"
    "s_cmp_eq_u32 %[nslot], 0xc000\n"
    "s_cselect_b32 %[nslot], 0, %[nslot]\n"
    "v_add_u32 v126, %[nslot], %[kaddr]\n"
    "v_and_b32 v127, %[nslot], %[vsel]\n"
    "v_add_u32 v127, v127, %[vaddr]\n"
    "s_add_u32 %[dslot], %[dslot], 0x4000\n"
    "s_cmp_eq_u32 %[dslot], %[dend]\n"
    "s_cselect_b32 %[dslot], %[dbeg], %[dslot]\n"
    "v_add_u32 %[goff], 0x4000, %[goff]\n"
    "s_add_u32 %[cnt], %[cnt], 1\n"
    "s_cmp_lt_u32 %[cnt], 8\n"
    "s_cbranch_scc1 .Lloop%=\n"
    "s_nop 1\n"
    "v_mfma_f32_32x32x16_bf16 v[56:71], v[120:123], v[92:95], v[56:71]\n"
    "s_nop 15\n"
    "s_nop 3\n"
    "v_rcp_f32 v24, v64\n"
    "s_nop 0\n"
    "v_mul_f32 v56, v56, v24\n"
    "v_mul_f32 v57, v57, v24\n"
    "v_mul_f32 v58, v58, v24\n"
    "v_mul_f32 v59, v59, v24\n"
    "v_mul_f32 v60, v60, v24\n"
    "v_mul_f32 v61, v61, v24\n"
    "v_mul_f32 v62, v62, v24\n"
    "v_mul_f32 v63, v63, v24\n"
    "v_cvt_pk_bf16_f32 %[oa0], v56, v57\n"
    "v_cvt_pk_bf16_f32 %[oa1], v58, v59\n"
    "v_cvt_pk_bf16_f32 %[oa2], v60, v61\n"
    "v_cvt_pk_bf16_f32 %[oa3], v62, v63\n"
    "s_waitcnt lgkmcnt(0)\n"
    "s_mov_b32 m0, %[m0save]\n"
        : [oa0] "=&v"(oa0), [oa1] "=&v"(oa1), [oa2] "=&v"(oa2), [oa3] "=&v"(oa3),
          [goff] "+v"(goff), [dslot] "+s"(dslot), [nslot] "+s"(nslot), [cnt] "=&s"(cnt), [m0save] "=&s"(m0save)
        : [qa0] "v"(qa[0]), [qa1] "v"(qa[1]), [qa2] "v"(qa[2]), [qa3] "v"(qa[3]),
          [nma] "v"(nma), [kaddr] "v"(kaddr), [vaddr] "v"(vaddr), [vsel] "v"(vsel), [gbase] "s"(kvb), [dbeg] "s"(dbeg), [dend] "s"(dend), [psel] "s"(0x07060302u), [vmask] "s"(0x0000ffff0000ffffull), [vone] "v"(((lane & 27) == 16) ? 0x3f803f80u : 0u), [dbgaddr] "s"(lds0 + A_LDS + wid * 64)
        : "memory", "scc", "vcc",
          "v24", "v25", "v26", "v27", "v28", "v29", "v30", "v31", "v32", "v33", "v34", "v35", "v36", "v37", "v38", "v39", "v40", "v41", "v42", "v43", "v44", "v45", "v46", "v47", "v48", "v49", "v50", "v51", "v52", "v53", "v54", "v55", "v56", "v57", "v58", "v59", "v60", "v61", "v62", "v63", "v64", "v65", "v66", "v67", "v68", "v69", "v70", "v71", "v72", "v73", "v74", "v75", "v76", "v77", "v78", "v79", "v80", "v81", "v82", "v83", "v84", "v85", "v86", "v87", "v88", "v89", "v90", "v91", "v92", "v93", "v94", "v95", "v96", "v97", "v98", "v99", "v100", "v101", "v102", "v103", "v104", "v105", "v106", "v107", "v108", "v109", "v110", "v111", "v112", "v113", "v114", "v115", "v116", "v117", "v118", "v119", "v120", "v121", "v122", "v123", "v124", "v125", "v126", "v127"
    );
    STAMP(4);
    st16_wt(ws + WS_OF + ((size_t)tile0 * 32 + head) * 1024 + lane * 16, (u32x4){oa0, oa1, oa2, oa3});
    PUBLISH(FL_O + jb * 32 + head);
}

__device__ __forceinline__ void z_role(const uint16_t* __restrict__ x, const uint16_t* __restrict__ win, char* __restrict__ ws, char* lds, int g, u64& tag) {
    const int tid = threadIdx.x, lane = tid & 63, wid = __builtin_amdgcn_readfirstlane(tid >> 6), wr = wid >> 2, wc = wid & 3, l31 = lane & 31, hh = lane >> 5;
    const int xcd = g & 7, i = g >> 3;
    const int rb = 4 * (xcd >> 1) + (i & 3), cbz = 7 * ((xcd & 1) ^ 1) + (i >> 2);
    f32x16 acc[2];
    proj_main<false>(x, win, NIN, rb, cbz * 128, 12 + cbz, ws, lds, ((i & 3) * 4 + (i >> 2) * 3) & 15, tag, acc);
    STAMP(1);
    const float* st = (const float*)(lds + P_ST);
    unsigned zp[2][8];
#pragma unroll
    for (int rt = 0; rt < 2; ++rt) { const int tr = wr * 64 + rt * 32; const float mu = st[tr + l31], rs = st[128 + tr + l31];
#pragma unroll
        for (int q = 0; q < 4; ++q) { const f32x4 c = *(const f32x4*)(st + 256 + wc * 32 + 8 * q + 4 * hh);
            zp[rt][2 * q] = cvtpk(gelu_t((acc[rt][4 * q] - mu * c[0]) * rs), gelu_t((acc[rt][4 * q + 1] - mu * c[1]) * rs));
            zp[rt][2 * q + 1] = cvtpk(gelu_t((acc[rt][4 * q + 2] - mu * c[2]) * rs), gelu_t((acc[rt][4 * q + 3] - mu * c[3]) * rs)); } }
    if (cbz < 7) {
#pragma unroll
        for (int rt = 0; rt < 2; ++rt) { const int tt = rb * 4 + wr * 2 + rt;
            char* dst = ws + WS_Z1 + ((size_t)tt * 28 + cbz * 4 + wc) * 2048 + lane * 32;
            st16_wt(dst, (u32x4){zp[rt][0], zp[rt][1], zp[rt][2], zp[rt][3]}); st16_wt(dst + 16, (u32x4){zp[rt][4], zp[rt][5], zp[rt][6], zp[rt][7]}); }
    } else {
        const int cj = cbz - 7;
        float* pst = (float*)lds;
        const unsigned ONES = 0x3f803f80u;
#pragma unroll
        for (int rt = 0; rt < 2; ++rt) { float s1 = 0.f, s2 = 0.f;
#pragma unroll
            for (int e = 0; e < 8; ++e) { s1 = dot2(zp[rt][e], ONES, s1); s2 = dot2(zp[rt][e], zp[rt][e], s2); }
            s1 = swap32_add(s1); s2 = swap32_add(s2);
            if (hh == 0) *(f32x2*)(pst + (wc * 128 + wr * 64 + rt * 32 + l31) * 2) = (f32x2){s1, s2}; }
        __syncthreads();
        char* gst = ws + WS_ST + (size_t)rb * 7 * 2048;
        float* fin = (float*)(lds + 4096);
        if (tid < 128) { f32x2 s = *(const f32x2*)(pst + tid * 2);
#pragma unroll
            for (int j = 1; j < 4; ++j) s += *(const f32x2*)(pst + (j * 128 + tid) * 2);
            st16_wt(gst + (cj * 128 + tid) * 16, (u32x4){__float_as_uint(s[0]), __float_as_uint(s[1]), (unsigned)tag, (unsigned)(tag >> 32)});
            u32x4 gr[7];
            for (unsigned spins = 0; spins < (1u << 18); ++spins) {
                asm volatile("global_load_dwordx4 %0, %7, off sc1\n\tglobal_load_dwordx4 %1, %7, off offset:2048 sc1\n\tglobal_load_dwordx4 %2, %8, off sc1\n\tglobal_load_dwordx4 %3, %8, off offset:2048 sc1\n\t"
                             "global_load_dwordx4 %4, %9, off sc1\n\tglobal_load_dwordx4 %5, %9, off offset:2048 sc1\n\tglobal_load_dwordx4 %6, %10, off sc1\n\ts_waitcnt vmcnt(0)"
                             : "=&v"(gr[0]), "=&v"(gr[1]), "=&v"(gr[2]), "=&v"(gr[3]), "=&v"(gr[4]), "=&v"(gr[5]), "=&v"(gr[6])
                             : "v"(gst + tid * 16), "v"(gst + 4096 + tid * 16), "v"(gst + 8192 + tid * 16), "v"(gst + 12288 + tid * 16) : "memory");
                bool ok = true;
#pragma unroll
                for (int j = 0; j < 7; ++j) ok &= gr[j][2] == (unsigned)tag && gr[j][3] == (unsigned)(tag >> 32);
                if (__all(ok)) break;
                __builtin_amdgcn_s_sleep(POLL_SLEEP); }
            float s1 = 0.f, s2 = 0.f;
#pragma unroll
            for (int j = 0; j < 7; ++j) { s1 += __uint_as_float(gr[j][0]); s2 += __uint_as_float(gr[j][1]); }
            const float mu = s1 * (1.0f / FH); const float var = fmaxf(s2 * (1.0f / FH) - mu * mu, 0.f);
            fin[tid] = mu; fin[128 + tid] = __builtin_amdgcn_rsqf(var + LN_EPS); }
        __syncthreads();
        char* img = lds + 8192;
#pragma unroll
        for (int rt = 0; rt < 2; ++rt) { const int tok = wr * 64 + rt * 32 + l31; const float mu = fin[tok], rs = fin[128 + tok];
#pragma unroll
            for (int q = 0; q < 4; ++q) { const unsigned a = zp[rt][2 * q], c = zp[rt][2 * q + 1];
                const u32x2 o = {cvtpk((bflo(a) - mu) * rs, (bfhi(a) - mu) * rs), cvtpk((bflo(c) - mu) * rs, (bfhi(c) - mu) * rs)};
                const int f = wc * 32 + 8 * q + 4 * hh;
                *(u32x2*)(img + tok * 256 + (((f >> 3) ^ wsw(tok)) * 16) + (f & 4) * 2) = o; } }
        __syncthreads();
        { const int blk = (lane >> 4) & 1, q = (lane & 15) >> 2, p = lane & 3;
#pragma unroll
          for (int kq = 0; kq < 4; ++kq) { const int ks = wr * 4 + kq; s16x4 t2[2];
#pragma unroll
              for (int t = 0; t < 2; ++t) { const int kr = 16 * ks + 8 * hh + 4 * t + q; const int ch = 4 * wc + 2 * blk + (p >> 1);
                  t2[t] = trread((lds_cptr)img + kr * 256 + ((ch ^ wsw(kr)) * 16) + 8 * (p & 1)); }
              st16_wt(ws + WS_Z2N + ((size_t)(cj * 4 + wc) * 128 + rb * 8 + ks) * 1024 + lane * 16, __builtin_bit_cast(u32x4, cat(t2[0], t2[1]))); } }
    }
    PUBLISH(FL_Z + rb * 14 + cbz);
}

constexpr int S_SLOT = 24576, S_LDS = 3 * S_SLOT;
__device__ __forceinline__ void spatial_role(const uint16_t* __restrict__ wsp, const uint16_t* __restrict__ bsp, char* __restrict__ ws, char* lds, int g, u64 tag) {
    const int tid = threadIdx.x, lane = tid & 63, wid8 = __builtin_amdgcn_readfirstlane(tid >> 6), wid = wid8 & 3, l31 = lane & 31, hh = lane >> 5;
    const int i = g >> 3, tb = 4 * (g & 7) + (i & 3), fb = i >> 2;
    const unsigned lds0 = (unsigned)(uintptr_t)lds;
    STAMP(3);
    if (wid8 >= 4) {
        const int r0 = 16 * wid + (lane >> 3), r1 = r0 + 8;
        const char* sb_ = (const char*)(wsp + (size_t)tb * 64 * SEQ);
        const unsigned vo0 = (r0 * SEQ + ((lane & 7) ^ ((r0 >> 1) & 7)) * 8) * 2, vo1 = (r1 * SEQ + ((lane & 7) ^ ((r1 >> 1) & 7)) * 8) * 2;
        const char* zs = ws + WS_Z2N + (size_t)(fb * 4 + wid) * 131072; const unsigned zvo = lane * 16;
        unsigned ready = 0;
        GAS u64* zf = flagp(ws, FL_Z + (lane & 15) * 14 + 7 + fb);
        auto need = [&](int rbk) {
            for (unsigned spins = 0; !((ready >> rbk) & 1) && spins < (1u << 18); ++spins) {
                ready = (unsigned)__ballot(__hip_atomic_load(zf, RLX_AGENT) == tag) & 0xffffu;
                if (!((ready >> rbk) & 1)) __builtin_amdgcn_s_sleep(POLL_SLEEP); } };
        auto issue = [&](int it, int slot) { const unsigned so = lds0 + slot * S_SLOT;
            need(it >> 1);
            set_m0(so + wid * 2048); glds_s<0, false>(sb_ + it * 128, vo0);
            set_m0(so + wid * 2048 + 1024); glds_s<0, false>(sb_ + it * 128, vo1);
            set_m0(so + 8192 + wid * 4096);
            glds_s<0, true>(zs + it * 4096, zvo); glds_s<1024, true>(zs + it * 4096, zvo); glds_s<2048, true>(zs + it * 4096, zvo); glds_s<3072, true>(zs + it * 4096, zvo); };
        issue(0, 0); issue(1, 1); issue(2, 2);
        WAIT_BAR(12);
        int slot = 0;
#pragma unroll 1
        for (int it = 1; it < 31; ++it) { WAIT_BAR(6); if (it + 2 < 32) issue(it + 2, slot); slot = slot == 2 ? 0 : slot + 1; }
        WAIT_BAR(0);
        BAR();
    } else {
        unsigned boff[2][4];
#pragma unroll
        for (int rt = 0; rt < 2; ++rt)
#pragma unroll
            for (int ks = 0; ks < 4; ++ks) { const int r = rt * 32 + l31; boff[rt][ks] = (r * 128 + ((hh ^ ((r >> 1) & 7)) * 16)) ^ (ks * 32); }
        const unsigned aoff = 8192 + wid * 4096 + lane * 16;
        struct SF { bf16x8 a, b[2]; };
        auto rd = [&](SF& f, int slot, int ks) { lds_cptr sb = (lds_cptr)lds + slot * S_SLOT;
            f.a = ldsfrag(sb + aoff + ks * 1024); f.b[0] = ldsfrag(sb + boff[0][ks]); f.b[1] = ldsfrag(sb + boff[1][ks]); };
        f32x16 acc[2] = {f32x16{}, f32x16{}};
        auto mm = [&](const SF& f) { acc[0] = MFMA(f.a, f.b[0], acc[0]); acc[1] = MFMA(f.a, f.b[1], acc[1]); };
        SF f[4];
        BAR();
#pragma unroll
        for (int ks = 0; ks < 4; ++ks) rd(f[ks], 0, ks);
        auto step = [&](int nsl) {
            LGKM_BAR();
#pragma unroll
            for (int ks = 0; ks < 4; ++ks) { lds_cptr sb = (lds_cptr)lds + nsl * S_SLOT;
                acc[0] = MFMA(f[ks].a, f[ks].b[0], acc[0]); SBAR();
                f[ks].b[0] = ldsfrag(sb + boff[0][ks]); SBAR();
                PACE();
                acc[1] = MFMA(f[ks].a, f[ks].b[1], acc[1]); SBAR();
                f[ks].b[1] = ldsfrag(sb + boff[1][ks]); f[ks].a = ldsfrag(sb + aoff + ks * 1024); SBAR();
                PACE(); } };
#pragma unroll 1
        for (int io = 0; io < 10; ++io) { step(1); step(2); step(0); }
        step(1); step(2);
        wait_flags(flagp(ws, FL_Z + (tb >> 1) * 14 + fb), lane == 0, tag);
#pragma unroll
        for (int rt = 0; rt < 2; ++rt) { const int tt = tb * 2 + rt; const float bias = __uint_as_float((unsigned)bsp[tt * 32 + l31] << 16);
            const char* z1p = ws + WS_Z1 + ((size_t)tt * 28 + fb * 4 + wid) * 2048 + lane * 32;
            const u32x4 za = ld16_wt(z1p), zb = ld16_wt(z1p + 16);
            u32x4 ua, ub;
#pragma unroll
            for (int e = 0; e < 4; ++e) {
                ua[e] = cvtpk(bflo(za[e]) * (acc[rt][2 * e] + bias), bfhi(za[e]) * (acc[rt][2 * e + 1] + bias));
                ub[e] = cvtpk(bflo(zb[e]) * (acc[rt][8 + 2 * e] + bias), bfhi(zb[e]) * (acc[rt][8 + 2 * e + 1] + bias)); }
            char* ud = ws + WS_U + ((size_t)tt * 56 + (fb * 4 + wid) * 2) * 1024 + lane * 16;
            st16_wt(ud, ua); st16_wt(ud + 1024, ub); }
    }
    PUBLISH(FL_U + tb * 7 + fb);
}

constexpr int F_SLOT = 8192 + 8192, F_LDS = 4 * F_SLOT;
__device__ __forceinline__ void final_role(const uint16_t* __restrict__ x, const uint16_t* __restrict__ wo, const uint16_t* __restrict__ wout, char* __restrict__ ws, uint16_t* __restrict__ out, char* lds, int g, u64 tag) {
    const int tb = 4 * (g & 7) + ((g >> 3) & 3), db = g >> 5;
    const int tid = threadIdx.x, lane = tid & 63, wid8 = __builtin_amdgcn_readfirstlane(tid >> 6), wid = wid8 & 3, wr = wid >> 1, wc = wid & 1, l31 = lane & 31, hh = lane >> 5;
    const unsigned lds0 = (unsigned)(uintptr_t)lds;
    auto wswz = [](int r) { return ((r & 2) << 1) | ((r >> 2) & 3); };
    f32x16 as = f32x16{}, ag = f32x16{};
    const int k0 = 16 * wid + (lane >> 3), k1 = k0 + 8;
    const unsigned wo0 = (k0 * DM + db * 64 + ((lane & 7) ^ wswz(k0)) * 8) * 2, wo1 = (k1 * DM + db * 64 + ((lane & 7) ^ wswz(k1)) * 8) * 2;
    const int att = tb * 2 + (wid & 1), aks = 2 * (wid >> 1);
    unsigned woff[2];
    { const int blk = (lane >> 4) & 1, q = (lane & 15) >> 2, p = lane & 3; const int kr = 4 * hh + q; const int ch = 4 * wc + 2 * blk + (p >> 1);
      woff[0] = kr * 128 + ((ch ^ wswz(kr)) * 16) + 8 * (p & 1); woff[1] = (woff[0] + 1024) ^ 32; }
    const unsigned aoff = 8192 + wr * 4096 + lane * 16;
    struct FF { bf16x8 w, a; };
    auto rd = [&](FF& f, int slot, int ks) { lds_cptr sb = (lds_cptr)lds + slot * F_SLOT;
        f.w = cat(trread(sb + woff[0] + ks * 2048), trread(sb + woff[1] + ks * 2048)); f.a = ldsfrag(sb + aoff + ks * 1024); };
    auto pass = [&](const uint16_t* wb, const char* ab, int nfr, int n, f32x16& acc, bool pace) {
        if (wid8 >= 4) {
            auto issue = [&](int it) { const unsigned so = lds0 + (it & 3) * F_SLOT; const char* w = (const char*)(wb + (size_t)it * 64 * DM);
                set_m0(so + wid * 2048); glds_s<0, false>(w, wo0);
                set_m0(so + wid * 2048 + 1024); glds_s<0, false>(w, wo1);
                const char* a = ab + ((size_t)att * nfr + it * 4 + aks) * 1024;
                set_m0(so + 8192 + (wid & 1) * 4096 + aks * 1024); glds_s<0, true>(a, lane * 16); glds_s<1024, true>(a, lane * 16); };
            issue(0); issue(1); issue(2); issue(3);
            WAIT_BAR(12);
#pragma unroll 1
            for (int i = 1; i < n - 2; ++i) { WAIT_BAR(8); if (i + 3 < n) issue(i + 3); }
            WAIT_BAR(4);
            WAIT_BAR(0);
            BAR();
        } else {
            FF fa[4], fb[4];
            BAR();
#pragma unroll
            for (int ks = 0; ks < 4; ++ks) rd(fa[ks], 0, ks);
            auto step = [&](int nsl, FF (&cur)[4], FF (&nxt)[4]) {
                LGKM_BAR();
#pragma unroll
                for (int ks = 0; ks < 4; ++ks) { acc = MFMA(cur[ks].w, cur[ks].a, acc); SBAR(); rd(nxt[ks], nsl, ks); SBAR(); if (pace) PACE(); } };
#pragma unroll 1
            for (int it = 0; it < n; it += 2) { step((it + 1) & 3, fa, fb); step((it + 2) & 3, fb, fa); }
        } };
    if (wid8 == 0) wait_flags(flagp(ws, FL_U + tb * 7 + lane), lane < 7, tag);
    __syncthreads();
    STAMP(5);
    pass(wout, ws + WS_U, 56, 14, ag, true);
    STAMP(6);
    __syncthreads();
#pragma unroll
    for (int q = 0; q < 8; ++q) {
        const int kr = 8 * wid8 + (lane >> 3);
        set_m0(lds0 + q * 8192 + wid8 * 1024);
        glds_s<0, false>((const char*)(wo + (size_t)q * 64 * DM), (kr * DM + db * 64 + ((lane & 7) ^ wswz(kr)) * 8) * 2); }
    const int tok = tb * 64 + wr * 32 + l31;
    float gate[16]; u32x2 xv[4];
    if (wid8 < 4) {
#pragma unroll
        for (int q = 0; q < 4; ++q) xv[q] = *(const u32x2*)(x + (size_t)tok * DM + db * 64 + wc * 32 + 8 * q + 4 * hh);
#pragma unroll
        for (int r = 0; r < 16; ++r) gate[r] = silu_f(ag[r]); }
    if (wid8 == 0) wait_flags(flagp(ws, FL_O + (tb >> 2) * 32 + lane), lane < 32, tag);
    WAITV(0);
    __syncthreads();
    STAMP(7);
    const int kh = wid8 >> 2;
    u32x4 of[16];
    { const char* ob = ws + WS_OF + ((size_t)(tb * 2 + wr) * 32 + 16 * kh) * 1024; const unsigned v0 = lane * 16, v1 = v0 + 4096, v2 = v0 + 8192, v3 = v0 + 12288;
      asm volatile(
        "global_load_dwordx4 %0, %16, %20 sc1\n\tglobal_load_dwordx4 %1, %16, %20 offset:1024 sc1\n\tglobal_load_dwordx4 %2, %16, %20 offset:2048 sc1\n\tglobal_load_dwordx4 %3, %16, %20 offset:3072 sc1\n\t"
        "global_load_dwordx4 %4, %17, %20 sc1\n\tglobal_load_dwordx4 %5, %17, %20 offset:1024 sc1\n\tglobal_load_dwordx4 %6, %17, %20 offset:2048 sc1\n\tglobal_load_dwordx4 %7, %17, %20 offset:3072 sc1\n\t"
        "global_load_dwordx4 %8, %18, %20 sc1\n\tglobal_load_dwordx4 %9, %18, %20 offset:1024 sc1\n\tglobal_load_dwordx4 %10, %18, %20 offset:2048 sc1\n\tglobal_load_dwordx4 %11, %18, %20 offset:3072 sc1\n\t"
        "global_load_dwordx4 %12, %19, %20 sc1\n\tglobal_load_dwordx4 %13, %19, %20 offset:1024 sc1\n\tglobal_load_dwordx4 %14, %19, %20 offset:2048 sc1\n\tglobal_load_dwordx4 %15, %19, %20 offset:3072 sc1\n\t"
        "s_waitcnt vmcnt(0)"
        : "=&v"(of[0]), "=&v"(of[1]), "=&v"(of[2]), "=&v"(of[3]), "=&v"(of[4]), "=&v"(of[5]), "=&v"(of[6]), "=&v"(of[7]),
          "=&v"(of[8]), "=&v"(of[9]), "=&v"(of[10]), "=&v"(of[11]), "=&v"(of[12]), "=&v"(of[13]), "=&v"(of[14]), "=&v"(of[15])
        : "v"(v0), "v"(v1), "v"(v2), "v"(v3), "s"(ob) : "memory"); }
    { lds_cptr wb_ = (lds_cptr)lds + kh * 32768;
#pragma unroll
      for (int ks = 0; ks < 16; ++ks)
          as = MFMA(cat(trread(wb_ + woff[0] + ks * 2048), trread(wb_ + woff[1] + ks * 2048)), __builtin_bit_cast(bf16x8, of[ks]), as); }
    __syncthreads();
    if (kh == 1) {
#pragma unroll
        for (int q = 0; q < 4; ++q) *(f32x4*)(lds + (wid * 4 + q) * 1024 + lane * 16) = (f32x4){as[4 * q], as[4 * q + 1], as[4 * q + 2], as[4 * q + 3]}; }
    __syncthreads();
    STAMP(8);
    if (kh == 0) {
#pragma unroll
        for (int q = 0; q < 4; ++q) { const f32x4 p = *(const f32x4*)(lds + (wid * 4 + q) * 1024 + lane * 16);
            const float r0 = bflo(xv[q][0]) + (as[4 * q] + p[0]) * gate[4 * q], r1 = bfhi(xv[q][0]) + (as[4 * q + 1] + p[1]) * gate[4 * q + 1];
            const float r2 = bflo(xv[q][1]) + (as[4 * q + 2] + p[2]) * gate[4 * q + 2], r3 = bfhi(xv[q][1]) + (as[4 * q + 3] + p[3]) * gate[4 * q + 3];
            *(u32x2*)(out + (size_t)tok * DM + db * 64 + wc * 32 + 8 * q + 4 * hh) = (u32x2){cvtpk(r0, r1), cvtpk(r2, r3)}; }
    }
}

#define APRIO 3
#define GDELAY 0
#define GPRIO 2
constexpr int ALL_LDS = S_LDS;
__global__ __launch_bounds__(512, 4) void k_all(const uint16_t* __restrict__ x, const uint16_t* __restrict__ wqkv, const uint16_t* __restrict__ wo, const uint16_t* __restrict__ win,
                                                const uint16_t* __restrict__ wsp, const uint16_t* __restrict__ bsp, const uint16_t* __restrict__ wout, char* __restrict__ ws, uint16_t* __restrict__ out) {
    extern __shared__ __attribute__((aligned(16))) char lds[];
    const int b = blockIdx.x;
    u64 tag;
    STAMP(0);
    if (b < 256) {
        __builtin_amdgcn_s_setprio(APRIO);
        if (ADELAY > 0 && b < 192) { const u64 t0 = __builtin_amdgcn_s_memrealtime(); while (__builtin_amdgcn_s_memrealtime() - t0 < ADELAY) __builtin_amdgcn_s_sleep(8); }
        if (b < 192) qkv_role(x, wqkv, ws, lds, b, tag); else stats_role(x, wqkv, win, ws, lds, b - 192, tag);
        __builtin_amdgcn_s_setprio(0);
        __syncthreads();
        attn_role(ws, lds, b, tag);
        STAMP(5);
    } else {
        const int g = b - 256;
        __builtin_amdgcn_s_setprio(GPRIO);
            if (GDELAY > 0) { const u64 t0 = __builtin_amdgcn_s_memrealtime(); while (__builtin_amdgcn_s_memrealtime() - t0 < GDELAY) __builtin_amdgcn_s_sleep(8); }
        if (g < 224) { z_role(x, win, ws, lds, g, tag); STAMP(2); __syncthreads(); spatial_role(wsp, bsp, ws, lds, g, tag); STAMP(4); __syncthreads(); }
        else { u64 ep; EPOCH_REQ(ep); asm volatile("s_waitcnt vmcnt(0)" : "+v"(ep) :: "memory"); tag = EPOCH_TAG(ep); }
        if (threadIdx.x == 0) __hip_atomic_store(flagp(ws, FL_G + g), tag, RLX_AGENT);
        final_role(x, wo, wout, ws, out, lds, g, tag);
        if (g == 255 && threadIdx.x < 64) {
            const int lane = threadIdx.x;
#pragma unroll 1
            for (int c = 0; c < 4; ++c) { wait_flags(flagp(ws, FL_O + c * 64 + lane), true, tag); wait_flags(flagp(ws, FL_G + c * 64 + lane), true, tag); }
            if (lane == 0) __hip_atomic_store(flagp(ws, FL_EPOCH), tag, RLX_AGENT);
        }
        STAMP(9);
    }
}

extern "C" void kernel_launch(void* const* d_in, const int* in_sizes, int n_in, void* d_out, int out_size, void* d_ws, size_t ws_size, hipStream_t stream) {
    const uint16_t* x = (const uint16_t*)d_in[0]; const uint16_t* wqkv = (const uint16_t*)d_in[1]; const uint16_t* wo = (const uint16_t*)d_in[2];
    const uint16_t* win = (const uint16_t*)d_in[4]; const uint16_t* wsp = (const uint16_t*)d_in[5]; const uint16_t* bsp = (const uint16_t*)d_in[6]; const uint16_t* wout = (const uint16_t*)d_in[7];
    (void)hipFuncSetAttribute((const void*)k_all, hipFuncAttributeMaxDynamicSharedMemorySize, ALL_LDS);
    k_all<<<512, 512, ALL_LDS, stream>>>(x, wqkv, wo, win, wsp, bsp, wout, (char*)d_ws, (uint16_t*)d_out);
}
```

```cpp
#include <hip/hip_runtime.h>
#include <hip/hip_bf16.h>
#include <stdint.h>
#define WARM_IN 1
#include <stdio.h>

typedef short bf16x8 __attribute__((ext_vector_type(8)));
typedef short s16x4 __attribute__((ext_vector_type(4)));
typedef __bf16 bf16x2_t __attribute__((ext_vector_type(2)));
typedef float f32x16 __attribute__((ext_vector_type(16)));
typedef float f32x4 __attribute__((ext_vector_type(4)));
typedef float f32x2 __attribute__((ext_vector_type(2)));
typedef unsigned u32x4 __attribute__((ext_vector_type(4)));
typedef unsigned u32x2 __attribute__((ext_vector_type(2)));
typedef unsigned long long u64;
#define GAS __attribute__((address_space(1)))
#define LAS __attribute__((address_space(3)))
typedef LAS const char* lds_cptr;
typedef short v4i16_t __attribute__((ext_vector_type(4)));
#define RLX_AGENT __ATOMIC_RELAXED, __HIP_MEMORY_SCOPE_AGENT

constexpr int SEQ = 2048, DM = 512, NQKV = 1536, NIN = 1792, FH = 896;
constexpr float QSCALE = 0.25f * 1.4426950408889634f;
constexpr float LN_EPS = 1e-5f;
constexpr size_t MB = 1024 * 1024;
constexpr size_t WS_QF = 0, WS_KV = 2 * MB, WS_Z1 = 6 * MB, WS_Z2N = 10 * MB, WS_U = 14 * MB, WS_OF = 18 * MB;
constexpr size_t WS_KM = 20 * MB, WS_ST = 20 * MB + 65536, WS_RS = 20 * MB + 65536 + 229376, WS_CS = 20 * MB + 65536 + 245760  , WS_FLAG = 21 * MB;
constexpr int FL_RS = 0, FL_CS = 64, FL_QKV = 128, FL_ST = 320, FL_Z = 432, FL_O = 656, FL_U = 912, FL_G = 1136, FL_EPOCH = 1392;

#define MFMA(a, b, c) __builtin_amdgcn_mfma_f32_32x32x16_bf16(a, b, c, 0, 0, 0)
#define SBAR() __builtin_amdgcn_sched_barrier(0)
__device__ __forceinline__ unsigned cvtpk(float lo, float hi) { unsigned r; asm("v_cvt_pk_bf16_f32 %0, %1, %2" : "=v"(r) : "v"(lo), "v"(hi)); return r; }
__device__ __forceinline__ float bflo(unsigned u) { return __uint_as_float(u << 16); }
__device__ __forceinline__ float bfhi(unsigned u) { return __uint_as_float(u & 0xffff0000u); }
__device__ __forceinline__ float dot2(unsigned a, unsigned b, float acc) {
    return __builtin_amdgcn_fdot2_f32_bf16(__builtin_bit_cast(bf16x2_t, a), __builtin_bit_cast(bf16x2_t, b), acc, false); }
__device__ __forceinline__ void glds16(const void* g, unsigned lds_base) {
    asm volatile("s_mov_b32 m0, %1\n\ts_nop 0\n\tglobal_load_lds_dwordx4 %0, off" :: "v"(g), "s"(lds_base) : "memory", "m0"); }
template <int IMM, bool WT> __device__ __forceinline__ void glds_s(const void* sbase, unsigned voff) {
    if (WT) asm volatile("global_load_lds_dwordx4 %0, %1 offset:%2 sc1" :: "v"(voff), "s"(sbase), "n"(IMM) : "memory");
    else asm volatile("global_load_lds_dwordx4 %0, %1 offset:%2" :: "v"(voff), "s"(sbase), "n"(IMM) : "memory"); }
__device__ __forceinline__ void set_m0(unsigned v) { asm volatile("s_mov_b32 m0, %0\n\ts_nop 0" :: "s"(v) : "memory", "m0"); }
__device__ __forceinline__ s16x4 trread(lds_cptr p) { return __builtin_bit_cast(s16x4, __builtin_amdgcn_ds_read_tr16_b64_v4i16((LAS v4i16_t*)p)); }
__device__ __forceinline__ bf16x8 ldsfrag(lds_cptr p) { return *(LAS const bf16x8*)p; }
__device__ __forceinline__ bf16x8 cat(s16x4 a, s16x4 b) { return (bf16x8){a[0], a[1], a[2], a[3], b[0], b[1], b[2], b[3]}; }
__device__ __forceinline__ float swap32_add(float v) { auto rr = __builtin_amdgcn_permlane32_swap(__float_as_uint(v), __float_as_uint(v), false, false); return __uint_as_float(rr[0]) + __uint_as_float(rr[1]); }
__device__ __forceinline__ float swap32_max(float v) { auto rr = __builtin_amdgcn_permlane32_swap(__float_as_uint(v), __float_as_uint(v), false, false); return fmaxf(__uint_as_float(rr[0]), __uint_as_float(rr[1])); }
template <int CTRL> __device__ __forceinline__ float dppf(float v) { return __int_as_float(__builtin_amdgcn_update_dpp(0, __float_as_int(v), CTRL, 0xf, 0xf, false)); }
__device__ __forceinline__ float row16_add(float v) { v += dppf<0x128>(v); v += dppf<0x124>(v); v += dppf<0x122>(v); v += dppf<0x121>(v); return v; }
__device__ __forceinline__ float row16_max(float v) { v = fmaxf(v, dppf<0x128>(v)); v = fmaxf(v, dppf<0x124>(v)); v = fmaxf(v, dppf<0x122>(v)); v = fmaxf(v, dppf<0x121>(v)); return v; }
__device__ __forceinline__ float swap16_max(float v) { auto rr = __builtin_amdgcn_permlane16_swap(__float_as_uint(v), __float_as_uint(v), false, false); return fmaxf(__uint_as_float(rr[0]), __uint_as_float(rr[1])); }
__device__ __forceinline__ float gelu_t(float x) {
    const float c1 = -2.3022082f, c2 = -0.10294324f;
    float e = __builtin_amdgcn_exp2f(x * __builtin_fmaf(x * x, c2, c1));
    return x * __builtin_amdgcn_rcpf(1.0f + e); }
__device__ __forceinline__ float silu_f(float x) { return x * __builtin_amdgcn_rcpf(1.0f + __builtin_amdgcn_exp2f(-1.4426950408889634f * x)); }
#define STAMP(slot) do {} while (0)
#define PF_DIST 9
#define PACE_N 2
#define PACE() do { asm volatile("s_nop %0" :: "n"(PACE_N)); SBAR(); } while (0)
#define POLL_SLEEP 2
#define WAITV(n) asm volatile("s_waitcnt vmcnt(" #n ")" ::: "memory")
#define WAIT_BAR(n) asm volatile("s_waitcnt vmcnt(" #n ")\n\ts_barrier" ::: "memory")
#define BAR() asm volatile("s_barrier" ::: "memory")
#define LGKM_BAR() do { __builtin_amdgcn_s_waitcnt(0xC07F); asm volatile("" ::: "memory"); __builtin_amdgcn_s_barrier(); asm volatile("" ::: "memory"); } while (0)
__device__ __forceinline__ unsigned wsw(unsigned row) { return ((row & 3) << 2) | ((row >> 2) & 3); }
__device__ __forceinline__ void st16_wt(void* p, u32x4 v) {
    asm volatile("global_store_dwordx4 %0, %1, off sc1\n\ts_nop 1" :: "v"(p), "v"(v)); }
__device__ __forceinline__ void st8_wt(void* p, u64 v) { __hip_atomic_store((GAS u64*)p, v, RLX_AGENT); }
__device__ __forceinline__ u64 ld8_wt(const void* p) { return __hip_atomic_load((GAS u64*)p, RLX_AGENT); }
__device__ __forceinline__ u32x4 ld16_wt(const void* p) { u32x4 v; asm volatile("global_load_dwordx4 %0, %1, off sc1\n\ts_waitcnt vmcnt(0)" : "=&v"(v) : "v"(p) : "memory"); return v; }
__device__ __forceinline__ void glds16_wt(const void* g, unsigned lds_base) {
    asm volatile("s_mov_b32 m0, %1\n\ts_nop 0\n\tglobal_load_lds_dwordx4 %0, off sc1" :: "v"(g), "s"(lds_base) : "memory", "m0"); }
__device__ __forceinline__ GAS u64* flagp(char* ws, int idx) { return (GAS u64*)(ws + WS_FLAG) + idx * 16; }
__device__ __forceinline__ void wait_flags(GAS u64* f, bool want, u64 tag) {
    for (unsigned spins = 0; spins < (1u << 20); ++spins) {
        const u64 v = want ? __hip_atomic_load(f, RLX_AGENT) : tag;
        if (__all(v == tag)) return;
        __builtin_amdgcn_s_sleep(POLL_SLEEP); } }
#define ACQUIRE() do { __builtin_amdgcn_fence(__ATOMIC_ACQUIRE, "agent"); asm volatile("s_waitcnt vmcnt(0)" ::: "memory"); } while (0)
#define EPOCH_REQ(ep) asm volatile("global_load_dwordx2 %0, %1, off sc1" : "=v"(ep) : "v"(flagp(ws, FL_EPOCH)) : "memory")
#define EPOCH_TAG(ep) ((((u64)(unsigned)__builtin_amdgcn_readfirstlane((unsigned)((ep) >> 32)) << 32) | (unsigned)__builtin_amdgcn_readfirstlane((unsigned)(ep))) + 1)
#define PUBLISH(idx) do { WAITV(0); __syncthreads(); if (threadIdx.x == 0) __hip_atomic_store(flagp(ws, idx), tag, RLX_AGENT); } while (0)

__device__ __forceinline__ void stats_role(const uint16_t* __restrict__ x, const uint16_t* __restrict__ wqkv, const uint16_t* __restrict__ win, char* __restrict__ ws, char* lds, int j, u64& tag) {
    u64 ep; EPOCH_REQ(ep);
    const int tid = threadIdx.x, lane = tid & 63, wid = tid >> 6, rr = lane >> 4, cc = lane & 15;
    const unsigned ONES = 0x3f803f80u;
    u32x4 xv[4], w[8];
    const int row = 32 * j + 4 * wid + rr;
#pragma unroll
    for (int i = 0; i < 4; ++i) xv[i] = *(const u32x4*)(x + (size_t)row * DM + i * 128 + cc * 8);
    const int cbk = j >> 1;
    if (j < 52) {
        const uint16_t* W = cbk < 12 ? wqkv + cbk * 128 : win + (cbk - 12) * 128; const int ldw = cbk < 12 ? NQKV : NIN;
        const uint16_t* p = W + (size_t)((j & 1) * 256 + 32 * wid + rr) * ldw + cc * 8;
#pragma unroll
        for (int i = 0; i < 8; ++i) w[i] = *(const u32x4*)(p + (size_t)(i * 4) * ldw);
    }
    asm volatile("s_waitcnt vmcnt(0)" : "+v"(ep) :: "memory");
    tag = EPOCH_TAG(ep);
    float s1 = 0.f, s2 = 0.f;
#pragma unroll
    for (int i = 0; i < 4; ++i)
#pragma unroll
        for (int e = 0; e < 4; ++e) { s1 = dot2(xv[i][e], ONES, s1); s2 = dot2(xv[i][e], xv[i][e], s2); }
    s1 = row16_add(s1); s2 = row16_add(s2);
    { const float mu = s1 * (1.0f / DM), var = fmaxf(s2 * (1.0f / DM) - mu * mu, 0.f);
      if (cc == 0) st8_wt(ws + WS_RS + (size_t)row * 8, __builtin_bit_cast(u64, (f32x2){mu, __builtin_amdgcn_rsqf(var + LN_EPS)})); }
    if (j < 52) {
        float cs[8] = {0.f, 0.f, 0.f, 0.f, 0.f, 0.f, 0.f, 0.f};
        unsigned sel_lo = 0x00003f80u, sel_hi = 0x3f800000u;
        asm volatile("" : "+v"(sel_lo), "+v"(sel_hi));
#pragma unroll
        for (int i = 0; i < 8; ++i)
#pragma unroll
            for (int e = 0; e < 4; ++e) { cs[2 * e] = dot2(w[i][e], sel_lo, cs[2 * e]); cs[2 * e + 1] = dot2(w[i][e], sel_hi, cs[2 * e + 1]); }
        float* red = (float*)lds;
        *(f32x4*)(red + (wid * 4 + rr) * 128 + cc * 8) = (f32x4){cs[0], cs[1], cs[2], cs[3]}; *(f32x4*)(red + (wid * 4 + rr) * 128 + cc * 8 + 4) = (f32x4){cs[4], cs[5], cs[6], cs[7]};
        __syncthreads();
        if (tid < 64) { f32x2 a = {0.f, 0.f};
#pragma unroll
            for (int q = 0; q < 32; ++q) a += *(const f32x2*)(red + q * 128 + 2 * tid);
            st8_wt(ws + WS_CS + (size_t)((j & 1) * 3328 + cbk * 128 + 2 * tid) * 4, __builtin_bit_cast(u64, a)); }
    }
    STAMP(11);
    WAITV(0); __syncthreads();
    STAMP(1);
    if (tid == 0) { __hip_atomic_store(flagp(ws, FL_RS + j), tag, RLX_AGENT); if (j < 52) __hip_atomic_store(flagp(ws, FL_CS + j), tag, RLX_AGENT); }
}

constexpr int P_SLOT = 16384, P_ST = 4 * P_SLOT, P_LDS = 4 * P_SLOT + 4096;
struct PFrag { bf16x8 x[2], w; };
template <bool SWAP>
__device__ __forceinline__ void proj_main(const uint16_t* __restrict__ x, const uint16_t* __restrict__ W, int ldw, int rb, int col0, int cblk, char* __restrict__ ws, char* lds, int rot, u64& tag, f32x16 (&acc)[2]) {
    u64 ep; EPOCH_REQ(ep);
    const int tid = threadIdx.x, lane = tid & 63, wid = __builtin_amdgcn_readfirstlane(tid >> 6), wr = wid >> 2, wc = wid & 3, l31 = lane & 31, hh = lane >> 5;
    const unsigned lds0 = (unsigned)(uintptr_t)lds;
    const int xr = 16 * wid + (lane >> 2), wk = 4 * wid + (lane >> 4);
    const char* xb = (const char*)(x + (size_t)rb * 128 * DM); const char* wb = (const char*)(W + col0);
    const unsigned xvo = (xr * DM + ((lane & 3) ^ ((xr >> 2) & 3)) * 8) * 2, wvo = (wk * ldw + ((lane & 15) ^ wsw(wk)) * 8) * 2;
    const unsigned xd = lds0 + wid * 1024, wd = lds0 + 8192 + wid * 1024;
    auto issue1 = [&](int it, int which) { const unsigned so = (it & 3) * P_SLOT; const int kk = (it + rot) & 15;
        if (which == 0) { set_m0(xd + so); glds_s<0, false>(xb + kk * 64, xvo); } else { set_m0(wd + so); glds_s<0, false>(wb + (size_t)kk * 64 * ldw, wvo); } SBAR(); };
#pragma unroll
    for (int it = 0; it < 4; ++it) { issue1(it, 0); issue1(it, 1); }
    unsigned xoff[2][2], woff[2][2];
    const int blk = (lane >> 4) & 1, q = (lane & 15) >> 2, p = lane & 3;
#pragma unroll
    for (int ks = 0; ks < 2; ++ks) {
#pragma unroll
        for (int rt = 0; rt < 2; ++rt) { const int r = wr * 64 + rt * 32 + l31; xoff[rt][ks] = r * 64 + (((2 * ks + hh) ^ ((r >> 2) & 3)) * 16); }
        const int kr = 8 * hh + q, ch = 4 * wc + 2 * blk + (p >> 1);
        woff[0][ks] = 8192 + ks * 4096 + kr * 256 + ((ch ^ wsw(kr)) * 16) + 8 * (p & 1); woff[1][ks] = (woff[0][ks] + 1024) ^ 16; }
    auto rd = [&](PFrag& f, int slot, int j) { lds_cptr sb = (lds_cptr)lds + slot * P_SLOT;
        f.x[0] = ldsfrag(sb + xoff[0][j]); f.x[1] = ldsfrag(sb + xoff[1][j]); f.w = cat(trread(sb + woff[0][j]), trread(sb + woff[1][j])); };
    auto mm = [&](const PFrag& f) {
#pragma unroll
        for (int rt = 0; rt < 2; ++rt) acc[rt] = SWAP ? MFMA(f.x[rt], f.w, acc[rt]) : MFMA(f.w, f.x[rt], acc[rt]); };
    acc[0] = f32x16{}; acc[1] = f32x16{};
    PFrag fa[2], fb[2];
    asm volatile("s_waitcnt vmcnt(6)\n\ts_barrier" : "+v"(ep) :: "memory");
    tag = EPOCH_TAG(ep);
    STAMP(10);
    rd(fa[0], 0, 0); rd(fa[1], 0, 1);
    auto mm1 = [&](const PFrag& f, int rt) { acc[rt] = SWAP ? MFMA(f.x[rt], f.w, acc[rt]) : MFMA(f.w, f.x[rt], acc[rt]); SBAR(); };
    auto step = [&](int it, int sl, PFrag (&cur)[2], PFrag (&nxt)[2]) {
        __builtin_amdgcn_s_waitcnt(0xC07F);
        if (it < 13) WAITV(4); else if (it == 13) WAITV(2); else WAITV(0);
        __builtin_amdgcn_s_barrier(); asm volatile("" ::: "memory");
        lds_cptr sb = (lds_cptr)lds + ((sl + 1) & 3) * P_SLOT;
        mm1(cur[0], 0);
        nxt[0].x[0] = ldsfrag(sb + xoff[0][0]); nxt[0].x[1] = ldsfrag(sb + xoff[1][0]); SBAR();
        mm1(cur[0], 1);
        nxt[0].w = cat(trread(sb + woff[0][0]), trread(sb + woff[1][0])); SBAR();
        if (it + 4 < 16) issue1(it + 4, 0);
        mm1(cur[1], 0);
        nxt[1].x[0] = ldsfrag(sb + xoff[0][1]); nxt[1].x[1] = ldsfrag(sb + xoff[1][1]); SBAR();
        mm1(cur[1], 1);
        nxt[1].w = cat(trread(sb + woff[0][1]), trread(sb + woff[1][1])); SBAR();
        if (it + 4 < 16) issue1(it + 4, 1); };
#pragma unroll 1
    for (int io = 0; io < 2; ++io) { step(io * 4, 0, fa, fb); step(io * 4 + 1, 1, fb, fa); step(io * 4 + 2, 2, fa, fb); step(io * 4 + 3, 3, fb, fa); }
    const char* fa_ = (const char*)(lane < 4 ? flagp(ws, FL_RS + rb * 4 + lane) : flagp(ws, FL_CS + 2 * cblk + (lane & 1)));
    const char* sa_ = tid < 128 ? ws + WS_RS + (size_t)(rb * 128 + tid) * 8 : ws + WS_CS + (size_t)(cblk * 128 + 2 * (tid & 63)) * 4;
    const char* sb_ = ws + WS_CS + (size_t)(3328 + cblk * 128 + 2 * (tid & 63)) * 4;
    u64 fl, stv, stw;
    asm volatile("global_load_dwordx2 %0, %1, off sc1" : "=v"(fl) : "v"(fa_) : "memory");
    step(8, 0, fa, fb); step(9, 1, fb, fa); step(10, 2, fa, fb); step(11, 3, fb, fa);
    asm volatile("" : "+v"(fl));
    const bool got = __all(lane >= 6 || fl == tag);
    asm volatile("global_load_dwordx2 %0, %2, off sc1\n\tglobal_load_dwordx2 %1, %3, off sc1" : "=&v"(stv), "=&v"(stw) : "v"(sa_), "v"(sb_) : "memory");
    step(12, 0, fa, fb); step(13, 1, fb, fa); step(14, 2, fa, fb); step(15, 3, fb, fa);
    asm volatile("" : "+v"(stv), "+v"(stw));
    if (!got) { wait_flags((GAS u64*)fa_, lane < 6, tag); stv = ld8_wt(sa_); stw = ld8_wt(sb_); }
    STAMP(11);
    float* st = (float*)(lds + P_ST);
    { const f32x2 v = __builtin_bit_cast(f32x2, stv);
      if (tid < 128) { st[tid] = v[0]; st[128 + tid] = v[1]; } else if (tid < 192) *(f32x2*)(st + 256 + 2 * (tid - 128)) = v + __builtin_bit_cast(f32x2, stw); }
    __syncthreads();
}

__device__ __forceinline__ void qkv_role(const uint16_t* __restrict__ x, const uint16_t* __restrict__ wqkv, char* __restrict__ ws, char* lds, int b, u64& tag) {
    const int xcd = b & 7, i = b >> 3;
    const int rb = 4 * (xcd >> 1) + (i & 3), cb = 6 * (xcd & 1) + (i >> 2);
    const int tid = threadIdx.x, lane = tid & 63, wid = __builtin_amdgcn_readfirstlane(tid >> 6), wr = wid >> 2, wc = wid & 3, l31 = lane & 31, hh = lane >> 5;
    f32x16 acc[2];
    const int rot = ((i & 3) * 4 + (i >> 2) * 3) & 15;
    if (cb >= 8) proj_main<true>(x, wqkv, NQKV, rb, cb * 128, cb, ws, lds, rot, tag, acc); else proj_main<false>(x, wqkv, NQKV, rb, cb * 128, cb, ws, lds, rot, tag, acc);
    STAMP(1);
    const float* st = (const float*)(lds + P_ST);
    if (cb >= 8) {
        const float cs = st[256 + wc * 32 + l31];
        const int head = (cb - 8) * 8 + wc * 2 + (l31 >> 4);
#pragma unroll
        for (int rt = 0; rt < 2; ++rt) { const int tr = wr * 64 + rt * 32;
            unsigned pk[8];
#pragma unroll
            for (int q = 0; q < 4; ++q) { const f32x4 m = *(const f32x4*)(st + tr + 8 * q + 4 * hh), r = *(const f32x4*)(st + 128 + tr + 8 * q + 4 * hh);
                pk[2 * q] = cvtpk((acc[rt][4 * q] - m[0] * cs) * r[0], (acc[rt][4 * q + 1] - m[1] * cs) * r[1]);
                pk[2 * q + 1] = cvtpk((acc[rt][4 * q + 2] - m[2] * cs) * r[2], (acc[rt][4 * q + 3] - m[3] * cs) * r[3]); }
            const int tile = rb * 4 + (tr >> 5);
            char* dst = ws + WS_KV + (size_t)head * 131072 + (tile >> 3) * 16384 + 8192 + (tile & 7) * 1024 + hh * 256 + (l31 & 15) * 16;
            st16_wt(dst, (u32x4){pk[0], pk[1], pk[2], pk[3]}); st16_wt(dst + 512, (u32x4){pk[4], pk[5], pk[6], pk[7]}); }
    } else {
        const bool isK = cb >= 4; const float sc = isK ? 1.0f : QSCALE;
        float kmx[2] = {0.f, 0.f};
#pragma unroll
        for (int rt = 0; rt < 2; ++rt) { const int tr = wr * 64 + rt * 32, tile = rb * 4 + (tr >> 5);
            const float mu = st[tr + l31], rs = st[128 + tr + l31] * sc;
#pragma unroll
            for (int hb = 0; hb < 2; ++hb) {
                float v[8];
#pragma unroll
                for (int q = 0; q < 2; ++q) { const f32x4 c = *(const f32x4*)(st + 256 + wc * 32 + 16 * hb + 8 * q + 4 * hh);
#pragma unroll
                    for (int e = 0; e < 4; ++e) v[4 * q + e] = (acc[rt][8 * hb + 4 * q + e] - mu * c[e]) * rs; }
                u32x4 pk;
#pragma unroll
                for (int e = 0; e < 4; ++e) pk[e] = cvtpk(v[2 * e], v[2 * e + 1]);
                const int head = (cb & 3) * 8 + wc * 2 + hb;
                char* dst = isK ? ws + WS_KV + (size_t)head * 131072 + (tile >> 3) * 16384 + (tile & 7) * 1024 + lane * 16 : ws + WS_QF + (size_t)head * 65536 + tile * 1024 + lane * 16;
                st16_wt(dst, pk);
                float n2 = 0.f;
#pragma unroll
                for (int e = 0; e < 4; ++e) n2 = dot2(pk[e], pk[e], n2);
                kmx[hb] = fmaxf(kmx[hb], swap32_add(n2));
            } }
        if (isK) {
#pragma unroll
            for (int hb = 0; hb < 2; ++hb) { const float m = swap16_max(row16_max(kmx[hb]));
                if (lane == 0) __hip_atomic_store((GAS unsigned*)(ws + WS_KM) + ((cb & 3) * 8 + wc * 2 + hb) * 32 + rb * 2 + wr, __float_as_uint(m), RLX_AGENT); } }
    }
    STAMP(12);
    PUBLISH(FL_QKV + cb * 16 + rb);
    STAMP(13);
}

constexpr int A_ONES = 3 * 16384, A_LDS = A_ONES + 8192 + 512;
__device__ __forceinline__ void attn_role(char* __restrict__ ws, char* lds, int b, u64 tag) {
    const int tid = threadIdx.x, lane = tid & 63, wid = __builtin_amdgcn_readfirstlane(tid >> 6);
    const int head = (b & 7) * 4 + ((b >> 3) & 3), jb = b >> 5;
    const unsigned lds0 = (unsigned)(uintptr_t)lds;
    const char* kvb = ws + WS_KV + (size_t)head * 131072;
    for (int i = tid; i < (8192 + 512) / 16; i += 512) { const unsigned o1 = ((i & 11) == 0) ? 0x3f803f80u : 0u; *(u32x4*)(lds + A_ONES + i * 16) = (u32x4){o1, o1, o1, o1}; }
    if (wid == 0) {
        const int hb = head >> 3;
        const int idx = lane < 16 ? (4 + hb) * 16 + lane : lane < 32 ? (8 + hb) * 16 + (lane - 16) : hb * 16 + 2 * jb + (lane & 1);
        wait_flags(flagp(ws, FL_QKV + idx), lane < 34, tag);
        STAMP(14);
        }
    __syncthreads();
    STAMP(2);
    unsigned goff = wid * 2048 + lane * 16;
    const int tile0 = jb * 8 + wid;
    u32x4 qa; unsigned kmu;
    asm volatile("global_load_dwordx4 %0, %2, off sc1\n\tglobal_load_dword %1, %3, off sc1" : "=&v"(qa), "=&v"(kmu)
                 : "v"(ws + WS_QF + (size_t)head * 65536 + tile0 * 1024 + lane * 16), "v"(ws + WS_KM + (size_t)(head * 32 + (lane & 31)) * 4) : "memory");
#pragma unroll
    for (int c = 0; c < 2; ++c) { set_m0(lds0 + c * 16384 + wid * 2048); glds_s<0, true>(kvb + c * 16384, goff); glds_s<1024, true>(kvb + c * 16384, goff); }
    asm volatile("s_waitcnt vmcnt(4)" : "+v"(qa), "+v"(kmu) :: "memory");
    const float km = swap16_max(row16_max(__uint_as_float(kmu)));
    float na = 0.f;
#pragma unroll
    for (int e = 0; e < 4; ++e) na = dot2(qa[e], qa[e], na);
    na = swap32_add(na);
    float ma = __builtin_amdgcn_sqrtf(na * km);
    if (__any(!(ma <= 40.0f))) {
        float xa = -3.0e38f;
        for (int t = 0; t < 64; ++t) {
            const bf16x8 kf = __builtin_bit_cast(bf16x8, ld16_wt(kvb + (t >> 3) * 16384 + (t & 7) * 1024 + lane * 16));
            const f32x16 sa = MFMA(kf, __builtin_bit_cast(bf16x8, qa), f32x16{});
#pragma unroll
            for (int r = 0; r < 16; ++r) xa = fmaxf(xa, sa[r]); }
        ma = swap32_max(xa);
    }
    const float nma = -ma;
    const unsigned kaddr = lds0 + lane * 16;
    const bool ones = (lane & 16) != 0;
    const unsigned vaddr = ones ? lds0 + A_ONES + (lane & 15) * 16 : lds0 + 8192 + (lane >> 5) * 256 + (lane & 15) * 16;
    const unsigned vsel = ones ? 0u : 0xffffffffu;
    goff += 2 * 16384;
    unsigned dbeg = lds0 + wid * 2048, dend = dbeg + 3 * 16384, dslot = dbeg + 2 * 16384, nslot = 0x4000, cnt, m0save;
    unsigned oa0, oa1, oa2, oa3;
    STAMP(3);
    asm volatile(
    "s_mov_b32 %[m0save], m0\n"
    "v_mov_b32 v96, %[qa0]\n"
    "v_mov_b32 v97, %[qa1]\n"
    "v_mov_b32 v98, %[qa2]\n"
    "v_mov_b32 v99, %[qa3]\n"
    "v_mov_b32 v72, %[nma]\n"
    "v_mov_b32 v56, 0\n"
    "v_mov_b32 v73, %[nma]\n"
    "v_mov_b32 v57, 0\n"
    "v_mov_b32 v74, %[nma]\n"
    "v_mov_b32 v58, 0\n"
    "v_mov_b32 v75, %[nma]\n"
    "v_mov_b32 v59, 0\n"
    "v_mov_b32 v76, %[nma]\n"
    "v_mov_b32 v60, 0\n"
    "v_mov_b32 v77, %[nma]\n"
    "v_mov_b32 v61, 0\n"
    "v_mov_b32 v78, %[nma]\n"
    "v_mov_b32 v62, 0\n"
    "v_mov_b32 v79, %[nma]\n"
    "v_mov_b32 v63, 0\n"
    "v_mov_b32 v80, %[nma]\n"
    "v_mov_b32 v64, 0\n"
    "v_mov_b32 v81, %[nma]\n"
    "v_mov_b32 v65, 0\n"
    "v_mov_b32 v82, %[nma]\n"
    "v_mov_b32 v66, 0\n"
    "v_mov_b32 v83, %[nma]\n"
    "v_mov_b32 v67, 0\n"
    "v_mov_b32 v84, %[nma]\n"
    "v_mov_b32 v68, 0\n"
    "v_mov_b32 v85, %[nma]\n"
    "v_mov_b32 v69, 0\n"
    "v_mov_b32 v86, %[nma]\n"
    "v_mov_b32 v70, 0\n"
    "v_mov_b32 v87, %[nma]\n"
    "v_mov_b32 v71, 0\n"
    "v_mov_b32 v120, 0\n"
    "v_mov_b32 v121, 0\n"
    "v_mov_b32 v122, 0\n"
    "v_mov_b32 v123, 0\n"
    "v_mov_b32 v88, 0\n"
    "v_mov_b32 v89, 0\n"
    "v_mov_b32 v90, 0\n"
    "v_mov_b32 v91, 0\n"
    "v_mov_b32 v92, 0\n"
    "v_mov_b32 v93, 0\n"
    "v_mov_b32 v94, 0\n"
    "v_mov_b32 v95, 0\n"
    "v_mov_b32 v124, %[kaddr]\n"
    "v_mov_b32 v125, %[vaddr]\n"
    "v_add_u32 v126, 0x4000, v124\n"
    "v_and_b32 v127, 0x4000, %[vsel]\n"
    "v_add_u32 v127, v127, v125\n"
    "s_waitcnt vmcnt(2) lgkmcnt(0)\n"
    "s_barrier\n"
    "ds_read_b128 v[100:103], v124\n"
    "ds_read_b128 v[104:107], v124 offset:1024\n"
    "ds_read_b128 v[108:111], v125\n"
    "ds_read_b128 v[112:115], v125 offset:512\n"
    "s_mov_b32 %[cnt], 0\n"
    "s_waitcnt lgkmcnt(0)\n"
    "v_mfma_f32_32x32x16_bf16 v[24:39], v[100:103], v[96:99], v[72:87]\n"
    "s_nop 15\n"
    ".Lloop%=:\n"
    "v_exp_f32 v24, v24\n"
    "v_exp_f32 v25, v25\n"
    "v_mfma_f32_32x32x16_bf16 v[56:71], v[120:123], v[92:95], v[56:71]\n"
    "v_exp_f32 v26, v26\n"
    "v_exp_f32 v27, v27\n"
    "v_cvt_pk_bf16_f32 v88, v24, v25\n"
    "v_exp_f32 v28, v28\n"
    "s_waitcnt lgkmcnt(0)\n"
    "v_mfma_f32_32x32x16_bf16 v[40:55], v[104:107], v[96:99], v[72:87]\n"
    "v_exp_f32 v29, v29\n"
    "v_cvt_pk_bf16_f32 v89, v26, v27\n"
    "ds_read_b128 v[100:103], v124 offset:2048\n"
    "v_exp_f32 v30, v30\n"
    "ds_read_b128 v[116:119], v125 offset:1024\n"
    "v_exp_f32 v31, v31\n"
    "v_cvt_pk_bf16_f32 v90, v28, v29\n"
    "ds_read_b128 v[120:123], v125 offset:1536\n"
    "v_exp_f32 v32, v32\n"
    "v_exp_f32 v33, v33\n"
    "v_cvt_pk_bf16_f32 v91, v30, v31\n"
    "v_exp_f32 v34, v34\n"
    "v_exp_f32 v35, v35\n"
    "v_mfma_f32_32x32x16_bf16 v[56:71], v[108:111], v[88:91], v[56:71]\n"
    "v_cvt_pk_bf16_f32 v92, v32, v33\n"
    "v_exp_f32 v36, v36\n"
    "v_exp_f32 v37, v37\n"
    "v_cvt_pk_bf16_f32 v93, v34, v35\n"
    "v_exp_f32 v38, v38\n"
    "v_exp_f32 v39, v39\n"
    "v_cvt_pk_bf16_f32 v94, v36, v37\n"
    "v_cvt_pk_bf16_f32 v95, v38, v39\n"
    "v_exp_f32 v40, v40\n"
    "v_exp_f32 v41, v41\n"
    "v_mfma_f32_32x32x16_bf16 v[56:71], v[112:115], v[92:95], v[56:71]\n"
    "v_exp_f32 v42, v42\n"
    "v_exp_f32 v43, v43\n"
    "v_cvt_pk_bf16_f32 v88, v40, v41\n"
    "v_exp_f32 v44, v44\n"
    "s_waitcnt lgkmcnt(0)\n"
    "v_mfma_f32_32x32x16_bf16 v[24:39], v[100:103], v[96:99], v[72:87]\n"
    "v_exp_f32 v45, v45\n"
    "v_cvt_pk_bf16_f32 v89, v42, v43\n"
    "ds_read_b128 v[104:107], v124 offset:3072\n"
    "v_exp_f32 v46, v46\n"
    "ds_read_b128 v[108:111], v125 offset:2048\n"
    "v_exp_f32 v47, v47\n"
    "v_cvt_pk_bf16_f32 v90, v44, v45\n"
    "ds_read_b128 v[112:115], v125 offset:2560\n"
    "v_exp_f32 v48, v48\n"
    "v_exp_f32 v49, v49\n"
    "v_cvt_pk_bf16_f32 v91, v46, v47\n"
    "v_exp_f32 v50, v50\n"
    "v_exp_f32 v51, v51\n"
    "v_mfma_f32_32x32x16_bf16 v[56:71], v[116:119], v[88:91], v[56:71]\n"
    "v_cvt_pk_bf16_f32 v92, v48, v49\n"
    "v_exp_f32 v52, v52\n"
    "v_exp_f32 v53, v53\n"
    "v_cvt_pk_bf16_f32 v93, v50, v51\n"
    "v_exp_f32 v54, v54\n"
    "v_exp_f32 v55, v55\n"
    "v_cvt_pk_bf16_f32 v94, v52, v53\n"
    "v_cvt_pk_bf16_f32 v95, v54, v55\n"
    "v_exp_f32 v24, v24\n"
    "v_exp_f32 v25, v25\n"
    "v_mfma_f32_32x32x16_bf16 v[56:71], v[120:123], v[92:95], v[56:71]\n"
    "v_exp_f32 v26, v26\n"
    "v_exp_f32 v27, v27\n"
    "v_cvt_pk_bf16_f32 v88, v24, v25\n"
    "v_exp_f32 v28, v28\n"
    "s_waitcnt lgkmcnt(0)\n"
    "v_mfma_f32_32x32x16_bf16 v[40:55], v[104:107], v[96:99], v[72:87]\n"
    "v_exp_f32 v29, v29\n"
    "v_cvt_pk_bf16_f32 v89, v26, v27\n"
    "ds_read_b128 v[100:103], v124 offset:4096\n"
    "v_exp_f32 v30, v30\n"
    "ds_read_b128 v[116:119], v125 offset:3072\n"
    "v_exp_f32 v31, v31\n"
    "v_cvt_pk_bf16_f32 v90, v28, v29\n"
    "ds_read_b128 v[120:123], v125 offset:3584\n"
    "v_exp_f32 v32, v32\n"
    "v_exp_f32 v33, v33\n"
    "v_cvt_pk_bf16_f32 v91, v30, v31\n"
    "v_exp_f32 v34, v34\n"
    "v_exp_f32 v35, v35\n"
    "v_mfma_f32_32x32x16_bf16 v[56:71], v[108:111], v[88:91], v[56:71]\n"
    "v_cvt_pk_bf16_f32 v92, v32, v33\n"
    "v_exp_f32 v36, v36\n"
    "v_exp_f32 v37, v37\n"
    "v_cvt_pk_bf16_f32 v93, v34, v35\n"
    "v_exp_f32 v38, v38\n"
    "v_exp_f32 v39, v39\n"
    "v_cvt_pk_bf16_f32 v94, v36, v37\n"
    "v_cvt_pk_bf16_f32 v95, v38, v39\n"
    "v_exp_f32 v40, v40\n"
    "v_exp_f32 v41, v41\n"
    "v_mfma_f32_32x32x16_bf16 v[56:71], v[112:115], v[92:95], v[56:71]\n"
    "v_exp_f32 v42, v42\n"
    "v_exp_f32 v43, v43\n"
    "v_cvt_pk_bf16_f32 v88, v40, v41\n"
    "v_exp_f32 v44, v44\n"
    "s_waitcnt lgkmcnt(0)\n"
    "v_mfma_f32_32x32x16_bf16 v[24:39], v[100:103], v[96:99], v[72:87]\n"
    "v_exp_f32 v45, v45\n"
    "v_cvt_pk_bf16_f32 v89, v42, v43\n"
    "ds_read_b128 v[104:107], v124 offset:5120\n"
    "v_exp_f32 v46, v46\n"
    "ds_read_b128 v[108:111], v125 offset:4096\n"
    "v_exp_f32 v47, v47\n"
    "v_cvt_pk_bf16_f32 v90, v44, v45\n"
    "ds_read_b128 v[112:115], v125 offset:4608\n"
    "v_exp_f32 v48, v48\n"
    "v_exp_f32 v49, v49\n"
    "v_cvt_pk_bf16_f32 v91, v46, v47\n"
    "v_exp_f32 v50, v50\n"
    "v_exp_f32 v51, v51\n"
    "v_mfma_f32_32x32x16_bf16 v[56:71], v[116:119], v[88:91], v[56:71]\n"
    "v_cvt_pk_bf16_f32 v92, v48, v49\n"
    "v_exp_f32 v52, v52\n"
    "v_exp_f32 v53, v53\n"
    "v_cvt_pk_bf16_f32 v93, v50, v51\n"
    "v_exp_f32 v54, v54\n"
    "v_exp_f32 v55, v55\n"
    "v_cvt_pk_bf16_f32 v94, v52, v53\n"
    "v_cvt_pk_bf16_f32 v95, v54, v55\n"
    "v_exp_f32 v24, v24\n"
    "v_exp_f32 v25, v25\n"
    "v_mfma_f32_32x32x16_bf16 v[56:71], v[120:123], v[92:95], v[56:71]\n"
    "v_exp_f32 v26, v26\n"
    "v_exp_f32 v27, v27\n"
    "v_cvt_pk_bf16_f32 v88, v24, v25\n"
    "v_exp_f32 v28, v28\n"
    "s_waitcnt lgkmcnt(0)\n"
    "v_mfma_f32_32x32x16_bf16 v[40:55], v[104:107], v[96:99], v[72:87]\n"
    "v_exp_f32 v29, v29\n"
    "v_cvt_pk_bf16_f32 v89, v26, v27\n"
    "ds_read_b128 v[100:103], v124 offset:6144\n"
    "v_exp_f32 v30, v30\n"
    "ds_read_b128 v[116:119], v125 offset:5120\n"
    "v_exp_f32 v31, v31\n"
    "v_cvt_pk_bf16_f32 v90, v28, v29\n"
    "ds_read_b128 v[120:123], v125 offset:5632\n"
    "v_exp_f32 v32, v32\n"
    "v_exp_f32 v33, v33\n"
    "v_cvt_pk_bf16_f32 v91, v30, v31\n"
    "v_exp_f32 v34, v34\n"
    "v_exp_f32 v35, v35\n"
    "v_mfma_f32_32x32x16_bf16 v[56:71], v[108:111], v[88:91], v[56:71]\n"
    "v_cvt_pk_bf16_f32 v92, v32, v33\n"
    "v_exp_f32 v36, v36\n"
    "v_exp_f32 v37, v37\n"
    "v_cvt_pk_bf16_f32 v93, v34, v35\n"
    "v_exp_f32 v38, v38\n"
    "v_exp_f32 v39, v39\n"
    "v_cvt_pk_bf16_f32 v94, v36, v37\n"
    "v_cvt_pk_bf16_f32 v95, v38, v39\n"
    "v_exp_f32 v40, v40\n"
    "v_exp_f32 v41, v41\n"
    "v_mfma_f32_32x32x16_bf16 v[56:71], v[112:115], v[92:95], v[56:71]\n"
    "v_exp_f32 v42, v42\n"
    "v_exp_f32 v43, v43\n"
    "v_cvt_pk_bf16_f32 v88, v40, v41\n"
    "v_exp_f32 v44, v44\n"
    "s_waitcnt lgkmcnt(0)\n"
    "v_mfma_f32_32x32x16_bf16 v[24:39], v[100:103], v[96:99], v[72:87]\n"
    "v_exp_f32 v45, v45\n"
    "v_cvt_pk_bf16_f32 v89, v42, v43\n"
    "ds_read_b128 v[104:107], v124 offset:7168\n"
    "v_exp_f32 v46, v46\n"
    "ds_read_b128 v[108:111], v125 offset:6144\n"
    "v_exp_f32 v47, v47\n"
    "v_cvt_pk_bf16_f32 v90, v44, v45\n"
    "ds_read_b128 v[112:115], v125 offset:6656\n"
    "v_exp_f32 v48, v48\n"
    "v_exp_f32 v49, v49\n"
    "v_cvt_pk_bf16_f32 v91, v46, v47\n"
    "v_exp_f32 v50, v50\n"
    "v_exp_f32 v51, v51\n"
    "v_mfma_f32_32x32x16_bf16 v[56:71], v[116:119], v[88:91], v[56:71]\n"
    "v_cvt_pk_bf16_f32 v92, v48, v49\n"
    "v_exp_f32 v52, v52\n"
    "v_exp_f32 v53, v53\n"
    "v_cvt_pk_bf16_f32 v93, v50, v51\n"
    "v_exp_f32 v54, v54\n"
    "v_exp_f32 v55, v55\n"
    "v_cvt_pk_bf16_f32 v94, v52, v53\n"
    "v_cvt_pk_bf16_f32 v95, v54, v55\n"
    "v_exp_f32 v24, v24\n"
    "v_exp_f32 v25, v25\n"
    "v_mfma_f32_32x32x16_bf16 v[56:71], v[120:123], v[92:95], v[56:71]\n"
    "v_exp_f32 v26, v26\n"
    "v_exp_f32 v27, v27\n"
    "v_cvt_pk_bf16_f32 v88, v24, v25\n"
    "v_exp_f32 v28, v28\n"
    "s_waitcnt lgkmcnt(0)\n"
    "v_mfma_f32_32x32x16_bf16 v[40:55], v[104:107], v[96:99], v[72:87]\n"
    "v_exp_f32 v29, v29\n"
    "v_cvt_pk_bf16_f32 v89, v26, v27\n"
    "s_waitcnt vmcnt(0)\n"
    "s_barrier\n"
    "ds_read_b128 v[100:103], v126 offset:0\n"
    "v_exp_f32 v30, v30\n"
    "ds_read_b128 v[116:119], v125 offset:7168\n"
    "v_exp_f32 v31, v31\n"
    "v_cvt_pk_bf16_f32 v90, v28, v29\n"
    "ds_read_b128 v[120:123], v125 offset:7680\n"
    "v_exp_f32 v32, v32\n"
    "v_exp_f32 v33, v33\n"
    "v_cvt_pk_bf16_f32 v91, v30, v31\n"
    "v_exp_f32 v34, v34\n"
    "v_exp_f32 v35, v35\n"
    "v_mfma_f32_32x32x16_bf16 v[56:71], v[108:111], v[88:91], v[56:71]\n"
    "s_cmp_ge_u32 %[cnt], 6\n"
    "s_cbranch_scc1 .Lnodma%=\n"
    "s_mov_b32 m0, %[dslot]\n"
    "s_nop 0\n"
    "global_load_lds_dwordx4 %[goff], %[gbase] offset:0\n"
    "global_load_lds_dwordx4 %[goff], %[gbase] offset:1024\n"
    ".Lnodma%=:\n"
    "v_cvt_pk_bf16_f32 v92, v32, v33\n"
    "v_exp_f32 v36, v36\n"
    "v_exp_f32 v37, v37\n"
    "v_cvt_pk_bf16_f32 v93, v34, v35\n"
    "v_exp_f32 v38, v38\n"
    "v_exp_f32 v39, v39\n"
    "v_cvt_pk_bf16_f32 v94, v36, v37\n"
    "v_cvt_pk_bf16_f32 v95, v38, v39\n"
    "v_exp_f32 v40, v40\n"
    "v_exp_f32 v41, v41\n"
    "v_mfma_f32_32x32x16_bf16 v[56:71], v[112:115], v[92:95], v[56:71]\n"
    "v_exp_f32 v42, v42\n"
    "v_exp_f32 v43, v43\n"
    "v_cvt_pk_bf16_f32 v88, v40, v41\n"
    "v_exp_f32 v44, v44\n"
    "s_waitcnt lgkmcnt(0)\n"
    "v_mfma_f32_32x32x16_bf16 v[24:39], v[100:103], v[96:99], v[72:87]\n"
    "v_exp_f32 v45, v45\n"
    "v_cvt_pk_bf16_f32 v89, v42, v43\n"
    "ds_read_b128 v[104:107], v126 offset:1024\n"
    "v_exp_f32 v46, v46\n"
    "ds_read_b128 v[108:111], v127 offset:0\n"
    "v_exp_f32 v47, v47\n"
    "v_cvt_pk_bf16_f32 v90, v44, v45\n"
    "ds_read_b128 v[112:115], v127 offset:512\n"
    "v_exp_f32 v48, v48\n"
    "v_exp_f32 v49, v49\n"
    "v_cvt_pk_bf16_f32 v91, v46, v47\n"
    "v_exp_f32 v50, v50\n"
    "v_exp_f32 v51, v51\n"
    "v_mfma_f32_32x32x16_bf16 v[56:71], v[116:119], v[88:91], v[56:71]\n"
    "v_cvt_pk_bf16_f32 v92, v48, v49\n"
    "v_exp_f32 v52, v52\n"
    "v_exp_f32 v53, v53\n"
    "v_cvt_pk_bf16_f32 v93, v50, v51\n"
    "v_exp_f32 v54, v54\n"
    "v_exp_f32 v55, v55\n"
    "v_cvt_pk_bf16_f32 v94, v52, v53\n"
    "v_cvt_pk_bf16_f32 v95, v54, v55\n"
    "v_mov_b32 v124, v126\n"
    "v_mov_b32 v125, v127\n"
    "s_add_u32 %[nslot], %[nslot], 0x4000\n"
    "s_cmp_eq_u32 %[nslot], 0xc000\n"
    "s_cselect_b32 %[nslot], 0, %[nslot]\n"
    "v_add_u32 v126, %[nslot], %[kaddr]\n"
    "v_and_b32 v127, %[nslot], %[vsel]\n"
    "v_add_u32 v127, v127, %[vaddr]\n"
    "s_add_u32 %[dslot], %[dslot], 0x4000\n"
    "s_cmp_eq_u32 %[dslot], %[dend]\n"
    "s_cselect_b32 %[dslot], %[dbeg], %[dslot]\n"
    "v_add_u32 %[goff], 0x4000, %[goff]\n"
    "s_add_u32 %[cnt], %[cnt], 1\n"
    "s_cmp_lt_u32 %[cnt], 8\n"
    "s_cbranch_scc1 .Lloop%=\n"
    "s_nop 1\n"
    "v_mfma_f32_32x32x16_bf16 v[56:71], v[120:123], v[92:95], v[56:71]\n"
    "s_nop 15\n"
    "s_nop 3\n"
    "v_rcp_f32 v24, v64\n"
    "s_nop 0\n"
    "v_mul_f32 v56, v56, v24\n"
    "v_mul_f32 v57, v57, v24\n"
    "v_mul_f32 v58, v58, v24\n"
    "v_mul_f32 v59, v59, v24\n"
    "v_mul_f32 v60, v60, v24\n"
    "v_mul_f32 v61, v61, v24\n"
    "v_mul_f32 v62, v62, v24\n"
    "v_mul_f32 v63, v63, v24\n"
    "v_cvt_pk_bf16_f32 %[oa0], v56, v57\n"
    "v_cvt_pk_bf16_f32 %[oa1], v58, v59\n"
    "v_cvt_pk_bf16_f32 %[oa2], v60, v61\n"
    "v_cvt_pk_bf16_f32 %[oa3], v62, v63\n"
    "s_waitcnt lgkmcnt(0)\n"
    "s_mov_b32 m0, %[m0save]\n"
        : [oa0] "=&v"(oa0), [oa1] "=&v"(oa1), [oa2] "=&v"(oa2), [oa3] "=&v"(oa3),
          [goff] "+v"(goff), [dslot] "+s"(dslot), [nslot] "+s"(nslot), [cnt] "=&s"(cnt), [m0save] "=&s"(m0save)
        : [qa0] "v"(qa[0]), [qa1] "v"(qa[1]), [qa2] "v"(qa[2]), [qa3] "v"(qa[3]),
          [nma] "v"(nma), [kaddr] "v"(kaddr), [vaddr] "v"(vaddr), [vsel] "v"(vsel), [gbase] "s"(kvb), [dbeg] "s"(dbeg), [dend] "s"(dend), [psel] "s"(0x07060302u), [vmask] "s"(0x0000ffff0000ffffull), [vone] "v"(((lane & 27) == 16) ? 0x3f803f80u : 0u), [dbgaddr] "s"(lds0 + A_LDS + wid * 64)
        : "memory", "scc", "vcc",
          "v24", "v25", "v26", "v27", "v28", "v29", "v30", "v31", "v32", "v33", "v34", "v35", "v36", "v37", "v38", "v39", "v40", "v41", "v42", "v43", "v44", "v45", "v46", "v47", "v48", "v49", "v50", "v51", "v52", "v53", "v54", "v55", "v56", "v57", "v58", "v59", "v60", "v61", "v62", "v63", "v64", "v65", "v66", "v67", "v68", "v69", "v70", "v71", "v72", "v73", "v74", "v75", "v76", "v77", "v78", "v79", "v80", "v81", "v82", "v83", "v84", "v85", "v86", "v87", "v88", "v89", "v90", "v91", "v92", "v93", "v94", "v95", "v96", "v97", "v98", "v99", "v100", "v101", "v102", "v103", "v104", "v105", "v106", "v107", "v108", "v109", "v110", "v111", "v112", "v113", "v114", "v115", "v116", "v117", "v118", "v119", "v120", "v121", "v122", "v123", "v124", "v125", "v126", "v127"
    );
    STAMP(4);
    st16_wt(ws + WS_OF + ((size_t)tile0 * 32 + head) * 1024 + lane * 16, (u32x4){oa0, oa1, oa2, oa3});
    PUBLISH(FL_O + jb * 32 + head);
}

__device__ __forceinline__ void z_role(const uint16_t* __restrict__ x, const uint16_t* __restrict__ win, char* __restrict__ ws, char* lds, int g, u64& tag) {
    const int tid = threadIdx.x, lane = tid & 63, wid = __builtin_amdgcn_readfirstlane(tid >> 6), wr = wid >> 2, wc = wid & 3, l31 = lane & 31, hh = lane >> 5;
    const int xcd = g & 7, i = g >> 3;
    const int rb = 4 * (xcd >> 1) + (i & 3), cbz = 7 * ((xcd & 1) ^ 1) + (i >> 2);
    f32x16 acc[2];
    proj_main<false>(x, win, NIN, rb, cbz * 128, 12 + cbz, ws, lds, ((i & 3) * 4 + (i >> 2) * 3) & 15, tag, acc);
    STAMP(1);
    const float* st = (const float*)(lds + P_ST);
    unsigned zp[2][8];
#pragma unroll
    for (int rt = 0; rt < 2; ++rt) { const int tr = wr * 64 + rt * 32; const float mu = st[tr + l31], rs = st[128 + tr + l31];
#pragma unroll
        for (int q = 0; q < 4; ++q) { const f32x4 c = *(const f32x4*)(st + 256 + wc * 32 + 8 * q + 4 * hh);
            zp[rt][2 * q] = cvtpk(gelu_t((acc[rt][4 * q] - mu * c[0]) * rs), gelu_t((acc[rt][4 * q + 1] - mu * c[1]) * rs));
            zp[rt][2 * q + 1] = cvtpk(gelu_t((acc[rt][4 * q + 2] - mu * c[2]) * rs), gelu_t((acc[rt][4 * q + 3] - mu * c[3]) * rs)); } }
    if (cbz < 7) {
#pragma unroll
        for (int rt = 0; rt < 2; ++rt) { const int tt = rb * 4 + wr * 2 + rt;
            char* dst = ws + WS_Z1 + ((size_t)tt * 28 + cbz * 4 + wc) * 2048 + lane * 32;
            st16_wt(dst, (u32x4){zp[rt][0], zp[rt][1], zp[rt][2], zp[rt][3]}); st16_wt(dst + 16, (u32x4){zp[rt][4], zp[rt][5], zp[rt][6], zp[rt][7]}); }
    } else {
        const int cj = cbz - 7;
        float* pst = (float*)lds;
        const unsigned ONES = 0x3f803f80u;
#pragma unroll
        for (int rt = 0; rt < 2; ++rt) { float s1 = 0.f, s2 = 0.f;
#pragma unroll
            for (int e = 0; e < 8; ++e) { s1 = dot2(zp[rt][e], ONES, s1); s2 = dot2(zp[rt][e], zp[rt][e], s2); }
            s1 = swap32_add(s1); s2 = swap32_add(s2);
            if (hh == 0) *(f32x2*)(pst + (wc * 128 + wr * 64 + rt * 32 + l31) * 2) = (f32x2){s1, s2}; }
        __syncthreads();
        char* gst = ws + WS_ST + (size_t)rb * 7 * 2048;
        float* fin = (float*)(lds + 4096);
        if (tid < 128) { f32x2 s = *(const f32x2*)(pst + tid * 2);
#pragma unroll
            for (int j = 1; j < 4; ++j) s += *(const f32x2*)(pst + (j * 128 + tid) * 2);
            st16_wt(gst + (cj * 128 + tid) * 16, (u32x4){__float_as_uint(s[0]), __float_as_uint(s[1]), (unsigned)tag, (unsigned)(tag >> 32)});
            u32x4 gr[7];
            for (unsigned spins = 0; spins < (1u << 18); ++spins) {
                asm volatile("global_load_dwordx4 %0, %7, off sc1\n\tglobal_load_dwordx4 %1, %7, off offset:2048 sc1\n\tglobal_load_dwordx4 %2, %8, off sc1\n\tglobal_load_dwordx4 %3, %8, off offset:2048 sc1\n\t"
                             "global_load_dwordx4 %4, %9, off sc1\n\tglobal_load_dwordx4 %5, %9, off offset:2048 sc1\n\tglobal_load_dwordx4 %6, %10, off sc1\n\ts_waitcnt vmcnt(0)"
                             : "=&v"(gr[0]), "=&v"(gr[1]), "=&v"(gr[2]), "=&v"(gr[3]), "=&v"(gr[4]), "=&v"(gr[5]), "=&v"(gr[6])
                             : "v"(gst + tid * 16), "v"(gst + 4096 + tid * 16), "v"(gst + 8192 + tid * 16), "v"(gst + 12288 + tid * 16) : "memory");
                bool ok = true;
#pragma unroll
                for (int j = 0; j < 7; ++j) ok &= gr[j][2] == (unsigned)tag && gr[j][3] == (unsigned)(tag >> 32);
                if (__all(ok)) break;
                __builtin_amdgcn_s_sleep(POLL_SLEEP); }
            float s1 = 0.f, s2 = 0.f;
#pragma unroll
            for (int j = 0; j < 7; ++j) { s1 += __uint_as_float(gr[j][0]); s2 += __uint_as_float(gr[j][1]); }
            const float mu = s1 * (1.0f / FH); const float var = fmaxf(s2 * (1.0f / FH) - mu * mu, 0.f);
            fin[tid] = mu; fin[128 + tid] = __builtin_amdgcn_rsqf(var + LN_EPS); }
        __syncthreads();
        char* img = lds + 8192;
#pragma unroll
        for (int rt = 0; rt < 2; ++rt) { const int tok = wr * 64 + rt * 32 + l31; const float mu = fin[tok], rs = fin[128 + tok];
#pragma unroll
            for (int q = 0; q < 4; ++q) { const unsigned a = zp[rt][2 * q], c = zp[rt][2 * q + 1];
                const u32x2 o = {cvtpk((bflo(a) - mu) * rs, (bfhi(a) - mu) * rs), cvtpk((bflo(c) - mu) * rs, (bfhi(c) - mu) * rs)};
                const int f = wc * 32 + 8 * q + 4 * hh;
                *(u32x2*)(img + tok * 256 + (((f >> 3) ^ wsw(tok)) * 16) + (f & 4) * 2) = o; } }
        __syncthreads();
        { const int blk = (lane >> 4) & 1, q = (lane & 15) >> 2, p = lane & 3;
#pragma unroll
          for (int kq = 0; kq < 4; ++kq) { const int ks = wr * 4 + kq; s16x4 t2[2];
#pragma unroll
              for (int t = 0; t < 2; ++t) { const int kr = 16 * ks + 8 * hh + 4 * t + q; const int ch = 4 * wc + 2 * blk + (p >> 1);
                  t2[t] = trread((lds_cptr)img + kr * 256 + ((ch ^ wsw(kr)) * 16) + 8 * (p & 1)); }
              st16_wt(ws + WS_Z2N + ((size_t)(cj * 4 + wc) * 128 + rb * 8 + ks) * 1024 + lane * 16, __builtin_bit_cast(u32x4, cat(t2[0], t2[1]))); } }
    }
    PUBLISH(FL_Z + rb * 14 + cbz);
}

constexpr int S_SLOT = 24576, S_LDS = 3 * S_SLOT;
__device__ __forceinline__ void spatial_role(const uint16_t* __restrict__ wsp, const uint16_t* __restrict__ bsp, char* __restrict__ ws, char* lds, int g, u64 tag) {
    const int tid = threadIdx.x, lane = tid & 63, wid8 = __builtin_amdgcn_readfirstlane(tid >> 6), wid = wid8 & 3, l31 = lane & 31, hh = lane >> 5;
    const int i = g >> 3, tb = 4 * (g & 7) + (i & 3), fb = i >> 2;
    const unsigned lds0 = (unsigned)(uintptr_t)lds;
    STAMP(3);
    if (wid8 >= 4) {
        const int r0 = 16 * wid + (lane >> 3), r1 = r0 + 8;
        const char* sb_ = (const char*)(wsp + (size_t)tb * 64 * SEQ);
        const unsigned vo0 = (r0 * SEQ + ((lane & 7) ^ ((r0 >> 1) & 7)) * 8) * 2, vo1 = (r1 * SEQ + ((lane & 7) ^ ((r1 >> 1) & 7)) * 8) * 2;
        const char* zs = ws + WS_Z2N + (size_t)(fb * 4 + wid) * 131072; const unsigned zvo = lane * 16;
        unsigned ready = 0;
        GAS u64* zf = flagp(ws, FL_Z + (lane & 15) * 14 + 7 + fb);
        auto need = [&](int rbk) {
            for (unsigned spins = 0; !((ready >> rbk) & 1) && spins < (1u << 18); ++spins) {
                ready = (unsigned)__ballot(__hip_atomic_load(zf, RLX_AGENT) == tag) & 0xffffu;
                if (!((ready >> rbk) & 1)) __builtin_amdgcn_s_sleep(POLL_SLEEP); } };
        auto issue = [&](int it, int slot) { const unsigned so = lds0 + slot * S_SLOT;
            need(it >> 1);
            set_m0(so + wid * 2048); glds_s<0, false>(sb_ + it * 128, vo0);
            set_m0(so + wid * 2048 + 1024); glds_s<0, false>(sb_ + it * 128, vo1);
            set_m0(so + 8192 + wid * 4096);
            glds_s<0, true>(zs + it * 4096, zvo); glds_s<1024, true>(zs + it * 4096, zvo); glds_s<2048, true>(zs + it * 4096, zvo); glds_s<3072, true>(zs + it * 4096, zvo); };
        issue(0, 0); issue(1, 1); issue(2, 2);
        WAIT_BAR(12);
        int slot = 0;
#pragma unroll 1
        for (int it = 1; it < 31; ++it) { WAIT_BAR(6); if (it + 2 < 32) issue(it + 2, slot); slot = slot == 2 ? 0 : slot + 1; }
        WAIT_BAR(0);
        BAR();
    } else {
        unsigned boff[2][4];
#pragma unroll
        for (int rt = 0; rt < 2; ++rt)
#pragma unroll
            for (int ks = 0; ks < 4; ++ks) { const int r = rt * 32 + l31; boff[rt][ks] = (r * 128 + ((hh ^ ((r >> 1) & 7)) * 16)) ^ (ks * 32); }
        const unsigned aoff = 8192 + wid * 4096 + lane * 16;
        struct SF { bf16x8 a, b[2]; };
        auto rd = [&](SF& f, int slot, int ks) { lds_cptr sb = (lds_cptr)lds + slot * S_SLOT;
            f.a = ldsfrag(sb + aoff + ks * 1024); f.b[0] = ldsfrag(sb + boff[0][ks]); f.b[1] = ldsfrag(sb + boff[1][ks]); };
        f32x16 acc[2] = {f32x16{}, f32x16{}};
        auto mm = [&](const SF& f) { acc[0] = MFMA(f.a, f.b[0], acc[0]); acc[1] = MFMA(f.a, f.b[1], acc[1]); };
        SF f[4];
        BAR();
#pragma unroll
        for (int ks = 0; ks < 4; ++ks) rd(f[ks], 0, ks);
        auto step = [&](int nsl) {
            LGKM_BAR();
#pragma unroll
            for (int ks = 0; ks < 4; ++ks) { lds_cptr sb = (lds_cptr)lds + nsl * S_SLOT;
                acc[0] = MFMA(f[ks].a, f[ks].b[0], acc[0]); SBAR();
                f[ks].b[0] = ldsfrag(sb + boff[0][ks]); SBAR();
                PACE();
                acc[1] = MFMA(f[ks].a, f[ks].b[1], acc[1]); SBAR();
                f[ks].b[1] = ldsfrag(sb + boff[1][ks]); f[ks].a = ldsfrag(sb + aoff + ks * 1024); SBAR();
                PACE(); } };
#pragma unroll 1
        for (int io = 0; io < 10; ++io) { step(1); step(2); step(0); }
        step(1); step(2);
        wait_flags(flagp(ws, FL_Z + (tb >> 1) * 14 + fb), lane == 0, tag);
#pragma unroll
        for (int rt = 0; rt < 2; ++rt) { const int tt = tb * 2 + rt; const float bias = __uint_as_float((unsigned)bsp[tt * 32 + l31] << 16);
            const char* z1p = ws + WS_Z1 + ((size_t)tt * 28 + fb * 4 + wid) * 2048 + lane * 32;
            const u32x4 za = ld16_wt(z1p), zb = ld16_wt(z1p + 16);
            u32x4 ua, ub;
#pragma unroll
            for (int e = 0; e < 4; ++e) {
                ua[e] = cvtpk(bflo(za[e]) * (acc[rt][2 * e] + bias), bfhi(za[e]) * (acc[rt][2 * e + 1] + bias));
                ub[e] = cvtpk(bflo(zb[e]) * (acc[rt][8 + 2 * e] + bias), bfhi(zb[e]) * (acc[rt][8 + 2 * e + 1] + bias)); }
            char* ud = ws + WS_U + ((size_t)tt * 56 + (fb * 4 + wid) * 2) * 1024 + lane * 16;
            st16_wt(ud, ua); st16_wt(ud + 1024, ub); }
    }
    PUBLISH(FL_U + tb * 7 + fb);
}

constexpr int F_SLOT = 8192 + 8192, F_LDS = 4 * F_SLOT;
__device__ __forceinline__ void final_role(const uint16_t* __restrict__ x, const uint16_t* __restrict__ wo, const uint16_t* __restrict__ wout, char* __restrict__ ws, uint16_t* __restrict__ out, char* lds, int g, u64 tag) {
    const int tb = 4 * (g & 7) + ((g >> 3) & 3), db = g >> 5;
    const int tid = threadIdx.x, lane = tid & 63, wid8 = __builtin_amdgcn_readfirstlane(tid >> 6), wid = wid8 & 3, wr = wid >> 1, wc = wid & 1, l31 = lane & 31, hh = lane >> 5;
    const unsigned lds0 = (unsigned)(uintptr_t)lds;
    auto wswz = [](int r) { return ((r & 2) << 1) | ((r >> 2) & 3); };
    f32x16 as = f32x16{}, ag = f32x16{};
    const int k0 = 16 * wid + (lane >> 3), k1 = k0 + 8;
    const unsigned wo0 = (k0 * DM + db * 64 + ((lane & 7) ^ wswz(k0)) * 8) * 2, wo1 = (k1 * DM + db * 64 + ((lane & 7) ^ wswz(k1)) * 8) * 2;
    const int att = tb * 2 + (wid & 1), aks = 2 * (wid >> 1);
    unsigned woff[2];
    { const int blk = (lane >> 4) & 1, q = (lane & 15) >> 2, p = lane & 3; const int kr = 4 * hh + q; const int ch = 4 * wc + 2 * blk + (p >> 1);
      woff[0] = kr * 128 + ((ch ^ wswz(kr)) * 16) + 8 * (p & 1); woff[1] = (woff[0] + 1024) ^ 32; }
    const unsigned aoff = 8192 + wr * 4096 + lane * 16;
    struct FF { bf16x8 w, a; };
    auto rd = [&](FF& f, int slot, int ks) { lds_cptr sb = (lds_cptr)lds + slot * F_SLOT;
        f.w = cat(trread(sb + woff[0] + ks * 2048), trread(sb + woff[1] + ks * 2048)); f.a = ldsfrag(sb + aoff + ks * 1024); };
    auto pass = [&](const uint16_t* wb, const char* ab, int nfr, int n, f32x16& acc, bool pace) {
        if (wid8 >= 4) {
            auto issue = [&](int it) { const unsigned so = lds0 + (it & 3) * F_SLOT; const char* w = (const char*)(wb + (size_t)it * 64 * DM);
                set_m0(so + wid * 2048); glds_s<0, false>(w, wo0);
                set_m0(so + wid * 2048 + 1024); glds_s<0, false>(w, wo1);
                const char* a = ab + ((size_t)att * nfr + it * 4 + aks) * 1024;
                set_m0(so + 8192 + (wid & 1) * 4096 + aks * 1024); glds_s<0, true>(a, lane * 16); glds_s<1024, true>(a, lane * 16); };
            issue(0); issue(1); issue(2); issue(3);
            WAIT_BAR(12);
#pragma unroll 1
            for (int i = 1; i < n - 2; ++i) { WAIT_BAR(8); if (i + 3 < n) issue(i + 3); }
            WAIT_BAR(4);
            WAIT_BAR(0);
            BAR();
        } else {
            FF fa[4], fb[4];
            BAR();
#pragma unroll
            for (int ks = 0; ks < 4; ++ks) rd(fa[ks], 0, ks);
            auto step = [&](int nsl, FF (&cur)[4], FF (&nxt)[4]) {
                LGKM_BAR();
#pragma unroll
                for (int ks = 0; ks < 4; ++ks) { acc = MFMA(cur[ks].w, cur[ks].a, acc); SBAR(); rd(nxt[ks], nsl, ks); SBAR(); if (pace) PACE(); } };
#pragma unroll 1
            for (int it = 0; it < n; it += 2) { step((it + 1) & 3, fa, fb); step((it + 2) & 3, fb, fa); }
        } };
    if (wid8 == 0) wait_flags(flagp(ws, FL_U + tb * 7 + lane), lane < 7, tag);
    __syncthreads();
    STAMP(5);
    pass(wout, ws + WS_U, 56, 14, ag, true);
    STAMP(6);
    __syncthreads();
#pragma unroll
    for (int q = 0; q < 8; ++q) {
        const int kr = 8 * wid8 + (lane >> 3);
        set_m0(lds0 + q * 8192 + wid8 * 1024);
        glds_s<0, false>((const char*)(wo + (size_t)q * 64 * DM), (kr * DM + db * 64 + ((lane & 7) ^ wswz(kr)) * 8) * 2); }
    const int tok = tb * 64 + wr * 32 + l31;
    float gate[16]; u32x2 xv[4];
    if (wid8 < 4) {
#pragma unroll
        for (int q = 0; q < 4; ++q) xv[q] = *(const u32x2*)(x + (size_t)tok * DM + db * 64 + wc * 32 + 8 * q + 4 * hh);
#pragma unroll
        for (int r = 0; r < 16; ++r) gate[r] = silu_f(ag[r]); }
    if (wid8 == 0) wait_flags(flagp(ws, FL_O + (tb >> 2) * 32 + lane), lane < 32, tag);
    WAITV(0);
    __syncthreads();
    STAMP(7);
    const int kh = wid8 >> 2;
    u32x4 of[16];
    { const char* ob = ws + WS_OF + ((size_t)(tb * 2 + wr) * 32 + 16 * kh) * 1024; const unsigned v0 = lane * 16, v1 = v0 + 4096, v2 = v0 + 8192, v3 = v0 + 12288;
      asm volatile(
        "global_load_dwordx4 %0, %16, %20 sc1\n\tglobal_load_dwordx4 %1, %16, %20 offset:1024 sc1\n\tglobal_load_dwordx4 %2, %16, %20 offset:2048 sc1\n\tglobal_load_dwordx4 %3, %16, %20 offset:3072 sc1\n\t"
        "global_load_dwordx4 %4, %17, %20 sc1\n\tglobal_load_dwordx4 %5, %17, %20 offset:1024 sc1\n\tglobal_load_dwordx4 %6, %17, %20 offset:2048 sc1\n\tglobal_load_dwordx4 %7, %17, %20 offset:3072 sc1\n\t"
        "global_load_dwordx4 %8, %18, %20 sc1\n\tglobal_load_dwordx4 %9, %18, %20 offset:1024 sc1\n\tglobal_load_dwordx4 %10, %18, %20 offset:2048 sc1\n\tglobal_load_dwordx4 %11, %18, %20 offset:3072 sc1\n\t"
        "global_load_dwordx4 %12, %19, %20 sc1\n\tglobal_load_dwordx4 %13, %19, %20 offset:1024 sc1\n\tglobal_load_dwordx4 %14, %19, %20 offset:2048 sc1\n\tglobal_load_dwordx4 %15, %19, %20 offset:3072 sc1\n\t"
        "s_waitcnt vmcnt(0)"
        : "=&v"(of[0]), "=&v"(of[1]), "=&v"(of[2]), "=&v"(of[3]), "=&v"(of[4]), "=&v"(of[5]), "=&v"(of[6]), "=&v"(of[7]),
          "=&v"(of[8]), "=&v"(of[9]), "=&v"(of[10]), "=&v"(of[11]), "=&v"(of[12]), "=&v"(of[13]), "=&v"(of[14]), "=&v"(of[15])
        : "v"(v0), "v"(v1), "v"(v2), "v"(v3), "s"(ob) : "memory"); }
    { lds_cptr wb_ = (lds_cptr)lds + kh * 32768;
#pragma unroll
      for (int ks = 0; ks < 16; ++ks)
          as = MFMA(cat(trread(wb_ + woff[0] + ks * 2048), trread(wb_ + woff[1] + ks * 2048)), __builtin_bit_cast(bf16x8, of[ks]), as); }
    __syncthreads();
    if (kh == 1) {
#pragma unroll
        for (int q = 0; q < 4; ++q) *(f32x4*)(lds + (wid * 4 + q) * 1024 + lane * 16) = (f32x4){as[4 * q], as[4 * q + 1], as[4 * q + 2], as[4 * q + 3]}; }
    __syncthreads();
    STAMP(8);
    if (kh == 0) {
#pragma unroll
        for (int q = 0; q < 4; ++q) { const f32x4 p = *(const f32x4*)(lds + (wid * 4 + q) * 1024 + lane * 16);
            const float r0 = bflo(xv[q][0]) + (as[4 * q] + p[0]) * gate[4 * q], r1 = bfhi(xv[q][0]) + (as[4 * q + 1] + p[1]) * gate[4 * q + 1];
            const float r2 = bflo(xv[q][1]) + (as[4 * q + 2] + p[2]) * gate[4 * q + 2], r3 = bfhi(xv[q][1]) + (as[4 * q + 3] + p[3]) * gate[4 * q + 3];
            *(u32x2*)(out + (size_t)tok * DM + db * 64 + wc * 32 + 8 * q + 4 * hh) = (u32x2){cvtpk(r0, r1), cvtpk(r2, r3)}; }
    }
}

#define ADELAY 300
#define APRIO 3
#define GDELAY 0
#define GPRIO 2
constexpr int ALL_LDS = S_LDS;
__global__ __launch_bounds__(512, 4) void k_all(const uint16_t* __restrict__ x, const uint16_t* __restrict__ wqkv, const uint16_t* __restrict__ wo, const uint16_t* __restrict__ win,
                                                const uint16_t* __restrict__ wsp, const uint16_t* __restrict__ bsp, const uint16_t* __restrict__ wout, char* __restrict__ ws, uint16_t* __restrict__ out) {
    extern __shared__ __attribute__((aligned(16))) char lds[];
    const int b = blockIdx.x;
    u64 tag;
    STAMP(0);
    if (b < 256) {
        __builtin_amdgcn_s_setprio(APRIO);
        if (ADELAY > 0 && b < 192) { const u64 t0 = __builtin_amdgcn_s_memrealtime(); while (__builtin_amdgcn_s_memrealtime() - t0 < ADELAY) __builtin_amdgcn_s_sleep(8); }
        if (b < 192) qkv_role(x, wqkv, ws, lds, b, tag); else stats_role(x, wqkv, win, ws, lds, b - 192, tag);
        __builtin_amdgcn_s_setprio(0);
        __syncthreads();
        attn_role(ws, lds, b, tag);
        STAMP(5);
    } else {
        const int g = b - 256;
        __builtin_amdgcn_s_setprio(GPRIO);
            if (GDELAY > 0) { const u64 t0 = __builtin_amdgcn_s_memrealtime(); while (__builtin_amdgcn_s_memrealtime() - t0 < GDELAY) __builtin_amdgcn_s_sleep(8); }
        if (g < 224) { z_role(x, win, ws, lds, g, tag); STAMP(2); __syncthreads(); spatial_role(wsp, bsp, ws, lds, g, tag); STAMP(4); __syncthreads(); }
        else { u64 ep; EPOCH_REQ(ep);
            { const int wv = (g - 224) * 8 + (threadIdx.x >> 6), lane = threadIdx.x & 63; unsigned junk = 0;
#pragma unroll
              for (int q = 0; q < 4; ++q) asm volatile("global_load_dword %0, %1, off" : "+v"(junk) : "v"((const char*)wsp + ((size_t)(q * 256 + wv) * 64 + lane) * 128) : "memory");
              if (wv < 112) asm volatile("global_load_dword %0, %1, off" : "+v"(junk) : "v"((const char*)wout + ((size_t)wv * 64 + lane) * 128) : "memory");
              else if (wv < 176) asm volatile("global_load_dword %0, %1, off" : "+v"(junk) : "v"((const char*)wo + ((size_t)(wv - 112) * 64 + lane) * 128) : "memory");
              asm volatile("s_waitcnt vmcnt(0)" : "+v"(junk) :: "memory"); }
            asm volatile("s_waitcnt vmcnt(0)" : "+v"(ep) :: "memory"); tag = EPOCH_TAG(ep); }
        if (threadIdx.x == 0) __hip_atomic_store(flagp(ws, FL_G + g), tag, RLX_AGENT);
        final_role(x, wo, wout, ws, out, lds, g, tag);
        if (g == 255 && threadIdx.x < 64) {
            const int lane = threadIdx.x;
#pragma unroll 1
            for (int c = 0; c < 4; ++c) { wait_flags(flagp(ws, FL_O + c * 64 + lane), true, tag); wait_flags(flagp(ws, FL_G + c * 64 + lane), true, tag); }
            if (lane == 0) __hip_atomic_store(flagp(ws, FL_EPOCH), tag, RLX_AGENT);
        }
        STAMP(9);
    }
}

extern "C" void kernel_launch(void* const* d_in, const int* in_sizes, int n_in, void* d_out, int out_size, void* d_ws, size_t ws_size, hipStream_t stream) {
    const uint16_t* x = (const uint16_t*)d_in[0]; const uint16_t* wqkv = (const uint16_t*)d_in[1]; const uint16_t* wo = (const uint16_t*)d_in[2];
    const uint16_t* win = (const uint16_t*)d_in[4]; const uint16_t* wsp = (const uint16_t*)d_in[5]; const uint16_t* bsp = (const uint16_t*)d_in[6]; const uint16_t* wout = (const uint16_t*)d_in[7];
    (void)hipFuncSetAttribute((const void*)k_all, hipFuncAttributeMaxDynamicSharedMemorySize, ALL_LDS);
    k_all<<<512, 512, ALL_LDS, stream>>>(x, wqkv, wo, win, wsp, bsp, wout, (char*)d_ws, (uint16_t*)d_out);
}
```

```cpp
#include <hip/hip_runtime.h>
#include <hip/hip_bf16.h>
#include <stdint.h>
#define WARM_IN 1
#include <stdio.h>

typedef short bf16x8 __attribute__((ext_vector_type(8)));
typedef short s16x4 __attribute__((ext_vector_type(4)));
typedef __bf16 bf16x2_t __attribute__((ext_vector_type(2)));
typedef float f32x16 __attribute__((ext_vector_type(16)));
typedef float f32x4 __attribute__((ext_vector_type(4)));
typedef float f32x2 __attribute__((ext_vector_type(2)));
typedef unsigned u32x4 __attribute__((ext_vector_type(4)));
typedef unsigned u32x2 __attribute__((ext_vector_type(2)));
typedef unsigned long long u64;
#define GAS __attribute__((address_space(1)))
#define LAS __attribute__((address_space(3)))
typedef LAS const char* lds_cptr;
typedef short v4i16_t __attribute__((ext_vector_type(4)));
#define RLX_AGENT __ATOMIC_RELAXED, __HIP_MEMORY_SCOPE_AGENT

constexpr int SEQ = 2048, DM = 512, NQKV = 1536, NIN = 1792, FH = 896;
constexpr float QSCALE = 0.25f * 1.4426950408889634f;
constexpr float LN_EPS = 1e-5f;
constexpr size_t MB = 1024 * 1024;
constexpr size_t WS_QF = 0, WS_KV = 2 * MB, WS_Z1 = 6 * MB, WS_Z2N = 10 * MB, WS_U = 14 * MB, WS_OF = 18 * MB;
constexpr size_t WS_KM = 20 * MB, WS_ST = 20 * MB + 65536, WS_RS = 20 * MB + 65536 + 229376, WS_CS = 20 * MB + 65536 + 245760  , WS_FLAG = 21 * MB;
constexpr int FL_RS = 0, FL_CS = 64, FL_QKV = 128, FL_ST = 320, FL_Z = 432, FL_O = 656, FL_U = 912, FL_G = 1136, FL_EPOCH = 1392;

#define MFMA(a, b, c) __builtin_amdgcn_mfma_f32_32x32x16_bf16(a, b, c, 0, 0, 0)
#define SBAR() __builtin_amdgcn_sched_barrier(0)
__device__ __forceinline__ unsigned cvtpk(float lo, float hi) { unsigned r; asm("v_cvt_pk_bf16_f32 %0, %1, %2" : "=v"(r) : "v"(lo), "v"(hi)); return r; }
__device__ __forceinline__ float bflo(unsigned u) { return __uint_as_float(u << 16); }
__device__ __forceinline__ float bfhi(unsigned u) { return __uint_as_float(u & 0xffff0000u); }
__device__ __forceinline__ float dot2(unsigned a, unsigned b, float acc) {
    return __builtin_amdgcn_fdot2_f32_bf16(__builtin_bit_cast(bf16x2_t, a), __builtin_bit_cast(bf16x2_t, b), acc, false); }
__device__ __forceinline__ void glds16(const void* g, unsigned lds_base) {
    asm volatile("s_mov_b32 m0, %1\n\ts_nop 0\n\tglobal_load_lds_dwordx4 %0, off" :: "v"(g), "s"(lds_base) : "memory", "m0"); }
template <int IMM, bool WT> __device__ __forceinline__ void glds_s(const void* sbase, unsigned voff) {
    if (WT) asm volatile("global_load_lds_dwordx4 %0, %1 offset:%2 sc1" :: "v"(voff), "s"(sbase), "n"(IMM) : "memory");
    else asm volatile("global_load_lds_dwordx4 %0, %1 offset:%2" :: "v"(voff), "s"(sbase), "n"(IMM) : "memory"); }
__device__ __forceinline__ void set_m0(unsigned v) { asm volatile("s_mov_b32 m0, %0\n\ts_nop 0" :: "s"(v) : "memory", "m0"); }
__device__ __forceinline__ s16x4 trread(lds_cptr p) { return __builtin_bit_cast(s16x4, __builtin_amdgcn_ds_read_tr16_b64_v4i16((LAS v4i16_t*)p)); }
__device__ __forceinline__ bf16x8 ldsfrag(lds_cptr p) { return *(LAS const bf16x8*)p; }
__device__ __forceinline__ bf16x8 cat(s16x4 a, s16x4 b) { return (bf16x8){a[0], a[1], a[2], a[3], b[0], b[1], b[2], b[3]}; }
__device__ __forceinline__ float swap32_add(float v) { auto rr = __builtin_amdgcn_permlane32_swap(__float_as_uint(v), __float_as_uint(v), false, false); return __uint_as_float(rr[0]) + __uint_as_float(rr[1]); }
__device__ __forceinline__ float swap32_max(float v) { auto rr = __builtin_amdgcn_permlane32_swap(__float_as_uint(v), __float_as_uint(v), false, false); return fmaxf(__uint_as_float(rr[0]), __uint_as_float(rr[1])); }
template <int CTRL> __device__ __forceinline__ float dppf(float v) { return __int_as_float(__builtin_amdgcn_update_dpp(0, __float_as_int(v), CTRL, 0xf, 0xf, false)); }
__device__ __forceinline__ float row16_add(float v) { v += dppf<0x128>(v); v += dppf<0x124>(v); v += dppf<0x122>(v); v += dppf<0x121>(v); return v; }
__device__ __forceinline__ float row16_max(float v) { v = fmaxf(v, dppf<0x128>(v)); v = fmaxf(v, dppf<0x124>(v)); v = fmaxf(v, dppf<0x122>(v)); v = fmaxf(v, dppf<0x121>(v)); return v; }
__device__ __forceinline__ float swap16_max(float v) { auto rr = __builtin_amdgcn_permlane16_swap(__float_as_uint(v), __float_as_uint(v), false, false); return fmaxf(__uint_as_float(rr[0]), __uint_as_float(rr[1])); }
__device__ __forceinline__ float gelu_t(float x) {
    const float c1 = -2.3022082f, c2 = -0.10294324f;
    float e = __builtin_amdgcn_exp2f(x * __builtin_fmaf(x * x, c2, c1));
    return x * __builtin_amdgcn_rcpf(1.0f + e); }
__device__ __forceinline__ float silu_f(float x) { return x * __builtin_amdgcn_rcpf(1.0f + __builtin_amdgcn_exp2f(-1.4426950408889634f * x)); }
#define STAMP(slot) do {} while (0)
#define PF_DIST 9
#define PACE_N 2
#define PACE() do { asm volatile("s_nop %0" :: "n"(PACE_N)); SBAR(); } while (0)
#define POLL_SLEEP 2
#define WAITV(n) asm volatile("s_waitcnt vmcnt(" #n ")" ::: "memory")
#define WAIT_BAR(n) asm volatile("s_waitcnt vmcnt(" #n ")\n\ts_barrier" ::: "memory")
#define BAR() asm volatile("s_barrier" ::: "memory")
#define LGKM_BAR() do { __builtin_amdgcn_s_waitcnt(0xC07F); asm volatile("" ::: "memory"); __builtin_amdgcn_s_barrier(); asm volatile("" ::: "memory"); } while (0)
__device__ __forceinline__ unsigned wsw(unsigned row) { return ((row & 3) << 2) | ((row >> 2) & 3); }
__device__ __forceinline__ void st16_wt(void* p, u32x4 v) {
    asm volatile("global_store_dwordx4 %0, %1, off sc1\n\ts_nop 1" :: "v"(p), "v"(v)); }
__device__ __forceinline__ void st8_wt(void* p, u64 v) { __hip_atomic_store((GAS u64*)p, v, RLX_AGENT); }
__device__ __forceinline__ u64 ld8_wt(const void* p) { return __hip_atomic_load((GAS u64*)p, RLX_AGENT); }
__device__ __forceinline__ u32x4 ld16_wt(const void* p) { u32x4 v; asm volatile("global_load_dwordx4 %0, %1, off sc1\n\ts_waitcnt vmcnt(0)" : "=&v"(v) : "v"(p) : "memory"); return v; }
__device__ __forceinline__ void glds16_wt(const void* g, unsigned lds_base) {
    asm volatile("s_mov_b32 m0, %1\n\ts_nop 0\n\tglobal_load_lds_dwordx4 %0, off sc1" :: "v"(g), "s"(lds_base) : "memory", "m0"); }
__device__ __forceinline__ GAS u64* flagp(char* ws, int idx) { return (GAS u64*)(ws + WS_FLAG) + idx * 16; }
__device__ __forceinline__ void wait_flags(GAS u64* f, bool want, u64 tag) {
    for (unsigned spins = 0; spins < (1u << 20); ++spins) {
        const u64 v = want ? __hip_atomic_load(f, RLX_AGENT) : tag;
        if (__all(v == tag)) return;
        __builtin_amdgcn_s_sleep(POLL_SLEEP); } }
#define ACQUIRE() do { __builtin_amdgcn_fence(__ATOMIC_ACQUIRE, "agent"); asm volatile("s_waitcnt vmcnt(0)" ::: "memory"); } while (0)
#define EPOCH_REQ(ep) asm volatile("global_load_dwordx2 %0, %1, off sc1" : "=v"(ep) : "v"(flagp(ws, FL_EPOCH)) : "memory")
#define EPOCH_TAG(ep) ((((u64)(unsigned)__builtin_amdgcn_readfirstlane((unsigned)((ep) >> 32)) << 32) | (unsigned)__builtin_amdgcn_readfirstlane((unsigned)(ep))) + 1)
#define PUBLISH(idx) do { WAITV(0); __syncthreads(); if (threadIdx.x == 0) __hip_atomic_store(flagp(ws, idx), tag, RLX_AGENT); } while (0)

__device__ __forceinline__ void stats_role(const uint16_t* __restrict__ x, const uint16_t* __restrict__ wqkv, const uint16_t* __restrict__ win, char* __restrict__ ws, char* lds, int j, u64& tag) {
    u64 ep; EPOCH_REQ(ep);
    const int tid = threadIdx.x, lane = tid & 63, wid = tid >> 6, rr = lane >> 4, cc = lane & 15;
    const unsigned ONES = 0x3f803f80u;
    u32x4 xv[4], w[8];
    const int row = 32 * j + 4 * wid + rr;
#pragma unroll
    for (int i = 0; i < 4; ++i) xv[i] = *(const u32x4*)(x + (size_t)row * DM + i * 128 + cc * 8);
    const int cbk = j >> 1;
    if (j < 52) {
        const uint16_t* W = cbk < 12 ? wqkv + cbk * 128 : win + (cbk - 12) * 128; const int ldw = cbk < 12 ? NQKV : NIN;
        const uint16_t* p = W + (size_t)((j & 1) * 256 + 32 * wid + rr) * ldw + cc * 8;
#pragma unroll
        for (int i = 0; i < 8; ++i) w[i] = *(const u32x4*)(p + (size_t)(i * 4) * ldw);
    }
    asm volatile("s_waitcnt vmcnt(0)" : "+v"(ep) :: "memory");
    tag = EPOCH_TAG(ep);
    float s1 = 0.f, s2 = 0.f;
#pragma unroll
    for (int i = 0; i < 4; ++i)
#pragma unroll
        for (int e = 0; e < 4; ++e) { s1 = dot2(xv[i][e], ONES, s1); s2 = dot2(xv[i][e], xv[i][e], s2); }
    s1 = row16_add(s1); s2 = row16_add(s2);
    { const float mu = s1 * (1.0f / DM), var = fmaxf(s2 * (1.0f / DM) - mu * mu, 0.f);
      if (cc == 0) st8_wt(ws + WS_RS + (size_t)row * 8, __builtin_bit_cast(u64, (f32x2){mu, __builtin_amdgcn_rsqf(var + LN_EPS)})); }
    if (j < 52) {
        float cs[8] = {0.f, 0.f, 0.f, 0.f, 0.f, 0.f, 0.f, 0.f};
        unsigned sel_lo = 0x00003f80u, sel_hi = 0x3f800000u;
        asm volatile("" : "+v"(sel_lo), "+v"(sel_hi));
#pragma unroll
        for (int i = 0; i < 8; ++i)
#pragma unroll
            for (int e = 0; e < 4; ++e) { cs[2 * e] = dot2(w[i][e], sel_lo, cs[2 * e]); cs[2 * e + 1] = dot2(w[i][e], sel_hi, cs[2 * e + 1]); }
        float* red = (float*)lds;
        *(f32x4*)(red + (wid * 4 + rr) * 128 + cc * 8) = (f32x4){cs[0], cs[1], cs[2], cs[3]}; *(f32x4*)(red + (wid * 4 + rr) * 128 + cc * 8 + 4) = (f32x4){cs[4], cs[5], cs[6], cs[7]};
        __syncthreads();
        if (tid < 64) { f32x2 a = {0.f, 0.f};
#pragma unroll
            for (int q = 0; q < 32; ++q) a += *(const f32x2*)(red + q * 128 + 2 * tid);
            st8_wt(ws + WS_CS + (size_t)((j & 1) * 3328 + cbk * 128 + 2 * tid) * 4, __builtin_bit_cast(u64, a)); }
    }
    STAMP(11);
    WAITV(0); __syncthreads();
    STAMP(1);
    if (tid == 0) { __hip_atomic_store(flagp(ws, FL_RS + j), tag, RLX_AGENT); if (j < 52) __hip_atomic_store(flagp(ws, FL_CS + j), tag, RLX_AGENT); }
}

constexpr int P_SLOT = 16384, P_ST = 4 * P_SLOT, P_LDS = 4 * P_SLOT + 4096;
struct PFrag { bf16x8 x[2], w; };
template <bool SWAP>
__device__ __forceinline__ void proj_main(const uint16_t* __restrict__ x, const uint16_t* __restrict__ W, int ldw, int rb, int col0, int cblk, char* __restrict__ ws, char* lds, int rot, u64& tag, f32x16 (&acc)[2]) {
    u64 ep; EPOCH_REQ(ep);
    const int tid = threadIdx.x, lane = tid & 63, wid = __builtin_amdgcn_readfirstlane(tid >> 6), wr = wid >> 2, wc = wid & 3, l31 = lane & 31, hh = lane >> 5;
    const unsigned lds0 = (unsigned)(uintptr_t)lds;
    const int xr = 16 * wid + (lane >> 2), wk = 4 * wid + (lane >> 4);
    const char* xb = (const char*)(x + (size_t)rb * 128 * DM); const char* wb = (const char*)(W + col0);
    const unsigned xvo = (xr * DM + ((lane & 3) ^ ((xr >> 2) & 3)) * 8) * 2, wvo = (wk * ldw + ((lane & 15) ^ wsw(wk)) * 8) * 2;
    const unsigned xd = lds0 + wid * 1024, wd = lds0 + 8192 + wid * 1024;
    auto issue1 = [&](int it, int which) { const unsigned so = (it & 3) * P_SLOT; const int kk = (it + rot) & 15;
        if (which == 0) { set_m0(xd + so); glds_s<0, false>(xb + kk * 64, xvo); } else { set_m0(wd + so); glds_s<0, false>(wb + (size_t)kk * 64 * ldw, wvo); } SBAR(); };
#pragma unroll
    for (int it = 0; it < 4; ++it) { issue1(it, 0); issue1(it, 1); }
    unsigned xoff[2][2], woff[2][2];
    const int blk = (lane >> 4) & 1, q = (lane & 15) >> 2, p = lane & 3;
#pragma unroll
    for (int ks = 0; ks < 2; ++ks) {
#pragma unroll
        for (int rt = 0; rt < 2; ++rt) { const int r = wr * 64 + rt * 32 + l31; xoff[rt][ks] = r * 64 + (((2 * ks + hh) ^ ((r >> 2) & 3)) * 16); }
        const int kr = 8 * hh + q, ch = 4 * wc + 2 * blk + (p >> 1);
        woff[0][ks] = 8192 + ks * 4096 + kr * 256 + ((ch ^ wsw(kr)) * 16) + 8 * (p & 1); woff[1][ks] = (woff[0][ks] + 1024) ^ 16; }
    auto rd = [&](PFrag& f, int slot, int j) { lds_cptr sb = (lds_cptr)lds + slot * P_SLOT;
        f.x[0] = ldsfrag(sb + xoff[0][j]); f.x[1] = ldsfrag(sb + xoff[1][j]); f.w = cat(trread(sb + woff[0][j]), trread(sb + woff[1][j])); };
    auto mm = [&](const PFrag& f) {
#pragma unroll
        for (int rt = 0; rt < 2; ++rt) acc[rt] = SWAP ? MFMA(f.x[rt], f.w, acc[rt]) : MFMA(f.w, f.x[rt], acc[rt]); };
    acc[0] = f32x16{}; acc[1] = f32x16{};
    PFrag fa[2], fb[2];
    asm volatile("s_waitcnt vmcnt(6)\n\ts_barrier" : "+v"(ep) :: "memory");
    tag = EPOCH_TAG(ep);
    STAMP(10);
    rd(fa[0], 0, 0); rd(fa[1], 0, 1);
    auto mm1 = [&](const PFrag& f, int rt) { acc[rt] = SWAP ? MFMA(f.x[rt], f.w, acc[rt]) : MFMA(f.w, f.x[rt], acc[rt]); SBAR(); };
    auto step = [&](int it, int sl, PFrag (&cur)[2], PFrag (&nxt)[2]) {
        __builtin_amdgcn_s_waitcnt(0xC07F);
        if (it < 13) WAITV(4); else if (it == 13) WAITV(2); else WAITV(0);
        __builtin_amdgcn_s_barrier(); asm volatile("" ::: "memory");
        lds_cptr sb = (lds_cptr)lds + ((sl + 1) & 3) * P_SLOT;
        mm1(cur[0], 0);
        nxt[0].x[0] = ldsfrag(sb + xoff[0][0]); nxt[0].x[1] = ldsfrag(sb + xoff[1][0]); SBAR();
        mm1(cur[0], 1);
        nxt[0].w = cat(trread(sb + woff[0][0]), trread(sb + woff[1][0])); SBAR();
        if (it + 4 < 16) issue1(it + 4, 0);
        mm1(cur[1], 0);
        nxt[1].x[0] = ldsfrag(sb + xoff[0][1]); nxt[1].x[1] = ldsfrag(sb + xoff[1][1]); SBAR();
        mm1(cur[1], 1);
        nxt[1].w = cat(trread(sb + woff[0][1]), trread(sb + woff[1][1])); SBAR();
        if (it + 4 < 16) issue1(it + 4, 1); };
#pragma unroll 1
    for (int io = 0; io < 2; ++io) { step(io * 4, 0, fa, fb); step(io * 4 + 1, 1, fb, fa); step(io * 4 + 2, 2, fa, fb); step(io * 4 + 3, 3, fb, fa); }
    const char* fa_ = (const char*)(lane < 4 ? flagp(ws, FL_RS + rb * 4 + lane) : flagp(ws, FL_CS + 2 * cblk + (lane & 1)));
    const char* sa_ = tid < 128 ? ws + WS_RS + (size_t)(rb * 128 + tid) * 8 : ws + WS_CS + (size_t)(cblk * 128 + 2 * (tid & 63)) * 4;
    const char* sb_ = ws + WS_CS + (size_t)(3328 + cblk * 128 + 2 * (tid & 63)) * 4;
    u64 fl, stv, stw;
    asm volatile("global_load_dwordx2 %0, %1, off sc1" : "=v"(fl) : "v"(fa_) : "memory");
    step(8, 0, fa, fb); step(9, 1, fb, fa); step(10, 2, fa, fb); step(11, 3, fb, fa);
    asm volatile("" : "+v"(fl));
    const bool got = __all(lane >= 6 || fl == tag);
    asm volatile("global_load_dwordx2 %0, %2, off sc1\n\tglobal_load_dwordx2 %1, %3, off sc1" : "=&v"(stv), "=&v"(stw) : "v"(sa_), "v"(sb_) : "memory");
    step(12, 0, fa, fb); step(13, 1, fb, fa); step(14, 2, fa, fb); step(15, 3, fb, fa);
    asm volatile("" : "+v"(stv), "+v"(stw));
    if (!got) { wait_flags((GAS u64*)fa_, lane < 6, tag); stv = ld8_wt(sa_); stw = ld8_wt(sb_); }
    STAMP(11);
    float* st = (float*)(lds + P_ST);
    { const f32x2 v = __builtin_bit_cast(f32x2, stv);
      if (tid < 128) { st[tid] = v[0]; st[128 + tid] = v[1]; } else if (tid < 192) *(f32x2*)(st + 256 + 2 * (tid - 128)) = v + __builtin_bit_cast(f32x2, stw); }
    __syncthreads();
}

__device__ __forceinline__ void qkv_role(const uint16_t* __restrict__ x, const uint16_t* __restrict__ wqkv, char* __restrict__ ws, char* lds, int b, u64& tag) {
    const int xcd = b & 7, i = b >> 3;
    const int rb = 4 * (xcd >> 1) + (i & 3), cb = 6 * (xcd & 1) + (i >> 2);
    const int tid = threadIdx.x, lane = tid & 63, wid = __builtin_amdgcn_readfirstlane(tid >> 6), wr = wid >> 2, wc = wid & 3, l31 = lane & 31, hh = lane >> 5;
    f32x16 acc[2];
    const int rot = ((i & 3) * 4 + (i >> 2) * 3) & 15;
    if (cb >= 8) proj_main<true>(x, wqkv, NQKV, rb, cb * 128, cb, ws, lds, rot, tag, acc); else proj_main<false>(x, wqkv, NQKV, rb, cb * 128, cb, ws, lds, rot, tag, acc);
    STAMP(1);
    const float* st = (const float*)(lds + P_ST);
    if (cb >= 8) {
        const float cs = st[256 + wc * 32 + l31];
        const int head = (cb - 8) * 8 + wc * 2 + (l31 >> 4);
#pragma unroll
        for (int rt = 0; rt < 2; ++rt) { const int tr = wr * 64 + rt * 32;
            unsigned pk[8];
#pragma unroll
            for (int q = 0; q < 4; ++q) { const f32x4 m = *(const f32x4*)(st + tr + 8 * q + 4 * hh), r = *(const f32x4*)(st + 128 + tr + 8 * q + 4 * hh);
                pk[2 * q] = cvtpk((acc[rt][4 * q] - m[0] * cs) * r[0], (acc[rt][4 * q + 1] - m[1] * cs) * r[1]);
                pk[2 * q + 1] = cvtpk((acc[rt][4 * q + 2] - m[2] * cs) * r[2], (acc[rt][4 * q + 3] - m[3] * cs) * r[3]); }
            const int tile = rb * 4 + (tr >> 5);
            char* dst = ws + WS_KV + (size_t)head * 131072 + (tile >> 3) * 16384 + 8192 + (tile & 7) * 1024 + hh * 256 + (l31 & 15) * 16;
            st16_wt(dst, (u32x4){pk[0], pk[1], pk[2], pk[3]}); st16_wt(dst + 512, (u32x4){pk[4], pk[5], pk[6], pk[7]}); }
    } else {
        const bool isK = cb >= 4; const float sc = isK ? 1.0f : QSCALE;
        float kmx[2] = {0.f, 0.f};
#pragma unroll
        for (int rt = 0; rt < 2; ++rt) { const int tr = wr * 64 + rt * 32, tile = rb * 4 + (tr >> 5);
            const float mu = st[tr + l31], rs = st[128 + tr + l31] * sc;
#pragma unroll
            for (int hb = 0; hb < 2; ++hb) {
                float v[8];
#pragma unroll
                for (int q = 0; q < 2; ++q) { const f32x4 c = *(const f32x4*)(st + 256 + wc * 32 + 16 * hb + 8 * q + 4 * hh);
#pragma unroll
                    for (int e = 0; e < 4; ++e) v[4 * q + e] = (acc[rt][8 * hb + 4 * q + e] - mu * c[e]) * rs; }
                u32x4 pk;
#pragma unroll
                for (int e = 0; e < 4; ++e) pk[e] = cvtpk(v[2 * e], v[2 * e + 1]);
                const int head = (cb & 3) * 8 + wc * 2 + hb;
                char* dst = isK ? ws + WS_KV + (size_t)head * 131072 + (tile >> 3) * 16384 + (tile & 7) * 1024 + lane * 16 : ws + WS_QF + (size_t)head * 65536 + tile * 1024 + lane * 16;
                st16_wt(dst, pk);
                float n2 = 0.f;
#pragma unroll
                for (int e = 0; e < 4; ++e) n2 = dot2(pk[e], pk[e], n2);
                kmx[hb] = fmaxf(kmx[hb], swap32_add(n2));
            } }
        if (isK) {
#pragma unroll
            for (int hb = 0; hb < 2; ++hb) { const float m = swap16_max(row16_max(kmx[hb]));
                if (lane == 0) __hip_atomic_store((GAS unsigned*)(ws + WS_KM) + ((cb & 3) * 8 + wc * 2 + hb) * 32 + rb * 2 + wr, __float_as_uint(m), RLX_AGENT); } }
    }
    STAMP(12);
    PUBLISH(FL_QKV + cb * 16 + rb);
    STAMP(13);
}

constexpr int A_ONES = 3 * 16384, A_LDS = A_ONES + 8192 + 512;
__device__ __forceinline__ void attn_role(char* __restrict__ ws, char* lds, int b, u64 tag) {
    const int tid = threadIdx.x, lane = tid & 63, wid = __builtin_amdgcn_readfirstlane(tid >> 6);
    const int head = (b & 7) * 4 + ((b >> 3) & 3), jb = b >> 5;
    const unsigned lds0 = (unsigned)(uintptr_t)lds;
    const char* kvb = ws + WS_KV + (size_t)head * 131072;
    for (int i = tid; i < (8192 + 512) / 16; i += 512) { const unsigned o1 = ((i & 11) == 0) ? 0x3f803f80u : 0u; *(u32x4*)(lds + A_ONES + i * 16) = (u32x4){o1, o1, o1, o1}; }
    if (wid == 0) {
        const int hb = head >> 3;
        const int idx = lane < 16 ? (4 + hb) * 16 + lane : lane < 32 ? (8 + hb) * 16 + (lane - 16) : hb * 16 + 2 * jb + (lane & 1);
        wait_flags(flagp(ws, FL_QKV + idx), lane < 34, tag);
        STAMP(14);
        }
    __syncthreads();
    STAMP(2);
    unsigned goff = wid * 2048 + lane * 16;
    const int tile0 = jb * 8 + wid;
    u32x4 qa; unsigned kmu;
    asm volatile("global_load_dwordx4 %0, %2, off sc1\n\tglobal_load_dword %1, %3, off sc1" : "=&v"(qa), "=&v"(kmu)
                 : "v"(ws + WS_QF + (size_t)head * 65536 + tile0 * 1024 + lane * 16), "v"(ws + WS_KM + (size_t)(head * 32 + (lane & 31)) * 4) : "memory");
#pragma unroll
    for (int c = 0; c < 2; ++c) { set_m0(lds0 + c * 16384 + wid * 2048); glds_s<0, true>(kvb + c * 16384, goff); glds_s<1024, true>(kvb + c * 16384, goff); }
    asm volatile("s_waitcnt vmcnt(4)" : "+v"(qa), "+v"(kmu) :: "memory");
    const float km = swap16_max(row16_max(__uint_as_float(kmu)));
    float na = 0.f;
#pragma unroll
    for (int e = 0; e < 4; ++e) na = dot2(qa[e], qa[e], na);
    na = swap32_add(na);
    float ma = __builtin_amdgcn_sqrtf(na * km);
    if (__any(!(ma <= 40.0f))) {
        float xa = -3.0e38f;
        for (int t = 0; t < 64; ++t) {
            const bf16x8 kf = __builtin_bit_cast(bf16x8, ld16_wt(kvb + (t >> 3) * 16384 + (t & 7) * 1024 + lane * 16));
            const f32x16 sa = MFMA(kf, __builtin_bit_cast(bf16x8, qa), f32x16{});
#pragma unroll
            for (int r = 0; r < 16; ++r) xa = fmaxf(xa, sa[r]); }
        ma = swap32_max(xa);
    }
    const float nma = -ma;
    const unsigned kaddr = lds0 + lane * 16;
    const bool ones = (lane & 16) != 0;
    const unsigned vaddr = ones ? lds0 + A_ONES + (lane & 15) * 16 : lds0 + 8192 + (lane >> 5) * 256 + (lane & 15) * 16;
    const unsigned vsel = ones ? 0u : 0xffffffffu;
    goff += 2 * 16384;
    unsigned dbeg = lds0 + wid * 2048, dend = dbeg + 3 * 16384, dslot = dbeg + 2 * 16384, nslot = 0x4000, cnt, m0save;
    unsigned oa0, oa1, oa2, oa3;
    STAMP(3);
    asm volatile(
    "s_mov_b32 %[m0save], m0\n"
    "v_mov_b32 v96, %[qa0]\n"
    "v_mov_b32 v97, %[qa1]\n"
    "v_mov_b32 v98, %[qa2]\n"
    "v_mov_b32 v99, %[qa3]\n"
    "v_mov_b32 v72, %[nma]\n"
    "v_mov_b32 v56, 0\n"
    "v_mov_b32 v73, %[nma]\n"
    "v_mov_b32 v57, 0\n"
    "v_mov_b32 v74, %[nma]\n"
    "v_mov_b32 v58, 0\n"
    "v_mov_b32 v75, %[nma]\n"
    "v_mov_b32 v59, 0\n"
    "v_mov_b32 v76, %[nma]\n"
    "v_mov_b32 v60, 0\n"
    "v_mov_b32 v77, %[nma]\n"
    "v_mov_b32 v61, 0\n"
    "v_mov_b32 v78, %[nma]\n"
    "v_mov_b32 v62, 0\n"
    "v_mov_b32 v79, %[nma]\n"
    "v_mov_b32 v63, 0\n"
    "v_mov_b32 v80, %[nma]\n"
    "v_mov_b32 v64, 0\n"
    "v_mov_b32 v81, %[nma]\n"
    "v_mov_b32 v65, 0\n"
    "v_mov_b32 v82, %[nma]\n"
    "v_mov_b32 v66, 0\n"
    "v_mov_b32 v83, %[nma]\n"
    "v_mov_b32 v67, 0\n"
    "v_mov_b32 v84, %[nma]\n"
    "v_mov_b32 v68, 0\n"
    "v_mov_b32 v85, %[nma]\n"
    "v_mov_b32 v69, 0\n"
    "v_mov_b32 v86, %[nma]\n"
    "v_mov_b32 v70, 0\n"
    "v_mov_b32 v87, %[nma]\n"
    "v_mov_b32 v71, 0\n"
    "v_mov_b32 v120, 0\n"
    "v_mov_b32 v121, 0\n"
    "v_mov_b32 v122, 0\n"
    "v_mov_b32 v123, 0\n"
    "v_mov_b32 v88, 0\n"
    "v_mov_b32 v89, 0\n"
    "v_mov_b32 v90, 0\n"
    "v_mov_b32 v91, 0\n"
    "v_mov_b32 v92, 0\n"
    "v_mov_b32 v93, 0\n"
    "v_mov_b32 v94, 0\n"
    "v_mov_b32 v95, 0\n"
    "v_mov_b32 v124, %[kaddr]\n"
    "v_mov_b32 v125, %[vaddr]\n"
    "v_add_u32 v126, 0x4000, v124\n"
    "v_and_b32 v127, 0x4000, %[vsel]\n"
    "v_add_u32 v127, v127, v125\n"
    "s_waitcnt vmcnt(2) lgkmcnt(0)\n"
    "s_barrier\n"
    "ds_read_b128 v[100:103], v124\n"
    "ds_read_b128 v[104:107], v124 offset:1024\n"
    "ds_read_b128 v[108:111], v125\n"
    "ds_read_b128 v[112:115], v125 offset:512\n"
    "s_mov_b32 %[cnt], 0\n"
    "s_waitcnt lgkmcnt(0)\n"
    "v_mfma_f32_32x32x16_bf16 v[24:39], v[100:103], v[96:99], v[72:87]\n"
    "s_nop 15\n"
    ".Lloop%=:\n"
    "v_exp_f32 v24, v24\n"
    "v_exp_f32 v25, v25\n"
    "v_mfma_f32_32x32x16_bf16 v[56:71], v[120:123], v[92:95], v[56:71]\n"
    "v_exp_f32 v26, v26\n"
    "v_exp_f32 v27, v27\n"
    "v_cvt_pk_bf16_f32 v88, v24, v25\n"
    "v_exp_f32 v28, v28\n"
    "s_waitcnt lgkmcnt(0)\n"
    "v_mfma_f32_32x32x16_bf16 v[40:55], v[104:107], v[96:99], v[72:87]\n"
    "v_exp_f32 v29, v29\n"
    "v_cvt_pk_bf16_f32 v89, v26, v27\n"
    "ds_read_b128 v[100:103], v124 offset:2048\n"
    "v_exp_f32 v30, v30\n"
    "ds_read_b128 v[116:119], v125 offset:1024\n"
    "v_exp_f32 v31, v31\n"
    "v_cvt_pk_bf16_f32 v90, v28, v29\n"
    "ds_read_b128 v[120:123], v125 offset:1536\n"
    "v_exp_f32 v32, v32\n"
    "v_exp_f32 v33, v33\n"
    "v_cvt_pk_bf16_f32 v91, v30, v31\n"
    "v_exp_f32 v34, v34\n"
    "v_exp_f32 v35, v35\n"
    "v_mfma_f32_32x32x16_bf16 v[56:71], v[108:111], v[88:91], v[56:71]\n"
    "v_cvt_pk_bf16_f32 v92, v32, v33\n"
    "v_exp_f32 v36, v36\n"
    "v_exp_f32 v37, v37\n"
    "v_cvt_pk_bf16_f32 v93, v34, v35\n"
    "v_exp_f32 v38, v38\n"
    "v_exp_f32 v39, v39\n"
    "v_cvt_pk_bf16_f32 v94, v36, v37\n"
    "v_cvt_pk_bf16_f32 v95, v38, v39\n"
    "v_exp_f32 v40, v40\n"
    "v_exp_f32 v41, v41\n"
    "v_mfma_f32_32x32x16_bf16 v[56:71], v[112:115], v[92:95], v[56:71]\n"
    "v_exp_f32 v42, v42\n"
    "v_exp_f32 v43, v43\n"
    "v_cvt_pk_bf16_f32 v88, v40, v41\n"
    "v_exp_f32 v44, v44\n"
    "s_waitcnt lgkmcnt(0)\n"
    "v_mfma_f32_32x32x16_bf16 v[24:39], v[100:103], v[96:99], v[72:87]\n"
    "v_exp_f32 v45, v45\n"
    "v_cvt_pk_bf16_f32 v89, v42, v43\n"
    "ds_read_b128 v[104:107], v124 offset:3072\n"
    "v_exp_f32 v46, v46\n"
    "ds_read_b128 v[108:111], v125 offset:2048\n"
    "v_exp_f32 v47, v47\n"
    "v_cvt_pk_bf16_f32 v90, v44, v45\n"
    "ds_read_b128 v[112:115], v125 offset:2560\n"
    "v_exp_f32 v48, v48\n"
    "v_exp_f32 v49, v49\n"
    "v_cvt_pk_bf16_f32 v91, v46, v47\n"
    "v_exp_f32 v50, v50\n"
    "v_exp_f32 v51, v51\n"
    "v_mfma_f32_32x32x16_bf16 v[56:71], v[116:119], v[88:91], v[56:71]\n"
    "v_cvt_pk_bf16_f32 v92, v48, v49\n"
    "v_exp_f32 v52, v52\n"
    "v_exp_f32 v53, v53\n"
    "v_cvt_pk_bf16_f32 v93, v50, v51\n"
    "v_exp_f32 v54, v54\n"
    "v_exp_f32 v55, v55\n"
    "v_cvt_pk_bf16_f32 v94, v52, v53\n"
    "v_cvt_pk_bf16_f32 v95, v54, v55\n"
    "v_exp_f32 v24, v24\n"
    "v_exp_f32 v25, v25\n"
    "v_mfma_f32_32x32x16_bf16 v[56:71], v[120:123], v[92:95], v[56:71]\n"
    "v_exp_f32 v26, v26\n"
    "v_exp_f32 v27, v27\n"
    "v_cvt_pk_bf16_f32 v88, v24, v25\n"
    "v_exp_f32 v28, v28\n"
    "s_waitcnt lgkmcnt(0)\n"
    "v_mfma_f32_32x32x16_bf16 v[40:55], v[104:107], v[96:99], v[72:87]\n"
    "v_exp_f32 v29, v29\n"
    "v_cvt_pk_bf16_f32 v89, v26, v27\n"
    "ds_read_b128 v[100:103], v124 offset:4096\n"
    "v_exp_f32 v30, v30\n"
    "ds_read_b128 v[116:119], v125 offset:3072\n"
    "v_exp_f32 v31, v31\n"
    "v_cvt_pk_bf16_f32 v90, v28, v29\n"
    "ds_read_b128 v[120:123], v125 offset:3584\n"
    "v_exp_f32 v32, v32\n"
    "v_exp_f32 v33, v33\n"
    "v_cvt_pk_bf16_f32 v91, v30, v31\n"
    "v_exp_f32 v34, v34\n"
    "v_exp_f32 v35, v35\n"
    "v_mfma_f32_32x32x16_bf16 v[56:71], v[108:111], v[88:91], v[56:71]\n"
    "v_cvt_pk_bf16_f32 v92, v32, v33\n"
    "v_exp_f32 v36, v36\n"
    "v_exp_f32 v37, v37\n"
    "v_cvt_pk_bf16_f32 v93, v34, v35\n"
    "v_exp_f32 v38, v38\n"
    "v_exp_f32 v39, v39\n"
    "v_cvt_pk_bf16_f32 v94, v36, v37\n"
    "v_cvt_pk_bf16_f32 v95, v38, v39\n"
    "v_exp_f32 v40, v40\n"
    "v_exp_f32 v41, v41\n"
    "v_mfma_f32_32x32x16_bf16 v[56:71], v[112:115], v[92:95], v[56:71]\n"
    "v_exp_f32 v42, v42\n"
    "v_exp_f32 v43, v43\n"
    "v_cvt_pk_bf16_f32 v88, v40, v41\n"
    "v_exp_f32 v44, v44\n"
    "s_waitcnt lgkmcnt(0)\n"
    "v_mfma_f32_32x32x16_bf16 v[24:39], v[100:103], v[96:99], v[72:87]\n"
    "v_exp_f32 v45, v45\n"
    "v_cvt_pk_bf16_f32 v89, v42, v43\n"
    "ds_read_b128 v[104:107], v124 offset:5120\n"
    "v_exp_f32 v46, v46\n"
    "ds_read_b128 v[108:111], v125 offset:4096\n"
    "v_exp_f32 v47, v47\n"
    "v_cvt_pk_bf16_f32 v90, v44, v45\n"
    "ds_read_b128 v[112:115], v125 offset:4608\n"
    "v_exp_f32 v48, v48\n"
    "v_exp_f32 v49, v49\n"
    "v_cvt_pk_bf16_f32 v91, v46, v47\n"
    "v_exp_f32 v50, v50\n"
    "v_exp_f32 v51, v51\n"
    "v_mfma_f32_32x32x16_bf16 v[56:71], v[116:119], v[88:91], v[56:71]\n"
    "v_cvt_pk_bf16_f32 v92, v48, v49\n"
    "v_exp_f32 v52, v52\n"
    "v_exp_f32 v53, v53\n"
    "v_cvt_pk_bf16_f32 v93, v50, v51\n"
    "v_exp_f32 v54, v54\n"
    "v_exp_f32 v55, v55\n"
    "v_cvt_pk_bf16_f32 v94, v52, v53\n"
    "v_cvt_pk_bf16_f32 v95, v54, v55\n"
    "v_exp_f32 v24, v24\n"
    "v_exp_f32 v25, v25\n"
    "v_mfma_f32_32x32x16_bf16 v[56:71], v[120:123], v[92:95], v[56:71]\n"
    "v_exp_f32 v26, v26\n"
    "v_exp_f32 v27, v27\n"
    "v_cvt_pk_bf16_f32 v88, v24, v25\n"
    "v_exp_f32 v28, v28\n"
    "s_waitcnt lgkmcnt(0)\n"
    "v_mfma_f32_32x32x16_bf16 v[40:55], v[104:107], v[96:99], v[72:87]\n"
    "v_exp_f32 v29, v29\n"
    "v_cvt_pk_bf16_f32 v89, v26, v27\n"
    "ds_read_b128 v[100:103], v124 offset:6144\n"
    "v_exp_f32 v30, v30\n"
    "ds_read_b128 v[116:119], v125 offset:5120\n"
    "v_exp_f32 v31, v31\n"
    "v_cvt_pk_bf16_f32 v90, v28, v29\n"
    "ds_read_b128 v[120:123], v125 offset:5632\n"
    "v_exp_f32 v32, v32\n"
    "v_exp_f32 v33, v33\n"
    "v_cvt_pk_bf16_f32 v91, v30, v31\n"
    "v_exp_f32 v34, v34\n"
    "v_exp_f32 v35, v35\n"
    "v_mfma_f32_32x32x16_bf16 v[56:71], v[108:111], v[88:91], v[56:71]\n"
    "v_cvt_pk_bf16_f32 v92, v32, v33\n"
    "v_exp_f32 v36, v36\n"
    "v_exp_f32 v37, v37\n"
    "v_cvt_pk_bf16_f32 v93, v34, v35\n"
    "v_exp_f32 v38, v38\n"
    "v_exp_f32 v39, v39\n"
    "v_cvt_pk_bf16_f32 v94, v36, v37\n"
    "v_cvt_pk_bf16_f32 v95, v38, v39\n"
    "v_exp_f32 v40, v40\n"
    "v_exp_f32 v41, v41\n"
    "v_mfma_f32_32x32x16_bf16 v[56:71], v[112:115], v[92:95], v[56:71]\n"
    "v_exp_f32 v42, v42\n"
    "v_exp_f32 v43, v43\n"
    "v_cvt_pk_bf16_f32 v88, v40, v41\n"
    "v_exp_f32 v44, v44\n"
    "s_waitcnt lgkmcnt(0)\n"
    "v_mfma_f32_32x32x16_bf16 v[24:39], v[100:103], v[96:99], v[72:87]\n"
    "v_exp_f32 v45, v45\n"
    "v_cvt_pk_bf16_f32 v89, v42, v43\n"
    "ds_read_b128 v[104:107], v124 offset:7168\n"
    "v_exp_f32 v46, v46\n"
    "ds_read_b128 v[108:111], v125 offset:6144\n"
    "v_exp_f32 v47, v47\n"
    "v_cvt_pk_bf16_f32 v90, v44, v45\n"
    "ds_read_b128 v[112:115], v125 offset:6656\n"
    "v_exp_f32 v48, v48\n"
    "v_exp_f32 v49, v49\n"
    "v_cvt_pk_bf16_f32 v91, v46, v47\n"
    "v_exp_f32 v50, v50\n"
    "v_exp_f32 v51, v51\n"
    "v_mfma_f32_32x32x16_bf16 v[56:71], v[116:119], v[88:91], v[56:71]\n"
    "v_cvt_pk_bf16_f32 v92, v48, v49\n"
    "v_exp_f32 v52, v52\n"
    "v_exp_f32 v53, v53\n"
    "v_cvt_pk_bf16_f32 v93, v50, v51\n"
    "v_exp_f32 v54, v54\n"
    "v_exp_f32 v55, v55\n"
    "v_cvt_pk_bf16_f32 v94, v52, v53\n"
    "v_cvt_pk_bf16_f32 v95, v54, v55\n"
    "v_exp_f32 v24, v24\n"
    "v_exp_f32 v25, v25\n"
    "v_mfma_f32_32x32x16_bf16 v[56:71], v[120:123], v[92:95], v[56:71]\n"
    "v_exp_f32 v26, v26\n"
    "v_exp_f32 v27, v27\n"
    "v_cvt_pk_bf16_f32 v88, v24, v25\n"
    "v_exp_f32 v28, v28\n"
    "s_waitcnt lgkmcnt(0)\n"
    "v_mfma_f32_32x32x16_bf16 v[40:55], v[104:107], v[96:99], v[72:87]\n"
    "v_exp_f32 v29, v29\n"
    "v_cvt_pk_bf16_f32 v89, v26, v27\n"
    "s_waitcnt vmcnt(0)\n"
    "s_barrier\n"
    "ds_read_b128 v[100:103], v126 offset:0\n"
    "v_exp_f32 v30, v30\n"
    "ds_read_b128 v[116:119], v125 offset:7168\n"
    "v_exp_f32 v31, v31\n"
    "v_cvt_pk_bf16_f32 v90, v28, v29\n"
    "ds_read_b128 v[120:123], v125 offset:7680\n"
    "v_exp_f32 v32, v32\n"
    "v_exp_f32 v33, v33\n"
    "v_cvt_pk_bf16_f32 v91, v30, v31\n"
    "v_exp_f32 v34, v34\n"
    "v_exp_f32 v35, v35\n"
    "v_mfma_f32_32x32x16_bf16 v[56:71], v[108:111], v[88:91], v[56:71]\n"
    "s_cmp_ge_u32 %[cnt], 6\n"
    "s_cbranch_scc1 .Lnodma%=\n"
    "s_mov_b32 m0, %[dslot]\n"
    "s_nop 0\n"
    "global_load_lds_dwordx4 %[goff], %[gbase] offset:0\n"
    "global_load_lds_dwordx4 %[goff], %[gbase] offset:1024\n"
    ".Lnodma%=:\n"
    "v_cvt_pk_bf16_f32 v92, v32, v33\n"
    "v_exp_f32 v36, v36\n"
    "v_exp_f32 v37, v37\n"
    "v_cvt_pk_bf16_f32 v93, v34, v35\n"
    "v_exp_f32 v38, v38\n"
    "v_exp_f32 v39, v39\n"
    "v_cvt_pk_bf16_f32 v94, v36, v37\n"
    "v_cvt_pk_bf16_f32 v95, v38, v39\n"
    "v_exp_f32 v40, v40\n"
    "v_exp_f32 v41, v41\n"
    "v_mfma_f32_32x32x16_bf16 v[56:71], v[112:115], v[92:95], v[56:71]\n"
    "v_exp_f32 v42, v42\n"
    "v_exp_f32 v43, v43\n"
    "v_cvt_pk_bf16_f32 v88, v40, v41\n"
    "v_exp_f32 v44, v44\n"
    "s_waitcnt lgkmcnt(0)\n"
    "v_mfma_f32_32x32x16_bf16 v[24:39], v[100:103], v[96:99], v[72:87]\n"
    "v_exp_f32 v45, v45\n"
    "v_cvt_pk_bf16_f32 v89, v42, v43\n"
    "ds_read_b128 v[104:107], v126 offset:1024\n"
    "v_exp_f32 v46, v46\n"
    "ds_read_b128 v[108:111], v127 offset:0\n"
    "v_exp_f32 v47, v47\n"
    "v_cvt_pk_bf16_f32 v90, v44, v45\n"
    "ds_read_b128 v[112:115], v127 offset:512\n"
    "v_exp_f32 v48, v48\n"
    "v_exp_f32 v49, v49\n"
    "v_cvt_pk_bf16_f32 v91, v46, v47\n"
    "v_exp_f32 v50, v50\n"
    "v_exp_f32 v51, v51\n"
    "v_mfma_f32_32x32x16_bf16 v[56:71], v[116:119], v[88:91], v[56:71]\n"
    "v_cvt_pk_bf16_f32 v92, v48, v49\n"
    "v_exp_f32 v52, v52\n"
    "v_exp_f32 v53, v53\n"
    "v_cvt_pk_bf16_f32 v93, v50, v51\n"
    "v_exp_f32 v54, v54\n"
    "v_exp_f32 v55, v55\n"
    "v_cvt_pk_bf16_f32 v94, v52, v53\n"
    "v_cvt_pk_bf16_f32 v95, v54, v55\n"
    "v_mov_b32 v124, v126\n"
    "v_mov_b32 v125, v127\n"
    "s_add_u32 %[nslot], %[nslot], 0x4000\n"
    "s_cmp_eq_u32 %[nslot], 0xc000\n"
    "s_cselect_b32 %[nslot], 0, %[nslot]\n"
    "v_add_u32 v126, %[nslot], %[kaddr]\n"
    "v_and_b32 v127, %[nslot], %[vsel]\n"
    "v_add_u32 v127, v127, %[vaddr]\n"
    "s_add_u32 %[dslot], %[dslot], 0x4000\n"
    "s_cmp_eq_u32 %[dslot], %[dend]\n"
    "s_cselect_b32 %[dslot], %[dbeg], %[dslot]\n"
    "v_add_u32 %[goff], 0x4000, %[goff]\n"
    "s_add_u32 %[cnt], %[cnt], 1\n"
    "s_cmp_lt_u32 %[cnt], 8\n"
    "s_cbranch_scc1 .Lloop%=\n"
    "s_nop 1\n"
    "v_mfma_f32_32x32x16_bf16 v[56:71], v[120:123], v[92:95], v[56:71]\n"
    "s_nop 15\n"
    "s_nop 3\n"
    "v_rcp_f32 v24, v64\n"
    "s_nop 0\n"
    "v_mul_f32 v56, v56, v24\n"
    "v_mul_f32 v57, v57, v24\n"
    "v_mul_f32 v58, v58, v24\n"
    "v_mul_f32 v59, v59, v24\n"
    "v_mul_f32 v60, v60, v24\n"
    "v_mul_f32 v61, v61, v24\n"
    "v_mul_f32 v62, v62, v24\n"
    "v_mul_f32 v63, v63, v24\n"
    "v_cvt_pk_bf16_f32 %[oa0], v56, v57\n"
    "v_cvt_pk_bf16_f32 %[oa1], v58, v59\n"
    "v_cvt_pk_bf16_f32 %[oa2], v60, v61\n"
    "v_cvt_pk_bf16_f32 %[oa3], v62, v63\n"
    "s_waitcnt lgkmcnt(0)\n"
    "s_mov_b32 m0, %[m0save]\n"
        : [oa0] "=&v"(oa0), [oa1] "=&v"(oa1), [oa2] "=&v"(oa2), [oa3] "=&v"(oa3),
          [goff] "+v"(goff), [dslot] "+s"(dslot), [nslot] "+s"(nslot), [cnt] "=&s"(cnt), [m0save] "=&s"(m0save)
        : [qa0] "v"(qa[0]), [qa1] "v"(qa[1]), [qa2] "v"(qa[2]), [qa3] "v"(qa[3]),
          [nma] "v"(nma), [kaddr] "v"(kaddr), [vaddr] "v"(vaddr), [vsel] "v"(vsel), [gbase] "s"(kvb), [dbeg] "s"(dbeg), [dend] "s"(dend), [psel] "s"(0x07060302u), [vmask] "s"(0x0000ffff0000ffffull), [vone] "v"(((lane & 27) == 16) ? 0x3f803f80u : 0u), [dbgaddr] "s"(lds0 + A_LDS + wid * 64)
        : "memory", "scc", "vcc",
          "v24", "v25", "v26", "v27", "v28", "v29", "v30", "v31", "v32", "v33", "v34", "v35", "v36", "v37", "v38", "v39", "v40", "v41", "v42", "v43", "v44", "v45", "v46", "v47", "v48", "v49", "v50", "v51", "v52", "v53", "v54", "v55", "v56", "v57", "v58", "v59", "v60", "v61", "v62", "v63", "v64", "v65", "v66", "v67", "v68", "v69", "v70", "v71", "v72", "v73", "v74", "v75", "v76", "v77", "v78", "v79", "v80", "v81", "v82", "v83", "v84", "v85", "v86", "v87", "v88", "v89", "v90", "v91", "v92", "v93", "v94", "v95", "v96", "v97", "v98", "v99", "v100", "v101", "v102", "v103", "v104", "v105", "v106", "v107", "v108", "v109", "v110", "v111", "v112", "v113", "v114", "v115", "v116", "v117", "v118", "v119", "v120", "v121", "v122", "v123", "v124", "v125", "v126", "v127"
    );
    STAMP(4);
    st16_wt(ws + WS_OF + ((size_t)tile0 * 32 + head) * 1024 + lane * 16, (u32x4){oa0, oa1, oa2, oa3});
    PUBLISH(FL_O + jb * 32 + head);
}

__device__ __forceinline__ void z_role(const uint16_t* __restrict__ x, const uint16_t* __restrict__ win, char* __restrict__ ws, char* lds, int g, u64& tag) {
    const int tid = threadIdx.x, lane = tid & 63, wid = __builtin_amdgcn_readfirstlane(tid >> 6), wr = wid >> 2, wc = wid & 3, l31 = lane & 31, hh = lane >> 5;
    const int xcd = g & 7, i = g >> 3;
    const int rb = 4 * (xcd >> 1) + (i & 3), cbz = 7 * ((xcd & 1) ^ 1) + (i >> 2);
    f32x16 acc[2];
    proj_main<false>(x, win, NIN, rb, cbz * 128, 12 + cbz, ws, lds, ((i & 3) * 4 + (i >> 2) * 3) & 15, tag, acc);
    STAMP(1);
    const float* st = (const float*)(lds + P_ST);
    unsigned zp[2][8];
#pragma unroll
    for (int rt = 0; rt < 2; ++rt) { const int tr = wr * 64 + rt * 32; const float mu = st[tr + l31], rs = st[128 + tr + l31];
#pragma unroll
        for (int q = 0; q < 4; ++q) { const f32x4 c = *(const f32x4*)(st + 256 + wc * 32 + 8 * q + 4 * hh);
            zp[rt][2 * q] = cvtpk(gelu_t((acc[rt][4 * q] - mu * c[0]) * rs), gelu_t((acc[rt][4 * q + 1] - mu * c[1]) * rs));
            zp[rt][2 * q + 1] = cvtpk(gelu_t((acc[rt][4 * q + 2] - mu * c[2]) * rs), gelu_t((acc[rt][4 * q + 3] - mu * c[3]) * rs)); } }
    if (cbz < 7) {
#pragma unroll
        for (int rt = 0; rt < 2; ++rt) { const int tt = rb * 4 + wr * 2 + rt;
            char* dst = ws + WS_Z1 + ((size_t)tt * 28 + cbz * 4 + wc) * 2048 + lane * 32;
            st16_wt(dst, (u32x4){zp[rt][0], zp[rt][1], zp[rt][2], zp[rt][3]}); st16_wt(dst + 16, (u32x4){zp[rt][4], zp[rt][5], zp[rt][6], zp[rt][7]}); }
    } else {
        const int cj = cbz - 7;
        float* pst = (float*)lds;
        const unsigned ONES = 0x3f803f80u;
#pragma unroll
        for (int rt = 0; rt < 2; ++rt) { float s1 = 0.f, s2 = 0.f;
#pragma unroll
            for (int e = 0; e < 8; ++e) { s1 = dot2(zp[rt][e], ONES, s1); s2 = dot2(zp[rt][e], zp[rt][e], s2); }
            s1 = swap32_add(s1); s2 = swap32_add(s2);
            if (hh == 0) *(f32x2*)(pst + (wc * 128 + wr * 64 + rt * 32 + l31) * 2) = (f32x2){s1, s2}; }
        __syncthreads();
        char* gst = ws + WS_ST + (size_t)rb * 7 * 2048;
        float* fin = (float*)(lds + 4096);
        if (tid < 128) { f32x2 s = *(const f32x2*)(pst + tid * 2);
#pragma unroll
            for (int j = 1; j < 4; ++j) s += *(const f32x2*)(pst + (j * 128 + tid) * 2);
            st16_wt(gst + (cj * 128 + tid) * 16, (u32x4){__float_as_uint(s[0]), __float_as_uint(s[1]), (unsigned)tag, (unsigned)(tag >> 32)});
            u32x4 gr[7];
            for (unsigned spins = 0; spins < (1u << 18); ++spins) {
                asm volatile("global_load_dwordx4 %0, %7, off sc1\n\tglobal_load_dwordx4 %1, %7, off offset:2048 sc1\n\tglobal_load_dwordx4 %2, %8, off sc1\n\tglobal_load_dwordx4 %3, %8, off offset:2048 sc1\n\t"
                             "global_load_dwordx4 %4, %9, off sc1\n\tglobal_load_dwordx4 %5, %9, off offset:2048 sc1\n\tglobal_load_dwordx4 %6, %10, off sc1\n\ts_waitcnt vmcnt(0)"
                             : "=&v"(gr[0]), "=&v"(gr[1]), "=&v"(gr[2]), "=&v"(gr[3]), "=&v"(gr[4]), "=&v"(gr[5]), "=&v"(gr[6])
                             : "v"(gst + tid * 16), "v"(gst + 4096 + tid * 16), "v"(gst + 8192 + tid * 16), "v"(gst + 12288 + tid * 16) : "memory");
                bool ok = true;
#pragma unroll
                for (int j = 0; j < 7; ++j) ok &= gr[j][2] == (unsigned)tag && gr[j][3] == (unsigned)(tag >> 32);
                if (__all(ok)) break;
                __builtin_amdgcn_s_sleep(POLL_SLEEP); }
            float s1 = 0.f, s2 = 0.f;
#pragma unroll
            for (int j = 0; j < 7; ++j) { s1 += __uint_as_float(gr[j][0]); s2 += __uint_as_float(gr[j][1]); }
            const float mu = s1 * (1.0f / FH); const float var = fmaxf(s2 * (1.0f / FH) - mu * mu, 0.f);
            fin[tid] = mu; fin[128 + tid] = __builtin_amdgcn_rsqf(var + LN_EPS); }
        __syncthreads();
        char* img = lds + 8192;
#pragma unroll
        for (int rt = 0; rt < 2; ++rt) { const int tok = wr * 64 + rt * 32 + l31; const float mu = fin[tok], rs = fin[128 + tok];
#pragma unroll
            for (int q = 0; q < 4; ++q) { const unsigned a = zp[rt][2 * q], c = zp[rt][2 * q + 1];
                const u32x2 o = {cvtpk((bflo(a) - mu) * rs, (bfhi(a) - mu) * rs), cvtpk((bflo(c) - mu) * rs, (bfhi(c) - mu) * rs)};
                const int f = wc * 32 + 8 * q + 4 * hh;
                *(u32x2*)(img + tok * 256 + (((f >> 3) ^ wsw(tok)) * 16) + (f & 4) * 2) = o; } }
        __syncthreads();
        { const int blk = (lane >> 4) & 1, q = (lane & 15) >> 2, p = lane & 3;
#pragma unroll
          for (int kq = 0; kq < 4; ++kq) { const int ks = wr * 4 + kq; s16x4 t2[2];
#pragma unroll
              for (int t = 0; t < 2; ++t) { const int kr = 16 * ks + 8 * hh + 4 * t + q; const int ch = 4 * wc + 2 * blk + (p >> 1);
                  t2[t] = trread((lds_cptr)img + kr * 256 + ((ch ^ wsw(kr)) * 16) + 8 * (p & 1)); }
              st16_wt(ws + WS_Z2N + ((size_t)(cj * 4 + wc) * 128 + rb * 8 + ks) * 1024 + lane * 16, __builtin_bit_cast(u32x4, cat(t2[0], t2[1]))); } }
    }
    PUBLISH(FL_Z + rb * 14 + cbz);
}

constexpr int S_SLOT = 24576, S_LDS = 3 * S_SLOT;
__device__ __forceinline__ void spatial_role(const uint16_t* __restrict__ wsp, const uint16_t* __restrict__ bsp, char* __restrict__ ws, char* lds, int g, u64 tag) {
    const int tid = threadIdx.x, lane = tid & 63, wid8 = __builtin_amdgcn_readfirstlane(tid >> 6), wid = wid8 & 3, l31 = lane & 31, hh = lane >> 5;
    const int i = g >> 3, tb = 4 * (g & 7) + (i & 3), fb = i >> 2;
    const unsigned lds0 = (unsigned)(uintptr_t)lds;
    STAMP(3);
    if (wid8 >= 4) {
        const int r0 = 16 * wid + (lane >> 3), r1 = r0 + 8;
        const char* sb_ = (const char*)(wsp + (size_t)tb * 64 * SEQ);
        const unsigned vo0 = (r0 * SEQ + ((lane & 7) ^ ((r0 >> 1) & 7)) * 8) * 2, vo1 = (r1 * SEQ + ((lane & 7) ^ ((r1 >> 1) & 7)) * 8) * 2;
        const char* zs = ws + WS_Z2N + (size_t)(fb * 4 + wid) * 131072; const unsigned zvo = lane * 16;
        unsigned ready = 0;
        GAS u64* zf = flagp(ws, FL_Z + (lane & 15) * 14 + 7 + fb);
        auto need = [&](int rbk) {
            for (unsigned spins = 0; !((ready >> rbk) & 1) && spins < (1u << 18); ++spins) {
                ready = (unsigned)__ballot(__hip_atomic_load(zf, RLX_AGENT) == tag) & 0xffffu;
                if (!((ready >> rbk) & 1)) __builtin_amdgcn_s_sleep(POLL_SLEEP); } };
        auto issue = [&](int it, int slot) { const unsigned so = lds0 + slot * S_SLOT;
            need(it >> 1);
            set_m0(so + wid * 2048); glds_s<0, false>(sb_ + it * 128, vo0);
            set_m0(so + wid * 2048 + 1024); glds_s<0, false>(sb_ + it * 128, vo1);
            set_m0(so + 8192 + wid * 4096);
            glds_s<0, true>(zs + it * 4096, zvo); glds_s<1024, true>(zs + it * 4096, zvo); glds_s<2048, true>(zs + it * 4096, zvo); glds_s<3072, true>(zs + it * 4096, zvo); };
        issue(0, 0); issue(1, 1); issue(2, 2);
        WAIT_BAR(12);
        int slot = 0;
#pragma unroll 1
        for (int it = 1; it < 31; ++it) { WAIT_BAR(6); if (it + 2 < 32) issue(it + 2, slot); slot = slot == 2 ? 0 : slot + 1; }
        WAIT_BAR(0);
        BAR();
    } else {
        unsigned boff[2][4];
#pragma unroll
        for (int rt = 0; rt < 2; ++rt)
#pragma unroll
            for (int ks = 0; ks < 4; ++ks) { const int r = rt * 32 + l31; boff[rt][ks] = (r * 128 + ((hh ^ ((r >> 1) & 7)) * 16)) ^ (ks * 32); }
        const unsigned aoff = 8192 + wid * 4096 + lane * 16;
        struct SF { bf16x8 a, b[2]; };
        auto rd = [&](SF& f, int slot, int ks) { lds_cptr sb = (lds_cptr)lds + slot * S_SLOT;
            f.a = ldsfrag(sb + aoff + ks * 1024); f.b[0] = ldsfrag(sb + boff[0][ks]); f.b[1] = ldsfrag(sb + boff[1][ks]); };
        f32x16 acc[2] = {f32x16{}, f32x16{}};
        auto mm = [&](const SF& f) { acc[0] = MFMA(f.a, f.b[0], acc[0]); acc[1] = MFMA(f.a, f.b[1], acc[1]); };
        SF f[4];
        BAR();
#pragma unroll
        for (int ks = 0; ks < 4; ++ks) rd(f[ks], 0, ks);
        auto step = [&](int nsl) {
            LGKM_BAR();
#pragma unroll
            for (int ks = 0; ks < 4; ++ks) { lds_cptr sb = (lds_cptr)lds + nsl * S_SLOT;
                acc[0] = MFMA(f[ks].a, f[ks].b[0], acc[0]); SBAR();
                f[ks].b[0] = ldsfrag(sb + boff[0][ks]); SBAR();
                PACE();
                acc[1] = MFMA(f[ks].a, f[ks].b[1], acc[1]); SBAR();
                f[ks].b[1] = ldsfrag(sb + boff[1][ks]); f[ks].a = ldsfrag(sb + aoff + ks * 1024); SBAR();
                PACE(); } };
#pragma unroll 1
        for (int io = 0; io < 10; ++io) { step(1); step(2); step(0); }
        step(1); step(2);
        wait_flags(flagp(ws, FL_Z + (tb >> 1) * 14 + fb), lane == 0, tag);
#pragma unroll
        for (int rt = 0; rt < 2; ++rt) { const int tt = tb * 2 + rt; const float bias = __uint_as_float((unsigned)bsp[tt * 32 + l31] << 16);
            const char* z1p = ws + WS_Z1 + ((size_t)tt * 28 + fb * 4 + wid) * 2048 + lane * 32;
            const u32x4 za = ld16_wt(z1p), zb = ld16_wt(z1p + 16);
            u32x4 ua, ub;
#pragma unroll
            for (int e = 0; e < 4; ++e) {
                ua[e] = cvtpk(bflo(za[e]) * (acc[rt][2 * e] + bias), bfhi(za[e]) * (acc[rt][2 * e + 1] + bias));
                ub[e] = cvtpk(bflo(zb[e]) * (acc[rt][8 + 2 * e] + bias), bfhi(zb[e]) * (acc[rt][8 + 2 * e + 1] + bias)); }
            char* ud = ws + WS_U + ((size_t)tt * 56 + (fb * 4 + wid) * 2) * 1024 + lane * 16;
            st16_wt(ud, ua); st16_wt(ud + 1024, ub); }
    }
    PUBLISH(FL_U + tb * 7 + fb);
}

constexpr int F_SLOT = 8192 + 8192, F_LDS = 4 * F_SLOT;
__device__ __forceinline__ void final_role(const uint16_t* __restrict__ x, const uint16_t* __restrict__ wo, const uint16_t* __restrict__ wout, char* __restrict__ ws, uint16_t* __restrict__ out, char* lds, int g, u64 tag) {
    const int tb = 4 * (g & 7) + ((g >> 3) & 3), db = g >> 5;
    const int tid = threadIdx.x, lane = tid & 63, wid8 = __builtin_amdgcn_readfirstlane(tid >> 6), wid = wid8 & 3, wr = wid >> 1, wc = wid & 1, l31 = lane & 31, hh = lane >> 5;
    const unsigned lds0 = (unsigned)(uintptr_t)lds;
    auto wswz = [](int r) { return ((r & 2) << 1) | ((r >> 2) & 3); };
    f32x16 as = f32x16{}, ag = f32x16{};
    const int k0 = 16 * wid + (lane >> 3), k1 = k0 + 8;
    const unsigned wo0 = (k0 * DM + db * 64 + ((lane & 7) ^ wswz(k0)) * 8) * 2, wo1 = (k1 * DM + db * 64 + ((lane & 7) ^ wswz(k1)) * 8) * 2;
    const int att = tb * 2 + (wid & 1), aks = 2 * (wid >> 1);
    unsigned woff[2];
    { const int blk = (lane >> 4) & 1, q = (lane & 15) >> 2, p = lane & 3; const int kr = 4 * hh + q; const int ch = 4 * wc + 2 * blk + (p >> 1);
      woff[0] = kr * 128 + ((ch ^ wswz(kr)) * 16) + 8 * (p & 1); woff[1] = (woff[0] + 1024) ^ 32; }
    const unsigned aoff = 8192 + wr * 4096 + lane * 16;
    struct FF { bf16x8 w, a; };
    auto rd = [&](FF& f, int slot, int ks) { lds_cptr sb = (lds_cptr)lds + slot * F_SLOT;
        f.w = cat(trread(sb + woff[0] + ks * 2048), trread(sb + woff[1] + ks * 2048)); f.a = ldsfrag(sb + aoff + ks * 1024); };
    auto pass = [&](const uint16_t* wb, const char* ab, int nfr, int n, f32x16& acc, bool pace) {
        if (wid8 >= 4) {
            auto issue = [&](int it) { const unsigned so = lds0 + (it & 3) * F_SLOT; const char* w = (const char*)(wb + (size_t)it * 64 * DM);
                set_m0(so + wid * 2048); glds_s<0, false>(w, wo0);
                set_m0(so + wid * 2048 + 1024); glds_s<0, false>(w, wo1);
                const char* a = ab + ((size_t)att * nfr + it * 4 + aks) * 1024;
                set_m0(so + 8192 + (wid & 1) * 4096 + aks * 1024); glds_s<0, true>(a, lane * 16); glds_s<1024, true>(a, lane * 16); };
            issue(0); issue(1); issue(2); issue(3);
            WAIT_BAR(12);
#pragma unroll 1
            for (int i = 1; i < n - 2; ++i) { WAIT_BAR(8); if (i + 3 < n) issue(i + 3); }
            WAIT_BAR(4);
            WAIT_BAR(0);
            BAR();
        } else {
            FF fa[4], fb[4];
            BAR();
#pragma unroll
            for (int ks = 0; ks < 4; ++ks) rd(fa[ks], 0, ks);
            auto step = [&](int nsl, FF (&cur)[4], FF (&nxt)[4]) {
                LGKM_BAR();
#pragma unroll
                for (int ks = 0; ks < 4; ++ks) { acc = MFMA(cur[ks].w, cur[ks].a, acc); SBAR(); rd(nxt[ks], nsl, ks); SBAR(); if (pace) PACE(); } };
#pragma unroll 1
            for (int it = 0; it < n; it += 2) { step((it + 1) & 3, fa, fb); step((it + 2) & 3, fb, fa); }
        } };
    if (wid8 == 0) wait_flags(flagp(ws, FL_U + tb * 7 + lane), lane < 7, tag);
    __syncthreads();
    STAMP(5);
    pass(wout, ws + WS_U, 56, 14, ag, true);
    STAMP(6);
    __syncthreads();
#pragma unroll
    for (int q = 0; q < 8; ++q) {
        const int kr = 8 * wid8 + (lane >> 3);
        set_m0(lds0 + q * 8192 + wid8 * 1024);
        glds_s<0, false>((const char*)(wo + (size_t)q * 64 * DM), (kr * DM + db * 64 + ((lane & 7) ^ wswz(kr)) * 8) * 2); }
    const int tok = tb * 64 + wr * 32 + l31;
    float gate[16]; u32x2 xv[4];
    if (wid8 < 4) {
#pragma unroll
        for (int q = 0; q < 4; ++q) xv[q] = *(const u32x2*)(x + (size_t)tok * DM + db * 64 + wc * 32 + 8 * q + 4 * hh);
#pragma unroll
        for (int r = 0; r < 16; ++r) gate[r] = silu_f(ag[r]); }
    if (wid8 == 0) wait_flags(flagp(ws, FL_O + (tb >> 2) * 32 + lane), lane < 32, tag);
    WAITV(0);
    __syncthreads();
    STAMP(7);
    const int kh = wid8 >> 2;
    u32x4 of[16];
    { const char* ob = ws + WS_OF + ((size_t)(tb * 2 + wr) * 32 + 16 * kh) * 1024; const unsigned v0 = lane * 16, v1 = v0 + 4096, v2 = v0 + 8192, v3 = v0 + 12288;
      asm volatile(
        "global_load_dwordx4 %0, %16, %20 sc1\n\tglobal_load_dwordx4 %1, %16, %20 offset:1024 sc1\n\tglobal_load_dwordx4 %2, %16, %20 offset:2048 sc1\n\tglobal_load_dwordx4 %3, %16, %20 offset:3072 sc1\n\t"
        "global_load_dwordx4 %4, %17, %20 sc1\n\tglobal_load_dwordx4 %5, %17, %20 offset:1024 sc1\n\tglobal_load_dwordx4 %6, %17, %20 offset:2048 sc1\n\tglobal_load_dwordx4 %7, %17, %20 offset:3072 sc1\n\t"
        "global_load_dwordx4 %8, %18, %20 sc1\n\tglobal_load_dwordx4 %9, %18, %20 offset:1024 sc1\n\tglobal_load_dwordx4 %10, %18, %20 offset:2048 sc1\n\tglobal_load_dwordx4 %11, %18, %20 offset:3072 sc1\n\t"
        "global_load_dwordx4 %12, %19, %20 sc1\n\tglobal_load_dwordx4 %13, %19, %20 offset:1024 sc1\n\tglobal_load_dwordx4 %14, %19, %20 offset:2048 sc1\n\tglobal_load_dwordx4 %15, %19, %20 offset:3072 sc1\n\t"
        "s_waitcnt vmcnt(0)"
        : "=&v"(of[0]), "=&v"(of[1]), "=&v"(of[2]), "=&v"(of[3]), "=&v"(of[4]), "=&v"(of[5]), "=&v"(of[6]), "=&v"(of[7]),
          "=&v"(of[8]), "=&v"(of[9]), "=&v"(of[10]), "=&v"(of[11]), "=&v"(of[12]), "=&v"(of[13]), "=&v"(of[14]), "=&v"(of[15])
        : "v"(v0), "v"(v1), "v"(v2), "v"(v3), "s"(ob) : "memory"); }
    { lds_cptr wb_ = (lds_cptr)lds + kh * 32768;
#pragma unroll
      for (int ks = 0; ks < 16; ++ks)
          as = MFMA(cat(trread(wb_ + woff[0] + ks * 2048), trread(wb_ + woff[1] + ks * 2048)), __builtin_bit_cast(bf16x8, of[ks]), as); }
    __syncthreads();
    if (kh == 1) {
#pragma unroll
        for (int q = 0; q < 4; ++q) *(f32x4*)(lds + (wid * 4 + q) * 1024 + lane * 16) = (f32x4){as[4 * q], as[4 * q + 1], as[4 * q + 2], as[4 * q + 3]}; }
    __syncthreads();
    STAMP(8);
    if (kh == 0) {
#pragma unroll
        for (int q = 0; q < 4; ++q) { const f32x4 p = *(const f32x4*)(lds + (wid * 4 + q) * 1024 + lane * 16);
            const float r0 = bflo(xv[q][0]) + (as[4 * q] + p[0]) * gate[4 * q], r1 = bfhi(xv[q][0]) + (as[4 * q + 1] + p[1]) * gate[4 * q + 1];
            const float r2 = bflo(xv[q][1]) + (as[4 * q + 2] + p[2]) * gate[4 * q + 2], r3 = bfhi(xv[q][1]) + (as[4 * q + 3] + p[3]) * gate[4 * q + 3];
            *(u32x2*)(out + (size_t)tok * DM + db * 64 + wc * 32 + 8 * q + 4 * hh) = (u32x2){cvtpk(r0, r1), cvtpk(r2, r3)}; }
    }
}

#define ADELAY 300
#define APRIO 3
#define GDELAY 0
#define GPRIO 2
constexpr int ALL_LDS = S_LDS;
__global__ __launch_bounds__(512, 4) void k_all(const uint16_t* __restrict__ x, const uint16_t* __restrict__ wqkv, const uint16_t* __restrict__ wo, const uint16_t* __restrict__ win,
                                                const uint16_t* __restrict__ wsp, const uint16_t* __restrict__ bsp, const uint16_t* __restrict__ wout, char* __restrict__ ws, uint16_t* __restrict__ out) {
    extern __shared__ __attribute__((aligned(16))) char lds[];
    const int b = blockIdx.x;
    u64 tag;
    STAMP(0);
    if (b < 256) {
        __builtin_amdgcn_s_setprio(APRIO);
        if (ADELAY > 0 && b < 192) {
            const u64 t0 = __builtin_amdgcn_s_memrealtime();
            const int i_ = b >> 3, cb_ = 6 * (b & 1) + (i_ >> 2), line = (i_ & 3) * 256 + (threadIdx.x & 255);
            unsigned junk = 0;
            while (__builtin_amdgcn_s_memrealtime() - t0 < ADELAY) __builtin_amdgcn_s_sleep(8);
            asm volatile("s_waitcnt vmcnt(0)" : "+v"(junk) :: "memory"); }
        if (b < 192) qkv_role(x, wqkv, ws, lds, b, tag); else stats_role(x, wqkv, win, ws, lds, b - 192, tag);
        __builtin_amdgcn_s_setprio(0);
        __syncthreads();
        attn_role(ws, lds, b, tag);
        STAMP(5);
    } else {
        const int g = b - 256;
        __builtin_amdgcn_s_setprio(GPRIO);
            if (GDELAY > 0) { const u64 t0 = __builtin_amdgcn_s_memrealtime(); while (__builtin_amdgcn_s_memrealtime() - t0 < GDELAY) __builtin_amdgcn_s_sleep(8); }
        if (g < 224) { z_role(x, win, ws, lds, g, tag); STAMP(2); __syncthreads(); spatial_role(wsp, bsp, ws, lds, g, tag); STAMP(4); __syncthreads(); }
        else { u64 ep; EPOCH_REQ(ep);
            { const int wv = (g - 224) * 8 + (threadIdx.x >> 6), lane = threadIdx.x & 63; unsigned junk = 0;
#pragma unroll
              for (int q = 0; q < 4; ++q) asm volatile("global_load_dword %0, %1, off" : "+v"(junk) : "v"((const char*)wsp + ((size_t)(q * 256 + wv) * 64 + lane) * 128) : "memory");
              if (wv < 112) asm volatile("global_load_dword %0, %1, off" : "+v"(junk) : "v"((const char*)wout + ((size_t)wv * 64 + lane) * 128) : "memory");
              else if (wv < 176) asm volatile("global_load_dword %0, %1, off" : "+v"(junk) : "v"((const char*)wo + ((size_t)(wv - 112) * 64 + lane) * 128) : "memory");
              asm volatile("s_waitcnt vmcnt(0)" : "+v"(junk) :: "memory"); }
            asm volatile("s_waitcnt vmcnt(0)" : "+v"(ep) :: "memory"); tag = EPOCH_TAG(ep); }
        if (threadIdx.x == 0) __hip_atomic_store(flagp(ws, FL_G + g), tag, RLX_AGENT);
        final_role(x, wo, wout, ws, out, lds, g, tag);
        if (g == 255 && threadIdx.x < 64) {
            const int lane = threadIdx.x;
#pragma unroll 1
            for (int c = 0; c < 4; ++c) { wait_flags(flagp(ws, FL_O + c * 64 + lane), true, tag); wait_flags(flagp(ws, FL_G + c * 64 + lane), true, tag); }
            if (lane == 0) __hip_atomic_store(flagp(ws, FL_EPOCH), tag, RLX_AGENT);
        }
        STAMP(9);
    }
}

extern "C" void kernel_launch(void* const* d_in, const int* in_sizes, int n_in, void* d_out, int out_size, void* d_ws, size_t ws_size, hipStream_t stream) {
    const uint16_t* x = (const uint16_t*)d_in[0]; const uint16_t* wqkv = (const uint16_t*)d_in[1]; const uint16_t* wo = (const uint16_t*)d_in[2];
    const uint16_t* win = (const uint16_t*)d_in[4]; const uint16_t* wsp = (const uint16_t*)d_in[5]; const uint16_t* bsp = (const uint16_t*)d_in[6]; const uint16_t* wout = (const uint16_t*)d_in[7];
    (void)hipFuncSetAttribute((const void*)k_all, hipFuncAttributeMaxDynamicSharedMemorySize, ALL_LDS);
    k_all<<<512, 512, ALL_LDS, stream>>>(x, wqkv, wo, win, wsp, bsp, wout, (char*)d_ws, (uint16_t*)d_out);
}
```

```cpp
#include <hip/hip_runtime.h>
#include <hip/hip_bf16.h>
#include <stdint.h>
#define SP_PRIO 1
#define PACE_N 0
#define WARM_IN 1
#include <stdio.h>

typedef short bf16x8 __attribute__((ext_vector_type(8)));
typedef short s16x4 __attribute__((ext_vector_type(4)));
typedef __bf16 bf16x2_t __attribute__((ext_vector_type(2)));
typedef float f32x16 __attribute__((ext_vector_type(16)));
typedef float f32x4 __attribute__((ext_vector_type(4)));
typedef float f32x2 __attribute__((ext_vector_type(2)));
typedef unsigned u32x4 __attribute__((ext_vector_type(4)));
typedef unsigned u32x2 __attribute__((ext_vector_type(2)));
typedef unsigned long long u64;
#define GAS __attribute__((address_space(1)))
#define LAS __attribute__((address_space(3)))
typedef LAS const char* lds_cptr;
typedef short v4i16_t __attribute__((ext_vector_type(4)));
#define RLX_AGENT __ATOMIC_RELAXED, __HIP_MEMORY_SCOPE_AGENT

constexpr int SEQ = 2048, DM = 512, NQKV = 1536, NIN = 1792, FH = 896;
constexpr float QSCALE = 0.25f * 1.4426950408889634f;
constexpr float LN_EPS = 1e-5f;
constexpr size_t MB = 1024 * 1024;
constexpr size_t WS_QF = 0, WS_KV = 2 * MB, WS_Z1 = 6 * MB, WS_Z2N = 10 * MB, WS_U = 14 * MB, WS_OF = 18 * MB;
constexpr size_t WS_KM = 20 * MB, WS_ST = 20 * MB + 65536, WS_RS = 20 * MB + 65536 + 229376, WS_CS = 20 * MB + 65536 + 245760  , WS_FLAG = 21 * MB;
constexpr int FL_RS = 0, FL_CS = 64, FL_QKV = 128, FL_ST = 320, FL_Z = 432, FL_O = 656, FL_U = 912, FL_G = 1136, FL_EPOCH = 1392;

#define MFMA(a, b, c) __builtin_amdgcn_mfma_f32_32x32x16_bf16(a, b, c, 0, 0, 0)
#define SBAR() __builtin_amdgcn_sched_barrier(0)
__device__ __forceinline__ unsigned cvtpk(float lo, float hi) { unsigned r; asm("v_cvt_pk_bf16_f32 %0, %1, %2" : "=v"(r) : "v"(lo), "v"(hi)); return r; }
__device__ __forceinline__ float bflo(unsigned u) { return __uint_as_float(u << 16); }
__device__ __forceinline__ float bfhi(unsigned u) { return __uint_as_float(u & 0xffff0000u); }
__device__ __forceinline__ float dot2(unsigned a, unsigned b, float acc) {
    return __builtin_amdgcn_fdot2_f32_bf16(__builtin_bit_cast(bf16x2_t, a), __builtin_bit_cast(bf16x2_t, b), acc, false); }
__device__ __forceinline__ void glds16(const void* g, unsigned lds_base) {
    asm volatile("s_mov_b32 m0, %1\n\ts_nop 0\n\tglobal_load_lds_dwordx4 %0, off" :: "v"(g), "s"(lds_base) : "memory", "m0"); }
template <int IMM, bool WT> __device__ __forceinline__ void glds_s(const void* sbase, unsigned voff) {
    if (WT) asm volatile("global_load_lds_dwordx4 %0, %1 offset:%2 sc1" :: "v"(voff), "s"(sbase), "n"(IMM) : "memory");
    else asm volatile("global_load_lds_dwordx4 %0, %1 offset:%2" :: "v"(voff), "s"(sbase), "n"(IMM) : "memory"); }
__device__ __forceinline__ void set_m0(unsigned v) { asm volatile("s_mov_b32 m0, %0\n\ts_nop 0" :: "s"(v) : "memory", "m0"); }
__device__ __forceinline__ s16x4 trread(lds_cptr p) { return __builtin_bit_cast(s16x4, __builtin_amdgcn_ds_read_tr16_b64_v4i16((LAS v4i16_t*)p)); }
__device__ __forceinline__ bf16x8 ldsfrag(lds_cptr p) { return *(LAS const bf16x8*)p; }
__device__ __forceinline__ bf16x8 cat(s16x4 a, s16x4 b) { return (bf16x8){a[0], a[1], a[2], a[3], b[0], b[1], b[2], b[3]}; }
__device__ __forceinline__ float swap32_add(float v) { auto rr = __builtin_amdgcn_permlane32_swap(__float_as_uint(v), __float_as_uint(v), false, false); return __uint_as_float(rr[0]) + __uint_as_float(rr[1]); }
__device__ __forceinline__ float swap32_max(float v) { auto rr = __builtin_amdgcn_permlane32_swap(__float_as_uint(v), __float_as_uint(v), false, false); return fmaxf(__uint_as_float(rr[0]), __uint_as_float(rr[1])); }
template <int CTRL> __device__ __forceinline__ float dppf(float v) { return __int_as_float(__builtin_amdgcn_update_dpp(0, __float_as_int(v), CTRL, 0xf, 0xf, false)); }
__device__ __forceinline__ float row16_add(float v) { v += dppf<0x128>(v); v += dppf<0x124>(v); v += dppf<0x122>(v); v += dppf<0x121>(v); return v; }
__device__ __forceinline__ float row16_max(float v) { v = fmaxf(v, dppf<0x128>(v)); v = fmaxf(v, dppf<0x124>(v)); v = fmaxf(v, dppf<0x122>(v)); v = fmaxf(v, dppf<0x121>(v)); return v; }
__device__ __forceinline__ float swap16_max(float v) { auto rr = __builtin_amdgcn_permlane16_swap(__float_as_uint(v), __float_as_uint(v), false, false); return fmaxf(__uint_as_float(rr[0]), __uint_as_float(rr[1])); }
__device__ __forceinline__ float gelu_t(float x) {
    const float c1 = -2.3022082f, c2 = -0.10294324f;
    float e = __builtin_amdgcn_exp2f(x * __builtin_fmaf(x * x, c2, c1));
    return x * __builtin_amdgcn_rcpf(1.0f + e); }
__device__ __forceinline__ float silu_f(float x) { return x * __builtin_amdgcn_rcpf(1.0f + __builtin_amdgcn_exp2f(-1.4426950408889634f * x)); }
#define STAMP(slot) do {} while (0)
#define PF_DIST 9
#define PACE() do { asm volatile("s_nop %0" :: "n"(PACE_N)); SBAR(); } while (0)
#define POLL_SLEEP 2
#define WAITV(n) asm volatile("s_waitcnt vmcnt(" #n ")" ::: "memory")
#define WAIT_BAR(n) asm volatile("s_waitcnt vmcnt(" #n ")\n\ts_barrier" ::: "memory")
#define BAR() asm volatile("s_barrier" ::: "memory")
#define LGKM_BAR() do { __builtin_amdgcn_s_waitcnt(0xC07F); asm volatile("" ::: "memory"); __builtin_amdgcn_s_barrier(); asm volatile("" ::: "memory"); } while (0)
__device__ __forceinline__ unsigned wsw(unsigned row) { return ((row & 3) << 2) | ((row >> 2) & 3); }
__device__ __forceinline__ void st16_wt(void* p, u32x4 v) {
    asm volatile("global_store_dwordx4 %0, %1, off sc1\n\ts_nop 1" :: "v"(p), "v"(v)); }
__device__ __forceinline__ void st8_wt(void* p, u64 v) { __hip_atomic_store((GAS u64*)p, v, RLX_AGENT); }
__device__ __forceinline__ u64 ld8_wt(const void* p) { return __hip_atomic_load((GAS u64*)p, RLX_AGENT); }
__device__ __forceinline__ u32x4 ld16_wt(const void* p) { u32x4 v; asm volatile("global_load_dwordx4 %0, %1, off sc1\n\ts_waitcnt vmcnt(0)" : "=&v"(v) : "v"(p) : "memory"); return v; }
__device__ __forceinline__ void glds16_wt(const void* g, unsigned lds_base) {
    asm volatile("s_mov_b32 m0, %1\n\ts_nop 0\n\tglobal_load_lds_dwordx4 %0, off sc1" :: "v"(g), "s"(lds_base) : "memory", "m0"); }
__device__ __forceinline__ GAS u64* flagp(char* ws, int idx) { return (GAS u64*)(ws + WS_FLAG) + idx * 16; }
__device__ __forceinline__ void wait_flags(GAS u64* f, bool want, u64 tag) {
    for (unsigned spins = 0; spins < (1u << 20); ++spins) {
        const u64 v = want ? __hip_atomic_load(f, RLX_AGENT) : tag;
        if (__all(v == tag)) return;
        __builtin_amdgcn_s_sleep(POLL_SLEEP); } }
#define ACQUIRE() do { __builtin_amdgcn_fence(__ATOMIC_ACQUIRE, "agent"); asm volatile("s_waitcnt vmcnt(0)" ::: "memory"); } while (0)
#define EPOCH_REQ(ep) asm volatile("global_load_dwordx2 %0, %1, off sc1" : "=v"(ep) : "v"(flagp(ws, FL_EPOCH)) : "memory")
#define EPOCH_TAG(ep) ((((u64)(unsigned)__builtin_amdgcn_readfirstlane((unsigned)((ep) >> 32)) << 32) | (unsigned)__builtin_amdgcn_readfirstlane((unsigned)(ep))) + 1)
#define PUBLISH(idx) do { WAITV(0); __syncthreads(); if (threadIdx.x == 0) __hip_atomic_store(flagp(ws, idx), tag, RLX_AGENT); } while (0)

__device__ __forceinline__ void stats_role(const uint16_t* __restrict__ x, const uint16_t* __restrict__ wqkv, const uint16_t* __restrict__ win, char* __restrict__ ws, char* lds, int j, u64& tag) {
    u64 ep; EPOCH_REQ(ep);
    const int tid = threadIdx.x, lane = tid & 63, wid = tid >> 6, rr = lane >> 4, cc = lane & 15;
    const unsigned ONES = 0x3f803f80u;
    u32x4 xv[4], w[8];
    const int row = 32 * j + 4 * wid + rr;
#pragma unroll
    for (int i = 0; i < 4; ++i) xv[i] = *(const u32x4*)(x + (size_t)row * DM + i * 128 + cc * 8);
    const int cbk = j >> 1;
    if (j < 52) {
        const uint16_t* W = cbk < 12 ? wqkv + cbk * 128 : win + (cbk - 12) * 128; const int ldw = cbk < 12 ? NQKV : NIN;
        const uint16_t* p = W + (size_t)((j & 1) * 256 + 32 * wid + rr) * ldw + cc * 8;
#pragma unroll
        for (int i = 0; i < 8; ++i) w[i] = *(const u32x4*)(p + (size_t)(i * 4) * ldw);
    }
    asm volatile("s_waitcnt vmcnt(0)" : "+v"(ep) :: "memory");
    tag = EPOCH_TAG(ep);
    float s1 = 0.f, s2 = 0.f;
#pragma unroll
    for (int i = 0; i < 4; ++i)
#pragma unroll
        for (int e = 0; e < 4; ++e) { s1 = dot2(xv[i][e], ONES, s1); s2 = dot2(xv[i][e], xv[i][e], s2); }
    s1 = row16_add(s1); s2 = row16_add(s2);
    { const float mu = s1 * (1.0f / DM), var = fmaxf(s2 * (1.0f / DM) - mu * mu, 0.f);
      if (cc == 0) st8_wt(ws + WS_RS + (size_t)row * 8, __builtin_bit_cast(u64, (f32x2){mu, __builtin_amdgcn_rsqf(var + LN_EPS)})); }
    if (j < 52) {
        float cs[8] = {0.f, 0.f, 0.f, 0.f, 0.f, 0.f, 0.f, 0.f};
        unsigned sel_lo = 0x00003f80u, sel_hi = 0x3f800000u;
        asm volatile("" : "+v"(sel_lo), "+v"(sel_hi));
#pragma unroll
        for (int i = 0; i < 8; ++i)
#pragma unroll
            for (int e = 0; e < 4; ++e) { cs[2 * e] = dot2(w[i][e], sel_lo, cs[2 * e]); cs[2 * e + 1] = dot2(w[i][e], sel_hi, cs[2 * e + 1]); }
        float* red = (float*)lds;
        *(f32x4*)(red + (wid * 4 + rr) * 128 + cc * 8) = (f32x4){cs[0], cs[1], cs[2], cs[3]}; *(f32x4*)(red + (wid * 4 + rr) * 128 + cc * 8 + 4) = (f32x4){cs[4], cs[5], cs[6], cs[7]};
        __syncthreads();
        if (tid < 64) { f32x2 a = {0.f, 0.f};
#pragma unroll
            for (int q = 0; q < 32; ++q) a += *(const f32x2*)(red + q * 128 + 2 * tid);
            st8_wt(ws + WS_CS + (size_t)((j & 1) * 3328 + cbk * 128 + 2 * tid) * 4, __builtin_bit_cast(u64, a)); }
    }
    STAMP(11);
    WAITV(0); __syncthreads();
    STAMP(1);
    if (tid == 0) { __hip_atomic_store(flagp(ws, FL_RS + j), tag, RLX_AGENT); if (j < 52) __hip_atomic_store(flagp(ws, FL_CS + j), tag, RLX_AGENT); }
}

constexpr int P_SLOT = 16384, P_ST = 4 * P_SLOT, P_LDS = 4 * P_SLOT + 4096;
struct PFrag { bf16x8 x[2], w; };
template <bool SWAP>
__device__ __forceinline__ void proj_main(const uint16_t* __restrict__ x, const uint16_t* __restrict__ W, int ldw, int rb, int col0, int cblk, char* __restrict__ ws, char* lds, int rot, u64& tag, f32x16 (&acc)[2]) {
    u64 ep; EPOCH_REQ(ep);
    const int tid = threadIdx.x, lane = tid & 63, wid = __builtin_amdgcn_readfirstlane(tid >> 6), wr = wid >> 2, wc = wid & 3, l31 = lane & 31, hh = lane >> 5;
    const unsigned lds0 = (unsigned)(uintptr_t)lds;
    const int xr = 16 * wid + (lane >> 2), wk = 4 * wid + (lane >> 4);
    const char* xb = (const char*)(x + (size_t)rb * 128 * DM); const char* wb = (const char*)(W + col0);
    const unsigned xvo = (xr * DM + ((lane & 3) ^ ((xr >> 2) & 3)) * 8) * 2, wvo = (wk * ldw + ((lane & 15) ^ wsw(wk)) * 8) * 2;
    const unsigned xd = lds0 + wid * 1024, wd = lds0 + 8192 + wid * 1024;
    auto issue1 = [&](int it, int which) { const unsigned so = (it & 3) * P_SLOT; const int kk = (it + rot) & 15;
        if (which == 0) { set_m0(xd + so); glds_s<0, false>(xb + kk * 64, xvo); } else { set_m0(wd + so); glds_s<0, false>(wb + (size_t)kk * 64 * ldw, wvo); } SBAR(); };
#pragma unroll
    for (int it = 0; it < 4; ++it) { issue1(it, 0); issue1(it, 1); }
    unsigned xoff[2][2], woff[2][2];
    const int blk = (lane >> 4) & 1, q = (lane & 15) >> 2, p = lane & 3;
#pragma unroll
    for (int ks = 0; ks < 2; ++ks) {
#pragma unroll
        for (int rt = 0; rt < 2; ++rt) { const int r = wr * 64 + rt * 32 + l31; xoff[rt][ks] = r * 64 + (((2 * ks + hh) ^ ((r >> 2) & 3)) * 16); }
        const int kr = 8 * hh + q, ch = 4 * wc + 2 * blk + (p >> 1);
        woff[0][ks] = 8192 + ks * 4096 + kr * 256 + ((ch ^ wsw(kr)) * 16) + 8 * (p & 1); woff[1][ks] = (woff[0][ks] + 1024) ^ 16; }
    auto rd = [&](PFrag& f, int slot, int j) { lds_cptr sb = (lds_cptr)lds + slot * P_SLOT;
        f.x[0] = ldsfrag(sb + xoff[0][j]); f.x[1] = ldsfrag(sb + xoff[1][j]); f.w = cat(trread(sb + woff[0][j]), trread(sb + woff[1][j])); };
    auto mm = [&](const PFrag& f) {
#pragma unroll
        for (int rt = 0; rt < 2; ++rt) acc[rt] = SWAP ? MFMA(f.x[rt], f.w, acc[rt]) : MFMA(f.w, f.x[rt], acc[rt]); };
    acc[0] = f32x16{}; acc[1] = f32x16{};
    PFrag fa[2], fb[2];
    asm volatile("s_waitcnt vmcnt(6)\n\ts_barrier" : "+v"(ep) :: "memory");
    tag = EPOCH_TAG(ep);
    STAMP(10);
    rd(fa[0], 0, 0); rd(fa[1], 0, 1);
    auto mm1 = [&](const PFrag& f, int rt) { acc[rt] = SWAP ? MFMA(f.x[rt], f.w, acc[rt]) : MFMA(f.w, f.x[rt], acc[rt]); SBAR(); };
    auto step = [&](int it, int sl, PFrag (&cur)[2], PFrag (&nxt)[2]) {
        __builtin_amdgcn_s_waitcnt(0xC07F);
        if (it < 13) WAITV(4); else if (it == 13) WAITV(2); else WAITV(0);
        __builtin_amdgcn_s_barrier(); asm volatile("" ::: "memory");
        lds_cptr sb = (lds_cptr)lds + ((sl + 1) & 3) * P_SLOT;
        mm1(cur[0], 0);
        nxt[0].x[0] = ldsfrag(sb + xoff[0][0]); nxt[0].x[1] = ldsfrag(sb + xoff[1][0]); SBAR();
        mm1(cur[0], 1);
        nxt[0].w = cat(trread(sb + woff[0][0]), trread(sb + woff[1][0])); SBAR();
        if (it + 4 < 16) issue1(it + 4, 0);
        mm1(cur[1], 0);
        nxt[1].x[0] = ldsfrag(sb + xoff[0][1]); nxt[1].x[1] = ldsfrag(sb + xoff[1][1]); SBAR();
        mm1(cur[1], 1);
        nxt[1].w = cat(trread(sb + woff[0][1]), trread(sb + woff[1][1])); SBAR();
        if (it + 4 < 16) issue1(it + 4, 1); };
#pragma unroll 1
    for (int io = 0; io < 2; ++io) { step(io * 4, 0, fa, fb); step(io * 4 + 1, 1, fb, fa); step(io * 4 + 2, 2, fa, fb); step(io * 4 + 3, 3, fb, fa); }
    const char* fa_ = (const char*)(lane < 4 ? flagp(ws, FL_RS + rb * 4 + lane) : flagp(ws, FL_CS + 2 * cblk + (lane & 1)));
    const char* sa_ = tid < 128 ? ws + WS_RS + (size_t)(rb * 128 + tid) * 8 : ws + WS_CS + (size_t)(cblk * 128 + 2 * (tid & 63)) * 4;
    const char* sb_ = ws + WS_CS + (size_t)(3328 + cblk * 128 + 2 * (tid & 63)) * 4;
    u64 fl, stv, stw;
    step(8, 0, fa, fb); step(9, 1, fb, fa);
    asm volatile("global_load_dwordx2 %0, %1, off sc1" : "=v"(fl) : "v"(fa_) : "memory");
    step(10, 2, fa, fb); step(11, 3, fb, fa); step(12, 0, fa, fb);
    asm volatile("" : "+v"(fl));
    const bool got = __all(lane >= 6 || fl == tag);
    asm volatile("global_load_dwordx2 %0, %2, off sc1\n\tglobal_load_dwordx2 %1, %3, off sc1" : "=&v"(stv), "=&v"(stw) : "v"(sa_), "v"(sb_) : "memory");
    step(13, 1, fb, fa); step(14, 2, fa, fb); step(15, 3, fb, fa);
    asm volatile("" : "+v"(stv), "+v"(stw));
    if (!got) { wait_flags((GAS u64*)fa_, lane < 6, tag); stv = ld8_wt(sa_); stw = ld8_wt(sb_); }
    STAMP(11);
    float* st = (float*)(lds + P_ST);
    { const f32x2 v = __builtin_bit_cast(f32x2, stv);
      if (tid < 128) { st[tid] = v[0]; st[128 + tid] = v[1]; } else if (tid < 192) *(f32x2*)(st + 256 + 2 * (tid - 128)) = v + __builtin_bit_cast(f32x2, stw); }
    __syncthreads();
}

__device__ __forceinline__ void qkv_role(const uint16_t* __restrict__ x, const uint16_t* __restrict__ wqkv, char* __restrict__ ws, char* lds, int b, u64& tag) {
    const int xcd = b & 7, i = b >> 3;
    const int rb = 4 * (xcd >> 1) + (i & 3), cb = 6 * (xcd & 1) + (i >> 2);
    const int tid = threadIdx.x, lane = tid & 63, wid = __builtin_amdgcn_readfirstlane(tid >> 6), wr = wid >> 2, wc = wid & 3, l31 = lane & 31, hh = lane >> 5;
    f32x16 acc[2];
    const int rot = ((i & 3) * 4 + (i >> 2) * 3) & 15;
    if (cb >= 8) proj_main<true>(x, wqkv, NQKV, rb, cb * 128, cb, ws, lds, rot, tag, acc); else proj_main<false>(x, wqkv, NQKV, rb, cb * 128, cb, ws, lds, rot, tag, acc);
    STAMP(1);
    const float* st = (const float*)(lds + P_ST);
    if (cb >= 8) {
        const float cs = st[256 + wc * 32 + l31];
        const int head = (cb - 8) * 8 + wc * 2 + (l31 >> 4);
#pragma unroll
        for (int rt = 0; rt < 2; ++rt) { const int tr = wr * 64 + rt * 32;
            unsigned pk[8];
#pragma unroll
            for (int q = 0; q < 4; ++q) { const f32x4 m = *(const f32x4*)(st + tr + 8 * q + 4 * hh), r = *(const f32x4*)(st + 128 + tr + 8 * q + 4 * hh);
                pk[2 * q] = cvtpk((acc[rt][4 * q] - m[0] * cs) * r[0], (acc[rt][4 * q + 1] - m[1] * cs) * r[1]);
                pk[2 * q + 1] = cvtpk((acc[rt][4 * q + 2] - m[2] * cs) * r[2], (acc[rt][4 * q + 3] - m[3] * cs) * r[3]); }
            const int tile = rb * 4 + (tr >> 5);
            char* dst = ws + WS_KV + (size_t)head * 131072 + (tile >> 3) * 16384 + 8192 + (tile & 7) * 1024 + hh * 256 + (l31 & 15) * 16;
            st16_wt(dst, (u32x4){pk[0], pk[1], pk[2], pk[3]}); st16_wt(dst + 512, (u32x4){pk[4], pk[5], pk[6], pk[7]}); }
    } else {
        const bool isK = cb >= 4; const float sc = isK ? 1.0f : QSCALE;
        float kmx[2] = {0.f, 0.f};
#pragma unroll
        for (int rt = 0; rt < 2; ++rt) { const int tr = wr * 64 + rt * 32, tile = rb * 4 + (tr >> 5);
            const float mu = st[tr + l31], rs = st[128 + tr + l31] * sc;
#pragma unroll
            for (int hb = 0; hb < 2; ++hb) {
                float v[8];
#pragma unroll
                for (int q = 0; q < 2; ++q) { const f32x4 c = *(const f32x4*)(st + 256 + wc * 32 + 16 * hb + 8 * q + 4 * hh);
#pragma unroll
                    for (int e = 0; e < 4; ++e) v[4 * q + e] = (acc[rt][8 * hb + 4 * q + e] - mu * c[e]) * rs; }
                u32x4 pk;
#pragma unroll
                for (int e = 0; e < 4; ++e) pk[e] = cvtpk(v[2 * e], v[2 * e + 1]);
                const int head = (cb & 3) * 8 + wc * 2 + hb;
                char* dst = isK ? ws + WS_KV + (size_t)head * 131072 + (tile >> 3) * 16384 + (tile & 7) * 1024 + lane * 16 : ws + WS_QF + (size_t)head * 65536 + tile * 1024 + lane * 16;
                st16_wt(dst, pk);
                float n2 = 0.f;
#pragma unroll
                for (int e = 0; e < 4; ++e) n2 = dot2(pk[e], pk[e], n2);
                kmx[hb] = fmaxf(kmx[hb], swap32_add(n2));
            } }
        if (isK) {
#pragma unroll
            for (int hb = 0; hb < 2; ++hb) { const float m = swap16_max(row16_max(kmx[hb]));
                if (lane == 0) __hip_atomic_store((GAS unsigned*)(ws + WS_KM) + ((cb & 3) * 8 + wc * 2 + hb) * 32 + rb * 2 + wr, __float_as_uint(m), RLX_AGENT); } }
    }
    STAMP(12);
    PUBLISH(FL_QKV + cb * 16 + rb);
    STAMP(13);
}

constexpr int A_ONES = 3 * 16384, A_LDS = A_ONES + 8192 + 512;
__device__ __forceinline__ void attn_role(char* __restrict__ ws, char* lds, int b, u64 tag) {
    const int tid = threadIdx.x, lane = tid & 63, wid = __builtin_amdgcn_readfirstlane(tid >> 6);
    const int head = (b & 7) * 4 + ((b >> 3) & 3), jb = b >> 5;
    const unsigned lds0 = (unsigned)(uintptr_t)lds;
    const char* kvb = ws + WS_KV + (size_t)head * 131072;
    for (int i = tid; i < (8192 + 512) / 16; i += 512) { const unsigned o1 = ((i & 11) == 0) ? 0x3f803f80u : 0u; *(u32x4*)(lds + A_ONES + i * 16) = (u32x4){o1, o1, o1, o1}; }
    if (wid == 0) {
        const int hb = head >> 3;
        const int idx = lane < 16 ? (4 + hb) * 16 + lane : lane < 32 ? (8 + hb) * 16 + (lane - 16) : hb * 16 + 2 * jb + (lane & 1);
        wait_flags(flagp(ws, FL_QKV + idx), lane < 34, tag);
        STAMP(14);
        }
    __syncthreads();
    STAMP(2);
    unsigned goff = wid * 2048 + lane * 16;
    const int tile0 = jb * 8 + wid;
    u32x4 qa; unsigned kmu;
    asm volatile("global_load_dwordx4 %0, %2, off sc1\n\tglobal_load_dword %1, %3, off sc1" : "=&v"(qa), "=&v"(kmu)
                 : "v"(ws + WS_QF + (size_t)head * 65536 + tile0 * 1024 + lane * 16), "v"(ws + WS_KM + (size_t)(head * 32 + (lane & 31)) * 4) : "memory");
#pragma unroll
    for (int c = 0; c < 2; ++c) { set_m0(lds0 + c * 16384 + wid * 2048); glds_s<0, true>(kvb + c * 16384, goff); glds_s<1024, true>(kvb + c * 16384, goff); }
    asm volatile("s_waitcnt vmcnt(4)" : "+v"(qa), "+v"(kmu) :: "memory");
    const float km = swap16_max(row16_max(__uint_as_float(kmu)));
    float na = 0.f;
#pragma unroll
    for (int e = 0; e < 4; ++e) na = dot2(qa[e], qa[e], na);
    na = swap32_add(na);
    float ma = __builtin_amdgcn_sqrtf(na * km);
    if (__any(!(ma <= 40.0f))) {
        float xa = -3.0e38f;
        for (int t = 0; t < 64; ++t) {
            const bf16x8 kf = __builtin_bit_cast(bf16x8, ld16_wt(kvb + (t >> 3) * 16384 + (t & 7) * 1024 + lane * 16));
            const f32x16 sa = MFMA(kf, __builtin_bit_cast(bf16x8, qa), f32x16{});
#pragma unroll
            for (int r = 0; r < 16; ++r) xa = fmaxf(xa, sa[r]); }
        ma = swap32_max(xa);
    }
    const float nma = -ma;
    const unsigned kaddr = lds0 + lane * 16;
    const bool ones = (lane & 16) != 0;
    const unsigned vaddr = ones ? lds0 + A_ONES + (lane & 15) * 16 : lds0 + 8192 + (lane >> 5) * 256 + (lane & 15) * 16;
    const unsigned vsel = ones ? 0u : 0xffffffffu;
    goff += 2 * 16384;
    unsigned dbeg = lds0 + wid * 2048, dend = dbeg + 3 * 16384, dslot = dbeg + 2 * 16384, nslot = 0x4000, cnt, m0save;
    unsigned oa0, oa1, oa2, oa3;
    STAMP(3);
    asm volatile(
    "s_mov_b32 %[m0save], m0\n"
    "v_mov_b32 v96, %[qa0]\n"
    "v_mov_b32 v97, %[qa1]\n"
    "v_mov_b32 v98, %[qa2]\n"
    "v_mov_b32 v99, %[qa3]\n"
    "v_mov_b32 v72, %[nma]\n"
    "v_mov_b32 v56, 0\n"
    "v_mov_b32 v73, %[nma]\n"
    "v_mov_b32 v57, 0\n"
    "v_mov_b32 v74, %[nma]\n"
    "v_mov_b32 v58, 0\n"
    "v_mov_b32 v75, %[nma]\n"
    "v_mov_b32 v59, 0\n"
    "v_mov_b32 v76, %[nma]\n"
    "v_mov_b32 v60, 0\n"
    "v_mov_b32 v77, %[nma]\n"
    "v_mov_b32 v61, 0\n"
    "v_mov_b32 v78, %[nma]\n"
    "v_mov_b32 v62, 0\n"
    "v_mov_b32 v79, %[nma]\n"
    "v_mov_b32 v63, 0\n"
    "v_mov_b32 v80, %[nma]\n"
    "v_mov_b32 v64, 0\n"
    "v_mov_b32 v81, %[nma]\n"
    "v_mov_b32 v65, 0\n"
    "v_mov_b32 v82, %[nma]\n"
    "v_mov_b32 v66, 0\n"
    "v_mov_b32 v83, %[nma]\n"
    "v_mov_b32 v67, 0\n"
    "v_mov_b32 v84, %[nma]\n"
    "v_mov_b32 v68, 0\n"
    "v_mov_b32 v85, %[nma]\n"
    "v_mov_b32 v69, 0\n"
    "v_mov_b32 v86, %[nma]\n"
    "v_mov_b32 v70, 0\n"
    "v_mov_b32 v87, %[nma]\n"
    "v_mov_b32 v71, 0\n"
    "v_mov_b32 v120, 0\n"
    "v_mov_b32 v121, 0\n"
    "v_mov_b32 v122, 0\n"
    "v_mov_b32 v123, 0\n"
    "v_mov_b32 v88, 0\n"
    "v_mov_b32 v89, 0\n"
    "v_mov_b32 v90, 0\n"
    "v_mov_b32 v91, 0\n"
    "v_mov_b32 v92, 0\n"
    "v_mov_b32 v93, 0\n"
    "v_mov_b32 v94, 0\n"
    "v_mov_b32 v95, 0\n"
    "v_mov_b32 v124, %[kaddr]\n"
    "v_mov_b32 v125, %[vaddr]\n"
    "v_add_u32 v126, 0x4000, v124\n"
    "v_and_b32 v127, 0x4000, %[vsel]\n"
    "v_add_u32 v127, v127, v125\n"
    "s_waitcnt vmcnt(2) lgkmcnt(0)\n"
    "s_barrier\n"
    "ds_read_b128 v[100:103], v124\n"
    "ds_read_b128 v[104:107], v124 offset:1024\n"
    "ds_read_b128 v[108:111], v125\n"
    "ds_read_b128 v[112:115], v125 offset:512\n"
    "s_mov_b32 %[cnt], 0\n"
    "s_waitcnt lgkmcnt(0)\n"
    "v_mfma_f32_32x32x16_bf16 v[24:39], v[100:103], v[96:99], v[72:87]\n"
    "s_nop 15\n"
    ".Lloop%=:\n"
    "v_exp_f32 v24, v24\n"
    "v_exp_f32 v25, v25\n"
    "v_mfma_f32_32x32x16_bf16 v[56:71], v[120:123], v[92:95], v[56:71]\n"
    "v_exp_f32 v26, v26\n"
    "v_exp_f32 v27, v27\n"
    "v_cvt_pk_bf16_f32 v88, v24, v25\n"
    "v_exp_f32 v28, v28\n"
    "s_waitcnt lgkmcnt(0)\n"
    "v_mfma_f32_32x32x16_bf16 v[40:55], v[104:107], v[96:99], v[72:87]\n"
    "v_exp_f32 v29, v29\n"
    "v_cvt_pk_bf16_f32 v89, v26, v27\n"
    "ds_read_b128 v[100:103], v124 offset:2048\n"
    "v_exp_f32 v30, v30\n"
    "ds_read_b128 v[116:119], v125 offset:1024\n"
    "v_exp_f32 v31, v31\n"
    "v_cvt_pk_bf16_f32 v90, v28, v29\n"
    "ds_read_b128 v[120:123], v125 offset:1536\n"
    "v_exp_f32 v32, v32\n"
    "v_exp_f32 v33, v33\n"
    "v_cvt_pk_bf16_f32 v91, v30, v31\n"
    "v_exp_f32 v34, v34\n"
    "v_exp_f32 v35, v35\n"
    "v_mfma_f32_32x32x16_bf16 v[56:71], v[108:111], v[88:91], v[56:71]\n"
    "v_cvt_pk_bf16_f32 v92, v32, v33\n"
    "v_exp_f32 v36, v36\n"
    "v_exp_f32 v37, v37\n"
    "v_cvt_pk_bf16_f32 v93, v34, v35\n"
    "v_exp_f32 v38, v38\n"
    "v_exp_f32 v39, v39\n"
    "v_cvt_pk_bf16_f32 v94, v36, v37\n"
    "v_cvt_pk_bf16_f32 v95, v38, v39\n"
    "v_exp_f32 v40, v40\n"
    "v_exp_f32 v41, v41\n"
    "v_mfma_f32_32x32x16_bf16 v[56:71], v[112:115], v[92:95], v[56:71]\n"
    "v_exp_f32 v42, v42\n"
    "v_exp_f32 v43, v43\n"
    "v_cvt_pk_bf16_f32 v88, v40, v41\n"
    "v_exp_f32 v44, v44\n"
    "s_waitcnt lgkmcnt(0)\n"
    "v_mfma_f32_32x32x16_bf16 v[24:39], v[100:103], v[96:99], v[72:87]\n"
    "v_exp_f32 v45, v45\n"
    "v_cvt_pk_bf16_f32 v89, v42, v43\n"
    "ds_read_b128 v[104:107], v124 offset:3072\n"
    "v_exp_f32 v46, v46\n"
    "ds_read_b128 v[108:111], v125 offset:2048\n"
    "v_exp_f32 v47, v47\n"
    "v_cvt_pk_bf16_f32 v90, v44, v45\n"
    "ds_read_b128 v[112:115], v125 offset:2560\n"
    "v_exp_f32 v48, v48\n"
    "v_exp_f32 v49, v49\n"
    "v_cvt_pk_bf16_f32 v91, v46, v47\n"
    "v_exp_f32 v50, v50\n"
    "v_exp_f32 v51, v51\n"
    "v_mfma_f32_32x32x16_bf16 v[56:71], v[116:119], v[88:91], v[56:71]\n"
    "v_cvt_pk_bf16_f32 v92, v48, v49\n"
    "v_exp_f32 v52, v52\n"
    "v_exp_f32 v53, v53\n"
    "v_cvt_pk_bf16_f32 v93, v50, v51\n"
    "v_exp_f32 v54, v54\n"
    "v_exp_f32 v55, v55\n"
    "v_cvt_pk_bf16_f32 v94, v52, v53\n"
    "v_cvt_pk_bf16_f32 v95, v54, v55\n"
    "v_exp_f32 v24, v24\n"
    "v_exp_f32 v25, v25\n"
    "v_mfma_f32_32x32x16_bf16 v[56:71], v[120:123], v[92:95], v[56:71]\n"
    "v_exp_f32 v26, v26\n"
    "v_exp_f32 v27, v27\n"
    "v_cvt_pk_bf16_f32 v88, v24, v25\n"
    "v_exp_f32 v28, v28\n"
    "s_waitcnt lgkmcnt(0)\n"
    "v_mfma_f32_32x32x16_bf16 v[40:55], v[104:107], v[96:99], v[72:87]\n"
    "v_exp_f32 v29, v29\n"
    "v_cvt_pk_bf16_f32 v89, v26, v27\n"
    "ds_read_b128 v[100:103], v124 offset:4096\n"
    "v_exp_f32 v30, v30\n"
    "ds_read_b128 v[116:119], v125 offset:3072\n"
    "v_exp_f32 v31, v31\n"
    "v_cvt_pk_bf16_f32 v90, v28, v29\n"
    "ds_read_b128 v[120:123], v125 offset:3584\n"
    "v_exp_f32 v32, v32\n"
    "v_exp_f32 v33, v33\n"
    "v_cvt_pk_bf16_f32 v91, v30, v31\n"
    "v_exp_f32 v34, v34\n"
    "v_exp_f32 v35, v35\n"
    "v_mfma_f32_32x32x16_bf16 v[56:71], v[108:111], v[88:91], v[56:71]\n"
    "v_cvt_pk_bf16_f32 v92, v32, v33\n"
    "v_exp_f32 v36, v36\n"
    "v_exp_f32 v37, v37\n"
    "v_cvt_pk_bf16_f32 v93, v34, v35\n"
    "v_exp_f32 v38, v38\n"
    "v_exp_f32 v39, v39\n"
    "v_cvt_pk_bf16_f32 v94, v36, v37\n"
    "v_cvt_pk_bf16_f32 v95, v38, v39\n"
    "v_exp_f32 v40, v40\n"
    "v_exp_f32 v41, v41\n"
    "v_mfma_f32_32x32x16_bf16 v[56:71], v[112:115], v[92:95], v[56:71]\n"
    "v_exp_f32 v42, v42\n"
    "v_exp_f32 v43, v43\n"
    "v_cvt_pk_bf16_f32 v88, v40, v41\n"
    "v_exp_f32 v44, v44\n"
    "s_waitcnt lgkmcnt(0)\n"
    "v_mfma_f32_32x32x16_bf16 v[24:39], v[100:103], v[96:99], v[72:87]\n"
    "v_exp_f32 v45, v45\n"
    "v_cvt_pk_bf16_f32 v89, v42, v43\n"
    "ds_read_b128 v[104:107], v124 offset:5120\n"
    "v_exp_f32 v46, v46\n"
    "ds_read_b128 v[108:111], v125 offset:4096\n"
    "v_exp_f32 v47, v47\n"
    "v_cvt_pk_bf16_f32 v90, v44, v45\n"
    "ds_read_b128 v[112:115], v125 offset:4608\n"
    "v_exp_f32 v48, v48\n"
    "v_exp_f32 v49, v49\n"
    "v_cvt_pk_bf16_f32 v91, v46, v47\n"
    "v_exp_f32 v50, v50\n"
    "v_exp_f32 v51, v51\n"
    "v_mfma_f32_32x32x16_bf16 v[56:71], v[116:119], v[88:91], v[56:71]\n"
    "v_cvt_pk_bf16_f32 v92, v48, v49\n"
    "v_exp_f32 v52, v52\n"
    "v_exp_f32 v53, v53\n"
    "v_cvt_pk_bf16_f32 v93, v50, v51\n"
    "v_exp_f32 v54, v54\n"
    "v_exp_f32 v55, v55\n"
    "v_cvt_pk_bf16_f32 v94, v52, v53\n"
    "v_cvt_pk_bf16_f32 v95, v54, v55\n"
    "v_exp_f32 v24, v24\n"
    "v_exp_f32 v25, v25\n"
    "v_mfma_f32_32x32x16_bf16 v[56:71], v[120:123], v[92:95], v[56:71]\n"
    "v_exp_f32 v26, v26\n"
    "v_exp_f32 v27, v27\n"
    "v_cvt_pk_bf16_f32 v88, v24, v25\n"
    "v_exp_f32 v28, v28\n"
    "s_waitcnt lgkmcnt(0)\n"
    "v_mfma_f32_32x32x16_bf16 v[40:55], v[104:107], v[96:99], v[72:87]\n"
    "v_exp_f32 v29, v29\n"
    "v_cvt_pk_bf16_f32 v89, v26, v27\n"
    "ds_read_b128 v[100:103], v124 offset:6144\n"
    "v_exp_f32 v30, v30\n"
    "ds_read_b128 v[116:119], v125 offset:5120\n"
    "v_exp_f32 v31, v31\n"
    "v_cvt_pk_bf16_f32 v90, v28, v29\n"
    "ds_read_b128 v[120:123], v125 offset:5632\n"
    "v_exp_f32 v32, v32\n"
    "v_exp_f32 v33, v33\n"
    "v_cvt_pk_bf16_f32 v91, v30, v31\n"
    "v_exp_f32 v34, v34\n"
    "v_exp_f32 v35, v35\n"
    "v_mfma_f32_32x32x16_bf16 v[56:71], v[108:111], v[88:91], v[56:71]\n"
    "v_cvt_pk_bf16_f32 v92, v32, v33\n"
    "v_exp_f32 v36, v36\n"
    "v_exp_f32 v37, v37\n"
    "v_cvt_pk_bf16_f32 v93, v34, v35\n"
    "v_exp_f32 v38, v38\n"
    "v_exp_f32 v39, v39\n"
    "v_cvt_pk_bf16_f32 v94, v36, v37\n"
    "v_cvt_pk_bf16_f32 v95, v38, v39\n"
    "v_exp_f32 v40, v40\n"
    "v_exp_f32 v41, v41\n"
    "v_mfma_f32_32x32x16_bf16 v[56:71], v[112:115], v[92:95], v[56:71]\n"
    "v_exp_f32 v42, v42\n"
    "v_exp_f32 v43, v43\n"
    "v_cvt_pk_bf16_f32 v88, v40, v41\n"
    "v_exp_f32 v44, v44\n"
    "s_waitcnt lgkmcnt(0)\n"
    "v_mfma_f32_32x32x16_bf16 v[24:39], v[100:103], v[96:99], v[72:87]\n"
    "v_exp_f32 v45, v45\n"
    "v_cvt_pk_bf16_f32 v89, v42, v43\n"
    "ds_read_b128 v[104:107], v124 offset:7168\n"
    "v_exp_f32 v46, v46\n"
    "ds_read_b128 v[108:111], v125 offset:6144\n"
    "v_exp_f32 v47, v47\n"
    "v_cvt_pk_bf16_f32 v90, v44, v45\n"
    "ds_read_b128 v[112:115], v125 offset:6656\n"
    "v_exp_f32 v48, v48\n"
    "v_exp_f32 v49, v49\n"
    "v_cvt_pk_bf16_f32 v91, v46, v47\n"
    "v_exp_f32 v50, v50\n"
    "v_exp_f32 v51, v51\n"
    "v_mfma_f32_32x32x16_bf16 v[56:71], v[116:119], v[88:91], v[56:71]\n"
    "v_cvt_pk_bf16_f32 v92, v48, v49\n"
    "v_exp_f32 v52, v52\n"
    "v_exp_f32 v53, v53\n"
    "v_cvt_pk_bf16_f32 v93, v50, v51\n"
    "v_exp_f32 v54, v54\n"
    "v_exp_f32 v55, v55\n"
    "v_cvt_pk_bf16_f32 v94, v52, v53\n"
    "v_cvt_pk_bf16_f32 v95, v54, v55\n"
    "v_exp_f32 v24, v24\n"
    "v_exp_f32 v25, v25\n"
    "v_mfma_f32_32x32x16_bf16 v[56:71], v[120:123], v[92:95], v[56:71]\n"
    "v_exp_f32 v26, v26\n"
    "v_exp_f32 v27, v27\n"
    "v_cvt_pk_bf16_f32 v88, v24, v25\n"
    "v_exp_f32 v28, v28\n"
    "s_waitcnt lgkmcnt(0)\n"
    "v_mfma_f32_32x32x16_bf16 v[40:55], v[104:107], v[96:99], v[72:87]\n"
    "v_exp_f32 v29, v29\n"
    "v_cvt_pk_bf16_f32 v89, v26, v27\n"
    "s_waitcnt vmcnt(0)\n"
    "s_barrier\n"
    "ds_read_b128 v[100:103], v126 offset:0\n"
    "v_exp_f32 v30, v30\n"
    "ds_read_b128 v[116:119], v125 offset:7168\n"
    "v_exp_f32 v31, v31\n"
    "v_cvt_pk_bf16_f32 v90, v28, v29\n"
    "ds_read_b128 v[120:123], v125 offset:7680\n"
    "v_exp_f32 v32, v32\n"
    "v_exp_f32 v33, v33\n"
    "v_cvt_pk_bf16_f32 v91, v30, v31\n"
    "v_exp_f32 v34, v34\n"
    "v_exp_f32 v35, v35\n"
    "v_mfma_f32_32x32x16_bf16 v[56:71], v[108:111], v[88:91], v[56:71]\n"
    "s_cmp_ge_u32 %[cnt], 6\n"
    "s_cbranch_scc1 .Lnodma%=\n"
    "s_mov_b32 m0, %[dslot]\n"
    "s_nop 0\n"
    "global_load_lds_dwordx4 %[goff], %[gbase] offset:0\n"
    "global_load_lds_dwordx4 %[goff], %[gbase] offset:1024\n"
    ".Lnodma%=:\n"
    "v_cvt_pk_bf16_f32 v92, v32, v33\n"
    "v_exp_f32 v36, v36\n"
    "v_exp_f32 v37, v37\n"
    "v_cvt_pk_bf16_f32 v93, v34, v35\n"
    "v_exp_f32 v38, v38\n"
    "v_exp_f32 v39, v39\n"
    "v_cvt_pk_bf16_f32 v94, v36, v37\n"
    "v_cvt_pk_bf16_f32 v95, v38, v39\n"
    "v_exp_f32 v40, v40\n"
    "v_exp_f32 v41, v41\n"
    "v_mfma_f32_32x32x16_bf16 v[56:71], v[112:115], v[92:95], v[56:71]\n"
    "v_exp_f32 v42, v42\n"
    "v_exp_f32 v43, v43\n"
    "v_cvt_pk_bf16_f32 v88, v40, v41\n"
    "v_exp_f32 v44, v44\n"
    "s_waitcnt lgkmcnt(0)\n"
    "v_mfma_f32_32x32x16_bf16 v[24:39], v[100:103], v[96:99], v[72:87]\n"
    "v_exp_f32 v45, v45\n"
    "v_cvt_pk_bf16_f32 v89, v42, v43\n"
    "ds_read_b128 v[104:107], v126 offset:1024\n"
    "v_exp_f32 v46, v46\n"
    "ds_read_b128 v[108:111], v127 offset:0\n"
    "v_exp_f32 v47, v47\n"
    "v_cvt_pk_bf16_f32 v90, v44, v45\n"
    "ds_read_b128 v[112:115], v127 offset:512\n"
    "v_exp_f32 v48, v48\n"
    "v_exp_f32 v49, v49\n"
    "v_cvt_pk_bf16_f32 v91, v46, v47\n"
    "v_exp_f32 v50, v50\n"
    "v_exp_f32 v51, v51\n"
    "v_mfma_f32_32x32x16_bf16 v[56:71], v[116:119], v[88:91], v[56:71]\n"
    "v_cvt_pk_bf16_f32 v92, v48, v49\n"
    "v_exp_f32 v52, v52\n"
    "v_exp_f32 v53, v53\n"
    "v_cvt_pk_bf16_f32 v93, v50, v51\n"
    "v_exp_f32 v54, v54\n"
    "v_exp_f32 v55, v55\n"
    "v_cvt_pk_bf16_f32 v94, v52, v53\n"
    "v_cvt_pk_bf16_f32 v95, v54, v55\n"
    "v_mov_b32 v124, v126\n"
    "v_mov_b32 v125, v127\n"
    "s_add_u32 %[nslot], %[nslot], 0x4000\n"
    "s_cmp_eq_u32 %[nslot], 0xc000\n"
    "s_cselect_b32 %[nslot], 0, %[nslot]\n"
    "v_add_u32 v126, %[nslot], %[kaddr]\n"
    "v_and_b32 v127, %[nslot], %[vsel]\n"
    "v_add_u32 v127, v127, %[vaddr]\n"
    "s_add_u32 %[dslot], %[dslot], 0x4000\n"
    "s_cmp_eq_u32 %[dslot], %[dend]\n"
    "s_cselect_b32 %[dslot], %[dbeg], %[dslot]\n"
    "v_add_u32 %[goff], 0x4000, %[goff]\n"
    "s_add_u32 %[cnt], %[cnt], 1\n"
    "s_cmp_lt_u32 %[cnt], 8\n"
    "s_cbranch_scc1 .Lloop%=\n"
    "s_nop 1\n"
    "v_mfma_f32_32x32x16_bf16 v[56:71], v[120:123], v[92:95], v[56:71]\n"
    "s_nop 15\n"
    "s_nop 3\n"
    "v_rcp_f32 v24, v64\n"
    "s_nop 0\n"
    "v_mul_f32 v56, v56, v24\n"
    "v_mul_f32 v57, v57, v24\n"
    "v_mul_f32 v58, v58, v24\n"
    "v_mul_f32 v59, v59, v24\n"
    "v_mul_f32 v60, v60, v24\n"
    "v_mul_f32 v61, v61, v24\n"
    "v_mul_f32 v62, v62, v24\n"
    "v_mul_f32 v63, v63, v24\n"
    "v_cvt_pk_bf16_f32 %[oa0], v56, v57\n"
    "v_cvt_pk_bf16_f32 %[oa1], v58, v59\n"
    "v_cvt_pk_bf16_f32 %[oa2], v60, v61\n"
    "v_cvt_pk_bf16_f32 %[oa3], v62, v63\n"
    "s_waitcnt lgkmcnt(0)\n"
    "s_mov_b32 m0, %[m0save]\n"
        : [oa0] "=&v"(oa0), [oa1] "=&v"(oa1), [oa2] "=&v"(oa2), [oa3] "=&v"(oa3),
          [goff] "+v"(goff), [dslot] "+s"(dslot), [nslot] "+s"(nslot), [cnt] "=&s"(cnt), [m0save] "=&s"(m0save)
        : [qa0] "v"(qa[0]), [qa1] "v"(qa[1]), [qa2] "v"(qa[2]), [qa3] "v"(qa[3]),
          [nma] "v"(nma), [kaddr] "v"(kaddr), [vaddr] "v"(vaddr), [vsel] "v"(vsel), [gbase] "s"(kvb), [dbeg] "s"(dbeg), [dend] "s"(dend), [psel] "s"(0x07060302u), [vmask] "s"(0x0000ffff0000ffffull), [vone] "v"(((lane & 27) == 16) ? 0x3f803f80u : 0u), [dbgaddr] "s"(lds0 + A_LDS + wid * 64)
        : "memory", "scc", "vcc",
          "v24", "v25", "v26", "v27", "v28", "v29", "v30", "v31", "v32", "v33", "v34", "v35", "v36", "v37", "v38", "v39", "v40", "v41", "v42", "v43", "v44", "v45", "v46", "v47", "v48", "v49", "v50", "v51", "v52", "v53", "v54", "v55", "v56", "v57", "v58", "v59", "v60", "v61", "v62", "v63", "v64", "v65", "v66", "v67", "v68", "v69", "v70", "v71", "v72", "v73", "v74", "v75", "v76", "v77", "v78", "v79", "v80", "v81", "v82", "v83", "v84", "v85", "v86", "v87", "v88", "v89", "v90", "v91", "v92", "v93", "v94", "v95", "v96", "v97", "v98", "v99", "v100", "v101", "v102", "v103", "v104", "v105", "v106", "v107", "v108", "v109", "v110", "v111", "v112", "v113", "v114", "v115", "v116", "v117", "v118", "v119", "v120", "v121", "v122", "v123", "v124", "v125", "v126", "v127"
    );
    STAMP(4);
    st16_wt(ws + WS_OF + ((size_t)tile0 * 32 + head) * 1024 + lane * 16, (u32x4){oa0, oa1, oa2, oa3});
    PUBLISH(FL_O + jb * 32 + head);
}

__device__ __forceinline__ void z_role(const uint16_t* __restrict__ x, const uint16_t* __restrict__ win, char* __restrict__ ws, char* lds, int g, u64& tag) {
    const int tid = threadIdx.x, lane = tid & 63, wid = __builtin_amdgcn_readfirstlane(tid >> 6), wr = wid >> 2, wc = wid & 3, l31 = lane & 31, hh = lane >> 5;
    const int xcd = g & 7, i = g >> 3;
    const int rb = 4 * (xcd >> 1) + (i & 3), cbz = 7 * ((xcd & 1) ^ 1) + (i >> 2);
    f32x16 acc[2];
    proj_main<false>(x, win, NIN, rb, cbz * 128, 12 + cbz, ws, lds, ((i & 3) * 4 + (i >> 2) * 3) & 15, tag, acc);
    STAMP(1);
    const float* st = (const float*)(lds + P_ST);
    unsigned zp[2][8];
#pragma unroll
    for (int rt = 0; rt < 2; ++rt) { const int tr = wr * 64 + rt * 32; const float mu = st[tr + l31], rs = st[128 + tr + l31];
#pragma unroll
        for (int q = 0; q < 4; ++q) { const f32x4 c = *(const f32x4*)(st + 256 + wc * 32 + 8 * q + 4 * hh);
            zp[rt][2 * q] = cvtpk(gelu_t((acc[rt][4 * q] - mu * c[0]) * rs), gelu_t((acc[rt][4 * q + 1] - mu * c[1]) * rs));
            zp[rt][2 * q + 1] = cvtpk(gelu_t((acc[rt][4 * q + 2] - mu * c[2]) * rs), gelu_t((acc[rt][4 * q + 3] - mu * c[3]) * rs)); } }
    if (cbz < 7) {
#pragma unroll
        for (int rt = 0; rt < 2; ++rt) { const int tt = rb * 4 + wr * 2 + rt;
            char* dst = ws + WS_Z1 + ((size_t)tt * 28 + cbz * 4 + wc) * 2048 + lane * 32;
            st16_wt(dst, (u32x4){zp[rt][0], zp[rt][1], zp[rt][2], zp[rt][3]}); st16_wt(dst + 16, (u32x4){zp[rt][4], zp[rt][5], zp[rt][6], zp[rt][7]}); }
    } else {
        const int cj = cbz - 7;
        float* pst = (float*)lds;
        const unsigned ONES = 0x3f803f80u;
#pragma unroll
        for (int rt = 0; rt < 2; ++rt) { float s1 = 0.f, s2 = 0.f;
#pragma unroll
            for (int e = 0; e < 8; ++e) { s1 = dot2(zp[rt][e], ONES, s1); s2 = dot2(zp[rt][e], zp[rt][e], s2); }
            s1 = swap32_add(s1); s2 = swap32_add(s2);
            if (hh == 0) *(f32x2*)(pst + (wc * 128 + wr * 64 + rt * 32 + l31) * 2) = (f32x2){s1, s2}; }
        __syncthreads();
        char* gst = ws + WS_ST + (size_t)rb * 7 * 2048;
        float* fin = (float*)(lds + 4096);
        if (tid < 128) { f32x2 s = *(const f32x2*)(pst + tid * 2);
#pragma unroll
            for (int j = 1; j < 4; ++j) s += *(const f32x2*)(pst + (j * 128 + tid) * 2);
            st16_wt(gst + (cj * 128 + tid) * 16, (u32x4){__float_as_uint(s[0]), __float_as_uint(s[1]), (unsigned)tag, (unsigned)(tag >> 32)});
            u32x4 gr[7];
            for (unsigned spins = 0; spins < (1u << 18); ++spins) {
                asm volatile("global_load_dwordx4 %0, %7, off sc1\n\tglobal_load_dwordx4 %1, %7, off offset:2048 sc1\n\tglobal_load_dwordx4 %2, %8, off sc1\n\tglobal_load_dwordx4 %3, %8, off offset:2048 sc1\n\t"
                             "global_load_dwordx4 %4, %9, off sc1\n\tglobal_load_dwordx4 %5, %9, off offset:2048 sc1\n\tglobal_load_dwordx4 %6, %10, off sc1\n\ts_waitcnt vmcnt(0)"
                             : "=&v"(gr[0]), "=&v"(gr[1]), "=&v"(gr[2]), "=&v"(gr[3]), "=&v"(gr[4]), "=&v"(gr[5]), "=&v"(gr[6])
                             : "v"(gst + tid * 16), "v"(gst + 4096 + tid * 16), "v"(gst + 8192 + tid * 16), "v"(gst + 12288 + tid * 16) : "memory");
                bool ok = true;
#pragma unroll
                for (int j = 0; j < 7; ++j) ok &= gr[j][2] == (unsigned)tag && gr[j][3] == (unsigned)(tag >> 32);
                if (__all(ok)) break;
                __builtin_amdgcn_s_sleep(POLL_SLEEP); }
            float s1 = 0.f, s2 = 0.f;
#pragma unroll
            for (int j = 0; j < 7; ++j) { s1 += __uint_as_float(gr[j][0]); s2 += __uint_as_float(gr[j][1]); }
            const float mu = s1 * (1.0f / FH); const float var = fmaxf(s2 * (1.0f / FH) - mu * mu, 0.f);
            fin[tid] = mu; fin[128 + tid] = __builtin_amdgcn_rsqf(var + LN_EPS); }
        __syncthreads();
        char* img = lds + 8192;
#pragma unroll
        for (int rt = 0; rt < 2; ++rt) { const int tok = wr * 64 + rt * 32 + l31; const float mu = fin[tok], rs = fin[128 + tok];
#pragma unroll
            for (int q = 0; q < 4; ++q) { const unsigned a = zp[rt][2 * q], c = zp[rt][2 * q + 1];
                const u32x2 o = {cvtpk((bflo(a) - mu) * rs, (bfhi(a) - mu) * rs), cvtpk((bflo(c) - mu) * rs, (bfhi(c) - mu) * rs)};
                const int f = wc * 32 + 8 * q + 4 * hh;
                *(u32x2*)(img + tok * 256 + (((f >> 3) ^ wsw(tok)) * 16) + (f & 4) * 2) = o; } }
        __syncthreads();
        { const int blk = (lane >> 4) & 1, q = (lane & 15) >> 2, p = lane & 3;
#pragma unroll
          for (int kq = 0; kq < 4; ++kq) { const int ks = wr * 4 + kq; s16x4 t2[2];
#pragma unroll
              for (int t = 0; t < 2; ++t) { const int kr = 16 * ks + 8 * hh + 4 * t + q; const int ch = 4 * wc + 2 * blk + (p >> 1);
                  t2[t] = trread((lds_cptr)img + kr * 256 + ((ch ^ wsw(kr)) * 16) + 8 * (p & 1)); }
              st16_wt(ws + WS_Z2N + ((size_t)(cj * 4 + wc) * 128 + rb * 8 + ks) * 1024 + lane * 16, __builtin_bit_cast(u32x4, cat(t2[0], t2[1]))); } }
    }
    PUBLISH(FL_Z + rb * 14 + cbz);
}

constexpr int S_SLOT = 24576, S_LDS = 3 * S_SLOT;
__device__ __forceinline__ void spatial_role(const uint16_t* __restrict__ wsp, const uint16_t* __restrict__ bsp, char* __restrict__ ws, char* lds, int g, u64 tag) {
    const int tid = threadIdx.x, lane = tid & 63, wid8 = __builtin_amdgcn_readfirstlane(tid >> 6), wid = wid8 & 3, l31 = lane & 31, hh = lane >> 5;
    const int i = g >> 3, tb = 4 * (g & 7) + (i & 3), fb = i >> 2;
    const unsigned lds0 = (unsigned)(uintptr_t)lds;
    STAMP(3);
    __builtin_amdgcn_s_setprio(SP_PRIO);
    if (wid8 >= 4) {
        const int r0 = 16 * wid + (lane >> 3), r1 = r0 + 8;
        const char* sb_ = (const char*)(wsp + (size_t)tb * 64 * SEQ);
        const unsigned vo0 = (r0 * SEQ + ((lane & 7) ^ ((r0 >> 1) & 7)) * 8) * 2, vo1 = (r1 * SEQ + ((lane & 7) ^ ((r1 >> 1) & 7)) * 8) * 2;
        const char* zs = ws + WS_Z2N + (size_t)(fb * 4 + wid) * 131072; const unsigned zvo = lane * 16;
        unsigned ready = 0;
        GAS u64* zf = flagp(ws, FL_Z + (lane & 15) * 14 + 7 + fb);
        auto need = [&](int rbk) {
            for (unsigned spins = 0; !((ready >> rbk) & 1) && spins < (1u << 18); ++spins) {
                ready = (unsigned)__ballot(__hip_atomic_load(zf, RLX_AGENT) == tag) & 0xffffu;
                if (!((ready >> rbk) & 1)) __builtin_amdgcn_s_sleep(POLL_SLEEP); } };
        auto issue = [&](int it, int slot) { const unsigned so = lds0 + slot * S_SLOT;
            need(it >> 1);
            set_m0(so + wid * 2048); glds_s<0, false>(sb_ + it * 128, vo0);
            set_m0(so + wid * 2048 + 1024); glds_s<0, false>(sb_ + it * 128, vo1);
            set_m0(so + 8192 + wid * 4096);
            glds_s<0, true>(zs + it * 4096, zvo); glds_s<1024, true>(zs + it * 4096, zvo); glds_s<2048, true>(zs + it * 4096, zvo); glds_s<3072, true>(zs + it * 4096, zvo); };
        issue(0, 0); issue(1, 1); issue(2, 2);
        WAIT_BAR(12);
        int slot = 0;
#pragma unroll 1
        for (int it = 1; it < 31; ++it) { WAIT_BAR(6); if (it + 2 < 32) issue(it + 2, slot); slot = slot == 2 ? 0 : slot + 1; }
        WAIT_BAR(0);
        BAR();
    } else {
        unsigned boff[2][4];
#pragma unroll
        for (int rt = 0; rt < 2; ++rt)
#pragma unroll
            for (int ks = 0; ks < 4; ++ks) { const int r = rt * 32 + l31; boff[rt][ks] = (r * 128 + ((hh ^ ((r >> 1) & 7)) * 16)) ^ (ks * 32); }
        const unsigned aoff = 8192 + wid * 4096 + lane * 16;
        struct SF { bf16x8 a, b[2]; };
        auto rd = [&](SF& f, int slot, int ks) { lds_cptr sb = (lds_cptr)lds + slot * S_SLOT;
            f.a = ldsfrag(sb + aoff + ks * 1024); f.b[0] = ldsfrag(sb + boff[0][ks]); f.b[1] = ldsfrag(sb + boff[1][ks]); };
        f32x16 acc[2] = {f32x16{}, f32x16{}};
        auto mm = [&](const SF& f) { acc[0] = MFMA(f.a, f.b[0], acc[0]); acc[1] = MFMA(f.a, f.b[1], acc[1]); };
        SF f[4];
        BAR();
#pragma unroll
        for (int ks = 0; ks < 4; ++ks) rd(f[ks], 0, ks);
        auto step = [&](int nsl) {
            LGKM_BAR();
#pragma unroll
            for (int ks = 0; ks < 4; ++ks) { lds_cptr sb = (lds_cptr)lds + nsl * S_SLOT;
                acc[0] = MFMA(f[ks].a, f[ks].b[0], acc[0]); SBAR();
                f[ks].b[0] = ldsfrag(sb + boff[0][ks]); SBAR();
                PACE();
                acc[1] = MFMA(f[ks].a, f[ks].b[1], acc[1]); SBAR();
                f[ks].b[1] = ldsfrag(sb + boff[1][ks]); f[ks].a = ldsfrag(sb + aoff + ks * 1024); SBAR();
                PACE(); } };
#pragma unroll 1
        for (int io = 0; io < 10; ++io) { step(1); step(2); step(0); }
        step(1); step(2);
        wait_flags(flagp(ws, FL_Z + (tb >> 1) * 14 + fb), lane == 0, tag);
#pragma unroll
        for (int rt = 0; rt < 2; ++rt) { const int tt = tb * 2 + rt; const float bias = __uint_as_float((unsigned)bsp[tt * 32 + l31] << 16);
            const char* z1p = ws + WS_Z1 + ((size_t)tt * 28 + fb * 4 + wid) * 2048 + lane * 32;
            const u32x4 za = ld16_wt(z1p), zb = ld16_wt(z1p + 16);
            u32x4 ua, ub;
#pragma unroll
            for (int e = 0; e < 4; ++e) {
                ua[e] = cvtpk(bflo(za[e]) * (acc[rt][2 * e] + bias), bfhi(za[e]) * (acc[rt][2 * e + 1] + bias));
                ub[e] = cvtpk(bflo(zb[e]) * (acc[rt][8 + 2 * e] + bias), bfhi(zb[e]) * (acc[rt][8 + 2 * e + 1] + bias)); }
            char* ud = ws + WS_U + ((size_t)tt * 56 + (fb * 4 + wid) * 2) * 1024 + lane * 16;
            st16_wt(ud, ua); st16_wt(ud + 1024, ub); }
    }
    PUBLISH(FL_U + tb * 7 + fb);
}

constexpr int F_SLOT = 8192 + 8192, F_LDS = 4 * F_SLOT;
__device__ __forceinline__ void final_role(const uint16_t* __restrict__ x, const uint16_t* __restrict__ wo, const uint16_t* __restrict__ wout, char* __restrict__ ws, uint16_t* __restrict__ out, char* lds, int g, u64 tag) {
    const int tb = 4 * (g & 7) + ((g >> 3) & 3), db = g >> 5;
    const int tid = threadIdx.x, lane = tid & 63, wid8 = __builtin_amdgcn_readfirstlane(tid >> 6), wid = wid8 & 3, wr = wid >> 1, wc = wid & 1, l31 = lane & 31, hh = lane >> 5;
    const unsigned lds0 = (unsigned)(uintptr_t)lds;
    auto wswz = [](int r) { return ((r & 2) << 1) | ((r >> 2) & 3); };
    f32x16 as = f32x16{}, ag = f32x16{};
    const int k0 = 16 * wid + (lane >> 3), k1 = k0 + 8;
    const unsigned wo0 = (k0 * DM + db * 64 + ((lane & 7) ^ wswz(k0)) * 8) * 2, wo1 = (k1 * DM + db * 64 + ((lane & 7) ^ wswz(k1)) * 8) * 2;
    const int att = tb * 2 + (wid & 1), aks = 2 * (wid >> 1);
    unsigned woff[2];
    { const int blk = (lane >> 4) & 1, q = (lane & 15) >> 2, p = lane & 3; const int kr = 4 * hh + q; const int ch = 4 * wc + 2 * blk + (p >> 1);
      woff[0] = kr * 128 + ((ch ^ wswz(kr)) * 16) + 8 * (p & 1); woff[1] = (woff[0] + 1024) ^ 32; }
    const unsigned aoff = 8192 + wr * 4096 + lane * 16;
    struct FF { bf16x8 w, a; };
    auto rd = [&](FF& f, int slot, int ks) { lds_cptr sb = (lds_cptr)lds + slot * F_SLOT;
        f.w = cat(trread(sb + woff[0] + ks * 2048), trread(sb + woff[1] + ks * 2048)); f.a = ldsfrag(sb + aoff + ks * 1024); };
    auto pass = [&](const uint16_t* wb, const char* ab, int nfr, int n, f32x16& acc, bool pace) {
        if (wid8 >= 4) {
            auto issue = [&](int it) { const unsigned so = lds0 + (it & 3) * F_SLOT; const char* w = (const char*)(wb + (size_t)it * 64 * DM);
                set_m0(so + wid * 2048); glds_s<0, false>(w, wo0);
                set_m0(so + wid * 2048 + 1024); glds_s<0, false>(w, wo1);
                const char* a = ab + ((size_t)att * nfr + it * 4 + aks) * 1024;
                set_m0(so + 8192 + (wid & 1) * 4096 + aks * 1024); glds_s<0, true>(a, lane * 16); glds_s<1024, true>(a, lane * 16); };
            issue(0); issue(1); issue(2); issue(3);
            WAIT_BAR(12);
#pragma unroll 1
            for (int i = 1; i < n - 2; ++i) { WAIT_BAR(8); if (i + 3 < n) issue(i + 3); }
            WAIT_BAR(4);
            WAIT_BAR(0);
            BAR();
        } else {
            FF fa[4], fb[4];
            BAR();
#pragma unroll
            for (int ks = 0; ks < 4; ++ks) rd(fa[ks], 0, ks);
            auto step = [&](int nsl, FF (&cur)[4], FF (&nxt)[4]) {
                LGKM_BAR();
#pragma unroll
                for (int ks = 0; ks < 4; ++ks) { acc = MFMA(cur[ks].w, cur[ks].a, acc); SBAR(); rd(nxt[ks], nsl, ks); SBAR(); if (pace) PACE(); } };
#pragma unroll 1
            for (int it = 0; it < n; it += 2) { step((it + 1) & 3, fa, fb); step((it + 2) & 3, fb, fa); }
        } };
    if (wid8 == 0) wait_flags(flagp(ws, FL_U + tb * 7 + lane), lane < 7, tag);
    __syncthreads();
    STAMP(5);
    pass(wout, ws + WS_U, 56, 14, ag, true);
    STAMP(6);
    __syncthreads();
#pragma unroll
    for (int q = 0; q < 8; ++q) {
        const int kr = 8 * wid8 + (lane >> 3);
        set_m0(lds0 + q * 8192 + wid8 * 1024);
        glds_s<0, false>((const char*)(wo + (size_t)q * 64 * DM), (kr * DM + db * 64 + ((lane & 7) ^ wswz(kr)) * 8) * 2); }
    const int tok = tb * 64 + wr * 32 + l31;
    float gate[16]; u32x2 xv[4];
    if (wid8 < 4) {
#pragma unroll
        for (int q = 0; q < 4; ++q) xv[q] = *(const u32x2*)(x + (size_t)tok * DM + db * 64 + wc * 32 + 8 * q + 4 * hh);
#pragma unroll
        for (int r = 0; r < 16; ++r) gate[r] = silu_f(ag[r]); }
    if (wid8 == 0) wait_flags(flagp(ws, FL_O + (tb >> 2) * 32 + lane), lane < 32, tag);
    WAITV(0);
    __syncthreads();
    STAMP(7);
    const int kh = wid8 >> 2;
    u32x4 of[16];
    { const char* ob = ws + WS_OF + ((size_t)(tb * 2 + wr) * 32 + 16 * kh) * 1024; const unsigned v0 = lane * 16, v1 = v0 + 4096, v2 = v0 + 8192, v3 = v0 + 12288;
      asm volatile(
        "global_load_dwordx4 %0, %16, %20 sc1\n\tglobal_load_dwordx4 %1, %16, %20 offset:1024 sc1\n\tglobal_load_dwordx4 %2, %16, %20 offset:2048 sc1\n\tglobal_load_dwordx4 %3, %16, %20 offset:3072 sc1\n\t"
        "global_load_dwordx4 %4, %17, %20 sc1\n\tglobal_load_dwordx4 %5, %17, %20 offset:1024 sc1\n\tglobal_load_dwordx4 %6, %17, %20 offset:2048 sc1\n\tglobal_load_dwordx4 %7, %17, %20 offset:3072 sc1\n\t"
        "global_load_dwordx4 %8, %18, %20 sc1\n\tglobal_load_dwordx4 %9, %18, %20 offset:1024 sc1\n\tglobal_load_dwordx4 %10, %18, %20 offset:2048 sc1\n\tglobal_load_dwordx4 %11, %18, %20 offset:3072 sc1\n\t"
        "global_load_dwordx4 %12, %19, %20 sc1\n\tglobal_load_dwordx4 %13, %19, %20 offset:1024 sc1\n\tglobal_load_dwordx4 %14, %19, %20 offset:2048 sc1\n\tglobal_load_dwordx4 %15, %19, %20 offset:3072 sc1\n\t"
        "s_waitcnt vmcnt(0)"
        : "=&v"(of[0]), "=&v"(of[1]), "=&v"(of[2]), "=&v"(of[3]), "=&v"(of[4]), "=&v"(of[5]), "=&v"(of[6]), "=&v"(of[7]),
          "=&v"(of[8]), "=&v"(of[9]), "=&v"(of[10]), "=&v"(of[11]), "=&v"(of[12]), "=&v"(of[13]), "=&v"(of[14]), "=&v"(of[15])
        : "v"(v0), "v"(v1), "v"(v2), "v"(v3), "s"(ob) : "memory"); }
    { lds_cptr wb_ = (lds_cptr)lds + kh * 32768;
#pragma unroll
      for (int ks = 0; ks < 16; ++ks)
          as = MFMA(cat(trread(wb_ + woff[0] + ks * 2048), trread(wb_ + woff[1] + ks * 2048)), __builtin_bit_cast(bf16x8, of[ks]), as); }
    __syncthreads();
    if (kh == 1) {
#pragma unroll
        for (int q = 0; q < 4; ++q) *(f32x4*)(lds + (wid * 4 + q) * 1024 + lane * 16) = (f32x4){as[4 * q], as[4 * q + 1], as[4 * q + 2], as[4 * q + 3]}; }
    __syncthreads();
    STAMP(8);
    if (kh == 0) {
#pragma unroll
        for (int q = 0; q < 4; ++q) { const f32x4 p = *(const f32x4*)(lds + (wid * 4 + q) * 1024 + lane * 16);
            const float r0 = bflo(xv[q][0]) + (as[4 * q] + p[0]) * gate[4 * q], r1 = bfhi(xv[q][0]) + (as[4 * q + 1] + p[1]) * gate[4 * q + 1];
            const float r2 = bflo(xv[q][1]) + (as[4 * q + 2] + p[2]) * gate[4 * q + 2], r3 = bfhi(xv[q][1]) + (as[4 * q + 3] + p[3]) * gate[4 * q + 3];
            *(u32x2*)(out + (size_t)tok * DM + db * 64 + wc * 32 + 8 * q + 4 * hh) = (u32x2){cvtpk(r0, r1), cvtpk(r2, r3)}; }
    }
}

#define ADELAY 300
#define APRIO 3
#define GDELAY 0
#define GPRIO 2
constexpr int ALL_LDS = S_LDS;
__global__ __launch_bounds__(512, 4) void k_all(const uint16_t* __restrict__ x, const uint16_t* __restrict__ wqkv, const uint16_t* __restrict__ wo, const uint16_t* __restrict__ win,
                                                const uint16_t* __restrict__ wsp, const uint16_t* __restrict__ bsp, const uint16_t* __restrict__ wout, char* __restrict__ ws, uint16_t* __restrict__ out) {
    extern __shared__ __attribute__((aligned(16))) char lds[];
    const int b = blockIdx.x;
    u64 tag;
    STAMP(0);
    if (b < 256) {
        __builtin_amdgcn_s_setprio(APRIO);
        if (ADELAY > 0 && b < 192) {
            const u64 t0 = __builtin_amdgcn_s_memrealtime();
            const int i_ = b >> 3, cb_ = 6 * (b & 1) + (i_ >> 2), line = (i_ & 3) * 256 + (threadIdx.x & 255);
            unsigned junk = 0;
            while (__builtin_amdgcn_s_memrealtime() - t0 < ADELAY) __builtin_amdgcn_s_sleep(8);
            asm volatile("s_waitcnt vmcnt(0)" : "+v"(junk) :: "memory"); }
        if (b < 192) qkv_role(x, wqkv, ws, lds, b, tag); else stats_role(x, wqkv, win, ws, lds, b - 192, tag);
        __builtin_amdgcn_s_setprio(0);
        __syncthreads();
        attn_role(ws, lds, b, tag);
        STAMP(5);
    } else {
        const int g = b - 256;
        __builtin_amdgcn_s_setprio(GPRIO);
            if (GDELAY > 0) { const u64 t0 = __builtin_amdgcn_s_memrealtime(); while (__builtin_amdgcn_s_memrealtime() - t0 < GDELAY) __builtin_amdgcn_s_sleep(8); }
        if (g < 224) { z_role(x, win, ws, lds, g, tag); STAMP(2); __syncthreads(); spatial_role(wsp, bsp, ws, lds, g, tag); STAMP(4); __syncthreads(); }
        else { u64 ep; EPOCH_REQ(ep);
            { const int wv = (g - 224) * 8 + (threadIdx.x >> 6), lane = threadIdx.x & 63; unsigned junk = 0;
#pragma unroll
              for (int q = 0; q < 4; ++q) asm volatile("global_load_dword %0, %1, off" : "+v"(junk) : "v"((const char*)wsp + ((size_t)(q * 256 + wv) * 64 + lane) * 128) : "memory");
              if (wv < 112) asm volatile("global_load_dword %0, %1, off" : "+v"(junk) : "v"((const char*)wout + ((size_t)wv * 64 + lane) * 128) : "memory");
              else if (wv < 176) asm volatile("global_load_dword %0, %1, off" : "+v"(junk) : "v"((const char*)wo + ((size_t)(wv - 112) * 64 + lane) * 128) : "memory");
              asm volatile("s_waitcnt vmcnt(0)" : "+v"(junk) :: "memory"); }
            asm volatile("s_waitcnt vmcnt(0)" : "+v"(ep) :: "memory"); tag = EPOCH_TAG(ep); }
        if (threadIdx.x == 0) __hip_atomic_store(flagp(ws, FL_G + g), tag, RLX_AGENT);
        final_role(x, wo, wout, ws, out, lds, g, tag);
        if (g == 255 && threadIdx.x < 64) {
            const int lane = threadIdx.x;
#pragma unroll 1
            for (int c = 0; c < 4; ++c) { wait_flags(flagp(ws, FL_O + c * 64 + lane), true, tag); wait_flags(flagp(ws, FL_G + c * 64 + lane), true, tag); }
            if (lane == 0) __hip_atomic_store(flagp(ws, FL_EPOCH), tag, RLX_AGENT);
        }
        STAMP(9);
    }
}

extern "C" void kernel_launch(void* const* d_in, const int* in_sizes, int n_in, void* d_out, int out_size, void* d_ws, size_t ws_size, hipStream_t stream) {
    const uint16_t* x = (const uint16_t*)d_in[0]; const uint16_t* wqkv = (const uint16_t*)d_in[1]; const uint16_t* wo = (const uint16_t*)d_in[2];
    const uint16_t* win = (const uint16_t*)d_in[4]; const uint16_t* wsp = (const uint16_t*)d_in[5]; const uint16_t* bsp = (const uint16_t*)d_in[6]; const uint16_t* wout = (const uint16_t*)d_in[7];
    (void)hipFuncSetAttribute((const void*)k_all, hipFuncAttributeMaxDynamicSharedMemorySize, ALL_LDS);
    k_all<<<512, 512, ALL_LDS, stream>>>(x, wqkv, wo, win, wsp, bsp, wout, (char*)d_ws, (uint16_t*)d_out);
}
```

```cpp
#include <hip/hip_runtime.h>
#include <hip/hip_bf16.h>
#include <stdint.h>
#define WS_TAIL 24
#define WARM_IN 1
#define CODE_LINES 320
#include <stdio.h>

typedef short bf16x8 __attribute__((ext_vector_type(8)));
typedef short s16x4 __attribute__((ext_vector_type(4)));
typedef __bf16 bf16x2_t __attribute__((ext_vector_type(2)));
typedef float f32x16 __attribute__((ext_vector_type(16)));
typedef float f32x4 __attribute__((ext_vector_type(4)));
typedef float f32x2 __attribute__((ext_vector_type(2)));
typedef unsigned u32x4 __attribute__((ext_vector_type(4)));
typedef unsigned u32x2 __attribute__((ext_vector_type(2)));
typedef unsigned long long u64;
#define GAS __attribute__((address_space(1)))
#define LAS __attribute__((address_space(3)))
typedef LAS const char* lds_cptr;
typedef short v4i16_t __attribute__((ext_vector_type(4)));
#define RLX_AGENT __ATOMIC_RELAXED, __HIP_MEMORY_SCOPE_AGENT

constexpr int SEQ = 2048, DM = 512, NQKV = 1536, NIN = 1792, FH = 896;
constexpr float QSCALE = 0.25f * 1.4426950408889634f;
constexpr float LN_EPS = 1e-5f;
constexpr size_t MB = 1024 * 1024;
constexpr size_t WS_QF = 0, WS_KV = 2 * MB, WS_Z1 = 6 * MB, WS_Z2N = 10 * MB, WS_U = 14 * MB, WS_OF = 18 * MB;
constexpr size_t WS_KM = 20 * MB, WS_ST = 20 * MB + 65536, WS_RS = 20 * MB + 65536 + 229376, WS_CS = 20 * MB + 65536 + 245760  , WS_FLAG = 21 * MB;
constexpr int FL_RS = 0, FL_CS = 64, FL_QKV = 128, FL_ST = 320, FL_Z = 432, FL_U = 656, FL_G = 880, FL_EPOCH = 1136, FL_O = 1152;

#define MFMA(a, b, c) __builtin_amdgcn_mfma_f32_32x32x16_bf16(a, b, c, 0, 0, 0)
#define SBAR() __builtin_amdgcn_sched_barrier(0)
__device__ __forceinline__ unsigned cvtpk(float lo, float hi) { unsigned r; asm("v_cvt_pk_bf16_f32 %0, %1, %2" : "=v"(r) : "v"(lo), "v"(hi)); return r; }
__device__ __forceinline__ float bflo(unsigned u) { return __uint_as_float(u << 16); }
__device__ __forceinline__ float bfhi(unsigned u) { return __uint_as_float(u & 0xffff0000u); }
__device__ __forceinline__ float dot2(unsigned a, unsigned b, float acc) {
    return __builtin_amdgcn_fdot2_f32_bf16(__builtin_bit_cast(bf16x2_t, a), __builtin_bit_cast(bf16x2_t, b), acc, false); }
__device__ __forceinline__ void glds16(const void* g, unsigned lds_base) {
    asm volatile("s_mov_b32 m0, %1\n\ts_nop 0\n\tglobal_load_lds_dwordx4 %0, off" :: "v"(g), "s"(lds_base) : "memory", "m0"); }
template <int IMM, bool WT> __device__ __forceinline__ void glds_s(const void* sbase, unsigned voff) {
    if (WT) asm volatile("global_load_lds_dwordx4 %0, %1 offset:%2 sc1" :: "v"(voff), "s"(sbase), "n"(IMM) : "memory");
    else asm volatile("global_load_lds_dwordx4 %0, %1 offset:%2" :: "v"(voff), "s"(sbase), "n"(IMM) : "memory"); }
__device__ __forceinline__ void set_m0(unsigned v) { asm volatile("s_mov_b32 m0, %0\n\ts_nop 0" :: "s"(v) : "memory", "m0"); }
__device__ __forceinline__ s16x4 trread(lds_cptr p) { return __builtin_bit_cast(s16x4, __builtin_amdgcn_ds_read_tr16_b64_v4i16((LAS v4i16_t*)p)); }
__device__ __forceinline__ bf16x8 ldsfrag(lds_cptr p) { return *(LAS const bf16x8*)p; }
__device__ __forceinline__ bf16x8 cat(s16x4 a, s16x4 b) { return (bf16x8){a[0], a[1], a[2], a[3], b[0], b[1], b[2], b[3]}; }
__device__ __forceinline__ float swap32_add(float v) { auto rr = __builtin_amdgcn_permlane32_swap(__float_as_uint(v), __float_as_uint(v), false, false); return __uint_as_float(rr[0]) + __uint_as_float(rr[1]); }
__device__ __forceinline__ float swap32_max(float v) { auto rr = __builtin_amdgcn_permlane32_swap(__float_as_uint(v), __float_as_uint(v), false, false); return fmaxf(__uint_as_float(rr[0]), __uint_as_float(rr[1])); }
template <int CTRL> __device__ __forceinline__ float dppf(float v) { return __int_as_float(__builtin_amdgcn_update_dpp(0, __float_as_int(v), CTRL, 0xf, 0xf, false)); }
__device__ __forceinline__ float row16_add(float v) { v += dppf<0x128>(v); v += dppf<0x124>(v); v += dppf<0x122>(v); v += dppf<0x121>(v); return v; }
__device__ __forceinline__ float row16_max(float v) { v = fmaxf(v, dppf<0x128>(v)); v = fmaxf(v, dppf<0x124>(v)); v = fmaxf(v, dppf<0x122>(v)); v = fmaxf(v, dppf<0x121>(v)); return v; }
__device__ __forceinline__ float swap16_max(float v) { auto rr = __builtin_amdgcn_permlane16_swap(__float_as_uint(v), __float_as_uint(v), false, false); return fmaxf(__uint_as_float(rr[0]), __uint_as_float(rr[1])); }
__device__ __forceinline__ float gelu_t(float x) {
    const float c1 = -2.3022082f, c2 = -0.10294324f;
    float e = __builtin_amdgcn_exp2f(x * __builtin_fmaf(x * x, c2, c1));
    return x * __builtin_amdgcn_rcpf(1.0f + e); }
__device__ __forceinline__ float silu_f(float x) { return x * __builtin_amdgcn_rcpf(1.0f + __builtin_amdgcn_exp2f(-1.4426950408889634f * x)); }
#define STAMP(slot) do {} while (0)
#define PF_DIST 9
#define PACE_N 2
#define PACE() do { asm volatile("s_nop %0" :: "n"(PACE_N)); SBAR(); } while (0)
#define POLL_SLEEP 2
#define WAITV(n) asm volatile("s_waitcnt vmcnt(" #n ")" ::: "memory")
#define WAIT_BAR(n) asm volatile("s_waitcnt vmcnt(" #n ")\n\ts_barrier" ::: "memory")
#define BAR() asm volatile("s_barrier" ::: "memory")
#define LGKM_BAR() do { __builtin_amdgcn_s_waitcnt(0xC07F); asm volatile("" ::: "memory"); __builtin_amdgcn_s_barrier(); asm volatile("" ::: "memory"); } while (0)
__device__ __forceinline__ unsigned wsw(unsigned row) { return ((row & 3) << 2) | ((row >> 2) & 3); }
__device__ __forceinline__ void st16_wt(void* p, u32x4 v) {
    asm volatile("global_store_dwordx4 %0, %1, off sc1\n\ts_nop 1" :: "v"(p), "v"(v)); }
__device__ __forceinline__ void st8_wt(void* p, u64 v) { __hip_atomic_store((GAS u64*)p, v, RLX_AGENT); }
__device__ __forceinline__ u64 ld8_wt(const void* p) { return __hip_atomic_load((GAS u64*)p, RLX_AGENT); }
__device__ __forceinline__ u32x4 ld16_wt(const void* p) { u32x4 v; asm volatile("global_load_dwordx4 %0, %1, off sc1\n\ts_waitcnt vmcnt(0)" : "=&v"(v) : "v"(p) : "memory"); return v; }
__device__ __forceinline__ void glds16_wt(const void* g, unsigned lds_base) {
    asm volatile("s_mov_b32 m0, %1\n\ts_nop 0\n\tglobal_load_lds_dwordx4 %0, off sc1" :: "v"(g), "s"(lds_base) : "memory", "m0"); }
__device__ __forceinline__ GAS u64* flagp(char* ws, int idx) { return (GAS u64*)(ws + WS_FLAG) + idx * 16; }
__device__ __forceinline__ void wait_flags(GAS u64* f, bool want, u64 tag) {
    for (unsigned spins = 0; spins < (1u << 20); ++spins) {
        const u64 v = want ? __hip_atomic_load(f, RLX_AGENT) : tag;
        if (__all(v == tag)) return;
        __builtin_amdgcn_s_sleep(POLL_SLEEP); } }
#define ACQUIRE() do { __builtin_amdgcn_fence(__ATOMIC_ACQUIRE, "agent"); asm volatile("s_waitcnt vmcnt(0)" ::: "memory"); } while (0)
#define EPOCH_REQ(ep) asm volatile("global_load_dwordx2 %0, %1, off sc1" : "=v"(ep) : "v"(flagp(ws, FL_EPOCH)) : "memory")
#define EPOCH_TAG(ep) ((((u64)(unsigned)__builtin_amdgcn_readfirstlane((unsigned)((ep) >> 32)) << 32) | (unsigned)__builtin_amdgcn_readfirstlane((unsigned)(ep))) + 1)
#define PUBLISH(idx) do { WAITV(0); __syncthreads(); if (threadIdx.x == 0) __hip_atomic_store(flagp(ws, idx), tag, RLX_AGENT); } while (0)

__device__ __forceinline__ void stats_role(const uint16_t* __restrict__ x, const uint16_t* __restrict__ wqkv, const uint16_t* __restrict__ win, char* __restrict__ ws, char* lds, int j, u64& tag) {
    u64 ep; EPOCH_REQ(ep);
    const int tid = threadIdx.x, lane = tid & 63, wid = tid >> 6, rr = lane >> 4, cc = lane & 15;
    const unsigned ONES = 0x3f803f80u;
    u32x4 xv[4], w[8];
    const int row = 32 * j + 4 * wid + rr;
#pragma unroll
    for (int i = 0; i < 4; ++i) xv[i] = *(const u32x4*)(x + (size_t)row * DM + i * 128 + cc * 8);
    const int cbk = j >> 1;
    if (j < 52) {
        const uint16_t* W = cbk < 12 ? wqkv + cbk * 128 : win + (cbk - 12) * 128; const int ldw = cbk < 12 ? NQKV : NIN;
        const uint16_t* p = W + (size_t)((j & 1) * 256 + 32 * wid + rr) * ldw + cc * 8;
#pragma unroll
        for (int i = 0; i < 8; ++i) w[i] = *(const u32x4*)(p + (size_t)(i * 4) * ldw);
    }
    asm volatile("s_waitcnt vmcnt(0)" : "+v"(ep) :: "memory");
    tag = EPOCH_TAG(ep);
    float s1 = 0.f, s2 = 0.f;
#pragma unroll
    for (int i = 0; i < 4; ++i)
#pragma unroll
        for (int e = 0; e < 4; ++e) { s1 = dot2(xv[i][e], ONES, s1); s2 = dot2(xv[i][e], xv[i][e], s2); }
    s1 = row16_add(s1); s2 = row16_add(s2);
    { const float mu = s1 * (1.0f / DM), var = fmaxf(s2 * (1.0f / DM) - mu * mu, 0.f);
      if (cc == 0) st8_wt(ws + WS_RS + (size_t)row * 8, __builtin_bit_cast(u64, (f32x2){mu, __builtin_amdgcn_rsqf(var + LN_EPS)})); }
    if (j < 52) {
        float cs[8] = {0.f, 0.f, 0.f, 0.f, 0.f, 0.f, 0.f, 0.f};
        unsigned sel_lo = 0x00003f80u, sel_hi = 0x3f800000u;
        asm volatile("" : "+v"(sel_lo), "+v"(sel_hi));
#pragma unroll
        for (int i = 0; i < 8; ++i)
#pragma unroll
            for (int e = 0; e < 4; ++e) { cs[2 * e] = dot2(w[i][e], sel_lo, cs[2 * e]); cs[2 * e + 1] = dot2(w[i][e], sel_hi, cs[2 * e + 1]); }
        float* red = (float*)lds;
        *(f32x4*)(red + (wid * 4 + rr) * 128 + cc * 8) = (f32x4){cs[0], cs[1], cs[2], cs[3]}; *(f32x4*)(red + (wid * 4 + rr) * 128 + cc * 8 + 4) = (f32x4){cs[4], cs[5], cs[6], cs[7]};
        __syncthreads();
        if (tid < 64) { f32x2 a = {0.f, 0.f};
#pragma unroll
            for (int q = 0; q < 32; ++q) a += *(const f32x2*)(red + q * 128 + 2 * tid);
            st8_wt(ws + WS_CS + (size_t)((j & 1) * 3328 + cbk * 128 + 2 * tid) * 4, __builtin_bit_cast(u64, a)); }
    }
    STAMP(11);
    WAITV(0); __syncthreads();
    STAMP(1);
    if (tid == 0) { __hip_atomic_store(flagp(ws, FL_RS + j), tag, RLX_AGENT); if (j < 52) __hip_atomic_store(flagp(ws, FL_CS + j), tag, RLX_AGENT); }
}

constexpr int P_SLOT = 16384, P_ST = 4 * P_SLOT, P_LDS = 4 * P_SLOT + 4096;
struct PFrag { bf16x8 x[2], w; };
template <bool SWAP>
__device__ __forceinline__ void proj_main(const uint16_t* __restrict__ x, const uint16_t* __restrict__ W, int ldw, int rb, int col0, int cblk, char* __restrict__ ws, char* lds, int rot, u64& tag, f32x16 (&acc)[2]) {
    u64 ep; EPOCH_REQ(ep);
    const int tid = threadIdx.x, lane = tid & 63, wid = __builtin_amdgcn_readfirstlane(tid >> 6), wr = wid >> 2, wc = wid & 3, l31 = lane & 31, hh = lane >> 5;
    const unsigned lds0 = (unsigned)(uintptr_t)lds;
    const int xr = 16 * wid + (lane >> 2), wk = 4 * wid + (lane >> 4);
    const char* xb = (const char*)(x + (size_t)rb * 128 * DM); const char* wb = (const char*)(W + col0);
    const unsigned xvo = (xr * DM + ((lane & 3) ^ ((xr >> 2) & 3)) * 8) * 2, wvo = (wk * ldw + ((lane & 15) ^ wsw(wk)) * 8) * 2;
    const unsigned xd = lds0 + wid * 1024, wd = lds0 + 8192 + wid * 1024;
    auto issue1 = [&](int it, int which) { const unsigned so = (it & 3) * P_SLOT; const int kk = (it + rot) & 15;
        if (which == 0) { set_m0(xd + so); glds_s<0, false>(xb + kk * 64, xvo); } else { set_m0(wd + so); glds_s<0, false>(wb + (size_t)kk * 64 * ldw, wvo); } SBAR(); };
#pragma unroll
    for (int it = 0; it < 4; ++it) { issue1(it, 0); issue1(it, 1); }
    unsigned xoff[2][2], woff[2][2];
    const int blk = (lane >> 4) & 1, q = (lane & 15) >> 2, p = lane & 3;
#pragma unroll
    for (int ks = 0; ks < 2; ++ks) {
#pragma unroll
        for (int rt = 0; rt < 2; ++rt) { const int r = wr * 64 + rt * 32 + l31; xoff[rt][ks] = r * 64 + (((2 * ks + hh) ^ ((r >> 2) & 3)) * 16); }
        const int kr = 8 * hh + q, ch = 4 * wc + 2 * blk + (p >> 1);
        woff[0][ks] = 8192 + ks * 4096 + kr * 256 + ((ch ^ wsw(kr)) * 16) + 8 * (p & 1); woff[1][ks] = (woff[0][ks] + 1024) ^ 16; }
    auto rd = [&](PFrag& f, int slot, int j) { lds_cptr sb = (lds_cptr)lds + slot * P_SLOT;
        f.x[0] = ldsfrag(sb + xoff[0][j]); f.x[1] = ldsfrag(sb + xoff[1][j]); f.w = cat(trread(sb + woff[0][j]), trread(sb + woff[1][j])); };
    auto mm = [&](const PFrag& f) {
#pragma unroll
        for (int rt = 0; rt < 2; ++rt) acc[rt] = SWAP ? MFMA(f.x[rt], f.w, acc[rt]) : MFMA(f.w, f.x[rt], acc[rt]); };
    acc[0] = f32x16{}; acc[1] = f32x16{};
    PFrag fa[2], fb[2];
    asm volatile("s_waitcnt vmcnt(6)\n\ts_barrier" : "+v"(ep) :: "memory");
    tag = EPOCH_TAG(ep);
    STAMP(10);
    rd(fa[0], 0, 0); rd(fa[1], 0, 1);
    auto mm1 = [&](const PFrag& f, int rt) { acc[rt] = SWAP ? MFMA(f.x[rt], f.w, acc[rt]) : MFMA(f.w, f.x[rt], acc[rt]); SBAR(); };
    auto step = [&](int it, int sl, PFrag (&cur)[2], PFrag (&nxt)[2]) {
        __builtin_amdgcn_s_waitcnt(0xC07F);
        if (it < 13) WAITV(4); else if (it == 13) WAITV(2); else WAITV(0);
        __builtin_amdgcn_s_barrier(); asm volatile("" ::: "memory");
        lds_cptr sb = (lds_cptr)lds + ((sl + 1) & 3) * P_SLOT;
        mm1(cur[0], 0);
        nxt[0].x[0] = ldsfrag(sb + xoff[0][0]); nxt[0].x[1] = ldsfrag(sb + xoff[1][0]); SBAR();
        mm1(cur[0], 1);
        nxt[0].w = cat(trread(sb + woff[0][0]), trread(sb + woff[1][0])); SBAR();
        if (it + 4 < 16) issue1(it + 4, 0);
        mm1(cur[1], 0);
        nxt[1].x[0] = ldsfrag(sb + xoff[0][1]); nxt[1].x[1] = ldsfrag(sb + xoff[1][1]); SBAR();
        mm1(cur[1], 1);
        nxt[1].w = cat(trread(sb + woff[0][1]), trread(sb + woff[1][1])); SBAR();
        if (it + 4 < 16) issue1(it + 4, 1); };
#pragma unroll 1
    for (int io = 0; io < 2; ++io) { step(io * 4, 0, fa, fb); step(io * 4 + 1, 1, fb, fa); step(io * 4 + 2, 2, fa, fb); step(io * 4 + 3, 3, fb, fa); }
    const char* fa_ = (const char*)(lane < 4 ? flagp(ws, FL_RS + rb * 4 + lane) : flagp(ws, FL_CS + 2 * cblk + (lane & 1)));
    const char* sa_ = tid < 128 ? ws + WS_RS + (size_t)(rb * 128 + tid) * 8 : ws + WS_CS + (size_t)(cblk * 128 + 2 * (tid & 63)) * 4;
    const char* sb_ = ws + WS_CS + (size_t)(3328 + cblk * 128 + 2 * (tid & 63)) * 4;
    u64 fl, stv, stw;
    step(8, 0, fa, fb); step(9, 1, fb, fa);
    asm volatile("global_load_dwordx2 %0, %1, off sc1" : "=v"(fl) : "v"(fa_) : "memory");
    step(10, 2, fa, fb); step(11, 3, fb, fa); step(12, 0, fa, fb);
    asm volatile("" : "+v"(fl));
    const bool got = __all(lane >= 6 || fl == tag);
    asm volatile("global_load_dwordx2 %0, %2, off sc1\n\tglobal_load_dwordx2 %1, %3, off sc1" : "=&v"(stv), "=&v"(stw) : "v"(sa_), "v"(sb_) : "memory");
    step(13, 1, fb, fa); step(14, 2, fa, fb); step(15, 3, fb, fa);
    asm volatile("" : "+v"(stv), "+v"(stw));
    if (!got) { wait_flags((GAS u64*)fa_, lane < 6, tag); stv = ld8_wt(sa_); stw = ld8_wt(sb_); }
    STAMP(11);
    float* st = (float*)(lds + P_ST);
    { const f32x2 v = __builtin_bit_cast(f32x2, stv);
      if (tid < 128) { st[tid] = v[0]; st[128 + tid] = v[1]; } else if (tid < 192) *(f32x2*)(st + 256 + 2 * (tid - 128)) = v + __builtin_bit_cast(f32x2, stw); }
    __syncthreads();
}

__device__ __forceinline__ void qkv_role(const uint16_t* __restrict__ x, const uint16_t* __restrict__ wqkv, char* __restrict__ ws, char* lds, int b, u64& tag) {
    const int xcd = b & 7, i = b >> 3;
    const int rb = 4 * (xcd >> 1) + (i & 3), cb = 6 * (xcd & 1) + (i >> 2);
    const int tid = threadIdx.x, lane = tid & 63, wid = __builtin_amdgcn_readfirstlane(tid >> 6), wr = wid >> 2, wc = wid & 3, l31 = lane & 31, hh = lane >> 5;
    f32x16 acc[2];
    const int rot = ((i & 3) * 4 + (i >> 2) * 3) & 15;
    if (cb >= 8) proj_main<true>(x, wqkv, NQKV, rb, cb * 128, cb, ws, lds, rot, tag, acc); else proj_main<false>(x, wqkv, NQKV, rb, cb * 128, cb, ws, lds, rot, tag, acc);
    STAMP(1);
    const float* st = (const float*)(lds + P_ST);
    if (cb >= 8) {
        const float cs = st[256 + wc * 32 + l31];
        const int head = (cb - 8) * 8 + wc * 2 + (l31 >> 4);
#pragma unroll
        for (int rt = 0; rt < 2; ++rt) { const int tr = wr * 64 + rt * 32;
            unsigned pk[8];
#pragma unroll
            for (int q = 0; q < 4; ++q) { const f32x4 m = *(const f32x4*)(st + tr + 8 * q + 4 * hh), r = *(const f32x4*)(st + 128 + tr + 8 * q + 4 * hh);
                pk[2 * q] = cvtpk((acc[rt][4 * q] - m[0] * cs) * r[0], (acc[rt][4 * q + 1] - m[1] * cs) * r[1]);
                pk[2 * q + 1] = cvtpk((acc[rt][4 * q + 2] - m[2] * cs) * r[2], (acc[rt][4 * q + 3] - m[3] * cs) * r[3]); }
            const int tile = rb * 4 + (tr >> 5);
            char* dst = ws + WS_KV + (size_t)head * 131072 + (tile >> 3) * 16384 + 8192 + (tile & 7) * 1024 + hh * 256 + (l31 & 15) * 16;
            st16_wt(dst, (u32x4){pk[0], pk[1], pk[2], pk[3]}); st16_wt(dst + 512, (u32x4){pk[4], pk[5], pk[6], pk[7]}); }
    } else {
        const bool isK = cb >= 4; const float sc = isK ? 1.0f : QSCALE;
        float kmx[2] = {0.f, 0.f};
#pragma unroll
        for (int rt = 0; rt < 2; ++rt) { const int tr = wr * 64 + rt * 32, tile = rb * 4 + (tr >> 5);
            const float mu = st[tr + l31], rs = st[128 + tr + l31] * sc;
#pragma unroll
            for (int hb = 0; hb < 2; ++hb) {
                float v[8];
#pragma unroll
                for (int q = 0; q < 2; ++q) { const f32x4 c = *(const f32x4*)(st + 256 + wc * 32 + 16 * hb + 8 * q + 4 * hh);
#pragma unroll
                    for (int e = 0; e < 4; ++e) v[4 * q + e] = (acc[rt][8 * hb + 4 * q + e] - mu * c[e]) * rs; }
                u32x4 pk;
#pragma unroll
                for (int e = 0; e < 4; ++e) pk[e] = cvtpk(v[2 * e], v[2 * e + 1]);
                const int head = (cb & 3) * 8 + wc * 2 + hb;
                char* dst = isK ? ws + WS_KV + (size_t)head * 131072 + (tile >> 3) * 16384 + (tile & 7) * 1024 + lane * 16 : ws + WS_QF + (size_t)head * 65536 + tile * 1024 + lane * 16;
                st16_wt(dst, pk);
                float n2 = 0.f;
#pragma unroll
                for (int e = 0; e < 4; ++e) n2 = dot2(pk[e], pk[e], n2);
                kmx[hb] = fmaxf(kmx[hb], swap32_add(n2));
            } }
        if (isK) {
#pragma unroll
            for (int hb = 0; hb < 2; ++hb) { const float m = swap16_max(row16_max(kmx[hb]));
                if (lane == 0) __hip_atomic_store((GAS unsigned*)(ws + WS_KM) + ((cb & 3) * 8 + wc * 2 + hb) * 32 + rb * 2 + wr, __float_as_uint(m), RLX_AGENT); } }
    }
    STAMP(12);
    PUBLISH(FL_QKV + cb * 16 + rb);
    STAMP(13);
}

constexpr int A_ONES = 3 * 16384, A_LDS = A_ONES + 8192 + 512;
__device__ __forceinline__ void attn_role(char* __restrict__ ws, char* lds, int b, u64 tag) {
    const int tid = threadIdx.x, lane = tid & 63, wid = __builtin_amdgcn_readfirstlane(tid >> 6);
    const int head = (b & 7) * 4 + ((b >> 3) & 3), jb = b >> 5;
    const unsigned lds0 = (unsigned)(uintptr_t)lds;
    const char* kvb = ws + WS_KV + (size_t)head * 131072;
    if (wid == 0) {
        const int hb = head >> 3;
        const int idx = lane < 16 ? (4 + hb) * 16 + lane : lane < 32 ? (8 + hb) * 16 + (lane - 16) : hb * 16 + 2 * jb + (lane & 1);
        wait_flags(flagp(ws, FL_QKV + idx), lane < 34, tag);
        STAMP(14);
        }
    __syncthreads();
    STAMP(2);
    unsigned goff = wid * 2048 + lane * 16;
    const int tile0 = jb * 8 + wid;
    u32x4 qa; unsigned kmu;
    asm volatile("global_load_dwordx4 %0, %2, off sc1\n\tglobal_load_dword %1, %3, off sc1" : "=&v"(qa), "=&v"(kmu)
                 : "v"(ws + WS_QF + (size_t)head * 65536 + tile0 * 1024 + lane * 16), "v"(ws + WS_KM + (size_t)(head * 32 + (lane & 31)) * 4) : "memory");
#pragma unroll
    for (int c = 0; c < 2; ++c) { set_m0(lds0 + c * 16384 + wid * 2048); glds_s<0, true>(kvb + c * 16384, goff); glds_s<1024, true>(kvb + c * 16384, goff); }
    asm volatile("s_waitcnt vmcnt(4)" : "+v"(qa), "+v"(kmu) :: "memory");
    const float km = swap16_max(row16_max(__uint_as_float(kmu)));
    float na = 0.f;
#pragma unroll
    for (int e = 0; e < 4; ++e) na = dot2(qa[e], qa[e], na);
    na = swap32_add(na);
    float ma = __builtin_amdgcn_sqrtf(na * km);
    if (__any(!(ma <= 40.0f))) {
        float xa = -3.0e38f;
        for (int t = 0; t < 64; ++t) {
            const bf16x8 kf = __builtin_bit_cast(bf16x8, ld16_wt(kvb + (t >> 3) * 16384 + (t & 7) * 1024 + lane * 16));
            const f32x16 sa = MFMA(kf, __builtin_bit_cast(bf16x8, qa), f32x16{});
#pragma unroll
            for (int r = 0; r < 16; ++r) xa = fmaxf(xa, sa[r]); }
        ma = swap32_max(xa);
    }
    const float nma = -ma;
    const unsigned kaddr = lds0 + lane * 16;
    const bool ones = (lane & 16) != 0;
    const unsigned vaddr = ones ? lds0 + A_ONES + (lane & 15) * 16 : lds0 + 8192 + (lane >> 5) * 256 + (lane & 15) * 16;
    const unsigned vsel = ones ? 0u : 0xffffffffu;
    goff += 2 * 16384;
    unsigned dbeg = lds0 + wid * 2048, dend = dbeg + 3 * 16384, dslot = dbeg + 2 * 16384, nslot = 0x4000, cnt, m0save;
    unsigned oa0, oa1, oa2, oa3;
    STAMP(3);
    asm volatile(
    "s_mov_b32 %[m0save], m0\n"
    "v_mov_b32 v96, %[qa0]\n"
    "v_mov_b32 v97, %[qa1]\n"
    "v_mov_b32 v98, %[qa2]\n"
    "v_mov_b32 v99, %[qa3]\n"
    "v_mov_b32 v72, %[nma]\n"
    "v_mov_b32 v56, 0\n"
    "v_mov_b32 v73, %[nma]\n"
    "v_mov_b32 v57, 0\n"
    "v_mov_b32 v74, %[nma]\n"
    "v_mov_b32 v58, 0\n"
    "v_mov_b32 v75, %[nma]\n"
    "v_mov_b32 v59, 0\n"
    "v_mov_b32 v76, %[nma]\n"
    "v_mov_b32 v60, 0\n"
    "v_mov_b32 v77, %[nma]\n"
    "v_mov_b32 v61, 0\n"
    "v_mov_b32 v78, %[nma]\n"
    "v_mov_b32 v62, 0\n"
    "v_mov_b32 v79, %[nma]\n"
    "v_mov_b32 v63, 0\n"
    "v_mov_b32 v80, %[nma]\n"
    "v_mov_b32 v64, 0\n"
    "v_mov_b32 v81, %[nma]\n"
    "v_mov_b32 v65, 0\n"
    "v_mov_b32 v82, %[nma]\n"
    "v_mov_b32 v66, 0\n"
    "v_mov_b32 v83, %[nma]\n"
    "v_mov_b32 v67, 0\n"
    "v_mov_b32 v84, %[nma]\n"
    "v_mov_b32 v68, 0\n"
    "v_mov_b32 v85, %[nma]\n"
    "v_mov_b32 v69, 0\n"
    "v_mov_b32 v86, %[nma]\n"
    "v_mov_b32 v70, 0\n"
    "v_mov_b32 v87, %[nma]\n"
    "v_mov_b32 v71, 0\n"
    "v_mov_b32 v120, 0\n"
    "v_mov_b32 v121, 0\n"
    "v_mov_b32 v122, 0\n"
    "v_mov_b32 v123, 0\n"
    "v_mov_b32 v88, 0\n"
    "v_mov_b32 v89, 0\n"
    "v_mov_b32 v90, 0\n"
    "v_mov_b32 v91, 0\n"
    "v_mov_b32 v92, 0\n"
    "v_mov_b32 v93, 0\n"
    "v_mov_b32 v94, 0\n"
    "v_mov_b32 v95, 0\n"
    "v_mov_b32 v124, %[kaddr]\n"
    "v_mov_b32 v125, %[vaddr]\n"
    "v_add_u32 v126, 0x4000, v124\n"
    "v_and_b32 v127, 0x4000, %[vsel]\n"
    "v_add_u32 v127, v127, v125\n"
    "s_waitcnt vmcnt(2) lgkmcnt(0)\n"
    "s_barrier\n"
    "ds_read_b128 v[100:103], v124\n"
    "ds_read_b128 v[104:107], v124 offset:1024\n"
    "ds_read_b128 v[108:111], v125\n"
    "ds_read_b128 v[112:115], v125 offset:512\n"
    "s_mov_b32 %[cnt], 0\n"
    "s_waitcnt lgkmcnt(0)\n"
    "v_mfma_f32_32x32x16_bf16 v[24:39], v[100:103], v[96:99], v[72:87]\n"
    "s_nop 15\n"
    ".Lloop%=:\n"
    "v_exp_f32 v24, v24\n"
    "v_exp_f32 v25, v25\n"
    "v_mfma_f32_32x32x16_bf16 v[56:71], v[120:123], v[92:95], v[56:71]\n"
    "v_exp_f32 v26, v26\n"
    "v_exp_f32 v27, v27\n"
    "v_cvt_pk_bf16_f32 v88, v24, v25\n"
    "v_exp_f32 v28, v28\n"
    "s_waitcnt lgkmcnt(0)\n"
    "v_mfma_f32_32x32x16_bf16 v[40:55], v[104:107], v[96:99], v[72:87]\n"
    "v_exp_f32 v29, v29\n"
    "v_cvt_pk_bf16_f32 v89, v26, v27\n"
    "ds_read_b128 v[100:103], v124 offset:2048\n"
    "v_exp_f32 v30, v30\n"
    "ds_read_b128 v[116:119], v125 offset:1024\n"
    "v_exp_f32 v31, v31\n"
    "v_cvt_pk_bf16_f32 v90, v28, v29\n"
    "ds_read_b128 v[120:123], v125 offset:1536\n"
    "v_exp_f32 v32, v32\n"
    "v_exp_f32 v33, v33\n"
    "v_cvt_pk_bf16_f32 v91, v30, v31\n"
    "v_exp_f32 v34, v34\n"
    "v_exp_f32 v35, v35\n"
    "v_mfma_f32_32x32x16_bf16 v[56:71], v[108:111], v[88:91], v[56:71]\n"
    "v_cvt_pk_bf16_f32 v92, v32, v33\n"
    "v_exp_f32 v36, v36\n"
    "v_exp_f32 v37, v37\n"
    "v_cvt_pk_bf16_f32 v93, v34, v35\n"
    "v_exp_f32 v38, v38\n"
    "v_exp_f32 v39, v39\n"
    "v_cvt_pk_bf16_f32 v94, v36, v37\n"
    "v_cvt_pk_bf16_f32 v95, v38, v39\n"
    "v_exp_f32 v40, v40\n"
    "v_exp_f32 v41, v41\n"
    "v_mfma_f32_32x32x16_bf16 v[56:71], v[112:115], v[92:95], v[56:71]\n"
    "v_exp_f32 v42, v42\n"
    "v_exp_f32 v43, v43\n"
    "v_cvt_pk_bf16_f32 v88, v40, v41\n"
    "v_exp_f32 v44, v44\n"
    "s_waitcnt lgkmcnt(0)\n"
    "v_mfma_f32_32x32x16_bf16 v[24:39], v[100:103], v[96:99], v[72:87]\n"
    "v_exp_f32 v45, v45\n"
    "v_cvt_pk_bf16_f32 v89, v42, v43\n"
    "ds_read_b128 v[104:107], v124 offset:3072\n"
    "v_exp_f32 v46, v46\n"
    "ds_read_b128 v[108:111], v125 offset:2048\n"
    "v_exp_f32 v47, v47\n"
    "v_cvt_pk_bf16_f32 v90, v44, v45\n"
    "ds_read_b128 v[112:115], v125 offset:2560\n"
    "v_exp_f32 v48, v48\n"
    "v_exp_f32 v49, v49\n"
    "v_cvt_pk_bf16_f32 v91, v46, v47\n"
    "v_exp_f32 v50, v50\n"
    "v_exp_f32 v51, v51\n"
    "v_mfma_f32_32x32x16_bf16 v[56:71], v[116:119], v[88:91], v[56:71]\n"
    "v_cvt_pk_bf16_f32 v92, v48, v49\n"
    "v_exp_f32 v52, v52\n"
    "v_exp_f32 v53, v53\n"
    "v_cvt_pk_bf16_f32 v93, v50, v51\n"
    "v_exp_f32 v54, v54\n"
    "v_exp_f32 v55, v55\n"
    "v_cvt_pk_bf16_f32 v94, v52, v53\n"
    "v_cvt_pk_bf16_f32 v95, v54, v55\n"
    "v_exp_f32 v24, v24\n"
    "v_exp_f32 v25, v25\n"
    "v_mfma_f32_32x32x16_bf16 v[56:71], v[120:123], v[92:95], v[56:71]\n"
    "v_exp_f32 v26, v26\n"
    "v_exp_f32 v27, v27\n"
    "v_cvt_pk_bf16_f32 v88, v24, v25\n"
    "v_exp_f32 v28, v28\n"
    "s_waitcnt lgkmcnt(0)\n"
    "v_mfma_f32_32x32x16_bf16 v[40:55], v[104:107], v[96:99], v[72:87]\n"
    "v_exp_f32 v29, v29\n"
    "v_cvt_pk_bf16_f32 v89, v26, v27\n"
    "ds_read_b128 v[100:103], v124 offset:4096\n"
    "v_exp_f32 v30, v30\n"
    "ds_read_b128 v[116:119], v125 offset:3072\n"
    "v_exp_f32 v31, v31\n"
    "v_cvt_pk_bf16_f32 v90, v28, v29\n"
    "ds_read_b128 v[120:123], v125 offset:3584\n"
    "v_exp_f32 v32, v32\n"
    "v_exp_f32 v33, v33\n"
    "v_cvt_pk_bf16_f32 v91, v30, v31\n"
    "v_exp_f32 v34, v34\n"
    "v_exp_f32 v35, v35\n"
    "v_mfma_f32_32x32x16_bf16 v[56:71], v[108:111], v[88:91], v[56:71]\n"
    "v_cvt_pk_bf16_f32 v92, v32, v33\n"
    "v_exp_f32 v36, v36\n"
    "v_exp_f32 v37, v37\n"
    "v_cvt_pk_bf16_f32 v93, v34, v35\n"
    "v_exp_f32 v38, v38\n"
    "v_exp_f32 v39, v39\n"
    "v_cvt_pk_bf16_f32 v94, v36, v37\n"
    "v_cvt_pk_bf16_f32 v95, v38, v39\n"
    "v_exp_f32 v40, v40\n"
    "v_exp_f32 v41, v41\n"
    "v_mfma_f32_32x32x16_bf16 v[56:71], v[112:115], v[92:95], v[56:71]\n"
    "v_exp_f32 v42, v42\n"
    "v_exp_f32 v43, v43\n"
    "v_cvt_pk_bf16_f32 v88, v40, v41\n"
    "v_exp_f32 v44, v44\n"
    "s_waitcnt lgkmcnt(0)\n"
    "v_mfma_f32_32x32x16_bf16 v[24:39], v[100:103], v[96:99], v[72:87]\n"
    "v_exp_f32 v45, v45\n"
    "v_cvt_pk_bf16_f32 v89, v42, v43\n"
    "ds_read_b128 v[104:107], v124 offset:5120\n"
    "v_exp_f32 v46, v46\n"
    "ds_read_b128 v[108:111], v125 offset:4096\n"
    "v_exp_f32 v47, v47\n"
    "v_cvt_pk_bf16_f32 v90, v44, v45\n"
    "ds_read_b128 v[112:115], v125 offset:4608\n"
    "v_exp_f32 v48, v48\n"
    "v_exp_f32 v49, v49\n"
    "v_cvt_pk_bf16_f32 v91, v46, v47\n"
    "v_exp_f32 v50, v50\n"
    "v_exp_f32 v51, v51\n"
    "v_mfma_f32_32x32x16_bf16 v[56:71], v[116:119], v[88:91], v[56:71]\n"
    "v_cvt_pk_bf16_f32 v92, v48, v49\n"
    "v_exp_f32 v52, v52\n"
    "v_exp_f32 v53, v53\n"
    "v_cvt_pk_bf16_f32 v93, v50, v51\n"
    "v_exp_f32 v54, v54\n"
    "v_exp_f32 v55, v55\n"
    "v_cvt_pk_bf16_f32 v94, v52, v53\n"
    "v_cvt_pk_bf16_f32 v95, v54, v55\n"
    "v_exp_f32 v24, v24\n"
    "v_exp_f32 v25, v25\n"
    "v_mfma_f32_32x32x16_bf16 v[56:71], v[120:123], v[92:95], v[56:71]\n"
    "v_exp_f32 v26, v26\n"
    "v_exp_f32 v27, v27\n"
    "v_cvt_pk_bf16_f32 v88, v24, v25\n"
    "v_exp_f32 v28, v28\n"
    "s_waitcnt lgkmcnt(0)\n"
    "v_mfma_f32_32x32x16_bf16 v[40:55], v[104:107], v[96:99], v[72:87]\n"
    "v_exp_f32 v29, v29\n"
    "v_cvt_pk_bf16_f32 v89, v26, v27\n"
    "ds_read_b128 v[100:103], v124 offset:6144\n"
    "v_exp_f32 v30, v30\n"
    "ds_read_b128 v[116:119], v125 offset:5120\n"
    "v_exp_f32 v31, v31\n"
    "v_cvt_pk_bf16_f32 v90, v28, v29\n"
    "ds_read_b128 v[120:123], v125 offset:5632\n"
    "v_exp_f32 v32, v32\n"
    "v_exp_f32 v33, v33\n"
    "v_cvt_pk_bf16_f32 v91, v30, v31\n"
    "v_exp_f32 v34, v34\n"
    "v_exp_f32 v35, v35\n"
    "v_mfma_f32_32x32x16_bf16 v[56:71], v[108:111], v[88:91], v[56:71]\n"
    "v_cvt_pk_bf16_f32 v92, v32, v33\n"
    "v_exp_f32 v36, v36\n"
    "v_exp_f32 v37, v37\n"
    "v_cvt_pk_bf16_f32 v93, v34, v35\n"
    "v_exp_f32 v38, v38\n"
    "v_exp_f32 v39, v39\n"
    "v_cvt_pk_bf16_f32 v94, v36, v37\n"
    "v_cvt_pk_bf16_f32 v95, v38, v39\n"
    "v_exp_f32 v40, v40\n"
    "v_exp_f32 v41, v41\n"
    "v_mfma_f32_32x32x16_bf16 v[56:71], v[112:115], v[92:95], v[56:71]\n"
    "v_exp_f32 v42, v42\n"
    "v_exp_f32 v43, v43\n"
    "v_cvt_pk_bf16_f32 v88, v40, v41\n"
    "v_exp_f32 v44, v44\n"
    "s_waitcnt lgkmcnt(0)\n"
    "v_mfma_f32_32x32x16_bf16 v[24:39], v[100:103], v[96:99], v[72:87]\n"
    "v_exp_f32 v45, v45\n"
    "v_cvt_pk_bf16_f32 v89, v42, v43\n"
    "ds_read_b128 v[104:107], v124 offset:7168\n"
    "v_exp_f32 v46, v46\n"
    "ds_read_b128 v[108:111], v125 offset:6144\n"
    "v_exp_f32 v47, v47\n"
    "v_cvt_pk_bf16_f32 v90, v44, v45\n"
    "ds_read_b128 v[112:115], v125 offset:6656\n"
    "v_exp_f32 v48, v48\n"
    "v_exp_f32 v49, v49\n"
    "v_cvt_pk_bf16_f32 v91, v46, v47\n"
    "v_exp_f32 v50, v50\n"
    "v_exp_f32 v51, v51\n"
    "v_mfma_f32_32x32x16_bf16 v[56:71], v[116:119], v[88:91], v[56:71]\n"
    "v_cvt_pk_bf16_f32 v92, v48, v49\n"
    "v_exp_f32 v52, v52\n"
    "v_exp_f32 v53, v53\n"
    "v_cvt_pk_bf16_f32 v93, v50, v51\n"
    "v_exp_f32 v54, v54\n"
    "v_exp_f32 v55, v55\n"
    "v_cvt_pk_bf16_f32 v94, v52, v53\n"
    "v_cvt_pk_bf16_f32 v95, v54, v55\n"
    "v_exp_f32 v24, v24\n"
    "v_exp_f32 v25, v25\n"
    "v_mfma_f32_32x32x16_bf16 v[56:71], v[120:123], v[92:95], v[56:71]\n"
    "v_exp_f32 v26, v26\n"
    "v_exp_f32 v27, v27\n"
    "v_cvt_pk_bf16_f32 v88, v24, v25\n"
    "v_exp_f32 v28, v28\n"
    "s_waitcnt lgkmcnt(0)\n"
    "v_mfma_f32_32x32x16_bf16 v[40:55], v[104:107], v[96:99], v[72:87]\n"
    "v_exp_f32 v29, v29\n"
    "v_cvt_pk_bf16_f32 v89, v26, v27\n"
    "s_waitcnt vmcnt(0)\n"
    "s_barrier\n"
    "ds_read_b128 v[100:103], v126 offset:0\n"
    "v_exp_f32 v30, v30\n"
    "ds_read_b128 v[116:119], v125 offset:7168\n"
    "v_exp_f32 v31, v31\n"
    "v_cvt_pk_bf16_f32 v90, v28, v29\n"
    "ds_read_b128 v[120:123], v125 offset:7680\n"
    "v_exp_f32 v32, v32\n"
    "v_exp_f32 v33, v33\n"
    "v_cvt_pk_bf16_f32 v91, v30, v31\n"
    "v_exp_f32 v34, v34\n"
    "v_exp_f32 v35, v35\n"
    "v_mfma_f32_32x32x16_bf16 v[56:71], v[108:111], v[88:91], v[56:71]\n"
    "s_cmp_ge_u32 %[cnt], 6\n"
    "s_cbranch_scc1 .Lnodma%=\n"
    "s_mov_b32 m0, %[dslot]\n"
    "s_nop 0\n"
    "global_load_lds_dwordx4 %[goff], %[gbase] offset:0\n"
    "global_load_lds_dwordx4 %[goff], %[gbase] offset:1024\n"
    ".Lnodma%=:\n"
    "v_cvt_pk_bf16_f32 v92, v32, v33\n"
    "v_exp_f32 v36, v36\n"
    "v_exp_f32 v37, v37\n"
    "v_cvt_pk_bf16_f32 v93, v34, v35\n"
    "v_exp_f32 v38, v38\n"
    "v_exp_f32 v39, v39\n"
    "v_cvt_pk_bf16_f32 v94, v36, v37\n"
    "v_cvt_pk_bf16_f32 v95, v38, v39\n"
    "v_exp_f32 v40, v40\n"
    "v_exp_f32 v41, v41\n"
    "v_mfma_f32_32x32x16_bf16 v[56:71], v[112:115], v[92:95], v[56:71]\n"
    "v_exp_f32 v42, v42\n"
    "v_exp_f32 v43, v43\n"
    "v_cvt_pk_bf16_f32 v88, v40, v41\n"
    "v_exp_f32 v44, v44\n"
    "s_waitcnt lgkmcnt(0)\n"
    "v_mfma_f32_32x32x16_bf16 v[24:39], v[100:103], v[96:99], v[72:87]\n"
    "v_exp_f32 v45, v45\n"
    "v_cvt_pk_bf16_f32 v89, v42, v43\n"
    "ds_read_b128 v[104:107], v126 offset:1024\n"
    "v_exp_f32 v46, v46\n"
    "ds_read_b128 v[108:111], v127 offset:0\n"
    "v_exp_f32 v47, v47\n"
    "v_cvt_pk_bf16_f32 v90, v44, v45\n"
    "ds_read_b128 v[112:115], v127 offset:512\n"
    "v_exp_f32 v48, v48\n"
    "v_exp_f32 v49, v49\n"
    "v_cvt_pk_bf16_f32 v91, v46, v47\n"
    "v_exp_f32 v50, v50\n"
    "v_exp_f32 v51, v51\n"
    "v_mfma_f32_32x32x16_bf16 v[56:71], v[116:119], v[88:91], v[56:71]\n"
    "v_cvt_pk_bf16_f32 v92, v48, v49\n"
    "v_exp_f32 v52, v52\n"
    "v_exp_f32 v53, v53\n"
    "v_cvt_pk_bf16_f32 v93, v50, v51\n"
    "v_exp_f32 v54, v54\n"
    "v_exp_f32 v55, v55\n"
    "v_cvt_pk_bf16_f32 v94, v52, v53\n"
    "v_cvt_pk_bf16_f32 v95, v54, v55\n"
    "v_mov_b32 v124, v126\n"
    "v_mov_b32 v125, v127\n"
    "s_add_u32 %[nslot], %[nslot], 0x4000\n"
    "s_cmp_eq_u32 %[nslot], 0xc000\n"
    "s_cselect_b32 %[nslot], 0, %[nslot]\n"
    "v_add_u32 v126, %[nslot], %[kaddr]\n"
    "v_and_b32 v127, %[nslot], %[vsel]\n"
    "v_add_u32 v127, v127, %[vaddr]\n"
    "s_add_u32 %[dslot], %[dslot], 0x4000\n"
    "s_cmp_eq_u32 %[dslot], %[dend]\n"
    "s_cselect_b32 %[dslot], %[dbeg], %[dslot]\n"
    "v_add_u32 %[goff], 0x4000, %[goff]\n"
    "s_add_u32 %[cnt], %[cnt], 1\n"
    "s_cmp_lt_u32 %[cnt], 8\n"
    "s_cbranch_scc1 .Lloop%=\n"
    "s_nop 1\n"
    "v_mfma_f32_32x32x16_bf16 v[56:71], v[120:123], v[92:95], v[56:71]\n"
    "s_nop 15\n"
    "s_nop 3\n"
    "v_rcp_f32 v24, v64\n"
    "s_nop 0\n"
    "v_mul_f32 v56, v56, v24\n"
    "v_mul_f32 v57, v57, v24\n"
    "v_mul_f32 v58, v58, v24\n"
    "v_mul_f32 v59, v59, v24\n"
    "v_mul_f32 v60, v60, v24\n"
    "v_mul_f32 v61, v61, v24\n"
    "v_mul_f32 v62, v62, v24\n"
    "v_mul_f32 v63, v63, v24\n"
    "v_cvt_pk_bf16_f32 %[oa0], v56, v57\n"
    "v_cvt_pk_bf16_f32 %[oa1], v58, v59\n"
    "v_cvt_pk_bf16_f32 %[oa2], v60, v61\n"
    "v_cvt_pk_bf16_f32 %[oa3], v62, v63\n"
    "s_waitcnt lgkmcnt(0)\n"
    "s_mov_b32 m0, %[m0save]\n"
        : [oa0] "=&v"(oa0), [oa1] "=&v"(oa1), [oa2] "=&v"(oa2), [oa3] "=&v"(oa3),
          [goff] "+v"(goff), [dslot] "+s"(dslot), [nslot] "+s"(nslot), [cnt] "=&s"(cnt), [m0save] "=&s"(m0save)
        : [qa0] "v"(qa[0]), [qa1] "v"(qa[1]), [qa2] "v"(qa[2]), [qa3] "v"(qa[3]),
          [nma] "v"(nma), [kaddr] "v"(kaddr), [vaddr] "v"(vaddr), [vsel] "v"(vsel), [gbase] "s"(kvb), [dbeg] "s"(dbeg), [dend] "s"(dend), [psel] "s"(0x07060302u), [vmask] "s"(0x0000ffff0000ffffull), [vone] "v"(((lane & 27) == 16) ? 0x3f803f80u : 0u), [dbgaddr] "s"(lds0 + A_LDS + wid * 64)
        : "memory", "scc", "vcc",
          "v24", "v25", "v26", "v27", "v28", "v29", "v30", "v31", "v32", "v33", "v34", "v35", "v36", "v37", "v38", "v39", "v40", "v41", "v42", "v43", "v44", "v45", "v46", "v47", "v48", "v49", "v50", "v51", "v52", "v53", "v54", "v55", "v56", "v57", "v58", "v59", "v60", "v61", "v62", "v63", "v64", "v65", "v66", "v67", "v68", "v69", "v70", "v71", "v72", "v73", "v74", "v75", "v76", "v77", "v78", "v79", "v80", "v81", "v82", "v83", "v84", "v85", "v86", "v87", "v88", "v89", "v90", "v91", "v92", "v93", "v94", "v95", "v96", "v97", "v98", "v99", "v100", "v101", "v102", "v103", "v104", "v105", "v106", "v107", "v108", "v109", "v110", "v111", "v112", "v113", "v114", "v115", "v116", "v117", "v118", "v119", "v120", "v121", "v122", "v123", "v124", "v125", "v126", "v127"
    );
    STAMP(4);
    st16_wt(ws + WS_OF + ((size_t)tile0 * 32 + head) * 1024 + lane * 16, (u32x4){oa0, oa1, oa2, oa3});
    WAITV(0);
    if (lane == 0) __hip_atomic_store(flagp(ws, FL_O + tile0 * 32 + head), tag, RLX_AGENT);
}

__device__ __forceinline__ void z_role(const uint16_t* __restrict__ x, const uint16_t* __restrict__ win, char* __restrict__ ws, char* lds, int g, u64& tag) {
    const int tid = threadIdx.x, lane = tid & 63, wid = __builtin_amdgcn_readfirstlane(tid >> 6), wr = wid >> 2, wc = wid & 3, l31 = lane & 31, hh = lane >> 5;
    const int xcd = g & 7, i = g >> 3;
    const int rb = 4 * (xcd >> 1) + (i & 3), cbz = 7 * ((xcd & 1) ^ 1) + (i >> 2);
    f32x16 acc[2];
    proj_main<false>(x, win, NIN, rb, cbz * 128, 12 + cbz, ws, lds, ((i & 3) * 4 + (i >> 2) * 3) & 15, tag, acc);
    STAMP(1);
    const float* st = (const float*)(lds + P_ST);
    unsigned zp[2][8];
#pragma unroll
    for (int rt = 0; rt < 2; ++rt) { const int tr = wr * 64 + rt * 32; const float mu = st[tr + l31], rs = st[128 + tr + l31];
#pragma unroll
        for (int q = 0; q < 4; ++q) { const f32x4 c = *(const f32x4*)(st + 256 + wc * 32 + 8 * q + 4 * hh);
            zp[rt][2 * q] = cvtpk(gelu_t((acc[rt][4 * q] - mu * c[0]) * rs), gelu_t((acc[rt][4 * q + 1] - mu * c[1]) * rs));
            zp[rt][2 * q + 1] = cvtpk(gelu_t((acc[rt][4 * q + 2] - mu * c[2]) * rs), gelu_t((acc[rt][4 * q + 3] - mu * c[3]) * rs)); } }
    if (cbz < 7) {
#pragma unroll
        for (int rt = 0; rt < 2; ++rt) { const int tt = rb * 4 + wr * 2 + rt;
            char* dst = ws + WS_Z1 + ((size_t)tt * 28 + cbz * 4 + wc) * 2048 + lane * 32;
            st16_wt(dst, (u32x4){zp[rt][0], zp[rt][1], zp[rt][2], zp[rt][3]}); st16_wt(dst + 16, (u32x4){zp[rt][4], zp[rt][5], zp[rt][6], zp[rt][7]}); }
    } else {
        const int cj = cbz - 7;
        float* pst = (float*)lds;
        const unsigned ONES = 0x3f803f80u;
#pragma unroll
        for (int rt = 0; rt < 2; ++rt) { float s1 = 0.f, s2 = 0.f;
#pragma unroll
            for (int e = 0; e < 8; ++e) { s1 = dot2(zp[rt][e], ONES, s1); s2 = dot2(zp[rt][e], zp[rt][e], s2); }
            s1 = swap32_add(s1); s2 = swap32_add(s2);
            if (hh == 0) *(f32x2*)(pst + (wc * 128 + wr * 64 + rt * 32 + l31) * 2) = (f32x2){s1, s2}; }
        __syncthreads();
        char* gst = ws + WS_ST + (size_t)rb * 7 * 2048;
        float* fin = (float*)(lds + 4096);
        if (tid < 128) { f32x2 s = *(const f32x2*)(pst + tid * 2);
#pragma unroll
            for (int j = 1; j < 4; ++j) s += *(const f32x2*)(pst + (j * 128 + tid) * 2);
            st16_wt(gst + (cj * 128 + tid) * 16, (u32x4){__float_as_uint(s[0]), __float_as_uint(s[1]), (unsigned)tag, (unsigned)(tag >> 32)});
            u32x4 gr[7];
            for (unsigned spins = 0; spins < (1u << 18); ++spins) {
                asm volatile("global_load_dwordx4 %0, %7, off sc1\n\tglobal_load_dwordx4 %1, %7, off offset:2048 sc1\n\tglobal_load_dwordx4 %2, %8, off sc1\n\tglobal_load_dwordx4 %3, %8, off offset:2048 sc1\n\t"
                             "global_load_dwordx4 %4, %9, off sc1\n\tglobal_load_dwordx4 %5, %9, off offset:2048 sc1\n\tglobal_load_dwordx4 %6, %10, off sc1\n\ts_waitcnt vmcnt(0)"
                             : "=&v"(gr[0]), "=&v"(gr[1]), "=&v"(gr[2]), "=&v"(gr[3]), "=&v"(gr[4]), "=&v"(gr[5]), "=&v"(gr[6])
                             : "v"(gst + tid * 16), "v"(gst + 4096 + tid * 16), "v"(gst + 8192 + tid * 16), "v"(gst + 12288 + tid * 16) : "memory");
                bool ok = true;
#pragma unroll
                for (int j = 0; j < 7; ++j) ok &= gr[j][2] == (unsigned)tag && gr[j][3] == (unsigned)(tag >> 32);
                if (__all(ok)) break;
                __builtin_amdgcn_s_sleep(POLL_SLEEP); }
            float s1 = 0.f, s2 = 0.f;
#pragma unroll
            for (int j = 0; j < 7; ++j) { s1 += __uint_as_float(gr[j][0]); s2 += __uint_as_float(gr[j][1]); }
            const float mu = s1 * (1.0f / FH); const float var = fmaxf(s2 * (1.0f / FH) - mu * mu, 0.f);
            fin[tid] = mu; fin[128 + tid] = __builtin_amdgcn_rsqf(var + LN_EPS); }
        __syncthreads();
        char* img = lds + 8192;
#pragma unroll
        for (int rt = 0; rt < 2; ++rt) { const int tok = wr * 64 + rt * 32 + l31; const float mu = fin[tok], rs = fin[128 + tok];
#pragma unroll
            for (int q = 0; q < 4; ++q) { const unsigned a = zp[rt][2 * q], c = zp[rt][2 * q + 1];
                const u32x2 o = {cvtpk((bflo(a) - mu) * rs, (bfhi(a) - mu) * rs), cvtpk((bflo(c) - mu) * rs, (bfhi(c) - mu) * rs)};
                const int f = wc * 32 + 8 * q + 4 * hh;
                *(u32x2*)(img + tok * 256 + (((f >> 3) ^ wsw(tok)) * 16) + (f & 4) * 2) = o; } }
        __syncthreads();
        { const int blk = (lane >> 4) & 1, q = (lane & 15) >> 2, p = lane & 3;
#pragma unroll
          for (int kq = 0; kq < 4; ++kq) { const int ks = wr * 4 + kq; s16x4 t2[2];
#pragma unroll
              for (int t = 0; t < 2; ++t) { const int kr = 16 * ks + 8 * hh + 4 * t + q; const int ch = 4 * wc + 2 * blk + (p >> 1);
                  t2[t] = trread((lds_cptr)img + kr * 256 + ((ch ^ wsw(kr)) * 16) + 8 * (p & 1)); }
              st16_wt(ws + WS_Z2N + ((size_t)(cj * 4 + wc) * 128 + rb * 8 + ks) * 1024 + lane * 16, __builtin_bit_cast(u32x4, cat(t2[0], t2[1]))); } }
    }
    PUBLISH(FL_Z + rb * 14 + cbz);
}

constexpr int S_SLOT = 24576, S_LDS = 3 * S_SLOT;
__device__ __forceinline__ void spatial_role(const uint16_t* __restrict__ wsp, const uint16_t* __restrict__ bsp, char* __restrict__ ws, char* lds, int g, u64 tag) {
    const int tid = threadIdx.x, lane = tid & 63, wid8 = __builtin_amdgcn_readfirstlane(tid >> 6), wid = wid8 & 3, l31 = lane & 31, hh = lane >> 5;
    const int i = g >> 3, tb = 4 * (g & 7) + (i & 3), fb = i >> 2;
    const unsigned lds0 = (unsigned)(uintptr_t)lds;
    STAMP(3);
    if (wid8 >= 4) {
        const int r0 = 16 * wid + (lane >> 3), r1 = r0 + 8;
        const char* sb_ = (const char*)(wsp + (size_t)tb * 64 * SEQ);
        const unsigned vo0 = (r0 * SEQ + ((lane & 7) ^ ((r0 >> 1) & 7)) * 8) * 2, vo1 = (r1 * SEQ + ((lane & 7) ^ ((r1 >> 1) & 7)) * 8) * 2;
        const char* zs = ws + WS_Z2N + (size_t)(fb * 4 + wid) * 131072; const unsigned zvo = lane * 16;
        unsigned ready = 0;
        GAS u64* zf = flagp(ws, FL_Z + (lane & 15) * 14 + 7 + fb);
        auto need = [&](int rbk) {
            for (unsigned spins = 0; !((ready >> rbk) & 1) && spins < (1u << 18); ++spins) {
                ready = (unsigned)__ballot(__hip_atomic_load(zf, RLX_AGENT) == tag) & 0xffffu;
                if (!((ready >> rbk) & 1)) __builtin_amdgcn_s_sleep(POLL_SLEEP); } };
        auto issue = [&](int it, int slot) { const unsigned so = lds0 + slot * S_SLOT;
            need(it >> 1);
            set_m0(so + wid * 2048); glds_s<0, false>(sb_ + it * 128, vo0);
            set_m0(so + wid * 2048 + 1024); glds_s<0, false>(sb_ + it * 128, vo1);
            set_m0(so + 8192 + wid * 4096);
            glds_s<0, true>(zs + it * 4096, zvo); glds_s<1024, true>(zs + it * 4096, zvo); glds_s<2048, true>(zs + it * 4096, zvo); glds_s<3072, true>(zs + it * 4096, zvo); };
        issue(0, 0); issue(1, 1); issue(2, 2);
        WAIT_BAR(12);
        int slot = 0;
#pragma unroll 1
        for (int it = 1; it < 31; ++it) { WAIT_BAR(6); if (it + 2 < 32) issue(it + 2, slot); slot = slot == 2 ? 0 : slot + 1; }
        WAIT_BAR(0);
        BAR();
    } else {
        unsigned boff[2][4];
#pragma unroll
        for (int rt = 0; rt < 2; ++rt)
#pragma unroll
            for (int ks = 0; ks < 4; ++ks) { const int r = rt * 32 + l31; boff[rt][ks] = (r * 128 + ((hh ^ ((r >> 1) & 7)) * 16)) ^ (ks * 32); }
        const unsigned aoff = 8192 + wid * 4096 + lane * 16;
        struct SF { bf16x8 a, b[2]; };
        auto rd = [&](SF& f, int slot, int ks) { lds_cptr sb = (lds_cptr)lds + slot * S_SLOT;
            f.a = ldsfrag(sb + aoff + ks * 1024); f.b[0] = ldsfrag(sb + boff[0][ks]); f.b[1] = ldsfrag(sb + boff[1][ks]); };
        f32x16 acc[2] = {f32x16{}, f32x16{}};
        auto mm = [&](const SF& f) { acc[0] = MFMA(f.a, f.b[0], acc[0]); acc[1] = MFMA(f.a, f.b[1], acc[1]); };
        SF f[4];
        BAR();
#pragma unroll
        for (int ks = 0; ks < 4; ++ks) rd(f[ks], 0, ks);
        auto step = [&](int nsl) {
            LGKM_BAR();
#pragma unroll
            for (int ks = 0; ks < 4; ++ks) { lds_cptr sb = (lds_cptr)lds + nsl * S_SLOT;
                acc[0] = MFMA(f[ks].a, f[ks].b[0], acc[0]); SBAR();
                f[ks].b[0] = ldsfrag(sb + boff[0][ks]); SBAR();
                PACE();
                acc[1] = MFMA(f[ks].a, f[ks].b[1], acc[1]); SBAR();
                f[ks].b[1] = ldsfrag(sb + boff[1][ks]); f[ks].a = ldsfrag(sb + aoff + ks * 1024); SBAR();
                PACE(); } };
#pragma unroll 1
        for (int io = 0; io < 10; ++io) { step(1); step(2); step(0); }
        step(1); step(2);
        wait_flags(flagp(ws, FL_Z + (tb >> 1) * 14 + fb), lane == 0, tag);
#pragma unroll
        for (int rt = 0; rt < 2; ++rt) { const int tt = tb * 2 + rt; const float bias = __uint_as_float((unsigned)bsp[tt * 32 + l31] << 16);
            const char* z1p = ws + WS_Z1 + ((size_t)tt * 28 + fb * 4 + wid) * 2048 + lane * 32;
            const u32x4 za = ld16_wt(z1p), zb = ld16_wt(z1p + 16);
            u32x4 ua, ub;
#pragma unroll
            for (int e = 0; e < 4; ++e) {
                ua[e] = cvtpk(bflo(za[e]) * (acc[rt][2 * e] + bias), bfhi(za[e]) * (acc[rt][2 * e + 1] + bias));
                ub[e] = cvtpk(bflo(zb[e]) * (acc[rt][8 + 2 * e] + bias), bfhi(zb[e]) * (acc[rt][8 + 2 * e + 1] + bias)); }
            char* ud = ws + WS_U + ((size_t)tt * 56 + (fb * 4 + wid) * 2) * 1024 + lane * 16;
            st16_wt(ud, ua); st16_wt(ud + 1024, ub); }
    }
    PUBLISH(FL_U + tb * 7 + fb);
}

constexpr int F_SLOT = 8192 + 8192, F_LDS = 4 * F_SLOT;
__device__ __forceinline__ void final_role(const uint16_t* __restrict__ x, const uint16_t* __restrict__ wo, const uint16_t* __restrict__ wout, char* __restrict__ ws, uint16_t* __restrict__ out, char* lds, int g, u64 tag) {
    const int tb = 4 * (g & 7) + ((g >> 3) & 3), db = g >> 5;
    const int tid = threadIdx.x, lane = tid & 63, wid8 = __builtin_amdgcn_readfirstlane(tid >> 6), wid = wid8 & 3, wr = wid >> 1, wc = wid & 1, l31 = lane & 31, hh = lane >> 5;
    const unsigned lds0 = (unsigned)(uintptr_t)lds;
    auto wswz = [](int r) { return ((r & 2) << 1) | ((r >> 2) & 3); };
    f32x16 as = f32x16{}, ag = f32x16{};
    const int k0 = 16 * wid + (lane >> 3), k1 = k0 + 8;
    const unsigned wo0 = (k0 * DM + db * 64 + ((lane & 7) ^ wswz(k0)) * 8) * 2, wo1 = (k1 * DM + db * 64 + ((lane & 7) ^ wswz(k1)) * 8) * 2;
    const int att = tb * 2 + (wid & 1), aks = 2 * (wid >> 1);
    unsigned woff[2];
    { const int blk = (lane >> 4) & 1, q = (lane & 15) >> 2, p = lane & 3; const int kr = 4 * hh + q; const int ch = 4 * wc + 2 * blk + (p >> 1);
      woff[0] = kr * 128 + ((ch ^ wswz(kr)) * 16) + 8 * (p & 1); woff[1] = (woff[0] + 1024) ^ 32; }
    const unsigned aoff = 8192 + wr * 4096 + lane * 16;
    struct FF { bf16x8 w, a; };
    auto rd = [&](FF& f, int slot, int ks) { lds_cptr sb = (lds_cptr)lds + slot * F_SLOT;
        f.w = cat(trread(sb + woff[0] + ks * 2048), trread(sb + woff[1] + ks * 2048)); f.a = ldsfrag(sb + aoff + ks * 1024); };
    auto pass = [&](const uint16_t* wb, const char* ab, int nfr, int n, f32x16& acc, bool pace) {
        if (wid8 >= 4) {
            auto issue = [&](int it) { const unsigned so = lds0 + (it & 3) * F_SLOT; const char* w = (const char*)(wb + (size_t)it * 64 * DM);
                set_m0(so + wid * 2048); glds_s<0, false>(w, wo0);
                set_m0(so + wid * 2048 + 1024); glds_s<0, false>(w, wo1);
                const char* a = ab + ((size_t)att * nfr + it * 4 + aks) * 1024;
                set_m0(so + 8192 + (wid & 1) * 4096 + aks * 1024); glds_s<0, true>(a, lane * 16); glds_s<1024, true>(a, lane * 16); };
            issue(0); issue(1); issue(2); issue(3);
            WAIT_BAR(12);
#pragma unroll 1
            for (int i = 1; i < n - 2; ++i) { WAIT_BAR(8); if (i + 3 < n) issue(i + 3); }
            WAIT_BAR(4);
            WAIT_BAR(0);
            BAR();
        } else {
            FF fa[4], fb[4];
            BAR();
#pragma unroll
            for (int ks = 0; ks < 4; ++ks) rd(fa[ks], 0, ks);
            auto step = [&](int nsl, FF (&cur)[4], FF (&nxt)[4]) {
                LGKM_BAR();
#pragma unroll
                for (int ks = 0; ks < 4; ++ks) { acc = MFMA(cur[ks].w, cur[ks].a, acc); SBAR(); rd(nxt[ks], nsl, ks); SBAR(); if (pace) PACE(); } };
#pragma unroll 1
            for (int it = 0; it < n; it += 2) { step((it + 1) & 3, fa, fb); step((it + 2) & 3, fb, fa); }
        } };
    if (wid8 == 0) wait_flags(flagp(ws, FL_U + tb * 7 + lane), lane < 7, tag);
    __syncthreads();
    STAMP(5);
    pass(wout, ws + WS_U, 56, 14, ag, true);
    STAMP(6);
    __syncthreads();
#pragma unroll
    for (int q = 0; q < 8; ++q) {
        const int kr = 8 * wid8 + (lane >> 3);
        set_m0(lds0 + q * 8192 + wid8 * 1024);
        glds_s<0, false>((const char*)(wo + (size_t)q * 64 * DM), (kr * DM + db * 64 + ((lane & 7) ^ wswz(kr)) * 8) * 2); }
    const int tok = tb * 64 + wr * 32 + l31;
    float gate[16]; u32x2 xv[4];
    if (wid8 < 4) {
#pragma unroll
        for (int q = 0; q < 4; ++q) xv[q] = *(const u32x2*)(x + (size_t)tok * DM + db * 64 + wc * 32 + 8 * q + 4 * hh);
#pragma unroll
        for (int r = 0; r < 16; ++r) gate[r] = silu_f(ag[r]); }
    if (wid8 == 0) wait_flags(flagp(ws, FL_O + tb * 64 + lane), true, tag);
    WAITV(0);
    __syncthreads();
    STAMP(7);
    const int kh = wid8 >> 2;
    u32x4 of[16];
    { const char* ob = ws + WS_OF + ((size_t)(tb * 2 + wr) * 32 + 16 * kh) * 1024; const unsigned v0 = lane * 16, v1 = v0 + 4096, v2 = v0 + 8192, v3 = v0 + 12288;
      asm volatile(
        "global_load_dwordx4 %0, %16, %20 sc1\n\tglobal_load_dwordx4 %1, %16, %20 offset:1024 sc1\n\tglobal_load_dwordx4 %2, %16, %20 offset:2048 sc1\n\tglobal_load_dwordx4 %3, %16, %20 offset:3072 sc1\n\t"
        "global_load_dwordx4 %4, %17, %20 sc1\n\tglobal_load_dwordx4 %5, %17, %20 offset:1024 sc1\n\tglobal_load_dwordx4 %6, %17, %20 offset:2048 sc1\n\tglobal_load_dwordx4 %7, %17, %20 offset:3072 sc1\n\t"
        "global_load_dwordx4 %8, %18, %20 sc1\n\tglobal_load_dwordx4 %9, %18, %20 offset:1024 sc1\n\tglobal_load_dwordx4 %10, %18, %20 offset:2048 sc1\n\tglobal_load_dwordx4 %11, %18, %20 offset:3072 sc1\n\t"
        "global_load_dwordx4 %12, %19, %20 sc1\n\tglobal_load_dwordx4 %13, %19, %20 offset:1024 sc1\n\tglobal_load_dwordx4 %14, %19, %20 offset:2048 sc1\n\tglobal_load_dwordx4 %15, %19, %20 offset:3072 sc1\n\t"
        "s_waitcnt vmcnt(0)"
        : "=&v"(of[0]), "=&v"(of[1]), "=&v"(of[2]), "=&v"(of[3]), "=&v"(of[4]), "=&v"(of[5]), "=&v"(of[6]), "=&v"(of[7]),
          "=&v"(of[8]), "=&v"(of[9]), "=&v"(of[10]), "=&v"(of[11]), "=&v"(of[12]), "=&v"(of[13]), "=&v"(of[14]), "=&v"(of[15])
        : "v"(v0), "v"(v1), "v"(v2), "v"(v3), "s"(ob) : "memory"); }
    { lds_cptr wb_ = (lds_cptr)lds + kh * 32768;
#pragma unroll
      for (int ks = 0; ks < 16; ++ks)
          as = MFMA(cat(trread(wb_ + woff[0] + ks * 2048), trread(wb_ + woff[1] + ks * 2048)), __builtin_bit_cast(bf16x8, of[ks]), as); }
    __syncthreads();
    if (kh == 1) {
#pragma unroll
        for (int q = 0; q < 4; ++q) *(f32x4*)(lds + (wid * 4 + q) * 1024 + lane * 16) = (f32x4){as[4 * q], as[4 * q + 1], as[4 * q + 2], as[4 * q + 3]}; }
    __syncthreads();
    STAMP(8);
    if (kh == 0) {
#pragma unroll
        for (int q = 0; q < 4; ++q) { const f32x4 p = *(const f32x4*)(lds + (wid * 4 + q) * 1024 + lane * 16);
            const float r0 = bflo(xv[q][0]) + (as[4 * q] + p[0]) * gate[4 * q], r1 = bfhi(xv[q][0]) + (as[4 * q + 1] + p[1]) * gate[4 * q + 1];
            const float r2 = bflo(xv[q][1]) + (as[4 * q + 2] + p[2]) * gate[4 * q + 2], r3 = bfhi(xv[q][1]) + (as[4 * q + 3] + p[3]) * gate[4 * q + 3];
            *(u32x2*)(out + (size_t)tok * DM + db * 64 + wc * 32 + 8 * q + 4 * hh) = (u32x2){cvtpk(r0, r1), cvtpk(r2, r3)}; }
    }
}

#define ADELAY 300
#define APRIO 3
#define GDELAY 0
#define GPRIO 2
constexpr int ALL_LDS = S_LDS;
__global__ __launch_bounds__(512, 4) void k_all(const uint16_t* __restrict__ x, const uint16_t* __restrict__ wqkv, const uint16_t* __restrict__ wo, const uint16_t* __restrict__ win,
                                                const uint16_t* __restrict__ wsp, const uint16_t* __restrict__ bsp, const uint16_t* __restrict__ wout, char* __restrict__ ws, uint16_t* __restrict__ out) {
    extern __shared__ __attribute__((aligned(16))) char lds[];
    const int b = blockIdx.x;
    u64 tag;
    STAMP(0);
    if (b < 256) {
        __builtin_amdgcn_s_setprio(APRIO);
        if (ADELAY > 0 && b < 192) {
            const u64 t0 = __builtin_amdgcn_s_memrealtime();
            const int i_ = b >> 3, cb_ = 6 * (b & 1) + (i_ >> 2), line = (i_ & 3) * 256 + (threadIdx.x & 255);
            unsigned junk = 0;
            while (__builtin_amdgcn_s_memrealtime() - t0 < ADELAY) __builtin_amdgcn_s_sleep(8);
            asm volatile("s_waitcnt vmcnt(0)" : "+v"(junk) :: "memory"); }
        if (b < 192) qkv_role(x, wqkv, ws, lds, b, tag); else stats_role(x, wqkv, win, ws, lds, b - 192, tag);
        __builtin_amdgcn_s_setprio(0);
        __syncthreads();
        attn_role(ws, lds, b, tag);
        STAMP(5);
    } else {
        const int g = b - 256;
        __builtin_amdgcn_s_setprio(GPRIO);
            if (GDELAY > 0) { const u64 t0 = __builtin_amdgcn_s_memrealtime(); while (__builtin_amdgcn_s_memrealtime() - t0 < GDELAY) __builtin_amdgcn_s_sleep(8); }
        if (g < 224) { z_role(x, win, ws, lds, g, tag); STAMP(2); __syncthreads(); spatial_role(wsp, bsp, ws, lds, g, tag); STAMP(4); __syncthreads(); }
        else { u64 ep; EPOCH_REQ(ep);
            { const int wv = (g - 224) * 8 + (threadIdx.x >> 6), lane = threadIdx.x & 63; unsigned junk = 0;
#pragma unroll
              for (int q = 0; q < 4; ++q) asm volatile("global_load_dword %0, %1, off" : "+v"(junk) : "v"((const char*)wsp + ((size_t)(q * 256 + wv) * 64 + lane) * 128) : "memory");
              if (wv < 112) asm volatile("global_load_dword %0, %1, off" : "+v"(junk) : "v"((const char*)wout + ((size_t)wv * 64 + lane) * 128) : "memory");
              else if (wv < 176) asm volatile("global_load_dword %0, %1, off" : "+v"(junk) : "v"((const char*)wo + ((size_t)(wv - 112) * 64 + lane) * 128) : "memory");
              asm volatile("s_waitcnt vmcnt(0)" : "+v"(junk) :: "memory"); }
            asm volatile("s_waitcnt vmcnt(0)" : "+v"(ep) :: "memory"); tag = EPOCH_TAG(ep); }
        if (threadIdx.x == 0) __hip_atomic_store(flagp(ws, FL_G + g), tag, RLX_AGENT);
        final_role(x, wo, wout, ws, out, lds, g, tag);
        if (g == 255 && threadIdx.x < 64) {
            const int lane = threadIdx.x;
#pragma unroll 1
            for (int c = 0; c < 4; ++c) { wait_flags(flagp(ws, FL_O + (c * 2 + (lane >> 5)) * 256 + (lane & 31)), true, tag); wait_flags(flagp(ws, FL_G + c * 64 + lane), true, tag); }
            if (lane == 0) __hip_atomic_store(flagp(ws, FL_EPOCH), tag, RLX_AGENT);
        }
        STAMP(9);
    }
}

extern "C" void kernel_launch(void* const* d_in, const int* in_sizes, int n_in, void* d_out, int out_size, void* d_ws, size_t ws_size, hipStream_t stream) {
    const uint16_t* x = (const uint16_t*)d_in[0]; const uint16_t* wqkv = (const uint16_t*)d_in[1]; const uint16_t* wo = (const uint16_t*)d_in[2];
    const uint16_t* win = (const uint16_t*)d_in[4]; const uint16_t* wsp = (const uint16_t*)d_in[5]; const uint16_t* bsp = (const uint16_t*)d_in[6]; const uint16_t* wout = (const uint16_t*)d_in[7];
    (void)hipFuncSetAttribute((const void*)k_all, hipFuncAttributeMaxDynamicSharedMemorySize, ALL_LDS);
    char* wsb = (char*)d_ws + ((ws_size - WS_TAIL * MB) & ~(size_t)(2 * MB - 1));
    k_all<<<512, 512, ALL_LDS, stream>>>(x, wqkv, wo, win, wsp, bsp, wout, wsb, (uint16_t*)d_out);
}
```

```cpp
#include <hip/hip_runtime.h>
#include <hip/hip_bf16.h>
#include <stdint.h>
#define WARM_TLB 1
#define WARM_IN 1
#define CODE_LINES 320
#include <stdio.h>

typedef short bf16x8 __attribute__((ext_vector_type(8)));
typedef short s16x4 __attribute__((ext_vector_type(4)));
typedef __bf16 bf16x2_t __attribute__((ext_vector_type(2)));
typedef float f32x16 __attribute__((ext_vector_type(16)));
typedef float f32x4 __attribute__((ext_vector_type(4)));
typedef float f32x2 __attribute__((ext_vector_type(2)));
typedef unsigned u32x4 __attribute__((ext_vector_type(4)));
typedef unsigned u32x2 __attribute__((ext_vector_type(2)));
typedef unsigned long long u64;
#define GAS __attribute__((address_space(1)))
#define LAS __attribute__((address_space(3)))
typedef LAS const char* lds_cptr;
typedef short v4i16_t __attribute__((ext_vector_type(4)));
#define RLX_AGENT __ATOMIC_RELAXED, __HIP_MEMORY_SCOPE_AGENT

constexpr int SEQ = 2048, DM = 512, NQKV = 1536, NIN = 1792, FH = 896;
constexpr float QSCALE = 0.25f * 1.4426950408889634f;
constexpr float LN_EPS = 1e-5f;
constexpr size_t MB = 1024 * 1024;
constexpr size_t WS_QF = 0, WS_KV = 2 * MB, WS_Z1 = 6 * MB, WS_Z2N = 10 * MB, WS_U = 14 * MB, WS_OF = 18 * MB;
constexpr size_t WS_KM = 20 * MB, WS_ST = 20 * MB + 65536, WS_RS = 20 * MB + 65536 + 229376, WS_CS = 20 * MB + 65536 + 245760  , WS_FLAG = 21 * MB;
constexpr int FL_RS = 0, FL_CS = 64, FL_QKV = 128, FL_ST = 320, FL_Z = 432, FL_U = 656, FL_G = 880, FL_EPOCH = 1136, FL_O = 1152;

#define MFMA(a, b, c) __builtin_amdgcn_mfma_f32_32x32x16_bf16(a, b, c, 0, 0, 0)
#define SBAR() __builtin_amdgcn_sched_barrier(0)
__device__ __forceinline__ unsigned cvtpk(float lo, float hi) { unsigned r; asm("v_cvt_pk_bf16_f32 %0, %1, %2" : "=v"(r) : "v"(lo), "v"(hi)); return r; }
__device__ __forceinline__ float bflo(unsigned u) { return __uint_as_float(u << 16); }
__device__ __forceinline__ float bfhi(unsigned u) { return __uint_as_float(u & 0xffff0000u); }
__device__ __forceinline__ float dot2(unsigned a, unsigned b, float acc) {
    return __builtin_amdgcn_fdot2_f32_bf16(__builtin_bit_cast(bf16x2_t, a), __builtin_bit_cast(bf16x2_t, b), acc, false); }
__device__ __forceinline__ void glds16(const void* g, unsigned lds_base) {
    asm volatile("s_mov_b32 m0, %1\n\ts_nop 0\n\tglobal_load_lds_dwordx4 %0, off" :: "v"(g), "s"(lds_base) : "memory", "m0"); }
template <int IMM, bool WT> __device__ __forceinline__ void glds_s(const void* sbase, unsigned voff) {
    if (WT) asm volatile("global_load_lds_dwordx4 %0, %1 offset:%2 sc1" :: "v"(voff), "s"(sbase), "n"(IMM) : "memory");
    else asm volatile("global_load_lds_dwordx4 %0, %1 offset:%2" :: "v"(voff), "s"(sbase), "n"(IMM) : "memory"); }
__device__ __forceinline__ void set_m0(unsigned v) { asm volatile("s_mov_b32 m0, %0\n\ts_nop 0" :: "s"(v) : "memory", "m0"); }
__device__ __forceinline__ s16x4 trread(lds_cptr p) { return __builtin_bit_cast(s16x4, __builtin_amdgcn_ds_read_tr16_b64_v4i16((LAS v4i16_t*)p)); }
__device__ __forceinline__ bf16x8 ldsfrag(lds_cptr p) { return *(LAS const bf16x8*)p; }
__device__ __forceinline__ bf16x8 cat(s16x4 a, s16x4 b) { return (bf16x8){a[0], a[1], a[2], a[3], b[0], b[1], b[2], b[3]}; }
__device__ __forceinline__ float swap32_add(float v) { auto rr = __builtin_amdgcn_permlane32_swap(__float_as_uint(v), __float_as_uint(v), false, false); return __uint_as_float(rr[0]) + __uint_as_float(rr[1]); }
__device__ __forceinline__ float swap32_max(float v) { auto rr = __builtin_amdgcn_permlane32_swap(__float_as_uint(v), __float_as_uint(v), false, false); return fmaxf(__uint_as_float(rr[0]), __uint_as_float(rr[1])); }
template <int CTRL> __device__ __forceinline__ float dppf(float v) { return __int_as_float(__builtin_amdgcn_update_dpp(0, __float_as_int(v), CTRL, 0xf, 0xf, false)); }
__device__ __forceinline__ float row16_add(float v) { v += dppf<0x128>(v); v += dppf<0x124>(v); v += dppf<0x122>(v); v += dppf<0x121>(v); return v; }
__device__ __forceinline__ float row16_max(float v) { v = fmaxf(v, dppf<0x128>(v)); v = fmaxf(v, dppf<0x124>(v)); v = fmaxf(v, dppf<0x122>(v)); v = fmaxf(v, dppf<0x121>(v)); return v; }
__device__ __forceinline__ float swap16_max(float v) { auto rr = __builtin_amdgcn_permlane16_swap(__float_as_uint(v), __float_as_uint(v), false, false); return fmaxf(__uint_as_float(rr[0]), __uint_as_float(rr[1])); }
__device__ __forceinline__ float gelu_t(float x) {
    const float c1 = -2.3022082f, c2 = -0.10294324f;
    float e = __builtin_amdgcn_exp2f(x * __builtin_fmaf(x * x, c2, c1));
    return x * __builtin_amdgcn_rcpf(1.0f + e); }
__device__ __forceinline__ float silu_f(float x) { return x * __builtin_amdgcn_rcpf(1.0f + __builtin_amdgcn_exp2f(-1.4426950408889634f * x)); }
#define STAMP(slot) do {} while (0)
#define PF_DIST 9
#define PACE_N 2
#define PACE() do { asm volatile("s_nop %0" :: "n"(PACE_N)); SBAR(); } while (0)
#define POLL_SLEEP 2
#define WAITV(n) asm volatile("s_waitcnt vmcnt(" #n ")" ::: "memory")
#define WAIT_BAR(n) asm volatile("s_waitcnt vmcnt(" #n ")\n\ts_barrier" ::: "memory")
#define BAR() asm volatile("s_barrier" ::: "memory")
#define LGKM_BAR() do { __builtin_amdgcn_s_waitcnt(0xC07F); asm volatile("" ::: "memory"); __builtin_amdgcn_s_barrier(); asm volatile("" ::: "memory"); } while (0)
__device__ __forceinline__ unsigned wsw(unsigned row) { return ((row & 3) << 2) | ((row >> 2) & 3); }
__device__ __forceinline__ void st16_wt(void* p, u32x4 v) {
    asm volatile("global_store_dwordx4 %0, %1, off sc1\n\ts_nop 1" :: "v"(p), "v"(v)); }
__device__ __forceinline__ void st8_wt(void* p, u64 v) { __hip_atomic_store((GAS u64*)p, v, RLX_AGENT); }
__device__ __forceinline__ u64 ld8_wt(const void* p) { return __hip_atomic_load((GAS u64*)p, RLX_AGENT); }
__device__ __forceinline__ u32x4 ld16_wt(const void* p) { u32x4 v; asm volatile("global_load_dwordx4 %0, %1, off sc1\n\ts_waitcnt vmcnt(0)" : "=&v"(v) : "v"(p) : "memory"); return v; }
__device__ __forceinline__ void glds16_wt(const void* g, unsigned lds_base) {
    asm volatile("s_mov_b32 m0, %1\n\ts_nop 0\n\tglobal_load_lds_dwordx4 %0, off sc1" :: "v"(g), "s"(lds_base) : "memory", "m0"); }
__device__ __forceinline__ GAS u64* flagp(char* ws, int idx) { return (GAS u64*)(ws + WS_FLAG) + idx * 16; }
__device__ __forceinline__ void wait_flags(GAS u64* f, bool want, u64 tag) {
    for (unsigned spins = 0; spins < (1u << 20); ++spins) {
        const u64 v = want ? __hip_atomic_load(f, RLX_AGENT) : tag;
        if (__all(v == tag)) return;
        __builtin_amdgcn_s_sleep(POLL_SLEEP); } }
#define ACQUIRE() do { __builtin_amdgcn_fence(__ATOMIC_ACQUIRE, "agent"); asm volatile("s_waitcnt vmcnt(0)" ::: "memory"); } while (0)
#define EPOCH_REQ(ep) asm volatile("global_load_dwordx2 %0, %1, off sc1" : "=v"(ep) : "v"(flagp(ws, FL_EPOCH)) : "memory")
#define EPOCH_TAG(ep) ((((u64)(unsigned)__builtin_amdgcn_readfirstlane((unsigned)((ep) >> 32)) << 32) | (unsigned)__builtin_amdgcn_readfirstlane((unsigned)(ep))) + 1)
#define PUBLISH(idx) do { WAITV(0); __syncthreads(); if (threadIdx.x == 0) __hip_atomic_store(flagp(ws, idx), tag, RLX_AGENT); } while (0)

__device__ __forceinline__ void stats_role(const uint16_t* __restrict__ x, const uint16_t* __restrict__ wqkv, const uint16_t* __restrict__ win, char* __restrict__ ws, char* lds, int j, u64& tag) {
    u64 ep; EPOCH_REQ(ep);
    const int tid = threadIdx.x, lane = tid & 63, wid = tid >> 6, rr = lane >> 4, cc = lane & 15;
    const unsigned ONES = 0x3f803f80u;
    u32x4 xv[4], w[8];
    const int row = 32 * j + 4 * wid + rr;
#pragma unroll
    for (int i = 0; i < 4; ++i) xv[i] = *(const u32x4*)(x + (size_t)row * DM + i * 128 + cc * 8);
    const int cbk = j >> 1;
    if (j < 52) {
        const uint16_t* W = cbk < 12 ? wqkv + cbk * 128 : win + (cbk - 12) * 128; const int ldw = cbk < 12 ? NQKV : NIN;
        const uint16_t* p = W + (size_t)((j & 1) * 256 + 32 * wid + rr) * ldw + cc * 8;
#pragma unroll
        for (int i = 0; i < 8; ++i) w[i] = *(const u32x4*)(p + (size_t)(i * 4) * ldw);
    }
    asm volatile("s_waitcnt vmcnt(0)" : "+v"(ep) :: "memory");
    tag = EPOCH_TAG(ep);
    float s1 = 0.f, s2 = 0.f;
#pragma unroll
    for (int i = 0; i < 4; ++i)
#pragma unroll
        for (int e = 0; e < 4; ++e) { s1 = dot2(xv[i][e], ONES, s1); s2 = dot2(xv[i][e], xv[i][e], s2); }
    s1 = row16_add(s1); s2 = row16_add(s2);
    { const float mu = s1 * (1.0f / DM), var = fmaxf(s2 * (1.0f / DM) - mu * mu, 0.f);
      if (cc == 0) st8_wt(ws + WS_RS + (size_t)row * 8, __builtin_bit_cast(u64, (f32x2){mu, __builtin_amdgcn_rsqf(var + LN_EPS)})); }
    if (j < 52) {
        float cs[8] = {0.f, 0.f, 0.f, 0.f, 0.f, 0.f, 0.f, 0.f};
        unsigned sel_lo = 0x00003f80u, sel_hi = 0x3f800000u;
        asm volatile("" : "+v"(sel_lo), "+v"(sel_hi));
#pragma unroll
        for (int i = 0; i < 8; ++i)
#pragma unroll
            for (int e = 0; e < 4; ++e) { cs[2 * e] = dot2(w[i][e], sel_lo, cs[2 * e]); cs[2 * e + 1] = dot2(w[i][e], sel_hi, cs[2 * e + 1]); }
        float* red = (float*)lds;
        *(f32x4*)(red + (wid * 4 + rr) * 128 + cc * 8) = (f32x4){cs[0], cs[1], cs[2], cs[3]}; *(f32x4*)(red + (wid * 4 + rr) * 128 + cc * 8 + 4) = (f32x4){cs[4], cs[5], cs[6], cs[7]};
        __syncthreads();
        if (tid < 64) { f32x2 a = {0.f, 0.f};
#pragma unroll
            for (int q = 0; q < 32; ++q) a += *(const f32x2*)(red + q * 128 + 2 * tid);
            st8_wt(ws + WS_CS + (size_t)((j & 1) * 3328 + cbk * 128 + 2 * tid) * 4, __builtin_bit_cast(u64, a)); }
    }
    STAMP(11);
    WAITV(0); __syncthreads();
    STAMP(1);
    if (tid == 0) { __hip_atomic_store(flagp(ws, FL_RS + j), tag, RLX_AGENT); if (j < 52) __hip_atomic_store(flagp(ws, FL_CS + j), tag, RLX_AGENT); }
}

constexpr int P_SLOT = 16384, P_ST = 4 * P_SLOT, P_LDS = 4 * P_SLOT + 4096;
struct PFrag { bf16x8 x[2], w; };
template <bool SWAP>
__device__ __forceinline__ void proj_main(const uint16_t* __restrict__ x, const uint16_t* __restrict__ W, int ldw, int rb, int col0, int cblk, char* __restrict__ ws, char* lds, int rot, u64& tag, f32x16 (&acc)[2]) {
    u64 ep; EPOCH_REQ(ep);
    const int tid = threadIdx.x, lane = tid & 63, wid = __builtin_amdgcn_readfirstlane(tid >> 6), wr = wid >> 2, wc = wid & 3, l31 = lane & 31, hh = lane >> 5;
    const unsigned lds0 = (unsigned)(uintptr_t)lds;
    const int xr = 16 * wid + (lane >> 2), wk = 4 * wid + (lane >> 4);
    const char* xb = (const char*)(x + (size_t)rb * 128 * DM); const char* wb = (const char*)(W + col0);
    const unsigned xvo = (xr * DM + ((lane & 3) ^ ((xr >> 2) & 3)) * 8) * 2, wvo = (wk * ldw + ((lane & 15) ^ wsw(wk)) * 8) * 2;
    const unsigned xd = lds0 + wid * 1024, wd = lds0 + 8192 + wid * 1024;
    auto issue1 = [&](int it, int which) { const unsigned so = (it & 3) * P_SLOT; const int kk = (it + rot) & 15;
        if (which == 0) { set_m0(xd + so); glds_s<0, false>(xb + kk * 64, xvo); } else { set_m0(wd + so); glds_s<0, false>(wb + (size_t)kk * 64 * ldw, wvo); } SBAR(); };
#pragma unroll
    for (int it = 0; it < 4; ++it) { issue1(it, 0); issue1(it, 1); }
    unsigned xoff[2][2], woff[2][2];
    const int blk = (lane >> 4) & 1, q = (lane & 15) >> 2, p = lane & 3;
#pragma unroll
    for (int ks = 0; ks < 2; ++ks) {
#pragma unroll
        for (int rt = 0; rt < 2; ++rt) { const int r = wr * 64 + rt * 32 + l31; xoff[rt][ks] = r * 64 + (((2 * ks + hh) ^ ((r >> 2) & 3)) * 16); }
        const int kr = 8 * hh + q, ch = 4 * wc + 2 * blk + (p >> 1);
        woff[0][ks] = 8192 + ks * 4096 + kr * 256 + ((ch ^ wsw(kr)) * 16) + 8 * (p & 1); woff[1][ks] = (woff[0][ks] + 1024) ^ 16; }
    auto rd = [&](PFrag& f, int slot, int j) { lds_cptr sb = (lds_cptr)lds + slot * P_SLOT;
        f.x[0] = ldsfrag(sb + xoff[0][j]); f.x[1] = ldsfrag(sb + xoff[1][j]); f.w = cat(trread(sb + woff[0][j]), trread(sb + woff[1][j])); };
    auto mm = [&](const PFrag& f) {
#pragma unroll
        for (int rt = 0; rt < 2; ++rt) acc[rt] = SWAP ? MFMA(f.x[rt], f.w, acc[rt]) : MFMA(f.w, f.x[rt], acc[rt]); };
    acc[0] = f32x16{}; acc[1] = f32x16{};
    PFrag fa[2], fb[2];
    asm volatile("s_waitcnt vmcnt(6)\n\ts_barrier" : "+v"(ep) :: "memory");
    tag = EPOCH_TAG(ep);
    STAMP(10);
    rd(fa[0], 0, 0); rd(fa[1], 0, 1);
    auto mm1 = [&](const PFrag& f, int rt) { acc[rt] = SWAP ? MFMA(f.x[rt], f.w, acc[rt]) : MFMA(f.w, f.x[rt], acc[rt]); SBAR(); };
    auto step = [&](int it, int sl, PFrag (&cur)[2], PFrag (&nxt)[2]) {
        __builtin_amdgcn_s_waitcnt(0xC07F);
        if (it < 13) WAITV(4); else if (it == 13) WAITV(2); else WAITV(0);
        __builtin_amdgcn_s_barrier(); asm volatile("" ::: "memory");
        lds_cptr sb = (lds_cptr)lds + ((sl + 1) & 3) * P_SLOT;
        mm1(cur[0], 0);
        nxt[0].x[0] = ldsfrag(sb + xoff[0][0]); nxt[0].x[1] = ldsfrag(sb + xoff[1][0]); SBAR();
        mm1(cur[0], 1);
        nxt[0].w = cat(trread(sb + woff[0][0]), trread(sb + woff[1][0])); SBAR();
        if (it + 4 < 16) issue1(it + 4, 0);
        mm1(cur[1], 0);
        nxt[1].x[0] = ldsfrag(sb + xoff[0][1]); nxt[1].x[1] = ldsfrag(sb + xoff[1][1]); SBAR();
        mm1(cur[1], 1);
        nxt[1].w = cat(trread(sb + woff[0][1]), trread(sb + woff[1][1])); SBAR();
        if (it + 4 < 16) issue1(it + 4, 1); };
#pragma unroll 1
    for (int io = 0; io < 2; ++io) { step(io * 4, 0, fa, fb); step(io * 4 + 1, 1, fb, fa); step(io * 4 + 2, 2, fa, fb); step(io * 4 + 3, 3, fb, fa); }
    const char* fa_ = (const char*)(lane < 4 ? flagp(ws, FL_RS + rb * 4 + lane) : flagp(ws, FL_CS + 2 * cblk + (lane & 1)));
    const char* sa_ = tid < 128 ? ws + WS_RS + (size_t)(rb * 128 + tid) * 8 : ws + WS_CS + (size_t)(cblk * 128 + 2 * (tid & 63)) * 4;
    const char* sb_ = ws + WS_CS + (size_t)(3328 + cblk * 128 + 2 * (tid & 63)) * 4;
    u64 fl, stv, stw;
    step(8, 0, fa, fb); step(9, 1, fb, fa);
    asm volatile("global_load_dwordx2 %0, %1, off sc1" : "=v"(fl) : "v"(fa_) : "memory");
    step(10, 2, fa, fb); step(11, 3, fb, fa); step(12, 0, fa, fb);
    asm volatile("" : "+v"(fl));
    const bool got = __all(lane >= 6 || fl == tag);
    asm volatile("global_load_dwordx2 %0, %2, off sc1\n\tglobal_load_dwordx2 %1, %3, off sc1" : "=&v"(stv), "=&v"(stw) : "v"(sa_), "v"(sb_) : "memory");
    step(13, 1, fb, fa); step(14, 2, fa, fb); step(15, 3, fb, fa);
    asm volatile("" : "+v"(stv), "+v"(stw));
    if (!got) { wait_flags((GAS u64*)fa_, lane < 6, tag); stv = ld8_wt(sa_); stw = ld8_wt(sb_); }
    STAMP(11);
    float* st = (float*)(lds + P_ST);
    { const f32x2 v = __builtin_bit_cast(f32x2, stv);
      if (tid < 128) { st[tid] = v[0]; st[128 + tid] = v[1]; } else if (tid < 192) *(f32x2*)(st + 256 + 2 * (tid - 128)) = v + __builtin_bit_cast(f32x2, stw); }
    __syncthreads();
}

__device__ __forceinline__ void qkv_role(const uint16_t* __restrict__ x, const uint16_t* __restrict__ wqkv, char* __restrict__ ws, char* lds, int b, u64& tag) {
    const int xcd = b & 7, i = b >> 3;
    const int rb = 4 * (xcd >> 1) + (i & 3), cb = 6 * (xcd & 1) + (i >> 2);
    const int tid = threadIdx.x, lane = tid & 63, wid = __builtin_amdgcn_readfirstlane(tid >> 6), wr = wid >> 2, wc = wid & 3, l31 = lane & 31, hh = lane >> 5;
    f32x16 acc[2];
    const int rot = ((i & 3) * 4 + (i >> 2) * 3) & 15;
    if (cb >= 8) proj_main<true>(x, wqkv, NQKV, rb, cb * 128, cb, ws, lds, rot, tag, acc); else proj_main<false>(x, wqkv, NQKV, rb, cb * 128, cb, ws, lds, rot, tag, acc);
    STAMP(1);
    const float* st = (const float*)(lds + P_ST);
    if (cb >= 8) {
        const float cs = st[256 + wc * 32 + l31];
        const int head = (cb - 8) * 8 + wc * 2 + (l31 >> 4);
#pragma unroll
        for (int rt = 0; rt < 2; ++rt) { const int tr = wr * 64 + rt * 32;
            unsigned pk[8];
#pragma unroll
            for (int q = 0; q < 4; ++q) { const f32x4 m = *(const f32x4*)(st + tr + 8 * q + 4 * hh), r = *(const f32x4*)(st + 128 + tr + 8 * q + 4 * hh);
                pk[2 * q] = cvtpk((acc[rt][4 * q] - m[0] * cs) * r[0], (acc[rt][4 * q + 1] - m[1] * cs) * r[1]);
                pk[2 * q + 1] = cvtpk((acc[rt][4 * q + 2] - m[2] * cs) * r[2], (acc[rt][4 * q + 3] - m[3] * cs) * r[3]); }
            const int tile = rb * 4 + (tr >> 5);
            char* dst = ws + WS_KV + (size_t)head * 131072 + (tile >> 3) * 16384 + 8192 + (tile & 7) * 1024 + hh * 256 + (l31 & 15) * 16;
            st16_wt(dst, (u32x4){pk[0], pk[1], pk[2], pk[3]}); st16_wt(dst + 512, (u32x4){pk[4], pk[5], pk[6], pk[7]}); }
    } else {
        const bool isK = cb >= 4; const float sc = isK ? 1.0f : QSCALE;
        float kmx[2] = {0.f, 0.f};
#pragma unroll
        for (int rt = 0; rt < 2; ++rt) { const int tr = wr * 64 + rt * 32, tile = rb * 4 + (tr >> 5);
            const float mu = st[tr + l31], rs = st[128 + tr + l31] * sc;
#pragma unroll
            for (int hb = 0; hb < 2; ++hb) {
                float v[8];
#pragma unroll
                for (int q = 0; q < 2; ++q) { const f32x4 c = *(const f32x4*)(st + 256 + wc * 32 + 16 * hb + 8 * q + 4 * hh);
#pragma unroll
                    for (int e = 0; e < 4; ++e) v[4 * q + e] = (acc[rt][8 * hb + 4 * q + e] - mu * c[e]) * rs; }
                u32x4 pk;
#pragma unroll
                for (int e = 0; e < 4; ++e) pk[e] = cvtpk(v[2 * e], v[2 * e + 1]);
                const int head = (cb & 3) * 8 + wc * 2 + hb;
                char* dst = isK ? ws + WS_KV + (size_t)head * 131072 + (tile >> 3) * 16384 + (tile & 7) * 1024 + lane * 16 : ws + WS_QF + (size_t)head * 65536 + tile * 1024 + lane * 16;
                st16_wt(dst, pk);
                float n2 = 0.f;
#pragma unroll
                for (int e = 0; e < 4; ++e) n2 = dot2(pk[e], pk[e], n2);
                kmx[hb] = fmaxf(kmx[hb], swap32_add(n2));
            } }
        if (isK) {
#pragma unroll
            for (int hb = 0; hb < 2; ++hb) { const float m = swap16_max(row16_max(kmx[hb]));
                if (lane == 0) __hip_atomic_store((GAS unsigned*)(ws + WS_KM) + ((cb & 3) * 8 + wc * 2 + hb) * 32 + rb * 2 + wr, __float_as_uint(m), RLX_AGENT); } }
    }
    STAMP(12);
    PUBLISH(FL_QKV + cb * 16 + rb);
    STAMP(13);
}

constexpr int A_ONES = 3 * 16384, A_LDS = A_ONES + 8192 + 512;
__device__ __forceinline__ void attn_role(char* __restrict__ ws, char* lds, int b, u64 tag) {
    const int tid = threadIdx.x, lane = tid & 63, wid = __builtin_amdgcn_readfirstlane(tid >> 6);
    const int head = (b & 7) * 4 + ((b >> 3) & 3), jb = b >> 5;
    const unsigned lds0 = (unsigned)(uintptr_t)lds;
    const char* kvb = ws + WS_KV + (size_t)head * 131072;
    if (wid == 0) {
        const int hb = head >> 3;
        const int idx = lane < 16 ? (4 + hb) * 16 + lane : lane < 32 ? (8 + hb) * 16 + (lane - 16) : hb * 16 + 2 * jb + (lane & 1);
        wait_flags(flagp(ws, FL_QKV + idx), lane < 34, tag);
        STAMP(14);
        }
    __syncthreads();
    STAMP(2);
    unsigned goff = wid * 2048 + lane * 16;
    const int tile0 = jb * 8 + wid;
    u32x4 qa; unsigned kmu;
    asm volatile("global_load_dwordx4 %0, %2, off sc1\n\tglobal_load_dword %1, %3, off sc1" : "=&v"(qa), "=&v"(kmu)
                 : "v"(ws + WS_QF + (size_t)head * 65536 + tile0 * 1024 + lane * 16), "v"(ws + WS_KM + (size_t)(head * 32 + (lane & 31)) * 4) : "memory");
#pragma unroll
    for (int c = 0; c < 2; ++c) { set_m0(lds0 + c * 16384 + wid * 2048); glds_s<0, true>(kvb + c * 16384, goff); glds_s<1024, true>(kvb + c * 16384, goff); }
    asm volatile("s_waitcnt vmcnt(4)" : "+v"(qa), "+v"(kmu) :: "memory");
    const float km = swap16_max(row16_max(__uint_as_float(kmu)));
    float na = 0.f;
#pragma unroll
    for (int e = 0; e < 4; ++e) na = dot2(qa[e], qa[e], na);
    na = swap32_add(na);
    float ma = __builtin_amdgcn_sqrtf(na * km);
    if (__any(!(ma <= 40.0f))) {
        float xa = -3.0e38f;
        for (int t = 0; t < 64; ++t) {
            const bf16x8 kf = __builtin_bit_cast(bf16x8, ld16_wt(kvb + (t >> 3) * 16384 + (t & 7) * 1024 + lane * 16));
            const f32x16 sa = MFMA(kf, __builtin_bit_cast(bf16x8, qa), f32x16{});
#pragma unroll
            for (int r = 0; r < 16; ++r) xa = fmaxf(xa, sa[r]); }
        ma = swap32_max(xa);
    }
    const float nma = -ma;
    const unsigned kaddr = lds0 + lane * 16;
    const bool ones = (lane & 16) != 0;
    const unsigned vaddr = ones ? lds0 + A_ONES + (lane & 15) * 16 : lds0 + 8192 + (lane >> 5) * 256 + (lane & 15) * 16;
    const unsigned vsel = ones ? 0u : 0xffffffffu;
    goff += 2 * 16384;
    unsigned dbeg = lds0 + wid * 2048, dend = dbeg + 3 * 16384, dslot = dbeg + 2 * 16384, nslot = 0x4000, cnt, m0save;
    unsigned oa0, oa1, oa2, oa3;
    STAMP(3);
    asm volatile(
    "s_mov_b32 %[m0save], m0\n"
    "v_mov_b32 v96, %[qa0]\n"
    "v_mov_b32 v97, %[qa1]\n"
    "v_mov_b32 v98, %[qa2]\n"
    "v_mov_b32 v99, %[qa3]\n"
    "v_mov_b32 v72, %[nma]\n"
    "v_mov_b32 v56, 0\n"
    "v_mov_b32 v73, %[nma]\n"
    "v_mov_b32 v57, 0\n"
    "v_mov_b32 v74, %[nma]\n"
    "v_mov_b32 v58, 0\n"
    "v_mov_b32 v75, %[nma]\n"
    "v_mov_b32 v59, 0\n"
    "v_mov_b32 v76, %[nma]\n"
    "v_mov_b32 v60, 0\n"
    "v_mov_b32 v77, %[nma]\n"
    "v_mov_b32 v61, 0\n"
    "v_mov_b32 v78, %[nma]\n"
    "v_mov_b32 v62, 0\n"
    "v_mov_b32 v79, %[nma]\n"
    "v_mov_b32 v63, 0\n"
    "v_mov_b32 v80, %[nma]\n"
    "v_mov_b32 v64, 0\n"
    "v_mov_b32 v81, %[nma]\n"
    "v_mov_b32 v65, 0\n"
    "v_mov_b32 v82, %[nma]\n"
    "v_mov_b32 v66, 0\n"
    "v_mov_b32 v83, %[nma]\n"
    "v_mov_b32 v67, 0\n"
    "v_mov_b32 v84, %[nma]\n"
    "v_mov_b32 v68, 0\n"
    "v_mov_b32 v85, %[nma]\n"
    "v_mov_b32 v69, 0\n"
    "v_mov_b32 v86, %[nma]\n"
    "v_mov_b32 v70, 0\n"
    "v_mov_b32 v87, %[nma]\n"
    "v_mov_b32 v71, 0\n"
    "v_mov_b32 v120, 0\n"
    "v_mov_b32 v121, 0\n"
    "v_mov_b32 v122, 0\n"
    "v_mov_b32 v123, 0\n"
    "v_mov_b32 v88, 0\n"
    "v_mov_b32 v89, 0\n"
    "v_mov_b32 v90, 0\n"
    "v_mov_b32 v91, 0\n"
    "v_mov_b32 v92, 0\n"
    "v_mov_b32 v93, 0\n"
    "v_mov_b32 v94, 0\n"
    "v_mov_b32 v95, 0\n"
    "v_mov_b32 v124, %[kaddr]\n"
    "v_mov_b32 v125, %[vaddr]\n"
    "v_add_u32 v126, 0x4000, v124\n"
    "v_and_b32 v127, 0x4000, %[vsel]\n"
    "v_add_u32 v127, v127, v125\n"
    "s_waitcnt vmcnt(2) lgkmcnt(0)\n"
    "s_barrier\n"
    "ds_read_b128 v[100:103], v124\n"
    "ds_read_b128 v[104:107], v124 offset:1024\n"
    "ds_read_b128 v[108:111], v125\n"
    "ds_read_b128 v[112:115], v125 offset:512\n"
    "s_mov_b32 %[cnt], 0\n"
    "s_waitcnt lgkmcnt(0)\n"
    "v_mfma_f32_32x32x16_bf16 v[24:39], v[100:103], v[96:99], v[72:87]\n"
    "s_nop 15\n"
    ".Lloop%=:\n"
    "v_exp_f32 v24, v24\n"
    "v_exp_f32 v25, v25\n"
    "v_mfma_f32_32x32x16_bf16 v[56:71], v[120:123], v[92:95], v[56:71]\n"
    "v_exp_f32 v26, v26\n"
    "v_exp_f32 v27, v27\n"
    "v_cvt_pk_bf16_f32 v88, v24, v25\n"
    "v_exp_f32 v28, v28\n"
    "s_waitcnt lgkmcnt(0)\n"
    "v_mfma_f32_32x32x16_bf16 v[40:55], v[104:107], v[96:99], v[72:87]\n"
    "v_exp_f32 v29, v29\n"
    "v_cvt_pk_bf16_f32 v89, v26, v27\n"
    "ds_read_b128 v[100:103], v124 offset:2048\n"
    "v_exp_f32 v30, v30\n"
    "ds_read_b128 v[116:119], v125 offset:1024\n"
    "v_exp_f32 v31, v31\n"
    "v_cvt_pk_bf16_f32 v90, v28, v29\n"
    "ds_read_b128 v[120:123], v125 offset:1536\n"
    "v_exp_f32 v32, v32\n"
    "v_exp_f32 v33, v33\n"
    "v_cvt_pk_bf16_f32 v91, v30, v31\n"
    "v_exp_f32 v34, v34\n"
    "v_exp_f32 v35, v35\n"
    "v_mfma_f32_32x32x16_bf16 v[56:71], v[108:111], v[88:91], v[56:71]\n"
    "v_cvt_pk_bf16_f32 v92, v32, v33\n"
    "v_exp_f32 v36, v36\n"
    "v_exp_f32 v37, v37\n"
    "v_cvt_pk_bf16_f32 v93, v34, v35\n"
    "v_exp_f32 v38, v38\n"
    "v_exp_f32 v39, v39\n"
    "v_cvt_pk_bf16_f32 v94, v36, v37\n"
    "v_cvt_pk_bf16_f32 v95, v38, v39\n"
    "v_exp_f32 v40, v40\n"
    "v_exp_f32 v41, v41\n"
    "v_mfma_f32_32x32x16_bf16 v[56:71], v[112:115], v[92:95], v[56:71]\n"
    "v_exp_f32 v42, v42\n"
    "v_exp_f32 v43, v43\n"
    "v_cvt_pk_bf16_f32 v88, v40, v41\n"
    "v_exp_f32 v44, v44\n"
    "s_waitcnt lgkmcnt(0)\n"
    "v_mfma_f32_32x32x16_bf16 v[24:39], v[100:103], v[96:99], v[72:87]\n"
    "v_exp_f32 v45, v45\n"
    "v_cvt_pk_bf16_f32 v89, v42, v43\n"
    "ds_read_b128 v[104:107], v124 offset:3072\n"
    "v_exp_f32 v46, v46\n"
    "ds_read_b128 v[108:111], v125 offset:2048\n"
    "v_exp_f32 v47, v47\n"
    "v_cvt_pk_bf16_f32 v90, v44, v45\n"
    "ds_read_b128 v[112:115], v125 offset:2560\n"
    "v_exp_f32 v48, v48\n"
    "v_exp_f32 v49, v49\n"
    "v_cvt_pk_bf16_f32 v91, v46, v47\n"
    "v_exp_f32 v50, v50\n"
    "v_exp_f32 v51, v51\n"
    "v_mfma_f32_32x32x16_bf16 v[56:71], v[116:119], v[88:91], v[56:71]\n"
    "v_cvt_pk_bf16_f32 v92, v48, v49\n"
    "v_exp_f32 v52, v52\n"
    "v_exp_f32 v53, v53\n"
    "v_cvt_pk_bf16_f32 v93, v50, v51\n"
    "v_exp_f32 v54, v54\n"
    "v_exp_f32 v55, v55\n"
    "v_cvt_pk_bf16_f32 v94, v52, v53\n"
    "v_cvt_pk_bf16_f32 v95, v54, v55\n"
    "v_exp_f32 v24, v24\n"
    "v_exp_f32 v25, v25\n"
    "v_mfma_f32_32x32x16_bf16 v[56:71], v[120:123], v[92:95], v[56:71]\n"
    "v_exp_f32 v26, v26\n"
    "v_exp_f32 v27, v27\n"
    "v_cvt_pk_bf16_f32 v88, v24, v25\n"
    "v_exp_f32 v28, v28\n"
    "s_waitcnt lgkmcnt(0)\n"
    "v_mfma_f32_32x32x16_bf16 v[40:55], v[104:107], v[96:99], v[72:87]\n"
    "v_exp_f32 v29, v29\n"
    "v_cvt_pk_bf16_f32 v89, v26, v27\n"
    "ds_read_b128 v[100:103], v124 offset:4096\n"
    "v_exp_f32 v30, v30\n"
    "ds_read_b128 v[116:119], v125 offset:3072\n"
    "v_exp_f32 v31, v31\n"
    "v_cvt_pk_bf16_f32 v90, v28, v29\n"
    "ds_read_b128 v[120:123], v125 offset:3584\n"
    "v_exp_f32 v32, v32\n"
    "v_exp_f32 v33, v33\n"
    "v_cvt_pk_bf16_f32 v91, v30, v31\n"
    "v_exp_f32 v34, v34\n"
    "v_exp_f32 v35, v35\n"
    "v_mfma_f32_32x32x16_bf16 v[56:71], v[108:111], v[88:91], v[56:71]\n"
    "v_cvt_pk_bf16_f32 v92, v32, v33\n"
    "v_exp_f32 v36, v36\n"
    "v_exp_f32 v37, v37\n"
    "v_cvt_pk_bf16_f32 v93, v34, v35\n"
    "v_exp_f32 v38, v38\n"
    "v_exp_f32 v39, v39\n"
    "v_cvt_pk_bf16_f32 v94, v36, v37\n"
    "v_cvt_pk_bf16_f32 v95, v38, v39\n"
    "v_exp_f32 v40, v40\n"
    "v_exp_f32 v41, v41\n"
    "v_mfma_f32_32x32x16_bf16 v[56:71], v[112:115], v[92:95], v[56:71]\n"
    "v_exp_f32 v42, v42\n"
    "v_exp_f32 v43, v43\n"
    "v_cvt_pk_bf16_f32 v88, v40, v41\n"
    "v_exp_f32 v44, v44\n"
    "s_waitcnt lgkmcnt(0)\n"
    "v_mfma_f32_32x32x16_bf16 v[24:39], v[100:103], v[96:99], v[72:87]\n"
    "v_exp_f32 v45, v45\n"
    "v_cvt_pk_bf16_f32 v89, v42, v43\n"
    "ds_read_b128 v[104:107], v124 offset:5120\n"
    "v_exp_f32 v46, v46\n"
    "ds_read_b128 v[108:111], v125 offset:4096\n"
    "v_exp_f32 v47, v47\n"
    "v_cvt_pk_bf16_f32 v90, v44, v45\n"
    "ds_read_b128 v[112:115], v125 offset:4608\n"
    "v_exp_f32 v48, v48\n"
    "v_exp_f32 v49, v49\n"
    "v_cvt_pk_bf16_f32 v91, v46, v47\n"
    "v_exp_f32 v50, v50\n"
    "v_exp_f32 v51, v51\n"
    "v_mfma_f32_32x32x16_bf16 v[56:71], v[116:119], v[88:91], v[56:71]\n"
    "v_cvt_pk_bf16_f32 v92, v48, v49\n"
    "v_exp_f32 v52, v52\n"
    "v_exp_f32 v53, v53\n"
    "v_cvt_pk_bf16_f32 v93, v50, v51\n"
    "v_exp_f32 v54, v54\n"
    "v_exp_f32 v55, v55\n"
    "v_cvt_pk_bf16_f32 v94, v52, v53\n"
    "v_cvt_pk_bf16_f32 v95, v54, v55\n"
    "v_exp_f32 v24, v24\n"
    "v_exp_f32 v25, v25\n"
    "v_mfma_f32_32x32x16_bf16 v[56:71], v[120:123], v[92:95], v[56:71]\n"
    "v_exp_f32 v26, v26\n"
    "v_exp_f32 v27, v27\n"
    "v_cvt_pk_bf16_f32 v88, v24, v25\n"
    "v_exp_f32 v28, v28\n"
    "s_waitcnt lgkmcnt(0)\n"
    "v_mfma_f32_32x32x16_bf16 v[40:55], v[104:107], v[96:99], v[72:87]\n"
    "v_exp_f32 v29, v29\n"
    "v_cvt_pk_bf16_f32 v89, v26, v27\n"
    "ds_read_b128 v[100:103], v124 offset:6144\n"
    "v_exp_f32 v30, v30\n"
    "ds_read_b128 v[116:119], v125 offset:5120\n"
    "v_exp_f32 v31, v31\n"
    "v_cvt_pk_bf16_f32 v90, v28, v29\n"
    "ds_read_b128 v[120:123], v125 offset:5632\n"
    "v_exp_f32 v32, v32\n"
    "v_exp_f32 v33, v33\n"
    "v_cvt_pk_bf16_f32 v91, v30, v31\n"
    "v_exp_f32 v34, v34\n"
    "v_exp_f32 v35, v35\n"
    "v_mfma_f32_32x32x16_bf16 v[56:71], v[108:111], v[88:91], v[56:71]\n"
    "v_cvt_pk_bf16_f32 v92, v32, v33\n"
    "v_exp_f32 v36, v36\n"
    "v_exp_f32 v37, v37\n"
    "v_cvt_pk_bf16_f32 v93, v34, v35\n"
    "v_exp_f32 v38, v38\n"
    "v_exp_f32 v39, v39\n"
    "v_cvt_pk_bf16_f32 v94, v36, v37\n"
    "v_cvt_pk_bf16_f32 v95, v38, v39\n"
    "v_exp_f32 v40, v40\n"
    "v_exp_f32 v41, v41\n"
    "v_mfma_f32_32x32x16_bf16 v[56:71], v[112:115], v[92:95], v[56:71]\n"
    "v_exp_f32 v42, v42\n"
    "v_exp_f32 v43, v43\n"
    "v_cvt_pk_bf16_f32 v88, v40, v41\n"
    "v_exp_f32 v44, v44\n"
    "s_waitcnt lgkmcnt(0)\n"
    "v_mfma_f32_32x32x16_bf16 v[24:39], v[100:103], v[96:99], v[72:87]\n"
    "v_exp_f32 v45, v45\n"
    "v_cvt_pk_bf16_f32 v89, v42, v43\n"
    "ds_read_b128 v[104:107], v124 offset:7168\n"
    "v_exp_f32 v46, v46\n"
    "ds_read_b128 v[108:111], v125 offset:6144\n"
    "v_exp_f32 v47, v47\n"
    "v_cvt_pk_bf16_f32 v90, v44, v45\n"
    "ds_read_b128 v[112:115], v125 offset:6656\n"
    "v_exp_f32 v48, v48\n"
    "v_exp_f32 v49, v49\n"
    "v_cvt_pk_bf16_f32 v91, v46, v47\n"
    "v_exp_f32 v50, v50\n"
    "v_exp_f32 v51, v51\n"
    "v_mfma_f32_32x32x16_bf16 v[56:71], v[116:119], v[88:91], v[56:71]\n"
    "v_cvt_pk_bf16_f32 v92, v48, v49\n"
    "v_exp_f32 v52, v52\n"
    "v_exp_f32 v53, v53\n"
    "v_cvt_pk_bf16_f32 v93, v50, v51\n"
    "v_exp_f32 v54, v54\n"
    "v_exp_f32 v55, v55\n"
    "v_cvt_pk_bf16_f32 v94, v52, v53\n"
    "v_cvt_pk_bf16_f32 v95, v54, v55\n"
    "v_exp_f32 v24, v24\n"
    "v_exp_f32 v25, v25\n"
    "v_mfma_f32_32x32x16_bf16 v[56:71], v[120:123], v[92:95], v[56:71]\n"
    "v_exp_f32 v26, v26\n"
    "v_exp_f32 v27, v27\n"
    "v_cvt_pk_bf16_f32 v88, v24, v25\n"
    "v_exp_f32 v28, v28\n"
    "s_waitcnt lgkmcnt(0)\n"
    "v_mfma_f32_32x32x16_bf16 v[40:55], v[104:107], v[96:99], v[72:87]\n"
    "v_exp_f32 v29, v29\n"
    "v_cvt_pk_bf16_f32 v89, v26, v27\n"
    "s_waitcnt vmcnt(0)\n"
    "s_barrier\n"
    "ds_read_b128 v[100:103], v126 offset:0\n"
    "v_exp_f32 v30, v30\n"
    "ds_read_b128 v[116:119], v125 offset:7168\n"
    "v_exp_f32 v31, v31\n"
    "v_cvt_pk_bf16_f32 v90, v28, v29\n"
    "ds_read_b128 v[120:123], v125 offset:7680\n"
    "v_exp_f32 v32, v32\n"
    "v_exp_f32 v33, v33\n"
    "v_cvt_pk_bf16_f32 v91, v30, v31\n"
    "v_exp_f32 v34, v34\n"
    "v_exp_f32 v35, v35\n"
    "v_mfma_f32_32x32x16_bf16 v[56:71], v[108:111], v[88:91], v[56:71]\n"
    "s_cmp_ge_u32 %[cnt], 6\n"
    "s_cbranch_scc1 .Lnodma%=\n"
    "s_mov_b32 m0, %[dslot]\n"
    "s_nop 0\n"
    "global_load_lds_dwordx4 %[goff], %[gbase] offset:0\n"
    "global_load_lds_dwordx4 %[goff], %[gbase] offset:1024\n"
    ".Lnodma%=:\n"
    "v_cvt_pk_bf16_f32 v92, v32, v33\n"
    "v_exp_f32 v36, v36\n"
    "v_exp_f32 v37, v37\n"
    "v_cvt_pk_bf16_f32 v93, v34, v35\n"
    "v_exp_f32 v38, v38\n"
    "v_exp_f32 v39, v39\n"
    "v_cvt_pk_bf16_f32 v94, v36, v37\n"
    "v_cvt_pk_bf16_f32 v95, v38, v39\n"
    "v_exp_f32 v40, v40\n"
    "v_exp_f32 v41, v41\n"
    "v_mfma_f32_32x32x16_bf16 v[56:71], v[112:115], v[92:95], v[56:71]\n"
    "v_exp_f32 v42, v42\n"
    "v_exp_f32 v43, v43\n"
    "v_cvt_pk_bf16_f32 v88, v40, v41\n"
    "v_exp_f32 v44, v44\n"
    "s_waitcnt lgkmcnt(0)\n"
    "v_mfma_f32_32x32x16_bf16 v[24:39], v[100:103], v[96:99], v[72:87]\n"
    "v_exp_f32 v45, v45\n"
    "v_cvt_pk_bf16_f32 v89, v42, v43\n"
    "ds_read_b128 v[104:107], v126 offset:1024\n"
    "v_exp_f32 v46, v46\n"
    "ds_read_b128 v[108:111], v127 offset:0\n"
    "v_exp_f32 v47, v47\n"
    "v_cvt_pk_bf16_f32 v90, v44, v45\n"
    "ds_read_b128 v[112:115], v127 offset:512\n"
    "v_exp_f32 v48, v48\n"
    "v_exp_f32 v49, v49\n"
    "v_cvt_pk_bf16_f32 v91, v46, v47\n"
    "v_exp_f32 v50, v50\n"
    "v_exp_f32 v51, v51\n"
    "v_mfma_f32_32x32x16_bf16 v[56:71], v[116:119], v[88:91], v[56:71]\n"
    "v_cvt_pk_bf16_f32 v92, v48, v49\n"
    "v_exp_f32 v52, v52\n"
    "v_exp_f32 v53, v53\n"
    "v_cvt_pk_bf16_f32 v93, v50, v51\n"
    "v_exp_f32 v54, v54\n"
    "v_exp_f32 v55, v55\n"
    "v_cvt_pk_bf16_f32 v94, v52, v53\n"
    "v_cvt_pk_bf16_f32 v95, v54, v55\n"
    "v_mov_b32 v124, v126\n"
    "v_mov_b32 v125, v127\n"
    "s_add_u32 %[nslot], %[nslot], 0x4000\n"
    "s_cmp_eq_u32 %[nslot], 0xc000\n"
    "s_cselect_b32 %[nslot], 0, %[nslot]\n"
    "v_add_u32 v126, %[nslot], %[kaddr]\n"
    "v_and_b32 v127, %[nslot], %[vsel]\n"
    "v_add_u32 v127, v127, %[vaddr]\n"
    "s_add_u32 %[dslot], %[dslot], 0x4000\n"
    "s_cmp_eq_u32 %[dslot], %[dend]\n"
    "s_cselect_b32 %[dslot], %[dbeg], %[dslot]\n"
    "v_add_u32 %[goff], 0x4000, %[goff]\n"
    "s_add_u32 %[cnt], %[cnt], 1\n"
    "s_cmp_lt_u32 %[cnt], 8\n"
    "s_cbranch_scc1 .Lloop%=\n"
    "s_nop 1\n"
    "v_mfma_f32_32x32x16_bf16 v[56:71], v[120:123], v[92:95], v[56:71]\n"
    "s_nop 15\n"
    "s_nop 3\n"
    "v_rcp_f32 v24, v64\n"
    "s_nop 0\n"
    "v_mul_f32 v56, v56, v24\n"
    "v_mul_f32 v57, v57, v24\n"
    "v_mul_f32 v58, v58, v24\n"
    "v_mul_f32 v59, v59, v24\n"
    "v_mul_f32 v60, v60, v24\n"
    "v_mul_f32 v61, v61, v24\n"
    "v_mul_f32 v62, v62, v24\n"
    "v_mul_f32 v63, v63, v24\n"
    "v_cvt_pk_bf16_f32 %[oa0], v56, v57\n"
    "v_cvt_pk_bf16_f32 %[oa1], v58, v59\n"
    "v_cvt_pk_bf16_f32 %[oa2], v60, v61\n"
    "v_cvt_pk_bf16_f32 %[oa3], v62, v63\n"
    "s_waitcnt lgkmcnt(0)\n"
    "s_mov_b32 m0, %[m0save]\n"
        : [oa0] "=&v"(oa0), [oa1] "=&v"(oa1), [oa2] "=&v"(oa2), [oa3] "=&v"(oa3),
          [goff] "+v"(goff), [dslot] "+s"(dslot), [nslot] "+s"(nslot), [cnt] "=&s"(cnt), [m0save] "=&s"(m0save)
        : [qa0] "v"(qa[0]), [qa1] "v"(qa[1]), [qa2] "v"(qa[2]), [qa3] "v"(qa[3]),
          [nma] "v"(nma), [kaddr] "v"(kaddr), [vaddr] "v"(vaddr), [vsel] "v"(vsel), [gbase] "s"(kvb), [dbeg] "s"(dbeg), [dend] "s"(dend), [psel] "s"(0x07060302u), [vmask] "s"(0x0000ffff0000ffffull), [vone] "v"(((lane & 27) == 16) ? 0x3f803f80u : 0u), [dbgaddr] "s"(lds0 + A_LDS + wid * 64)
        : "memory", "scc", "vcc",
          "v24", "v25", "v26", "v27", "v28", "v29", "v30", "v31", "v32", "v33", "v34", "v35", "v36", "v37", "v38", "v39", "v40", "v41", "v42", "v43", "v44", "v45", "v46", "v47", "v48", "v49", "v50", "v51", "v52", "v53", "v54", "v55", "v56", "v57", "v58", "v59", "v60", "v61", "v62", "v63", "v64", "v65", "v66", "v67", "v68", "v69", "v70", "v71", "v72", "v73", "v74", "v75", "v76", "v77", "v78", "v79", "v80", "v81", "v82", "v83", "v84", "v85", "v86", "v87", "v88", "v89", "v90", "v91", "v92", "v93", "v94", "v95", "v96", "v97", "v98", "v99", "v100", "v101", "v102", "v103", "v104", "v105", "v106", "v107", "v108", "v109", "v110", "v111", "v112", "v113", "v114", "v115", "v116", "v117", "v118", "v119", "v120", "v121", "v122", "v123", "v124", "v125", "v126", "v127"
    );
    STAMP(4);
    st16_wt(ws + WS_OF + ((size_t)tile0 * 32 + head) * 1024 + lane * 16, (u32x4){oa0, oa1, oa2, oa3});
    WAITV(0);
    if (lane == 0) __hip_atomic_store(flagp(ws, FL_O + tile0 * 32 + head), tag, RLX_AGENT);
}

__device__ __forceinline__ void z_role(const uint16_t* __restrict__ x, const uint16_t* __restrict__ win, char* __restrict__ ws, char* lds, int g, u64& tag) {
    const int tid = threadIdx.x, lane = tid & 63, wid = __builtin_amdgcn_readfirstlane(tid >> 6), wr = wid >> 2, wc = wid & 3, l31 = lane & 31, hh = lane >> 5;
    const int xcd = g & 7, i = g >> 3;
    const int rb = 4 * (xcd >> 1) + (i & 3), cbz = 7 * ((xcd & 1) ^ 1) + (i >> 2);
    f32x16 acc[2];
    proj_main<false>(x, win, NIN, rb, cbz * 128, 12 + cbz, ws, lds, ((i & 3) * 4 + (i >> 2) * 3) & 15, tag, acc);
    STAMP(1);
    const float* st = (const float*)(lds + P_ST);
    unsigned zp[2][8];
#pragma unroll
    for (int rt = 0; rt < 2; ++rt) { const int tr = wr * 64 + rt * 32; const float mu = st[tr + l31], rs = st[128 + tr + l31];
#pragma unroll
        for (int q = 0; q < 4; ++q) { const f32x4 c = *(const f32x4*)(st + 256 + wc * 32 + 8 * q + 4 * hh);
            zp[rt][2 * q] = cvtpk(gelu_t((acc[rt][4 * q] - mu * c[0]) * rs), gelu_t((acc[rt][4 * q + 1] - mu * c[1]) * rs));
            zp[rt][2 * q + 1] = cvtpk(gelu_t((acc[rt][4 * q + 2] - mu * c[2]) * rs), gelu_t((acc[rt][4 * q + 3] - mu * c[3]) * rs)); } }
    if (cbz < 7) {
#pragma unroll
        for (int rt = 0; rt < 2; ++rt) { const int tt = rb * 4 + wr * 2 + rt;
            char* dst = ws + WS_Z1 + ((size_t)tt * 28 + cbz * 4 + wc) * 2048 + lane * 32;
            st16_wt(dst, (u32x4){zp[rt][0], zp[rt][1], zp[rt][2], zp[rt][3]}); st16_wt(dst + 16, (u32x4){zp[rt][4], zp[rt][5], zp[rt][6], zp[rt][7]}); }
    } else {
        const int cj = cbz - 7;
        float* pst = (float*)lds;
        const unsigned ONES = 0x3f803f80u;
#pragma unroll
        for (int rt = 0; rt < 2; ++rt) { float s1 = 0.f, s2 = 0.f;
#pragma unroll
            for (int e = 0; e < 8; ++e) { s1 = dot2(zp[rt][e], ONES, s1); s2 = dot2(zp[rt][e], zp[rt][e], s2); }
            s1 = swap32_add(s1); s2 = swap32_add(s2);
            if (hh == 0) *(f32x2*)(pst + (wc * 128 + wr * 64 + rt * 32 + l31) * 2) = (f32x2){s1, s2}; }
        __syncthreads();
        char* gst = ws + WS_ST + (size_t)rb * 7 * 2048;
        float* fin = (float*)(lds + 4096);
        if (tid < 128) { f32x2 s = *(const f32x2*)(pst + tid * 2);
#pragma unroll
            for (int j = 1; j < 4; ++j) s += *(const f32x2*)(pst + (j * 128 + tid) * 2);
            st16_wt(gst + (cj * 128 + tid) * 16, (u32x4){__float_as_uint(s[0]), __float_as_uint(s[1]), (unsigned)tag, (unsigned)(tag >> 32)});
            u32x4 gr[7];
            for (unsigned spins = 0; spins < (1u << 18); ++spins) {
                asm volatile("global_load_dwordx4 %0, %7, off sc1\n\tglobal_load_dwordx4 %1, %7, off offset:2048 sc1\n\tglobal_load_dwordx4 %2, %8, off sc1\n\tglobal_load_dwordx4 %3, %8, off offset:2048 sc1\n\t"
                             "global_load_dwordx4 %4, %9, off sc1\n\tglobal_load_dwordx4 %5, %9, off offset:2048 sc1\n\tglobal_load_dwordx4 %6, %10, off sc1\n\ts_waitcnt vmcnt(0)"
                             : "=&v"(gr[0]), "=&v"(gr[1]), "=&v"(gr[2]), "=&v"(gr[3]), "=&v"(gr[4]), "=&v"(gr[5]), "=&v"(gr[6])
                             : "v"(gst + tid * 16), "v"(gst + 4096 + tid * 16), "v"(gst + 8192 + tid * 16), "v"(gst + 12288 + tid * 16) : "memory");
                bool ok = true;
#pragma unroll
                for (int j = 0; j < 7; ++j) ok &= gr[j][2] == (unsigned)tag && gr[j][3] == (unsigned)(tag >> 32);
                if (__all(ok)) break;
                __builtin_amdgcn_s_sleep(POLL_SLEEP); }
            float s1 = 0.f, s2 = 0.f;
#pragma unroll
            for (int j = 0; j < 7; ++j) { s1 += __uint_as_float(gr[j][0]); s2 += __uint_as_float(gr[j][1]); }
            const float mu = s1 * (1.0f / FH); const float var = fmaxf(s2 * (1.0f / FH) - mu * mu, 0.f);
            fin[tid] = mu; fin[128 + tid] = __builtin_amdgcn_rsqf(var + LN_EPS); }
        __syncthreads();
        char* img = lds + 8192;
#pragma unroll
        for (int rt = 0; rt < 2; ++rt) { const int tok = wr * 64 + rt * 32 + l31; const float mu = fin[tok], rs = fin[128 + tok];
#pragma unroll
            for (int q = 0; q < 4; ++q) { const unsigned a = zp[rt][2 * q], c = zp[rt][2 * q + 1];
                const u32x2 o = {cvtpk((bflo(a) - mu) * rs, (bfhi(a) - mu) * rs), cvtpk((bflo(c) - mu) * rs, (bfhi(c) - mu) * rs)};
                const int f = wc * 32 + 8 * q + 4 * hh;
                *(u32x2*)(img + tok * 256 + (((f >> 3) ^ wsw(tok)) * 16) + (f & 4) * 2) = o; } }
        __syncthreads();
        { const int blk = (lane >> 4) & 1, q = (lane & 15) >> 2, p = lane & 3;
#pragma unroll
          for (int kq = 0; kq < 4; ++kq) { const int ks = wr * 4 + kq; s16x4 t2[2];
#pragma unroll
              for (int t = 0; t < 2; ++t) { const int kr = 16 * ks + 8 * hh + 4 * t + q; const int ch = 4 * wc + 2 * blk + (p >> 1);
                  t2[t] = trread((lds_cptr)img + kr * 256 + ((ch ^ wsw(kr)) * 16) + 8 * (p & 1)); }
              st16_wt(ws + WS_Z2N + ((size_t)(cj * 4 + wc) * 128 + rb * 8 + ks) * 1024 + lane * 16, __builtin_bit_cast(u32x4, cat(t2[0], t2[1]))); } }
    }
    PUBLISH(FL_Z + rb * 14 + cbz);
}

constexpr int S_SLOT = 24576, S_LDS = 3 * S_SLOT;
__device__ __forceinline__ void spatial_role(const uint16_t* __restrict__ wsp, const uint16_t* __restrict__ bsp, char* __restrict__ ws, char* lds, int g, u64 tag) {
    const int tid = threadIdx.x, lane = tid & 63, wid8 = __builtin_amdgcn_readfirstlane(tid >> 6), wid = wid8 & 3, l31 = lane & 31, hh = lane >> 5;
    const int i = g >> 3, tb = 4 * (g & 7) + (i & 3), fb = i >> 2;
    const unsigned lds0 = (unsigned)(uintptr_t)lds;
    STAMP(3);
    if (wid8 >= 4) {
        const int r0 = 16 * wid + (lane >> 3), r1 = r0 + 8;
        const char* sb_ = (const char*)(wsp + (size_t)tb * 64 * SEQ);
        const unsigned vo0 = (r0 * SEQ + ((lane & 7) ^ ((r0 >> 1) & 7)) * 8) * 2, vo1 = (r1 * SEQ + ((lane & 7) ^ ((r1 >> 1) & 7)) * 8) * 2;
        const char* zs = ws + WS_Z2N + (size_t)(fb * 4 + wid) * 131072; const unsigned zvo = lane * 16;
        unsigned ready = 0;
        GAS u64* zf = flagp(ws, FL_Z + (lane & 15) * 14 + 7 + fb);
        auto need = [&](int rbk) {
            for (unsigned spins = 0; !((ready >> rbk) & 1) && spins < (1u << 18); ++spins) {
                ready = (unsigned)__ballot(__hip_atomic_load(zf, RLX_AGENT) == tag) & 0xffffu;
                if (!((ready >> rbk) & 1)) __builtin_amdgcn_s_sleep(POLL_SLEEP); } };
        auto issue = [&](int it, int slot) { const unsigned so = lds0 + slot * S_SLOT;
            need(it >> 1);
            set_m0(so + wid * 2048); glds_s<0, false>(sb_ + it * 128, vo0);
            set_m0(so + wid * 2048 + 1024); glds_s<0, false>(sb_ + it * 128, vo1);
            set_m0(so + 8192 + wid * 4096);
            glds_s<0, true>(zs + it * 4096, zvo); glds_s<1024, true>(zs + it * 4096, zvo); glds_s<2048, true>(zs + it * 4096, zvo); glds_s<3072, true>(zs + it * 4096, zvo); };
        issue(0, 0); issue(1, 1); issue(2, 2);
        WAIT_BAR(12);
        int slot = 0;
#pragma unroll 1
        for (int it = 1; it < 31; ++it) { WAIT_BAR(6); if (it + 2 < 32) issue(it + 2, slot); slot = slot == 2 ? 0 : slot + 1; }
        WAIT_BAR(0);
        BAR();
    } else {
        unsigned boff[2][4];
#pragma unroll
        for (int rt = 0; rt < 2; ++rt)
#pragma unroll
            for (int ks = 0; ks < 4; ++ks) { const int r = rt * 32 + l31; boff[rt][ks] = (r * 128 + ((hh ^ ((r >> 1) & 7)) * 16)) ^ (ks * 32); }
        const unsigned aoff = 8192 + wid * 4096 + lane * 16;
        struct SF { bf16x8 a, b[2]; };
        auto rd = [&](SF& f, int slot, int ks) { lds_cptr sb = (lds_cptr)lds + slot * S_SLOT;
            f.a = ldsfrag(sb + aoff + ks * 1024); f.b[0] = ldsfrag(sb + boff[0][ks]); f.b[1] = ldsfrag(sb + boff[1][ks]); };
        f32x16 acc[2] = {f32x16{}, f32x16{}};
        auto mm = [&](const SF& f) { acc[0] = MFMA(f.a, f.b[0], acc[0]); acc[1] = MFMA(f.a, f.b[1], acc[1]); };
        SF f[4];
        BAR();
#pragma unroll
        for (int ks = 0; ks < 4; ++ks) rd(f[ks], 0, ks);
        auto step = [&](int nsl) {
            LGKM_BAR();
#pragma unroll
            for (int ks = 0; ks < 4; ++ks) { lds_cptr sb = (lds_cptr)lds + nsl * S_SLOT;
                acc[0] = MFMA(f[ks].a, f[ks].b[0], acc[0]); SBAR();
                f[ks].b[0] = ldsfrag(sb + boff[0][ks]); SBAR();
                PACE();
                acc[1] = MFMA(f[ks].a, f[ks].b[1], acc[1]); SBAR();
                f[ks].b[1] = ldsfrag(sb + boff[1][ks]); f[ks].a = ldsfrag(sb + aoff + ks * 1024); SBAR();
                PACE(); } };
#pragma unroll 1
        for (int io = 0; io < 10; ++io) { step(1); step(2); step(0); }
        step(1); step(2);
        wait_flags(flagp(ws, FL_Z + (tb >> 1) * 14 + fb), lane == 0, tag);
#pragma unroll
        for (int rt = 0; rt < 2; ++rt) { const int tt = tb * 2 + rt; const float bias = __uint_as_float((unsigned)bsp[tt * 32 + l31] << 16);
            const char* z1p = ws + WS_Z1 + ((size_t)tt * 28 + fb * 4 + wid) * 2048 + lane * 32;
            const u32x4 za = ld16_wt(z1p), zb = ld16_wt(z1p + 16);
            u32x4 ua, ub;
#pragma unroll
            for (int e = 0; e < 4; ++e) {
                ua[e] = cvtpk(bflo(za[e]) * (acc[rt][2 * e] + bias), bfhi(za[e]) * (acc[rt][2 * e + 1] + bias));
                ub[e] = cvtpk(bflo(zb[e]) * (acc[rt][8 + 2 * e] + bias), bfhi(zb[e]) * (acc[rt][8 + 2 * e + 1] + bias)); }
            char* ud = ws + WS_U + ((size_t)tt * 56 + (fb * 4 + wid) * 2) * 1024 + lane * 16;
            st16_wt(ud, ua); st16_wt(ud + 1024, ub); }
    }
    PUBLISH(FL_U + tb * 7 + fb);
}

constexpr int F_SLOT = 8192 + 8192, F_LDS = 4 * F_SLOT;
__device__ __forceinline__ void final_role(const uint16_t* __restrict__ x, const uint16_t* __restrict__ wo, const uint16_t* __restrict__ wout, char* __restrict__ ws, uint16_t* __restrict__ out, char* lds, int g, u64 tag) {
    const int tb = 4 * (g & 7) + ((g >> 3) & 3), db = g >> 5;
    const int tid = threadIdx.x, lane = tid & 63, wid8 = __builtin_amdgcn_readfirstlane(tid >> 6), wid = wid8 & 3, wr = wid >> 1, wc = wid & 1, l31 = lane & 31, hh = lane >> 5;
    const unsigned lds0 = (unsigned)(uintptr_t)lds;
    auto wswz = [](int r) { return ((r & 2) << 1) | ((r >> 2) & 3); };
    f32x16 as = f32x16{}, ag = f32x16{};
    const int k0 = 16 * wid + (lane >> 3), k1 = k0 + 8;
    const unsigned wo0 = (k0 * DM + db * 64 + ((lane & 7) ^ wswz(k0)) * 8) * 2, wo1 = (k1 * DM + db * 64 + ((lane & 7) ^ wswz(k1)) * 8) * 2;
    const int att = tb * 2 + (wid & 1), aks = 2 * (wid >> 1);
    unsigned woff[2];
    { const int blk = (lane >> 4) & 1, q = (lane & 15) >> 2, p = lane & 3; const int kr = 4 * hh + q; const int ch = 4 * wc + 2 * blk + (p >> 1);
      woff[0] = kr * 128 + ((ch ^ wswz(kr)) * 16) + 8 * (p & 1); woff[1] = (woff[0] + 1024) ^ 32; }
    const unsigned aoff = 8192 + wr * 4096 + lane * 16;
    struct FF { bf16x8 w, a; };
    auto rd = [&](FF& f, int slot, int ks) { lds_cptr sb = (lds_cptr)lds + slot * F_SLOT;
        f.w = cat(trread(sb + woff[0] + ks * 2048), trread(sb + woff[1] + ks * 2048)); f.a = ldsfrag(sb + aoff + ks * 1024); };
    auto pass = [&](const uint16_t* wb, const char* ab, int nfr, int n, f32x16& acc, bool pace) {
        if (wid8 >= 4) {
            auto issue = [&](int it) { const unsigned so = lds0 + (it & 3) * F_SLOT; const char* w = (const char*)(wb + (size_t)it * 64 * DM);
                set_m0(so + wid * 2048); glds_s<0, false>(w, wo0);
                set_m0(so + wid * 2048 + 1024); glds_s<0, false>(w, wo1);
                const char* a = ab + ((size_t)att * nfr + it * 4 + aks) * 1024;
                set_m0(so + 8192 + (wid & 1) * 4096 + aks * 1024); glds_s<0, true>(a, lane * 16); glds_s<1024, true>(a, lane * 16); };
            issue(0); issue(1); issue(2); issue(3);
            WAIT_BAR(12);
#pragma unroll 1
            for (int i = 1; i < n - 2; ++i) { WAIT_BAR(8); if (i + 3 < n) issue(i + 3); }
            WAIT_BAR(4);
            WAIT_BAR(0);
            BAR();
        } else {
            FF fa[4], fb[4];
            BAR();
#pragma unroll
            for (int ks = 0; ks < 4; ++ks) rd(fa[ks], 0, ks);
            auto step = [&](int nsl, FF (&cur)[4], FF (&nxt)[4]) {
                LGKM_BAR();
#pragma unroll
                for (int ks = 0; ks < 4; ++ks) { acc = MFMA(cur[ks].w, cur[ks].a, acc); SBAR(); rd(nxt[ks], nsl, ks); SBAR(); if (pace) PACE(); } };
#pragma unroll 1
            for (int it = 0; it < n; it += 2) { step((it + 1) & 3, fa, fb); step((it + 2) & 3, fb, fa); }
        } };
    if (wid8 == 0) wait_flags(flagp(ws, FL_U + tb * 7 + lane), lane < 7, tag);
    __syncthreads();
    STAMP(5);
    pass(wout, ws + WS_U, 56, 14, ag, true);
    STAMP(6);
    __syncthreads();
#pragma unroll
    for (int q = 0; q < 8; ++q) {
        const int kr = 8 * wid8 + (lane >> 3);
        set_m0(lds0 + q * 8192 + wid8 * 1024);
        glds_s<0, false>((const char*)(wo + (size_t)q * 64 * DM), (kr * DM + db * 64 + ((lane & 7) ^ wswz(kr)) * 8) * 2); }
    const int tok = tb * 64 + wr * 32 + l31;
    float gate[16]; u32x2 xv[4];
    if (wid8 < 4) {
#pragma unroll
        for (int q = 0; q < 4; ++q) xv[q] = *(const u32x2*)(x + (size_t)tok * DM + db * 64 + wc * 32 + 8 * q + 4 * hh);
#pragma unroll
        for (int r = 0; r < 16; ++r) gate[r] = silu_f(ag[r]); }
    if (wid8 == 0) wait_flags(flagp(ws, FL_O + tb * 64 + lane), true, tag);
    WAITV(0);
    __syncthreads();
    STAMP(7);
    const int kh = wid8 >> 2;
    u32x4 of[16];
    { const char* ob = ws + WS_OF + ((size_t)(tb * 2 + wr) * 32 + 16 * kh) * 1024; const unsigned v0 = lane * 16, v1 = v0 + 4096, v2 = v0 + 8192, v3 = v0 + 12288;
      asm volatile(
        "global_load_dwordx4 %0, %16, %20 sc1\n\tglobal_load_dwordx4 %1, %16, %20 offset:1024 sc1\n\tglobal_load_dwordx4 %2, %16, %20 offset:2048 sc1\n\tglobal_load_dwordx4 %3, %16, %20 offset:3072 sc1\n\t"
        "global_load_dwordx4 %4, %17, %20 sc1\n\tglobal_load_dwordx4 %5, %17, %20 offset:1024 sc1\n\tglobal_load_dwordx4 %6, %17, %20 offset:2048 sc1\n\tglobal_load_dwordx4 %7, %17, %20 offset:3072 sc1\n\t"
        "global_load_dwordx4 %8, %18, %20 sc1\n\tglobal_load_dwordx4 %9, %18, %20 offset:1024 sc1\n\tglobal_load_dwordx4 %10, %18, %20 offset:2048 sc1\n\tglobal_load_dwordx4 %11, %18, %20 offset:3072 sc1\n\t"
        "global_load_dwordx4 %12, %19, %20 sc1\n\tglobal_load_dwordx4 %13, %19, %20 offset:1024 sc1\n\tglobal_load_dwordx4 %14, %19, %20 offset:2048 sc1\n\tglobal_load_dwordx4 %15, %19, %20 offset:3072 sc1\n\t"
        "s_waitcnt vmcnt(0)"
        : "=&v"(of[0]), "=&v"(of[1]), "=&v"(of[2]), "=&v"(of[3]), "=&v"(of[4]), "=&v"(of[5]), "=&v"(of[6]), "=&v"(of[7]),
          "=&v"(of[8]), "=&v"(of[9]), "=&v"(of[10]), "=&v"(of[11]), "=&v"(of[12]), "=&v"(of[13]), "=&v"(of[14]), "=&v"(of[15])
        : "v"(v0), "v"(v1), "v"(v2), "v"(v3), "s"(ob) : "memory"); }
    { lds_cptr wb_ = (lds_cptr)lds + kh * 32768;
#pragma unroll
      for (int ks = 0; ks < 16; ++ks)
          as = MFMA(cat(trread(wb_ + woff[0] + ks * 2048), trread(wb_ + woff[1] + ks * 2048)), __builtin_bit_cast(bf16x8, of[ks]), as); }
    __syncthreads();
    if (kh == 1) {
#pragma unroll
        for (int q = 0; q < 4; ++q) *(f32x4*)(lds + (wid * 4 + q) * 1024 + lane * 16) = (f32x4){as[4 * q], as[4 * q + 1], as[4 * q + 2], as[4 * q + 3]}; }
    __syncthreads();
    STAMP(8);
    if (kh == 0) {
#pragma unroll
        for (int q = 0; q < 4; ++q) { const f32x4 p = *(const f32x4*)(lds + (wid * 4 + q) * 1024 + lane * 16);
            const float r0 = bflo(xv[q][0]) + (as[4 * q] + p[0]) * gate[4 * q], r1 = bfhi(xv[q][0]) + (as[4 * q + 1] + p[1]) * gate[4 * q + 1];
            const float r2 = bflo(xv[q][1]) + (as[4 * q + 2] + p[2]) * gate[4 * q + 2], r3 = bfhi(xv[q][1]) + (as[4 * q + 3] + p[3]) * gate[4 * q + 3];
            *(u32x2*)(out + (size_t)tok * DM + db * 64 + wc * 32 + 8 * q + 4 * hh) = (u32x2){cvtpk(r0, r1), cvtpk(r2, r3)}; }
    }
}

#define ADELAY 300
#define APRIO 3
#define GDELAY 0
#define GPRIO 2
constexpr int ALL_LDS = S_LDS;
__global__ __launch_bounds__(512, 4) void k_all(const uint16_t* __restrict__ x, const uint16_t* __restrict__ wqkv, const uint16_t* __restrict__ wo, const uint16_t* __restrict__ win,
                                                const uint16_t* __restrict__ wsp, const uint16_t* __restrict__ bsp, const uint16_t* __restrict__ wout, char* __restrict__ ws, uint16_t* __restrict__ out) {
    extern __shared__ __attribute__((aligned(16))) char lds[];
    const int b = blockIdx.x;
    u64 tag;
    STAMP(0);
    if (b < 256) {
        __builtin_amdgcn_s_setprio(APRIO);
        if (ADELAY > 0 && b < 192) {
            const u64 t0 = __builtin_amdgcn_s_memrealtime();
            const int i_ = b >> 3, cb_ = 6 * (b & 1) + (i_ >> 2), line = (i_ & 3) * 256 + (threadIdx.x & 255);
            unsigned junk = 0;
            while (__builtin_amdgcn_s_memrealtime() - t0 < ADELAY) __builtin_amdgcn_s_sleep(8);
            asm volatile("s_waitcnt vmcnt(0)" : "+v"(junk) :: "memory"); }
        if (b < 192) qkv_role(x, wqkv, ws, lds, b, tag); else stats_role(x, wqkv, win, ws, lds, b - 192, tag);
        __builtin_amdgcn_s_setprio(0);
        __syncthreads();
        attn_role(ws, lds, b, tag);
        STAMP(5);
    } else {
        const int g = b - 256;
        __builtin_amdgcn_s_setprio(GPRIO);
            if (GDELAY > 0) { const u64 t0 = __builtin_amdgcn_s_memrealtime(); while (__builtin_amdgcn_s_memrealtime() - t0 < GDELAY) __builtin_amdgcn_s_sleep(8); }
        if (g < 224) { z_role(x, win, ws, lds, g, tag); STAMP(2); __syncthreads(); spatial_role(wsp, bsp, ws, lds, g, tag); STAMP(4); __syncthreads(); }
        else { u64 ep; EPOCH_REQ(ep);
            { const int wv = (g - 224) * 8 + (threadIdx.x >> 6), lane = threadIdx.x & 63; unsigned junk = 0;
#pragma unroll
              for (int q = 0; q < 4; ++q) asm volatile("global_load_dword %0, %1, off" : "+v"(junk) : "v"((const char*)wsp + ((size_t)(q * 256 + wv) * 64 + lane) * 128) : "memory");
              if (wv < 112) asm volatile("global_load_dword %0, %1, off" : "+v"(junk) : "v"((const char*)wout + ((size_t)wv * 64 + lane) * 128) : "memory");
              else if (wv < 176) asm volatile("global_load_dword %0, %1, off" : "+v"(junk) : "v"((const char*)wo + ((size_t)(wv - 112) * 64 + lane) * 128) : "memory");
              { const int t = ((g - 224) >> 3) * 512 + threadIdx.x;
                asm volatile("global_load_dword %0, %1, off" : "+v"(junk) : "v"((const char*)wsp + (size_t)t * 4096) : "memory");
                if (t < 224) asm volatile("global_load_dword %0, %1, off" : "+v"(junk) : "v"((const char*)wout + (size_t)t * 4096) : "memory");
                else if (t < 352) asm volatile("global_load_dword %0, %1, off" : "+v"(junk) : "v"((const char*)wo + (size_t)(t - 224) * 4096) : "memory");
                else if (t < 864) asm volatile("global_load_dword %0, %1, off" : "+v"(junk) : "v"((const char*)out + (size_t)(t - 352) * 4096) : "memory"); }
              asm volatile("s_waitcnt vmcnt(0)" : "+v"(junk) :: "memory"); }
            asm volatile("s_waitcnt vmcnt(0)" : "+v"(ep) :: "memory"); tag = EPOCH_TAG(ep); }
        if (threadIdx.x == 0) __hip_atomic_store(flagp(ws, FL_G + g), tag, RLX_AGENT);
        final_role(x, wo, wout, ws, out, lds, g, tag);
        if (g == 255 && threadIdx.x < 64) {
            const int lane = threadIdx.x;
#pragma unroll 1
            for (int c = 0; c < 4; ++c) { wait_flags(flagp(ws, FL_O + (c * 2 + (lane >> 5)) * 256 + (lane & 31)), true, tag); wait_flags(flagp(ws, FL_G + c * 64 + lane), true, tag); }
            if (lane == 0) __hip_atomic_store(flagp(ws, FL_EPOCH), tag, RLX_AGENT);
        }
        STAMP(9);
    }
}

extern "C" void kernel_launch(void* const* d_in, const int* in_sizes, int n_in, void* d_out, int out_size, void* d_ws, size_t ws_size, hipStream_t stream) {
    const uint16_t* x = (const uint16_t*)d_in[0]; const uint16_t* wqkv = (const uint16_t*)d_in[1]; const uint16_t* wo = (const uint16_t*)d_in[2];
    const uint16_t* win = (const uint16_t*)d_in[4]; const uint16_t* wsp = (const uint16_t*)d_in[5]; const uint16_t* bsp = (const uint16_t*)d_in[6]; const uint16_t* wout = (const uint16_t*)d_in[7];
    (void)hipFuncSetAttribute((const void*)k_all, hipFuncAttributeMaxDynamicSharedMemorySize, ALL_LDS);
    char* wsb = (char*)d_ws;
    k_all<<<512, 512, ALL_LDS, stream>>>(x, wqkv, wo, win, wsp, bsp, wout, wsb, (uint16_t*)d_out);
}
```

```cpp
#include <hip/hip_runtime.h>
#include <hip/hip_bf16.h>
#include <stdint.h>
#define ADELAY 0
#define WARM_IN 1
#define CODE_LINES 320
#define WDELAY 500
#include <stdio.h>

typedef short bf16x8 __attribute__((ext_vector_type(8)));
typedef short s16x4 __attribute__((ext_vector_type(4)));
typedef __bf16 bf16x2_t __attribute__((ext_vector_type(2)));
typedef float f32x16 __attribute__((ext_vector_type(16)));
typedef float f32x4 __attribute__((ext_vector_type(4)));
typedef float f32x2 __attribute__((ext_vector_type(2)));
typedef unsigned u32x4 __attribute__((ext_vector_type(4)));
typedef unsigned u32x2 __attribute__((ext_vector_type(2)));
typedef unsigned long long u64;
#define GAS __attribute__((address_space(1)))
#define LAS __attribute__((address_space(3)))
typedef LAS const char* lds_cptr;
typedef short v4i16_t __attribute__((ext_vector_type(4)));
#define RLX_AGENT __ATOMIC_RELAXED, __HIP_MEMORY_SCOPE_AGENT

constexpr int SEQ = 2048, DM = 512, NQKV = 1536, NIN = 1792, FH = 896;
constexpr float QSCALE = 0.25f * 1.4426950408889634f;
constexpr float LN_EPS = 1e-5f;
constexpr size_t MB = 1024 * 1024;
constexpr size_t WS_QF = 0, WS_KV = 2 * MB, WS_Z1 = 6 * MB, WS_Z2N = 10 * MB, WS_U = 14 * MB, WS_OF = 18 * MB;
constexpr size_t WS_KM = 20 * MB, WS_ST = 20 * MB + 65536, WS_RS = 20 * MB + 65536 + 229376, WS_CS = 20 * MB + 65536 + 245760  , WS_FLAG = 21 * MB;
constexpr int FL_RS = 0, FL_CS = 64, FL_QKV = 128, FL_ST = 320, FL_Z = 432, FL_U = 656, FL_G = 880, FL_EPOCH = 1136, FL_O = 1152;

#define MFMA(a, b, c) __builtin_amdgcn_mfma_f32_32x32x16_bf16(a, b, c, 0, 0, 0)
#define SBAR() __builtin_amdgcn_sched_barrier(0)
__device__ __forceinline__ unsigned cvtpk(float lo, float hi) { unsigned r; asm("v_cvt_pk_bf16_f32 %0, %1, %2" : "=v"(r) : "v"(lo), "v"(hi)); return r; }
__device__ __forceinline__ float bflo(unsigned u) { return __uint_as_float(u << 16); }
__device__ __forceinline__ float bfhi(unsigned u) { return __uint_as_float(u & 0xffff0000u); }
__device__ __forceinline__ float dot2(unsigned a, unsigned b, float acc) {
    return __builtin_amdgcn_fdot2_f32_bf16(__builtin_bit_cast(bf16x2_t, a), __builtin_bit_cast(bf16x2_t, b), acc, false); }
__device__ __forceinline__ void glds16(const void* g, unsigned lds_base) {
    asm volatile("s_mov_b32 m0, %1\n\ts_nop 0\n\tglobal_load_lds_dwordx4 %0, off" :: "v"(g), "s"(lds_base) : "memory", "m0"); }
template <int IMM, bool WT> __device__ __forceinline__ void glds_s(const void* sbase, unsigned voff) {
    if (WT) asm volatile("global_load_lds_dwordx4 %0, %1 offset:%2 sc1" :: "v"(voff), "s"(sbase), "n"(IMM) : "memory");
    else asm volatile("global_load_lds_dwordx4 %0, %1 offset:%2" :: "v"(voff), "s"(sbase), "n"(IMM) : "memory"); }
__device__ __forceinline__ void set_m0(unsigned v) { asm volatile("s_mov_b32 m0, %0\n\ts_nop 0" :: "s"(v) : "memory", "m0"); }
__device__ __forceinline__ s16x4 trread(lds_cptr p) { return __builtin_bit_cast(s16x4, __builtin_amdgcn_ds_read_tr16_b64_v4i16((LAS v4i16_t*)p)); }
__device__ __forceinline__ bf16x8 ldsfrag(lds_cptr p) { return *(LAS const bf16x8*)p; }
__device__ __forceinline__ bf16x8 cat(s16x4 a, s16x4 b) { return (bf16x8){a[0], a[1], a[2], a[3], b[0], b[1], b[2], b[3]}; }
__device__ __forceinline__ float swap32_add(float v) { auto rr = __builtin_amdgcn_permlane32_swap(__float_as_uint(v), __float_as_uint(v), false, false); return __uint_as_float(rr[0]) + __uint_as_float(rr[1]); }
__device__ __forceinline__ float swap32_max(float v) { auto rr = __builtin_amdgcn_permlane32_swap(__float_as_uint(v), __float_as_uint(v), false, false); return fmaxf(__uint_as_float(rr[0]), __uint_as_float(rr[1])); }
template <int CTRL> __device__ __forceinline__ float dppf(float v) { return __int_as_float(__builtin_amdgcn_update_dpp(0, __float_as_int(v), CTRL, 0xf, 0xf, false)); }
__device__ __forceinline__ float row16_add(float v) { v += dppf<0x128>(v); v += dppf<0x124>(v); v += dppf<0x122>(v); v += dppf<0x121>(v); return v; }
__device__ __forceinline__ float row16_max(float v) { v = fmaxf(v, dppf<0x128>(v)); v = fmaxf(v, dppf<0x124>(v)); v = fmaxf(v, dppf<0x122>(v)); v = fmaxf(v, dppf<0x121>(v)); return v; }
__device__ __forceinline__ float swap16_max(float v) { auto rr = __builtin_amdgcn_permlane16_swap(__float_as_uint(v), __float_as_uint(v), false, false); return fmaxf(__uint_as_float(rr[0]), __uint_as_float(rr[1])); }
__device__ __forceinline__ float gelu_t(float x) {
    const float c1 = -2.3022082f, c2 = -0.10294324f;
    float e = __builtin_amdgcn_exp2f(x * __builtin_fmaf(x * x, c2, c1));
    return x * __builtin_amdgcn_rcpf(1.0f + e); }
__device__ __forceinline__ float silu_f(float x) { return x * __builtin_amdgcn_rcpf(1.0f + __builtin_amdgcn_exp2f(-1.4426950408889634f * x)); }
#define STAMP(slot) do {} while (0)
#define PF_DIST 9
#define PACE_N 2
#define PACE() do { asm volatile("s_nop %0" :: "n"(PACE_N)); SBAR(); } while (0)
#define POLL_SLEEP 2
#define WAITV(n) asm volatile("s_waitcnt vmcnt(" #n ")" ::: "memory")
#define WAIT_BAR(n) asm volatile("s_waitcnt vmcnt(" #n ")\n\ts_barrier" ::: "memory")
#define BAR() asm volatile("s_barrier" ::: "memory")
#define LGKM_BAR() do { __builtin_amdgcn_s_waitcnt(0xC07F); asm volatile("" ::: "memory"); __builtin_amdgcn_s_barrier(); asm volatile("" ::: "memory"); } while (0)
__device__ __forceinline__ unsigned wsw(unsigned row) { return ((row & 3) << 2) | ((row >> 2) & 3); }
__device__ __forceinline__ void st16_wt(void* p, u32x4 v) {
    asm volatile("global_store_dwordx4 %0, %1, off sc1\n\ts_nop 1" :: "v"(p), "v"(v)); }
__device__ __forceinline__ void st8_wt(void* p, u64 v) { __hip_atomic_store((GAS u64*)p, v, RLX_AGENT); }
__device__ __forceinline__ u64 ld8_wt(const void* p) { return __hip_atomic_load((GAS u64*)p, RLX_AGENT); }
__device__ __forceinline__ u32x4 ld16_wt(const void* p) { u32x4 v; asm volatile("global_load_dwordx4 %0, %1, off sc1\n\ts_waitcnt vmcnt(0)" : "=&v"(v) : "v"(p) : "memory"); return v; }
__device__ __forceinline__ void glds16_wt(const void* g, unsigned lds_base) {
    asm volatile("s_mov_b32 m0, %1\n\ts_nop 0\n\tglobal_load_lds_dwordx4 %0, off sc1" :: "v"(g), "s"(lds_base) : "memory", "m0"); }
__device__ __forceinline__ GAS u64* flagp(char* ws, int idx) { return (GAS u64*)(ws + WS_FLAG) + idx * 16; }
__device__ __forceinline__ void wait_flags(GAS u64* f, bool want, u64 tag) {
    for (unsigned spins = 0; spins < (1u << 20); ++spins) {
        const u64 v = want ? __hip_atomic_load(f, RLX_AGENT) : tag;
        if (__all(v == tag)) return;
        __builtin_amdgcn_s_sleep(POLL_SLEEP); } }
#define ACQUIRE() do { __builtin_amdgcn_fence(__ATOMIC_ACQUIRE, "agent"); asm volatile("s_waitcnt vmcnt(0)" ::: "memory"); } while (0)
#define EPOCH_REQ(ep) asm volatile("global_load_dwordx2 %0, %1, off sc1" : "=v"(ep) : "v"(flagp(ws, FL_EPOCH)) : "memory")
#define EPOCH_TAG(ep) ((((u64)(unsigned)__builtin_amdgcn_readfirstlane((unsigned)((ep) >> 32)) << 32) | (unsigned)__builtin_amdgcn_readfirstlane((unsigned)(ep))) + 1)
#define PUBLISH(idx) do { WAITV(0); __syncthreads(); if (threadIdx.x == 0) __hip_atomic_store(flagp(ws, idx), tag, RLX_AGENT); } while (0)

__device__ __forceinline__ void stats_role(const uint16_t* __restrict__ x, const uint16_t* __restrict__ wqkv, const uint16_t* __restrict__ win, char* __restrict__ ws, char* lds, int j, u64& tag) {
    u64 ep; EPOCH_REQ(ep);
    const int tid = threadIdx.x, lane = tid & 63, wid = tid >> 6, rr = lane >> 4, cc = lane & 15;
    const unsigned ONES = 0x3f803f80u;
    u32x4 xv[4], w[8];
    const int row = 32 * j + 4 * wid + rr;
#pragma unroll
    for (int i = 0; i < 4; ++i) xv[i] = *(const u32x4*)(x + (size_t)row * DM + i * 128 + cc * 8);
    const int cbk = j >> 1;
    if (j < 52) {
        const uint16_t* W = cbk < 12 ? wqkv + cbk * 128 : win + (cbk - 12) * 128; const int ldw = cbk < 12 ? NQKV : NIN;
        const uint16_t* p = W + (size_t)((j & 1) * 256 + 32 * wid + rr) * ldw + cc * 8;
#pragma unroll
        for (int i = 0; i < 8; ++i) w[i] = *(const u32x4*)(p + (size_t)(i * 4) * ldw);
    }
    asm volatile("s_waitcnt vmcnt(0)" : "+v"(ep) :: "memory");
    tag = EPOCH_TAG(ep);
    float s1 = 0.f, s2 = 0.f;
#pragma unroll
    for (int i = 0; i < 4; ++i)
#pragma unroll
        for (int e = 0; e < 4; ++e) { s1 = dot2(xv[i][e], ONES, s1); s2 = dot2(xv[i][e], xv[i][e], s2); }
    s1 = row16_add(s1); s2 = row16_add(s2);
    { const float mu = s1 * (1.0f / DM), var = fmaxf(s2 * (1.0f / DM) - mu * mu, 0.f);
      if (cc == 0) st8_wt(ws + WS_RS + (size_t)row * 8, __builtin_bit_cast(u64, (f32x2){mu, __builtin_amdgcn_rsqf(var + LN_EPS)})); }
    if (j < 52) {
        float cs[8] = {0.f, 0.f, 0.f, 0.f, 0.f, 0.f, 0.f, 0.f};
        unsigned sel_lo = 0x00003f80u, sel_hi = 0x3f800000u;
        asm volatile("" : "+v"(sel_lo), "+v"(sel_hi));
#pragma unroll
        for (int i = 0; i < 8; ++i)
#pragma unroll
            for (int e = 0; e < 4; ++e) { cs[2 * e] = dot2(w[i][e], sel_lo, cs[2 * e]); cs[2 * e + 1] = dot2(w[i][e], sel_hi, cs[2 * e + 1]); }
        float* red = (float*)lds;
        *(f32x4*)(red + (wid * 4 + rr) * 128 + cc * 8) = (f32x4){cs[0], cs[1], cs[2], cs[3]}; *(f32x4*)(red + (wid * 4 + rr) * 128 + cc * 8 + 4) = (f32x4){cs[4], cs[5], cs[6], cs[7]};
        __syncthreads();
        if (tid < 64) { f32x2 a = {0.f, 0.f};
#pragma unroll
            for (int q = 0; q < 32; ++q) a += *(const f32x2*)(red + q * 128 + 2 * tid);
            st8_wt(ws + WS_CS + (size_t)((j & 1) * 3328 + cbk * 128 + 2 * tid) * 4, __builtin_bit_cast(u64, a)); }
    }
    STAMP(11);
    WAITV(0); __syncthreads();
    STAMP(1);
    if (tid == 0) { __hip_atomic_store(flagp(ws, FL_RS + j), tag, RLX_AGENT); if (j < 52) __hip_atomic_store(flagp(ws, FL_CS + j), tag, RLX_AGENT); }
}

constexpr int P_SLOT = 16384, P_ST = 4 * P_SLOT, P_LDS = 4 * P_SLOT + 4096;
struct PFrag { bf16x8 x[2], w; };
template <bool SWAP>
__device__ __forceinline__ void proj_main(const uint16_t* __restrict__ x, const uint16_t* __restrict__ W, int ldw, int rb, int col0, int cblk, char* __restrict__ ws, char* lds, int rot, u64& tag, f32x16 (&acc)[2]) {
    u64 ep; EPOCH_REQ(ep);
    const int tid = threadIdx.x, lane = tid & 63, wid = __builtin_amdgcn_readfirstlane(tid >> 6), wr = wid >> 2, wc = wid & 3, l31 = lane & 31, hh = lane >> 5;
    const unsigned lds0 = (unsigned)(uintptr_t)lds;
    const int xr = 16 * wid + (lane >> 2), wk = 4 * wid + (lane >> 4);
    const char* xb = (const char*)(x + (size_t)rb * 128 * DM); const char* wb = (const char*)(W + col0);
    const unsigned xvo = (xr * DM + ((lane & 3) ^ ((xr >> 2) & 3)) * 8) * 2, wvo = (wk * ldw + ((lane & 15) ^ wsw(wk)) * 8) * 2;
    const unsigned xd = lds0 + wid * 1024, wd = lds0 + 8192 + wid * 1024;
    auto issue1 = [&](int it, int which) { const unsigned so = (it & 3) * P_SLOT; const int kk = (it + rot) & 15;
        if (which == 0) { set_m0(xd + so); glds_s<0, false>(xb + kk * 64, xvo); } else { set_m0(wd + so); glds_s<0, false>(wb + (size_t)kk * 64 * ldw, wvo); } SBAR(); };
#pragma unroll
    for (int it = 0; it < 4; ++it) { issue1(it, 0); issue1(it, 1); }
    unsigned xoff[2][2], woff[2][2];
    const int blk = (lane >> 4) & 1, q = (lane & 15) >> 2, p = lane & 3;
#pragma unroll
    for (int ks = 0; ks < 2; ++ks) {
#pragma unroll
        for (int rt = 0; rt < 2; ++rt) { const int r = wr * 64 + rt * 32 + l31; xoff[rt][ks] = r * 64 + (((2 * ks + hh) ^ ((r >> 2) & 3)) * 16); }
        const int kr = 8 * hh + q, ch = 4 * wc + 2 * blk + (p >> 1);
        woff[0][ks] = 8192 + ks * 4096 + kr * 256 + ((ch ^ wsw(kr)) * 16) + 8 * (p & 1); woff[1][ks] = (woff[0][ks] + 1024) ^ 16; }
    auto rd = [&](PFrag& f, int slot, int j) { lds_cptr sb = (lds_cptr)lds + slot * P_SLOT;
        f.x[0] = ldsfrag(sb + xoff[0][j]); f.x[1] = ldsfrag(sb + xoff[1][j]); f.w = cat(trread(sb + woff[0][j]), trread(sb + woff[1][j])); };
    auto mm = [&](const PFrag& f) {
#pragma unroll
        for (int rt = 0; rt < 2; ++rt) acc[rt] = SWAP ? MFMA(f.x[rt], f.w, acc[rt]) : MFMA(f.w, f.x[rt], acc[rt]); };
    acc[0] = f32x16{}; acc[1] = f32x16{};
    PFrag fa[2], fb[2];
    asm volatile("s_waitcnt vmcnt(6)\n\ts_barrier" : "+v"(ep) :: "memory");
    tag = EPOCH_TAG(ep);
    STAMP(10);
    rd(fa[0], 0, 0); rd(fa[1], 0, 1);
    auto mm1 = [&](const PFrag& f, int rt) { acc[rt] = SWAP ? MFMA(f.x[rt], f.w, acc[rt]) : MFMA(f.w, f.x[rt], acc[rt]); SBAR(); };
    auto step = [&](int it, int sl, PFrag (&cur)[2], PFrag (&nxt)[2]) {
        __builtin_amdgcn_s_waitcnt(0xC07F);
        if (it < 13) WAITV(4); else if (it == 13) WAITV(2); else WAITV(0);
        __builtin_amdgcn_s_barrier(); asm volatile("" ::: "memory");
        lds_cptr sb = (lds_cptr)lds + ((sl + 1) & 3) * P_SLOT;
        mm1(cur[0], 0);
        nxt[0].x[0] = ldsfrag(sb + xoff[0][0]); nxt[0].x[1] = ldsfrag(sb + xoff[1][0]); SBAR();
        mm1(cur[0], 1);
        nxt[0].w = cat(trread(sb + woff[0][0]), trread(sb + woff[1][0])); SBAR();
        if (it + 4 < 16) issue1(it + 4, 0);
        mm1(cur[1], 0);
        nxt[1].x[0] = ldsfrag(sb + xoff[0][1]); nxt[1].x[1] = ldsfrag(sb + xoff[1][1]); SBAR();
        mm1(cur[1], 1);
        nxt[1].w = cat(trread(sb + woff[0][1]), trread(sb + woff[1][1])); SBAR();
        if (it + 4 < 16) issue1(it + 4, 1); };
#pragma unroll 1
    for (int io = 0; io < 2; ++io) { step(io * 4, 0, fa, fb); step(io * 4 + 1, 1, fb, fa); step(io * 4 + 2, 2, fa, fb); step(io * 4 + 3, 3, fb, fa); }
    const char* fa_ = (const char*)(lane < 4 ? flagp(ws, FL_RS + rb * 4 + lane) : flagp(ws, FL_CS + 2 * cblk + (lane & 1)));
    const char* sa_ = tid < 128 ? ws + WS_RS + (size_t)(rb * 128 + tid) * 8 : ws + WS_CS + (size_t)(cblk * 128 + 2 * (tid & 63)) * 4;
    const char* sb_ = ws + WS_CS + (size_t)(3328 + cblk * 128 + 2 * (tid & 63)) * 4;
    u64 fl, stv, stw;
    step(8, 0, fa, fb); step(9, 1, fb, fa);
    asm volatile("global_load_dwordx2 %0, %1, off sc1" : "=v"(fl) : "v"(fa_) : "memory");
    step(10, 2, fa, fb); step(11, 3, fb, fa); step(12, 0, fa, fb);
    asm volatile("" : "+v"(fl));
    const bool got = __all(lane >= 6 || fl == tag);
    asm volatile("global_load_dwordx2 %0, %2, off sc1\n\tglobal_load_dwordx2 %1, %3, off sc1" : "=&v"(stv), "=&v"(stw) : "v"(sa_), "v"(sb_) : "memory");
    step(13, 1, fb, fa); step(14, 2, fa, fb); step(15, 3, fb, fa);
    asm volatile("" : "+v"(stv), "+v"(stw));
    if (!got) { wait_flags((GAS u64*)fa_, lane < 6, tag); stv = ld8_wt(sa_); stw = ld8_wt(sb_); }
    STAMP(11);
    float* st = (float*)(lds + P_ST);
    { const f32x2 v = __builtin_bit_cast(f32x2, stv);
      if (tid < 128) { st[tid] = v[0]; st[128 + tid] = v[1]; } else if (tid < 192) *(f32x2*)(st + 256 + 2 * (tid - 128)) = v + __builtin_bit_cast(f32x2, stw); }
    __syncthreads();
}

__device__ __forceinline__ void qkv_role(const uint16_t* __restrict__ x, const uint16_t* __restrict__ wqkv, char* __restrict__ ws, char* lds, int b, u64& tag) {
    const int xcd = b & 7, i = b >> 3;
    const int rb = 4 * (xcd >> 1) + (i & 3), cb = 6 * (xcd & 1) + (i >> 2);
    const int tid = threadIdx.x, lane = tid & 63, wid = __builtin_amdgcn_readfirstlane(tid >> 6), wr = wid >> 2, wc = wid & 3, l31 = lane & 31, hh = lane >> 5;
    f32x16 acc[2];
    const int rot = ((i & 3) * 4 + (i >> 2) * 3) & 15;
    if (cb >= 8) proj_main<true>(x, wqkv, NQKV, rb, cb * 128, cb, ws, lds, rot, tag, acc); else proj_main<false>(x, wqkv, NQKV, rb, cb * 128, cb, ws, lds, rot, tag, acc);
    STAMP(1);
    const float* st = (const float*)(lds + P_ST);
    if (cb >= 8) {
        const float cs = st[256 + wc * 32 + l31];
        const int head = (cb - 8) * 8 + wc * 2 + (l31 >> 4);
#pragma unroll
        for (int rt = 0; rt < 2; ++rt) { const int tr = wr * 64 + rt * 32;
            unsigned pk[8];
#pragma unroll
            for (int q = 0; q < 4; ++q) { const f32x4 m = *(const f32x4*)(st + tr + 8 * q + 4 * hh), r = *(const f32x4*)(st + 128 + tr + 8 * q + 4 * hh);
                pk[2 * q] = cvtpk((acc[rt][4 * q] - m[0] * cs) * r[0], (acc[rt][4 * q + 1] - m[1] * cs) * r[1]);
                pk[2 * q + 1] = cvtpk((acc[rt][4 * q + 2] - m[2] * cs) * r[2], (acc[rt][4 * q + 3] - m[3] * cs) * r[3]); }
            const int tile = rb * 4 + (tr >> 5);
            char* dst = ws + WS_KV + (size_t)head * 131072 + (tile >> 3) * 16384 + 8192 + (tile & 7) * 1024 + hh * 256 + (l31 & 15) * 16;
            st16_wt(dst, (u32x4){pk[0], pk[1], pk[2], pk[3]}); st16_wt(dst + 512, (u32x4){pk[4], pk[5], pk[6], pk[7]}); }
    } else {
        const bool isK = cb >= 4; const float sc = isK ? 1.0f : QSCALE;
        float kmx[2] = {0.f, 0.f};
#pragma unroll
        for (int rt = 0; rt < 2; ++rt) { const int tr = wr * 64 + rt * 32, tile = rb * 4 + (tr >> 5);
            const float mu = st[tr + l31], rs = st[128 + tr + l31] * sc;
#pragma unroll
            for (int hb = 0; hb < 2; ++hb) {
                float v[8];
#pragma unroll
                for (int q = 0; q < 2; ++q) { const f32x4 c = *(const f32x4*)(st + 256 + wc * 32 + 16 * hb + 8 * q + 4 * hh);
#pragma unroll
                    for (int e = 0; e < 4; ++e) v[4 * q + e] = (acc[rt][8 * hb + 4 * q + e] - mu * c[e]) * rs; }
                u32x4 pk;
#pragma unroll
                for (int e = 0; e < 4; ++e) pk[e] = cvtpk(v[2 * e], v[2 * e + 1]);
                const int head = (cb & 3) * 8 + wc * 2 + hb;
                char* dst = isK ? ws + WS_KV + (size_t)head * 131072 + (tile >> 3) * 16384 + (tile & 7) * 1024 + lane * 16 : ws + WS_QF + (size_t)head * 65536 + tile * 1024 + lane * 16;
                st16_wt(dst, pk);
                float n2 = 0.f;
#pragma unroll
                for (int e = 0; e < 4; ++e) n2 = dot2(pk[e], pk[e], n2);
                kmx[hb] = fmaxf(kmx[hb], swap32_add(n2));
            } }
        if (isK) {
#pragma unroll
            for (int hb = 0; hb < 2; ++hb) { const float m = swap16_max(row16_max(kmx[hb]));
                if (lane == 0) __hip_atomic_store((GAS unsigned*)(ws + WS_KM) + ((cb & 3) * 8 + wc * 2 + hb) * 32 + rb * 2 + wr, __float_as_uint(m), RLX_AGENT); } }
    }
    STAMP(12);
    PUBLISH(FL_QKV + cb * 16 + rb);
    STAMP(13);
}

constexpr int A_ONES = 3 * 16384, A_LDS = A_ONES + 8192 + 512;
__device__ __forceinline__ void attn_role(char* __restrict__ ws, char* lds, int b, u64 tag) {
    const int tid = threadIdx.x, lane = tid & 63, wid = __builtin_amdgcn_readfirstlane(tid >> 6);
    const int head = (b & 7) * 4 + ((b >> 3) & 3), jb = b >> 5;
    const unsigned lds0 = (unsigned)(uintptr_t)lds;
    const char* kvb = ws + WS_KV + (size_t)head * 131072;
    if (wid == 0) {
        const int hb = head >> 3;
        const int idx = lane < 16 ? (4 + hb) * 16 + lane : lane < 32 ? (8 + hb) * 16 + (lane - 16) : hb * 16 + 2 * jb + (lane & 1);
        wait_flags(flagp(ws, FL_QKV + idx), lane < 34, tag);
        STAMP(14);
        }
    __syncthreads();
    STAMP(2);
    unsigned goff = wid * 2048 + lane * 16;
    const int tile0 = jb * 8 + wid;
    u32x4 qa; unsigned kmu;
    asm volatile("global_load_dwordx4 %0, %2, off sc1\n\tglobal_load_dword %1, %3, off sc1" : "=&v"(qa), "=&v"(kmu)
                 : "v"(ws + WS_QF + (size_t)head * 65536 + tile0 * 1024 + lane * 16), "v"(ws + WS_KM + (size_t)(head * 32 + (lane & 31)) * 4) : "memory");
#pragma unroll
    for (int c = 0; c < 2; ++c) { set_m0(lds0 + c * 16384 + wid * 2048); glds_s<0, true>(kvb + c * 16384, goff); glds_s<1024, true>(kvb + c * 16384, goff); }
    asm volatile("s_waitcnt vmcnt(4)" : "+v"(qa), "+v"(kmu) :: "memory");
    const float km = swap16_max(row16_max(__uint_as_float(kmu)));
    float na = 0.f;
#pragma unroll
    for (int e = 0; e < 4; ++e) na = dot2(qa[e], qa[e], na);
    na = swap32_add(na);
    float ma = __builtin_amdgcn_sqrtf(na * km);
    if (__any(!(ma <= 40.0f))) {
        float xa = -3.0e38f;
        for (int t = 0; t < 64; ++t) {
            const bf16x8 kf = __builtin_bit_cast(bf16x8, ld16_wt(kvb + (t >> 3) * 16384 + (t & 7) * 1024 + lane * 16));
            const f32x16 sa = MFMA(kf, __builtin_bit_cast(bf16x8, qa), f32x16{});
#pragma unroll
            for (int r = 0; r < 16; ++r) xa = fmaxf(xa, sa[r]); }
        ma = swap32_max(xa);
    }
    const float nma = -ma;
    const unsigned kaddr = lds0 + lane * 16;
    const bool ones = (lane & 16) != 0;
    const unsigned vaddr = ones ? lds0 + A_ONES + (lane & 15) * 16 : lds0 + 8192 + (lane >> 5) * 256 + (lane & 15) * 16;
    const unsigned vsel = ones ? 0u : 0xffffffffu;
    goff += 2 * 16384;
    unsigned dbeg = lds0 + wid * 2048, dend = dbeg + 3 * 16384, dslot = dbeg + 2 * 16384, nslot = 0x4000, cnt, m0save;
    unsigned oa0, oa1, oa2, oa3;
    STAMP(3);
    asm volatile(
    "s_mov_b32 %[m0save], m0\n"
    "v_mov_b32 v96, %[qa0]\n"
    "v_mov_b32 v97, %[qa1]\n"
    "v_mov_b32 v98, %[qa2]\n"
    "v_mov_b32 v99, %[qa3]\n"
    "v_mov_b32 v72, %[nma]\n"
    "v_mov_b32 v56, 0\n"
    "v_mov_b32 v73, %[nma]\n"
    "v_mov_b32 v57, 0\n"
    "v_mov_b32 v74, %[nma]\n"
    "v_mov_b32 v58, 0\n"
    "v_mov_b32 v75, %[nma]\n"
    "v_mov_b32 v59, 0\n"
    "v_mov_b32 v76, %[nma]\n"
    "v_mov_b32 v60, 0\n"
    "v_mov_b32 v77, %[nma]\n"
    "v_mov_b32 v61, 0\n"
    "v_mov_b32 v78, %[nma]\n"
    "v_mov_b32 v62, 0\n"
    "v_mov_b32 v79, %[nma]\n"
    "v_mov_b32 v63, 0\n"
    "v_mov_b32 v80, %[nma]\n"
    "v_mov_b32 v64, 0\n"
    "v_mov_b32 v81, %[nma]\n"
    "v_mov_b32 v65, 0\n"
    "v_mov_b32 v82, %[nma]\n"
    "v_mov_b32 v66, 0\n"
    "v_mov_b32 v83, %[nma]\n"
    "v_mov_b32 v67, 0\n"
    "v_mov_b32 v84, %[nma]\n"
    "v_mov_b32 v68, 0\n"
    "v_mov_b32 v85, %[nma]\n"
    "v_mov_b32 v69, 0\n"
    "v_mov_b32 v86, %[nma]\n"
    "v_mov_b32 v70, 0\n"
    "v_mov_b32 v87, %[nma]\n"
    "v_mov_b32 v71, 0\n"
    "v_mov_b32 v120, 0\n"
    "v_mov_b32 v121, 0\n"
    "v_mov_b32 v122, 0\n"
    "v_mov_b32 v123, 0\n"
    "v_mov_b32 v88, 0\n"
    "v_mov_b32 v89, 0\n"
    "v_mov_b32 v90, 0\n"
    "v_mov_b32 v91, 0\n"
    "v_mov_b32 v92, 0\n"
    "v_mov_b32 v93, 0\n"
    "v_mov_b32 v94, 0\n"
    "v_mov_b32 v95, 0\n"
    "v_mov_b32 v124, %[kaddr]\n"
    "v_mov_b32 v125, %[vaddr]\n"
    "v_add_u32 v126, 0x4000, v124\n"
    "v_and_b32 v127, 0x4000, %[vsel]\n"
    "v_add_u32 v127, v127, v125\n"
    "s_waitcnt vmcnt(2) lgkmcnt(0)\n"
    "s_barrier\n"
    "ds_read_b128 v[100:103], v124\n"
    "ds_read_b128 v[104:107], v124 offset:1024\n"
    "ds_read_b128 v[108:111], v125\n"
    "ds_read_b128 v[112:115], v125 offset:512\n"
    "s_mov_b32 %[cnt], 0\n"
    "s_waitcnt lgkmcnt(0)\n"
    "v_mfma_f32_32x32x16_bf16 v[24:39], v[100:103], v[96:99], v[72:87]\n"
    "s_nop 15\n"
    ".Lloop%=:\n"
    "v_exp_f32 v24, v24\n"
    "v_exp_f32 v25, v25\n"
    "v_mfma_f32_32x32x16_bf16 v[56:71], v[120:123], v[92:95], v[56:71]\n"
    "v_exp_f32 v26, v26\n"
    "v_exp_f32 v27, v27\n"
    "v_cvt_pk_bf16_f32 v88, v24, v25\n"
    "v_exp_f32 v28, v28\n"
    "s_waitcnt lgkmcnt(0)\n"
    "v_mfma_f32_32x32x16_bf16 v[40:55], v[104:107], v[96:99], v[72:87]\n"
    "v_exp_f32 v29, v29\n"
    "v_cvt_pk_bf16_f32 v89, v26, v27\n"
    "ds_read_b128 v[100:103], v124 offset:2048\n"
    "v_exp_f32 v30, v30\n"
    "ds_read_b128 v[116:119], v125 offset:1024\n"
    "v_exp_f32 v31, v31\n"
    "v_cvt_pk_bf16_f32 v90, v28, v29\n"
    "ds_read_b128 v[120:123], v125 offset:1536\n"
    "v_exp_f32 v32, v32\n"
    "v_exp_f32 v33, v33\n"
    "v_cvt_pk_bf16_f32 v91, v30, v31\n"
    "v_exp_f32 v34, v34\n"
    "v_exp_f32 v35, v35\n"
    "v_mfma_f32_32x32x16_bf16 v[56:71], v[108:111], v[88:91], v[56:71]\n"
    "v_cvt_pk_bf16_f32 v92, v32, v33\n"
    "v_exp_f32 v36, v36\n"
    "v_exp_f32 v37, v37\n"
    "v_cvt_pk_bf16_f32 v93, v34, v35\n"
    "v_exp_f32 v38, v38\n"
    "v_exp_f32 v39, v39\n"
    "v_cvt_pk_bf16_f32 v94, v36, v37\n"
    "v_cvt_pk_bf16_f32 v95, v38, v39\n"
    "v_exp_f32 v40, v40\n"
    "v_exp_f32 v41, v41\n"
    "v_mfma_f32_32x32x16_bf16 v[56:71], v[112:115], v[92:95], v[56:71]\n"
    "v_exp_f32 v42, v42\n"
    "v_exp_f32 v43, v43\n"
    "v_cvt_pk_bf16_f32 v88, v40, v41\n"
    "v_exp_f32 v44, v44\n"
    "s_waitcnt lgkmcnt(0)\n"
    "v_mfma_f32_32x32x16_bf16 v[24:39], v[100:103], v[96:99], v[72:87]\n"
    "v_exp_f32 v45, v45\n"
    "v_cvt_pk_bf16_f32 v89, v42, v43\n"
    "ds_read_b128 v[104:107], v124 offset:3072\n"
    "v_exp_f32 v46, v46\n"
    "ds_read_b128 v[108:111], v125 offset:2048\n"
    "v_exp_f32 v47, v47\n"
    "v_cvt_pk_bf16_f32 v90, v44, v45\n"
    "ds_read_b128 v[112:115], v125 offset:2560\n"
    "v_exp_f32 v48, v48\n"
    "v_exp_f32 v49, v49\n"
    "v_cvt_pk_bf16_f32 v91, v46, v47\n"
    "v_exp_f32 v50, v50\n"
    "v_exp_f32 v51, v51\n"
    "v_mfma_f32_32x32x16_bf16 v[56:71], v[116:119], v[88:91], v[56:71]\n"
    "v_cvt_pk_bf16_f32 v92, v48, v49\n"
    "v_exp_f32 v52, v52\n"
    "v_exp_f32 v53, v53\n"
    "v_cvt_pk_bf16_f32 v93, v50, v51\n"
    "v_exp_f32 v54, v54\n"
    "v_exp_f32 v55, v55\n"
    "v_cvt_pk_bf16_f32 v94, v52, v53\n"
    "v_cvt_pk_bf16_f32 v95, v54, v55\n"
    "v_exp_f32 v24, v24\n"
    "v_exp_f32 v25, v25\n"
    "v_mfma_f32_32x32x16_bf16 v[56:71], v[120:123], v[92:95], v[56:71]\n"
    "v_exp_f32 v26, v26\n"
    "v_exp_f32 v27, v27\n"
    "v_cvt_pk_bf16_f32 v88, v24, v25\n"
    "v_exp_f32 v28, v28\n"
    "s_waitcnt lgkmcnt(0)\n"
    "v_mfma_f32_32x32x16_bf16 v[40:55], v[104:107], v[96:99], v[72:87]\n"
    "v_exp_f32 v29, v29\n"
    "v_cvt_pk_bf16_f32 v89, v26, v27\n"
    "ds_read_b128 v[100:103], v124 offset:4096\n"
    "v_exp_f32 v30, v30\n"
    "ds_read_b128 v[116:119], v125 offset:3072\n"
    "v_exp_f32 v31, v31\n"
    "v_cvt_pk_bf16_f32 v90, v28, v29\n"
    "ds_read_b128 v[120:123], v125 offset:3584\n"
    "v_exp_f32 v32, v32\n"
    "v_exp_f32 v33, v33\n"
    "v_cvt_pk_bf16_f32 v91, v30, v31\n"
    "v_exp_f32 v34, v34\n"
    "v_exp_f32 v35, v35\n"
    "v_mfma_f32_32x32x16_bf16 v[56:71], v[108:111], v[88:91], v[56:71]\n"
    "v_cvt_pk_bf16_f32 v92, v32, v33\n"
    "v_exp_f32 v36, v36\n"
    "v_exp_f32 v37, v37\n"
    "v_cvt_pk_bf16_f32 v93, v34, v35\n"
    "v_exp_f32 v38, v38\n"
    "v_exp_f32 v39, v39\n"
    "v_cvt_pk_bf16_f32 v94, v36, v37\n"
    "v_cvt_pk_bf16_f32 v95, v38, v39\n"
    "v_exp_f32 v40, v40\n"
    "v_exp_f32 v41, v41\n"
    "v_mfma_f32_32x32x16_bf16 v[56:71], v[112:115], v[92:95], v[56:71]\n"
    "v_exp_f32 v42, v42\n"
    "v_exp_f32 v43, v43\n"
    "v_cvt_pk_bf16_f32 v88, v40, v41\n"
    "v_exp_f32 v44, v44\n"
    "s_waitcnt lgkmcnt(0)\n"
    "v_mfma_f32_32x32x16_bf16 v[24:39], v[100:103], v[96:99], v[72:87]\n"
    "v_exp_f32 v45, v45\n"
    "v_cvt_pk_bf16_f32 v89, v42, v43\n"
    "ds_read_b128 v[104:107], v124 offset:5120\n"
    "v_exp_f32 v46, v46\n"
    "ds_read_b128 v[108:111], v125 offset:4096\n"
    "v_exp_f32 v47, v47\n"
    "v_cvt_pk_bf16_f32 v90, v44, v45\n"
    "ds_read_b128 v[112:115], v125 offset:4608\n"
    "v_exp_f32 v48, v48\n"
    "v_exp_f32 v49, v49\n"
    "v_cvt_pk_bf16_f32 v91, v46, v47\n"
    "v_exp_f32 v50, v50\n"
    "v_exp_f32 v51, v51\n"
    "v_mfma_f32_32x32x16_bf16 v[56:71], v[116:119], v[88:91], v[56:71]\n"
    "v_cvt_pk_bf16_f32 v92, v48, v49\n"
    "v_exp_f32 v52, v52\n"
    "v_exp_f32 v53, v53\n"
    "v_cvt_pk_bf16_f32 v93, v50, v51\n"
    "v_exp_f32 v54, v54\n"
    "v_exp_f32 v55, v55\n"
    "v_cvt_pk_bf16_f32 v94, v52, v53\n"
    "v_cvt_pk_bf16_f32 v95, v54, v55\n"
    "v_exp_f32 v24, v24\n"
    "v_exp_f32 v25, v25\n"
    "v_mfma_f32_32x32x16_bf16 v[56:71], v[120:123], v[92:95], v[56:71]\n"
    "v_exp_f32 v26, v26\n"
    "v_exp_f32 v27, v27\n"
    "v_cvt_pk_bf16_f32 v88, v24, v25\n"
    "v_exp_f32 v28, v28\n"
    "s_waitcnt lgkmcnt(0)\n"
    "v_mfma_f32_32x32x16_bf16 v[40:55], v[104:107], v[96:99], v[72:87]\n"
    "v_exp_f32 v29, v29\n"
    "v_cvt_pk_bf16_f32 v89, v26, v27\n"
    "ds_read_b128 v[100:103], v124 offset:6144\n"
    "v_exp_f32 v30, v30\n"
    "ds_read_b128 v[116:119], v125 offset:5120\n"
    "v_exp_f32 v31, v31\n"
    "v_cvt_pk_bf16_f32 v90, v28, v29\n"
    "ds_read_b128 v[120:123], v125 offset:5632\n"
    "v_exp_f32 v32, v32\n"
    "v_exp_f32 v33, v33\n"
    "v_cvt_pk_bf16_f32 v91, v30, v31\n"
    "v_exp_f32 v34, v34\n"
    "v_exp_f32 v35, v35\n"
    "v_mfma_f32_32x32x16_bf16 v[56:71], v[108:111], v[88:91], v[56:71]\n"
    "v_cvt_pk_bf16_f32 v92, v32, v33\n"
    "v_exp_f32 v36, v36\n"
    "v_exp_f32 v37, v37\n"
    "v_cvt_pk_bf16_f32 v93, v34, v35\n"
    "v_exp_f32 v38, v38\n"
    "v_exp_f32 v39, v39\n"
    "v_cvt_pk_bf16_f32 v94, v36, v37\n"
    "v_cvt_pk_bf16_f32 v95, v38, v39\n"
    "v_exp_f32 v40, v40\n"
    "v_exp_f32 v41, v41\n"
    "v_mfma_f32_32x32x16_bf16 v[56:71], v[112:115], v[92:95], v[56:71]\n"
    "v_exp_f32 v42, v42\n"
    "v_exp_f32 v43, v43\n"
    "v_cvt_pk_bf16_f32 v88, v40, v41\n"
    "v_exp_f32 v44, v44\n"
    "s_waitcnt lgkmcnt(0)\n"
    "v_mfma_f32_32x32x16_bf16 v[24:39], v[100:103], v[96:99], v[72:87]\n"
    "v_exp_f32 v45, v45\n"
    "v_cvt_pk_bf16_f32 v89, v42, v43\n"
    "ds_read_b128 v[104:107], v124 offset:7168\n"
    "v_exp_f32 v46, v46\n"
    "ds_read_b128 v[108:111], v125 offset:6144\n"
    "v_exp_f32 v47, v47\n"
    "v_cvt_pk_bf16_f32 v90, v44, v45\n"
    "ds_read_b128 v[112:115], v125 offset:6656\n"
    "v_exp_f32 v48, v48\n"
    "v_exp_f32 v49, v49\n"
    "v_cvt_pk_bf16_f32 v91, v46, v47\n"
    "v_exp_f32 v50, v50\n"
    "v_exp_f32 v51, v51\n"
    "v_mfma_f32_32x32x16_bf16 v[56:71], v[116:119], v[88:91], v[56:71]\n"
    "v_cvt_pk_bf16_f32 v92, v48, v49\n"
    "v_exp_f32 v52, v52\n"
    "v_exp_f32 v53, v53\n"
    "v_cvt_pk_bf16_f32 v93, v50, v51\n"
    "v_exp_f32 v54, v54\n"
    "v_exp_f32 v55, v55\n"
    "v_cvt_pk_bf16_f32 v94, v52, v53\n"
    "v_cvt_pk_bf16_f32 v95, v54, v55\n"
    "v_exp_f32 v24, v24\n"
    "v_exp_f32 v25, v25\n"
    "v_mfma_f32_32x32x16_bf16 v[56:71], v[120:123], v[92:95], v[56:71]\n"
    "v_exp_f32 v26, v26\n"
    "v_exp_f32 v27, v27\n"
    "v_cvt_pk_bf16_f32 v88, v24, v25\n"
    "v_exp_f32 v28, v28\n"
    "s_waitcnt lgkmcnt(0)\n"
    "v_mfma_f32_32x32x16_bf16 v[40:55], v[104:107], v[96:99], v[72:87]\n"
    "v_exp_f32 v29, v29\n"
    "v_cvt_pk_bf16_f32 v89, v26, v27\n"
    "s_waitcnt vmcnt(0)\n"
    "s_barrier\n"
    "ds_read_b128 v[100:103], v126 offset:0\n"
    "v_exp_f32 v30, v30\n"
    "ds_read_b128 v[116:119], v125 offset:7168\n"
    "v_exp_f32 v31, v31\n"
    "v_cvt_pk_bf16_f32 v90, v28, v29\n"
    "ds_read_b128 v[120:123], v125 offset:7680\n"
    "v_exp_f32 v32, v32\n"
    "v_exp_f32 v33, v33\n"
    "v_cvt_pk_bf16_f32 v91, v30, v31\n"
    "v_exp_f32 v34, v34\n"
    "v_exp_f32 v35, v35\n"
    "v_mfma_f32_32x32x16_bf16 v[56:71], v[108:111], v[88:91], v[56:71]\n"
    "s_cmp_ge_u32 %[cnt], 6\n"
    "s_cbranch_scc1 .Lnodma%=\n"
    "s_mov_b32 m0, %[dslot]\n"
    "s_nop 0\n"
    "global_load_lds_dwordx4 %[goff], %[gbase] offset:0\n"
    "global_load_lds_dwordx4 %[goff], %[gbase] offset:1024\n"
    ".Lnodma%=:\n"
    "v_cvt_pk_bf16_f32 v92, v32, v33\n"
    "v_exp_f32 v36, v36\n"
    "v_exp_f32 v37, v37\n"
    "v_cvt_pk_bf16_f32 v93, v34, v35\n"
    "v_exp_f32 v38, v38\n"
    "v_exp_f32 v39, v39\n"
    "v_cvt_pk_bf16_f32 v94, v36, v37\n"
    "v_cvt_pk_bf16_f32 v95, v38, v39\n"
    "v_exp_f32 v40, v40\n"
    "v_exp_f32 v41, v41\n"
    "v_mfma_f32_32x32x16_bf16 v[56:71], v[112:115], v[92:95], v[56:71]\n"
    "v_exp_f32 v42, v42\n"
    "v_exp_f32 v43, v43\n"
    "v_cvt_pk_bf16_f32 v88, v40, v41\n"
    "v_exp_f32 v44, v44\n"
    "s_waitcnt lgkmcnt(0)\n"
    "v_mfma_f32_32x32x16_bf16 v[24:39], v[100:103], v[96:99], v[72:87]\n"
    "v_exp_f32 v45, v45\n"
    "v_cvt_pk_bf16_f32 v89, v42, v43\n"
    "ds_read_b128 v[104:107], v126 offset:1024\n"
    "v_exp_f32 v46, v46\n"
    "ds_read_b128 v[108:111], v127 offset:0\n"
    "v_exp_f32 v47, v47\n"
    "v_cvt_pk_bf16_f32 v90, v44, v45\n"
    "ds_read_b128 v[112:115], v127 offset:512\n"
    "v_exp_f32 v48, v48\n"
    "v_exp_f32 v49, v49\n"
    "v_cvt_pk_bf16_f32 v91, v46, v47\n"
    "v_exp_f32 v50, v50\n"
    "v_exp_f32 v51, v51\n"
    "v_mfma_f32_32x32x16_bf16 v[56:71], v[116:119], v[88:91], v[56:71]\n"
    "v_cvt_pk_bf16_f32 v92, v48, v49\n"
    "v_exp_f32 v52, v52\n"
    "v_exp_f32 v53, v53\n"
    "v_cvt_pk_bf16_f32 v93, v50, v51\n"
    "v_exp_f32 v54, v54\n"
    "v_exp_f32 v55, v55\n"
    "v_cvt_pk_bf16_f32 v94, v52, v53\n"
    "v_cvt_pk_bf16_f32 v95, v54, v55\n"
    "v_mov_b32 v124, v126\n"
    "v_mov_b32 v125, v127\n"
    "s_add_u32 %[nslot], %[nslot], 0x4000\n"
    "s_cmp_eq_u32 %[nslot], 0xc000\n"
    "s_cselect_b32 %[nslot], 0, %[nslot]\n"
    "v_add_u32 v126, %[nslot], %[kaddr]\n"
    "v_and_b32 v127, %[nslot], %[vsel]\n"
    "v_add_u32 v127, v127, %[vaddr]\n"
    "s_add_u32 %[dslot], %[dslot], 0x4000\n"
    "s_cmp_eq_u32 %[dslot], %[dend]\n"
    "s_cselect_b32 %[dslot], %[dbeg], %[dslot]\n"
    "v_add_u32 %[goff], 0x4000, %[goff]\n"
    "s_add_u32 %[cnt], %[cnt], 1\n"
    "s_cmp_lt_u32 %[cnt], 8\n"
    "s_cbranch_scc1 .Lloop%=\n"
    "s_nop 1\n"
    "v_mfma_f32_32x32x16_bf16 v[56:71], v[120:123], v[92:95], v[56:71]\n"
    "s_nop 15\n"
    "s_nop 3\n"
    "v_rcp_f32 v24, v64\n"
    "s_nop 0\n"
    "v_mul_f32 v56, v56, v24\n"
    "v_mul_f32 v57, v57, v24\n"
    "v_mul_f32 v58, v58, v24\n"
    "v_mul_f32 v59, v59, v24\n"
    "v_mul_f32 v60, v60, v24\n"
    "v_mul_f32 v61, v61, v24\n"
    "v_mul_f32 v62, v62, v24\n"
    "v_mul_f32 v63, v63, v24\n"
    "v_cvt_pk_bf16_f32 %[oa0], v56, v57\n"
    "v_cvt_pk_bf16_f32 %[oa1], v58, v59\n"
    "v_cvt_pk_bf16_f32 %[oa2], v60, v61\n"
    "v_cvt_pk_bf16_f32 %[oa3], v62, v63\n"
    "s_waitcnt lgkmcnt(0)\n"
    "s_mov_b32 m0, %[m0save]\n"
        : [oa0] "=&v"(oa0), [oa1] "=&v"(oa1), [oa2] "=&v"(oa2), [oa3] "=&v"(oa3),
          [goff] "+v"(goff), [dslot] "+s"(dslot), [nslot] "+s"(nslot), [cnt] "=&s"(cnt), [m0save] "=&s"(m0save)
        : [qa0] "v"(qa[0]), [qa1] "v"(qa[1]), [qa2] "v"(qa[2]), [qa3] "v"(qa[3]),
          [nma] "v"(nma), [kaddr] "v"(kaddr), [vaddr] "v"(vaddr), [vsel] "v"(vsel), [gbase] "s"(kvb), [dbeg] "s"(dbeg), [dend] "s"(dend), [psel] "s"(0x07060302u), [vmask] "s"(0x0000ffff0000ffffull), [vone] "v"(((lane & 27) == 16) ? 0x3f803f80u : 0u), [dbgaddr] "s"(lds0 + A_LDS + wid * 64)
        : "memory", "scc", "vcc",
          "v24", "v25", "v26", "v27", "v28", "v29", "v30", "v31", "v32", "v33", "v34", "v35", "v36", "v37", "v38", "v39", "v40", "v41", "v42", "v43", "v44", "v45", "v46", "v47", "v48", "v49", "v50", "v51", "v52", "v53", "v54", "v55", "v56", "v57", "v58", "v59", "v60", "v61", "v62", "v63", "v64", "v65", "v66", "v67", "v68", "v69", "v70", "v71", "v72", "v73", "v74", "v75", "v76", "v77", "v78", "v79", "v80", "v81", "v82", "v83", "v84", "v85", "v86", "v87", "v88", "v89", "v90", "v91", "v92", "v93", "v94", "v95", "v96", "v97", "v98", "v99", "v100", "v101", "v102", "v103", "v104", "v105", "v106", "v107", "v108", "v109", "v110", "v111", "v112", "v113", "v114", "v115", "v116", "v117", "v118", "v119", "v120", "v121", "v122", "v123", "v124", "v125", "v126", "v127"
    );
    STAMP(4);
    st16_wt(ws + WS_OF + ((size_t)tile0 * 32 + head) * 1024 + lane * 16, (u32x4){oa0, oa1, oa2, oa3});
    WAITV(0);
    if (lane == 0) __hip_atomic_store(flagp(ws, FL_O + tile0 * 32 + head), tag, RLX_AGENT);
}

__device__ __forceinline__ void z_role(const uint16_t* __restrict__ x, const uint16_t* __restrict__ win, char* __restrict__ ws, char* lds, int g, u64& tag) {
    const int tid = threadIdx.x, lane = tid & 63, wid = __builtin_amdgcn_readfirstlane(tid >> 6), wr = wid >> 2, wc = wid & 3, l31 = lane & 31, hh = lane >> 5;
    const int xcd = g & 7, i = g >> 3;
    const int rb = 4 * (xcd >> 1) + (i & 3), cbz = 7 * ((xcd & 1) ^ 1) + (i >> 2);
    f32x16 acc[2];
    proj_main<false>(x, win, NIN, rb, cbz * 128, 12 + cbz, ws, lds, ((i & 3) * 4 + (i >> 2) * 3) & 15, tag, acc);
    STAMP(1);
    const float* st = (const float*)(lds + P_ST);
    unsigned zp[2][8];
#pragma unroll
    for (int rt = 0; rt < 2; ++rt) { const int tr = wr * 64 + rt * 32; const float mu = st[tr + l31], rs = st[128 + tr + l31];
#pragma unroll
        for (int q = 0; q < 4; ++q) { const f32x4 c = *(const f32x4*)(st + 256 + wc * 32 + 8 * q + 4 * hh);
            zp[rt][2 * q] = cvtpk(gelu_t((acc[rt][4 * q] - mu * c[0]) * rs), gelu_t((acc[rt][4 * q + 1] - mu * c[1]) * rs));
            zp[rt][2 * q + 1] = cvtpk(gelu_t((acc[rt][4 * q + 2] - mu * c[2]) * rs), gelu_t((acc[rt][4 * q + 3] - mu * c[3]) * rs)); } }
    if (cbz < 7) {
#pragma unroll
        for (int rt = 0; rt < 2; ++rt) { const int tt = rb * 4 + wr * 2 + rt;
            char* dst = ws + WS_Z1 + ((size_t)tt * 28 + cbz * 4 + wc) * 2048 + lane * 32;
            st16_wt(dst, (u32x4){zp[rt][0], zp[rt][1], zp[rt][2], zp[rt][3]}); st16_wt(dst + 16, (u32x4){zp[rt][4], zp[rt][5], zp[rt][6], zp[rt][7]}); }
    } else {
        const int cj = cbz - 7;
        float* pst = (float*)lds;
        const unsigned ONES = 0x3f803f80u;
#pragma unroll
        for (int rt = 0; rt < 2; ++rt) { float s1 = 0.f, s2 = 0.f;
#pragma unroll
            for (int e = 0; e < 8; ++e) { s1 = dot2(zp[rt][e], ONES, s1); s2 = dot2(zp[rt][e], zp[rt][e], s2); }
            s1 = swap32_add(s1); s2 = swap32_add(s2);
            if (hh == 0) *(f32x2*)(pst + (wc * 128 + wr * 64 + rt * 32 + l31) * 2) = (f32x2){s1, s2}; }
        __syncthreads();
        char* gst = ws + WS_ST + (size_t)rb * 7 * 2048;
        float* fin = (float*)(lds + 4096);
        if (tid < 128) { f32x2 s = *(const f32x2*)(pst + tid * 2);
#pragma unroll
            for (int j = 1; j < 4; ++j) s += *(const f32x2*)(pst + (j * 128 + tid) * 2);
            st16_wt(gst + (cj * 128 + tid) * 16, (u32x4){__float_as_uint(s[0]), __float_as_uint(s[1]), (unsigned)tag, (unsigned)(tag >> 32)});
            u32x4 gr[7];
            for (unsigned spins = 0; spins < (1u << 18); ++spins) {
                asm volatile("global_load_dwordx4 %0, %7, off sc1\n\tglobal_load_dwordx4 %1, %7, off offset:2048 sc1\n\tglobal_load_dwordx4 %2, %8, off sc1\n\tglobal_load_dwordx4 %3, %8, off offset:2048 sc1\n\t"
                             "global_load_dwordx4 %4, %9, off sc1\n\tglobal_load_dwordx4 %5, %9, off offset:2048 sc1\n\tglobal_load_dwordx4 %6, %10, off sc1\n\ts_waitcnt vmcnt(0)"
                             : "=&v"(gr[0]), "=&v"(gr[1]), "=&v"(gr[2]), "=&v"(gr[3]), "=&v"(gr[4]), "=&v"(gr[5]), "=&v"(gr[6])
                             : "v"(gst + tid * 16), "v"(gst + 4096 + tid * 16), "v"(gst + 8192 + tid * 16), "v"(gst + 12288 + tid * 16) : "memory");
                bool ok = true;
#pragma unroll
                for (int j = 0; j < 7; ++j) ok &= gr[j][2] == (unsigned)tag && gr[j][3] == (unsigned)(tag >> 32);
                if (__all(ok)) break;
                __builtin_amdgcn_s_sleep(POLL_SLEEP); }
            float s1 = 0.f, s2 = 0.f;
#pragma unroll
            for (int j = 0; j < 7; ++j) { s1 += __uint_as_float(gr[j][0]); s2 += __uint_as_float(gr[j][1]); }
            const float mu = s1 * (1.0f / FH); const float var = fmaxf(s2 * (1.0f / FH) - mu * mu, 0.f);
            fin[tid] = mu; fin[128 + tid] = __builtin_amdgcn_rsqf(var + LN_EPS); }
        __syncthreads();
        char* img = lds + 8192;
#pragma unroll
        for (int rt = 0; rt < 2; ++rt) { const int tok = wr * 64 + rt * 32 + l31; const float mu = fin[tok], rs = fin[128 + tok];
#pragma unroll
            for (int q = 0; q < 4; ++q) { const unsigned a = zp[rt][2 * q], c = zp[rt][2 * q + 1];
                const u32x2 o = {cvtpk((bflo(a) - mu) * rs, (bfhi(a) - mu) * rs), cvtpk((bflo(c) - mu) * rs, (bfhi(c) - mu) * rs)};
                const int f = wc * 32 + 8 * q + 4 * hh;
                *(u32x2*)(img + tok * 256 + (((f >> 3) ^ wsw(tok)) * 16) + (f & 4) * 2) = o; } }
        __syncthreads();
        { const int blk = (lane >> 4) & 1, q = (lane & 15) >> 2, p = lane & 3;
#pragma unroll
          for (int kq = 0; kq < 4; ++kq) { const int ks = wr * 4 + kq; s16x4 t2[2];
#pragma unroll
              for (int t = 0; t < 2; ++t) { const int kr = 16 * ks + 8 * hh + 4 * t + q; const int ch = 4 * wc + 2 * blk + (p >> 1);
                  t2[t] = trread((lds_cptr)img + kr * 256 + ((ch ^ wsw(kr)) * 16) + 8 * (p & 1)); }
              st16_wt(ws + WS_Z2N + ((size_t)(cj * 4 + wc) * 128 + rb * 8 + ks) * 1024 + lane * 16, __builtin_bit_cast(u32x4, cat(t2[0], t2[1]))); } }
    }
    PUBLISH(FL_Z + rb * 14 + cbz);
}

constexpr int S_SLOT = 24576, S_LDS = 3 * S_SLOT;
__device__ __forceinline__ void spatial_role(const uint16_t* __restrict__ wsp, const uint16_t* __restrict__ bsp, char* __restrict__ ws, char* lds, int g, u64 tag) {
    const int tid = threadIdx.x, lane = tid & 63, wid8 = __builtin_amdgcn_readfirstlane(tid >> 6), wid = wid8 & 3, l31 = lane & 31, hh = lane >> 5;
    const int i = g >> 3, tb = 4 * (g & 7) + (i & 3), fb = i >> 2;
    const unsigned lds0 = (unsigned)(uintptr_t)lds;
    STAMP(3);
    if (wid8 >= 4) {
        const int r0 = 16 * wid + (lane >> 3), r1 = r0 + 8;
        const char* sb_ = (const char*)(wsp + (size_t)tb * 64 * SEQ);
        const unsigned vo0 = (r0 * SEQ + ((lane & 7) ^ ((r0 >> 1) & 7)) * 8) * 2, vo1 = (r1 * SEQ + ((lane & 7) ^ ((r1 >> 1) & 7)) * 8) * 2;
        const char* zs = ws + WS_Z2N + (size_t)(fb * 4 + wid) * 131072; const unsigned zvo = lane * 16;
        unsigned ready = 0;
        GAS u64* zf = flagp(ws, FL_Z + (lane & 15) * 14 + 7 + fb);
        auto need = [&](int rbk) {
            for (unsigned spins = 0; !((ready >> rbk) & 1) && spins < (1u << 18); ++spins) {
                ready = (unsigned)__ballot(__hip_atomic_load(zf, RLX_AGENT) == tag) & 0xffffu;
                if (!((ready >> rbk) & 1)) __builtin_amdgcn_s_sleep(POLL_SLEEP); } };
        auto issue = [&](int it, int slot) { const unsigned so = lds0 + slot * S_SLOT;
            need(it >> 1);
            set_m0(so + wid * 2048); glds_s<0, false>(sb_ + it * 128, vo0);
            set_m0(so + wid * 2048 + 1024); glds_s<0, false>(sb_ + it * 128, vo1);
            set_m0(so + 8192 + wid * 4096);
            glds_s<0, true>(zs + it * 4096, zvo); glds_s<1024, true>(zs + it * 4096, zvo); glds_s<2048, true>(zs + it * 4096, zvo); glds_s<3072, true>(zs + it * 4096, zvo); };
        issue(0, 0); issue(1, 1); issue(2, 2);
        WAIT_BAR(12);
        int slot = 0;
#pragma unroll 1
        for (int it = 1; it < 31; ++it) { WAIT_BAR(6); if (it + 2 < 32) issue(it + 2, slot); slot = slot == 2 ? 0 : slot + 1; }
        WAIT_BAR(0);
        BAR();
    } else {
        unsigned boff[2][4];
#pragma unroll
        for (int rt = 0; rt < 2; ++rt)
#pragma unroll
            for (int ks = 0; ks < 4; ++ks) { const int r = rt * 32 + l31; boff[rt][ks] = (r * 128 + ((hh ^ ((r >> 1) & 7)) * 16)) ^ (ks * 32); }
        const unsigned aoff = 8192 + wid * 4096 + lane * 16;
        struct SF { bf16x8 a, b[2]; };
        auto rd = [&](SF& f, int slot, int ks) { lds_cptr sb = (lds_cptr)lds + slot * S_SLOT;
            f.a = ldsfrag(sb + aoff + ks * 1024); f.b[0] = ldsfrag(sb + boff[0][ks]); f.b[1] = ldsfrag(sb + boff[1][ks]); };
        f32x16 acc[2] = {f32x16{}, f32x16{}};
        auto mm = [&](const SF& f) { acc[0] = MFMA(f.a, f.b[0], acc[0]); acc[1] = MFMA(f.a, f.b[1], acc[1]); };
        SF f[4];
        BAR();
#pragma unroll
        for (int ks = 0; ks < 4; ++ks) rd(f[ks], 0, ks);
        auto step = [&](int nsl) {
            LGKM_BAR();
#pragma unroll
            for (int ks = 0; ks < 4; ++ks) { lds_cptr sb = (lds_cptr)lds + nsl * S_SLOT;
                acc[0] = MFMA(f[ks].a, f[ks].b[0], acc[0]); SBAR();
                f[ks].b[0] = ldsfrag(sb + boff[0][ks]); SBAR();
                PACE();
                acc[1] = MFMA(f[ks].a, f[ks].b[1], acc[1]); SBAR();
                f[ks].b[1] = ldsfrag(sb + boff[1][ks]); f[ks].a = ldsfrag(sb + aoff + ks * 1024); SBAR();
                PACE(); } };
#pragma unroll 1
        for (int io = 0; io < 10; ++io) { step(1); step(2); step(0); }
        step(1); step(2);
        wait_flags(flagp(ws, FL_Z + (tb >> 1) * 14 + fb), lane == 0, tag);
#pragma unroll
        for (int rt = 0; rt < 2; ++rt) { const int tt = tb * 2 + rt; const float bias = __uint_as_float((unsigned)bsp[tt * 32 + l31] << 16);
            const char* z1p = ws + WS_Z1 + ((size_t)tt * 28 + fb * 4 + wid) * 2048 + lane * 32;
            const u32x4 za = ld16_wt(z1p), zb = ld16_wt(z1p + 16);
            u32x4 ua, ub;
#pragma unroll
            for (int e = 0; e < 4; ++e) {
                ua[e] = cvtpk(bflo(za[e]) * (acc[rt][2 * e] + bias), bfhi(za[e]) * (acc[rt][2 * e + 1] + bias));
                ub[e] = cvtpk(bflo(zb[e]) * (acc[rt][8 + 2 * e] + bias), bfhi(zb[e]) * (acc[rt][8 + 2 * e + 1] + bias)); }
            char* ud = ws + WS_U + ((size_t)tt * 56 + (fb * 4 + wid) * 2) * 1024 + lane * 16;
            st16_wt(ud, ua); st16_wt(ud + 1024, ub); }
    }
    PUBLISH(FL_U + tb * 7 + fb);
}

constexpr int F_SLOT = 8192 + 8192, F_LDS = 4 * F_SLOT;
__device__ __forceinline__ void final_role(const uint16_t* __restrict__ x, const uint16_t* __restrict__ wo, const uint16_t* __restrict__ wout, char* __restrict__ ws, uint16_t* __restrict__ out, char* lds, int g, u64 tag) {
    const int tb = 4 * (g & 7) + ((g >> 3) & 3), db = g >> 5;
    const int tid = threadIdx.x, lane = tid & 63, wid8 = __builtin_amdgcn_readfirstlane(tid >> 6), wid = wid8 & 3, wr = wid >> 1, wc = wid & 1, l31 = lane & 31, hh = lane >> 5;
    const unsigned lds0 = (unsigned)(uintptr_t)lds;
    auto wswz = [](int r) { return ((r & 2) << 1) | ((r >> 2) & 3); };
    f32x16 as = f32x16{}, ag = f32x16{};
    const int k0 = 16 * wid + (lane >> 3), k1 = k0 + 8;
    const unsigned wo0 = (k0 * DM + db * 64 + ((lane & 7) ^ wswz(k0)) * 8) * 2, wo1 = (k1 * DM + db * 64 + ((lane & 7) ^ wswz(k1)) * 8) * 2;
    const int att = tb * 2 + (wid & 1), aks = 2 * (wid >> 1);
    unsigned woff[2];
    { const int blk = (lane >> 4) & 1, q = (lane & 15) >> 2, p = lane & 3; const int kr = 4 * hh + q; const int ch = 4 * wc + 2 * blk + (p >> 1);
      woff[0] = kr * 128 + ((ch ^ wswz(kr)) * 16) + 8 * (p & 1); woff[1] = (woff[0] + 1024) ^ 32; }
    const unsigned aoff = 8192 + wr * 4096 + lane * 16;
    struct FF { bf16x8 w, a; };
    auto rd = [&](FF& f, int slot, int ks) { lds_cptr sb = (lds_cptr)lds + slot * F_SLOT;
        f.w = cat(trread(sb + woff[0] + ks * 2048), trread(sb + woff[1] + ks * 2048)); f.a = ldsfrag(sb + aoff + ks * 1024); };
    auto pass = [&](const uint16_t* wb, const char* ab, int nfr, int n, f32x16& acc, bool pace) {
        if (wid8 >= 4) {
            auto issue = [&](int it) { const unsigned so = lds0 + (it & 3) * F_SLOT; const char* w = (const char*)(wb + (size_t)it * 64 * DM);
                set_m0(so + wid * 2048); glds_s<0, false>(w, wo0);
                set_m0(so + wid * 2048 + 1024); glds_s<0, false>(w, wo1);
                const char* a = ab + ((size_t)att * nfr + it * 4 + aks) * 1024;
                set_m0(so + 8192 + (wid & 1) * 4096 + aks * 1024); glds_s<0, true>(a, lane * 16); glds_s<1024, true>(a, lane * 16); };
            issue(0); issue(1); issue(2); issue(3);
            WAIT_BAR(12);
#pragma unroll 1
            for (int i = 1; i < n - 2; ++i) { WAIT_BAR(8); if (i + 3 < n) issue(i + 3); }
            WAIT_BAR(4);
            WAIT_BAR(0);
            BAR();
        } else {
            FF fa[4], fb[4];
            BAR();
#pragma unroll
            for (int ks = 0; ks < 4; ++ks) rd(fa[ks], 0, ks);
            auto step = [&](int nsl, FF (&cur)[4], FF (&nxt)[4]) {
                LGKM_BAR();
#pragma unroll
                for (int ks = 0; ks < 4; ++ks) { acc = MFMA(cur[ks].w, cur[ks].a, acc); SBAR(); rd(nxt[ks], nsl, ks); SBAR(); if (pace) PACE(); } };
#pragma unroll 1
            for (int it = 0; it < n; it += 2) { step((it + 1) & 3, fa, fb); step((it + 2) & 3, fb, fa); }
        } };
    if (wid8 == 0) wait_flags(flagp(ws, FL_U + tb * 7 + lane), lane < 7, tag);
    __syncthreads();
    STAMP(5);
    pass(wout, ws + WS_U, 56, 14, ag, true);
    STAMP(6);
    __syncthreads();
#pragma unroll
    for (int q = 0; q < 8; ++q) {
        const int kr = 8 * wid8 + (lane >> 3);
        set_m0(lds0 + q * 8192 + wid8 * 1024);
        glds_s<0, false>((const char*)(wo + (size_t)q * 64 * DM), (kr * DM + db * 64 + ((lane & 7) ^ wswz(kr)) * 8) * 2); }
    const int tok = tb * 64 + wr * 32 + l31;
    float gate[16]; u32x2 xv[4];
    if (wid8 < 4) {
#pragma unroll
        for (int q = 0; q < 4; ++q) xv[q] = *(const u32x2*)(x + (size_t)tok * DM + db * 64 + wc * 32 + 8 * q + 4 * hh);
#pragma unroll
        for (int r = 0; r < 16; ++r) gate[r] = silu_f(ag[r]); }
    if (wid8 == 0) wait_flags(flagp(ws, FL_O + tb * 64 + lane), true, tag);
    WAITV(0);
    __syncthreads();
    STAMP(7);
    const int kh = wid8 >> 2;
    u32x4 of[16];
    { const char* ob = ws + WS_OF + ((size_t)(tb * 2 + wr) * 32 + 16 * kh) * 1024; const unsigned v0 = lane * 16, v1 = v0 + 4096, v2 = v0 + 8192, v3 = v0 + 12288;
      asm volatile(
        "global_load_dwordx4 %0, %16, %20 sc1\n\tglobal_load_dwordx4 %1, %16, %20 offset:1024 sc1\n\tglobal_load_dwordx4 %2, %16, %20 offset:2048 sc1\n\tglobal_load_dwordx4 %3, %16, %20 offset:3072 sc1\n\t"
        "global_load_dwordx4 %4, %17, %20 sc1\n\tglobal_load_dwordx4 %5, %17, %20 offset:1024 sc1\n\tglobal_load_dwordx4 %6, %17, %20 offset:2048 sc1\n\tglobal_load_dwordx4 %7, %17, %20 offset:3072 sc1\n\t"
        "global_load_dwordx4 %8, %18, %20 sc1\n\tglobal_load_dwordx4 %9, %18, %20 offset:1024 sc1\n\tglobal_load_dwordx4 %10, %18, %20 offset:2048 sc1\n\tglobal_load_dwordx4 %11, %18, %20 offset:3072 sc1\n\t"
        "global_load_dwordx4 %12, %19, %20 sc1\n\tglobal_load_dwordx4 %13, %19, %20 offset:1024 sc1\n\tglobal_load_dwordx4 %14, %19, %20 offset:2048 sc1\n\tglobal_load_dwordx4 %15, %19, %20 offset:3072 sc1\n\t"
        "s_waitcnt vmcnt(0)"
        : "=&v"(of[0]), "=&v"(of[1]), "=&v"(of[2]), "=&v"(of[3]), "=&v"(of[4]), "=&v"(of[5]), "=&v"(of[6]), "=&v"(of[7]),
          "=&v"(of[8]), "=&v"(of[9]), "=&v"(of[10]), "=&v"(of[11]), "=&v"(of[12]), "=&v"(of[13]), "=&v"(of[14]), "=&v"(of[15])
        : "v"(v0), "v"(v1), "v"(v2), "v"(v3), "s"(ob) : "memory"); }
    { lds_cptr wb_ = (lds_cptr)lds + kh * 32768;
#pragma unroll
      for (int ks = 0; ks < 16; ++ks)
          as = MFMA(cat(trread(wb_ + woff[0] + ks * 2048), trread(wb_ + woff[1] + ks * 2048)), __builtin_bit_cast(bf16x8, of[ks]), as); }
    __syncthreads();
    if (kh == 1) {
#pragma unroll
        for (int q = 0; q < 4; ++q) *(f32x4*)(lds + (wid * 4 + q) * 1024 + lane * 16) = (f32x4){as[4 * q], as[4 * q + 1], as[4 * q + 2], as[4 * q + 3]}; }
    __syncthreads();
    STAMP(8);
    if (kh == 0) {
#pragma unroll
        for (int q = 0; q < 4; ++q) { const f32x4 p = *(const f32x4*)(lds + (wid * 4 + q) * 1024 + lane * 16);
            const float r0 = bflo(xv[q][0]) + (as[4 * q] + p[0]) * gate[4 * q], r1 = bfhi(xv[q][0]) + (as[4 * q + 1] + p[1]) * gate[4 * q + 1];
            const float r2 = bflo(xv[q][1]) + (as[4 * q + 2] + p[2]) * gate[4 * q + 2], r3 = bfhi(xv[q][1]) + (as[4 * q + 3] + p[3]) * gate[4 * q + 3];
            *(u32x2*)(out + (size_t)tok * DM + db * 64 + wc * 32 + 8 * q + 4 * hh) = (u32x2){cvtpk(r0, r1), cvtpk(r2, r3)}; }
    }
}

#define APRIO 3
#define GDELAY 0
#define GPRIO 2
constexpr int ALL_LDS = S_LDS;
__global__ __launch_bounds__(512, 4) void k_all(const uint16_t* __restrict__ x, const uint16_t* __restrict__ wqkv, const uint16_t* __restrict__ wo, const uint16_t* __restrict__ win,
                                                const uint16_t* __restrict__ wsp, const uint16_t* __restrict__ bsp, const uint16_t* __restrict__ wout, char* __restrict__ ws, uint16_t* __restrict__ out) {
    extern __shared__ __attribute__((aligned(16))) char lds[];
    const int b = blockIdx.x;
    u64 tag;
    STAMP(0);
    if (b < 256) {
        __builtin_amdgcn_s_setprio(APRIO);
        if (ADELAY > 0 && b < 192) {
            const u64 t0 = __builtin_amdgcn_s_memrealtime();
            const int i_ = b >> 3, cb_ = 6 * (b & 1) + (i_ >> 2), line = (i_ & 3) * 256 + (threadIdx.x & 255);
            unsigned junk = 0;
            while (__builtin_amdgcn_s_memrealtime() - t0 < ADELAY) __builtin_amdgcn_s_sleep(8);
            asm volatile("s_waitcnt vmcnt(0)" : "+v"(junk) :: "memory"); }
        if (b < 192) qkv_role(x, wqkv, ws, lds, b, tag); else stats_role(x, wqkv, win, ws, lds, b - 192, tag);
        __builtin_amdgcn_s_setprio(0);
        __syncthreads();
        attn_role(ws, lds, b, tag);
        STAMP(5);
    } else {
        const int g = b - 256;
        __builtin_amdgcn_s_setprio(GPRIO);
            if (GDELAY > 0) { const u64 t0 = __builtin_amdgcn_s_memrealtime(); while (__builtin_amdgcn_s_memrealtime() - t0 < GDELAY) __builtin_amdgcn_s_sleep(8); }
        if (g < 224) { z_role(x, win, ws, lds, g, tag); STAMP(2); __syncthreads(); spatial_role(wsp, bsp, ws, lds, g, tag); STAMP(4); __syncthreads(); }
        else { u64 ep; EPOCH_REQ(ep);
            { const u64 t0 = __builtin_amdgcn_s_memrealtime(); while (__builtin_amdgcn_s_memrealtime() - t0 < WDELAY) __builtin_amdgcn_s_sleep(8); }
            { const int wv = (g - 224) * 8 + (threadIdx.x >> 6), lane = threadIdx.x & 63; unsigned junk = 0;
#pragma unroll
              for (int q = 0; q < 4; ++q) asm volatile("global_load_dword %0, %1, off" : "+v"(junk) : "v"((const char*)wsp + ((size_t)(q * 256 + wv) * 64 + lane) * 128) : "memory");
              if (wv < 112) asm volatile("global_load_dword %0, %1, off" : "+v"(junk) : "v"((const char*)wout + ((size_t)wv * 64 + lane) * 128) : "memory");
              else if (wv < 176) asm volatile("global_load_dword %0, %1, off" : "+v"(junk) : "v"((const char*)wo + ((size_t)(wv - 112) * 64 + lane) * 128) : "memory");
              asm volatile("s_waitcnt vmcnt(0)" : "+v"(junk) :: "memory"); }
            asm volatile("s_waitcnt vmcnt(0)" : "+v"(ep) :: "memory"); tag = EPOCH_TAG(ep); }
        if (threadIdx.x == 0) __hip_atomic_store(flagp(ws, FL_G + g), tag, RLX_AGENT);
        final_role(x, wo, wout, ws, out, lds, g, tag);
        if (g == 255 && threadIdx.x < 64) {
            const int lane = threadIdx.x;
#pragma unroll 1
            for (int c = 0; c < 4; ++c) { wait_flags(flagp(ws, FL_O + (c * 2 + (lane >> 5)) * 256 + (lane & 31)), true, tag); wait_flags(flagp(ws, FL_G + c * 64 + lane), true, tag); }
            if (lane == 0) __hip_atomic_store(flagp(ws, FL_EPOCH), tag, RLX_AGENT);
        }
        STAMP(9);
    }
}

extern "C" void kernel_launch(void* const* d_in, const int* in_sizes, int n_in, void* d_out, int out_size, void* d_ws, size_t ws_size, hipStream_t stream) {
    const uint16_t* x = (const uint16_t*)d_in[0]; const uint16_t* wqkv = (const uint16_t*)d_in[1]; const uint16_t* wo = (const uint16_t*)d_in[2];
    const uint16_t* win = (const uint16_t*)d_in[4]; const uint16_t* wsp = (const uint16_t*)d_in[5]; const uint16_t* bsp = (const uint16_t*)d_in[6]; const uint16_t* wout = (const uint16_t*)d_in[7];
    (void)hipFuncSetAttribute((const void*)k_all, hipFuncAttributeMaxDynamicSharedMemorySize, ALL_LDS);
    char* wsb = (char*)d_ws;
    k_all<<<512, 512, ALL_LDS, stream>>>(x, wqkv, wo, win, wsp, bsp, wout, wsb, (uint16_t*)d_out);
}
```

```cpp
#include <hip/hip_runtime.h>
#include <hip/hip_bf16.h>
#include <stdint.h>
#define WARM_IN 1
#define CODE_LINES 320
#define WDELAY 500
#include <stdio.h>

typedef short bf16x8 __attribute__((ext_vector_type(8)));
typedef short s16x4 __attribute__((ext_vector_type(4)));
typedef __bf16 bf16x2_t __attribute__((ext_vector_type(2)));
typedef float f32x16 __attribute__((ext_vector_type(16)));
typedef float f32x4 __attribute__((ext_vector_type(4)));
typedef float f32x2 __attribute__((ext_vector_type(2)));
typedef unsigned u32x4 __attribute__((ext_vector_type(4)));
typedef unsigned u32x2 __attribute__((ext_vector_type(2)));
typedef unsigned long long u64;
#define GAS __attribute__((address_space(1)))
#define LAS __attribute__((address_space(3)))
typedef LAS const char* lds_cptr;
typedef short v4i16_t __attribute__((ext_vector_type(4)));
#define RLX_AGENT __ATOMIC_RELAXED, __HIP_MEMORY_SCOPE_AGENT

constexpr int SEQ = 2048, DM = 512, NQKV = 1536, NIN = 1792, FH = 896;
constexpr float QSCALE = 0.25f * 1.4426950408889634f;
constexpr float LN_EPS = 1e-5f;
constexpr size_t MB = 1024 * 1024;
constexpr size_t WS_QF = 0, WS_KV = 2 * MB, WS_Z1 = 6 * MB, WS_Z2N = 10 * MB, WS_U = 14 * MB, WS_OF = 18 * MB;
constexpr size_t WS_KM = 20 * MB, WS_ST = 20 * MB + 65536, WS_RS = 20 * MB + 65536 + 229376, WS_CS = 20 * MB + 65536 + 245760  , WS_FLAG = 21 * MB;
constexpr int FL_RS = 0, FL_CS = 64, FL_QKV = 128, FL_ST = 320, FL_Z = 432, FL_U = 656, FL_G = 880, FL_EPOCH = 1136, FL_O = 1152;

#define MFMA(a, b, c) __builtin_amdgcn_mfma_f32_32x32x16_bf16(a, b, c, 0, 0, 0)
#define SBAR() __builtin_amdgcn_sched_barrier(0)
__device__ __forceinline__ unsigned cvtpk(float lo, float hi) { unsigned r; asm("v_cvt_pk_bf16_f32 %0, %1, %2" : "=v"(r) : "v"(lo), "v"(hi)); return r; }
__device__ __forceinline__ float bflo(unsigned u) { return __uint_as_float(u << 16); }
__device__ __forceinline__ float bfhi(unsigned u) { return __uint_as_float(u & 0xffff0000u); }
__device__ __forceinline__ float dot2(unsigned a, unsigned b, float acc) {
    return __builtin_amdgcn_fdot2_f32_bf16(__builtin_bit_cast(bf16x2_t, a), __builtin_bit_cast(bf16x2_t, b), acc, false); }
__device__ __forceinline__ void glds16(const void* g, unsigned lds_base) {
    asm volatile("s_mov_b32 m0, %1\n\ts_nop 0\n\tglobal_load_lds_dwordx4 %0, off" :: "v"(g), "s"(lds_base) : "memory", "m0"); }
template <int IMM, bool WT> __device__ __forceinline__ void glds_s(const void* sbase, unsigned voff) {
    if (WT) asm volatile("global_load_lds_dwordx4 %0, %1 offset:%2 sc1" :: "v"(voff), "s"(sbase), "n"(IMM) : "memory");
    else asm volatile("global_load_lds_dwordx4 %0, %1 offset:%2" :: "v"(voff), "s"(sbase), "n"(IMM) : "memory"); }
__device__ __forceinline__ void set_m0(unsigned v) { asm volatile("s_mov_b32 m0, %0\n\ts_nop 0" :: "s"(v) : "memory", "m0"); }
__device__ __forceinline__ s16x4 trread(lds_cptr p) { return __builtin_bit_cast(s16x4, __builtin_amdgcn_ds_read_tr16_b64_v4i16((LAS v4i16_t*)p)); }
__device__ __forceinline__ bf16x8 ldsfrag(lds_cptr p) { return *(LAS const bf16x8*)p; }
__device__ __forceinline__ bf16x8 cat(s16x4 a, s16x4 b) { return (bf16x8){a[0], a[1], a[2], a[3], b[0], b[1], b[2], b[3]}; }
__device__ __forceinline__ float swap32_add(float v) { auto rr = __builtin_amdgcn_permlane32_swap(__float_as_uint(v), __float_as_uint(v), false, false); return __uint_as_float(rr[0]) + __uint_as_float(rr[1]); }
__device__ __forceinline__ float swap32_max(float v) { auto rr = __builtin_amdgcn_permlane32_swap(__float_as_uint(v), __float_as_uint(v), false, false); return fmaxf(__uint_as_float(rr[0]), __uint_as_float(rr[1])); }
template <int CTRL> __device__ __forceinline__ float dppf(float v) { return __int_as_float(__builtin_amdgcn_update_dpp(0, __float_as_int(v), CTRL, 0xf, 0xf, false)); }
__device__ __forceinline__ float row16_add(float v) { v += dppf<0x128>(v); v += dppf<0x124>(v); v += dppf<0x122>(v); v += dppf<0x121>(v); return v; }
__device__ __forceinline__ float row16_max(float v) { v = fmaxf(v, dppf<0x128>(v)); v = fmaxf(v, dppf<0x124>(v)); v = fmaxf(v, dppf<0x122>(v)); v = fmaxf(v, dppf<0x121>(v)); return v; }
__device__ __forceinline__ float swap16_max(float v) { auto rr = __builtin_amdgcn_permlane16_swap(__float_as_uint(v), __float_as_uint(v), false, false); return fmaxf(__uint_as_float(rr[0]), __uint_as_float(rr[1])); }
__device__ __forceinline__ float gelu_t(float x) {
    const float c1 = -2.3022082f, c2 = -0.10294324f;
    float e = __builtin_amdgcn_exp2f(x * __builtin_fmaf(x * x, c2, c1));
    return x * __builtin_amdgcn_rcpf(1.0f + e); }
__device__ __forceinline__ float silu_f(float x) { return x * __builtin_amdgcn_rcpf(1.0f + __builtin_amdgcn_exp2f(-1.4426950408889634f * x)); }
#define STAMP(slot) do {} while (0)
#define PF_DIST 9
#define PACE_N 2
#define PACE() do { asm volatile("s_nop %0" :: "n"(PACE_N)); SBAR(); } while (0)
#define POLL_SLEEP 2
#define WAITV(n) asm volatile("s_waitcnt vmcnt(" #n ")" ::: "memory")
#define WAIT_BAR(n) asm volatile("s_waitcnt vmcnt(" #n ")\n\ts_barrier" ::: "memory")
#define BAR() asm volatile("s_barrier" ::: "memory")
#define LGKM_BAR() do { __builtin_amdgcn_s_waitcnt(0xC07F); asm volatile("" ::: "memory"); __builtin_amdgcn_s_barrier(); asm volatile("" ::: "memory"); } while (0)
__device__ __forceinline__ unsigned wsw(unsigned row) { return ((row & 3) << 2) | ((row >> 2) & 3); }
__device__ __forceinline__ void st16_wt(void* p, u32x4 v) {
    asm volatile("global_store_dwordx4 %0, %1, off sc1\n\ts_nop 1" :: "v"(p), "v"(v)); }
__device__ __forceinline__ void st8_wt(void* p, u64 v) { __hip_atomic_store((GAS u64*)p, v, RLX_AGENT); }
__device__ __forceinline__ u64 ld8_wt(const void* p) { return __hip_atomic_load((GAS u64*)p, RLX_AGENT); }
__device__ __forceinline__ u32x4 ld16_wt(const void* p) { u32x4 v; asm volatile("global_load_dwordx4 %0, %1, off sc1\n\ts_waitcnt vmcnt(0)" : "=&v"(v) : "v"(p) : "memory"); return v; }
__device__ __forceinline__ void glds16_wt(const void* g, unsigned lds_base) {
    asm volatile("s_mov_b32 m0, %1\n\ts_nop 0\n\tglobal_load_lds_dwordx4 %0, off sc1" :: "v"(g), "s"(lds_base) : "memory", "m0"); }
__device__ __forceinline__ GAS u64* flagp(char* ws, int idx) { return (GAS u64*)(ws + WS_FLAG) + idx * 16; }
__device__ __forceinline__ void wait_flags(GAS u64* f, bool want, u64 tag) {
    for (unsigned spins = 0; spins < (1u << 20); ++spins) {
        const u64 v = want ? __hip_atomic_load(f, RLX_AGENT) : tag;
        if (__all(v == tag)) return;
        __builtin_amdgcn_s_sleep(POLL_SLEEP); } }
#define ACQUIRE() do { __builtin_amdgcn_fence(__ATOMIC_ACQUIRE, "agent"); asm volatile("s_waitcnt vmcnt(0)" ::: "memory"); } while (0)
#define EPOCH_REQ(ep) asm volatile("global_load_dwordx2 %0, %1, off sc1" : "=v"(ep) : "v"(flagp(ws, FL_EPOCH)) : "memory")
#define EPOCH_TAG(ep) ((((u64)(unsigned)__builtin_amdgcn_readfirstlane((unsigned)((ep) >> 32)) << 32) | (unsigned)__builtin_amdgcn_readfirstlane((unsigned)(ep))) + 1)
#define PUBLISH(idx) do { WAITV(0); __syncthreads(); if (threadIdx.x == 0) __hip_atomic_store(flagp(ws, idx), tag, RLX_AGENT); } while (0)

__device__ __forceinline__ void stats_role(const uint16_t* __restrict__ x, const uint16_t* __restrict__ wqkv, const uint16_t* __restrict__ win, char* __restrict__ ws, char* lds, int j, u64& tag) {
    u64 ep; EPOCH_REQ(ep);
    const int tid = threadIdx.x, lane = tid & 63, wid = tid >> 6, rr = lane >> 4, cc = lane & 15;
    const unsigned ONES = 0x3f803f80u;
    u32x4 xv[4], w[8];
    const int row = 32 * j + 4 * wid + rr;
#pragma unroll
    for (int i = 0; i < 4; ++i) xv[i] = *(const u32x4*)(x + (size_t)row * DM + i * 128 + cc * 8);
    const int cbk = j >> 1;
    if (j < 52) {
        const uint16_t* W = cbk < 12 ? wqkv + cbk * 128 : win + (cbk - 12) * 128; const int ldw = cbk < 12 ? NQKV : NIN;
        const uint16_t* p = W + (size_t)((j & 1) * 256 + 32 * wid + rr) * ldw + cc * 8;
#pragma unroll
        for (int i = 0; i < 8; ++i) w[i] = *(const u32x4*)(p + (size_t)(i * 4) * ldw);
    }
    asm volatile("s_waitcnt vmcnt(0)" : "+v"(ep) :: "memory");
    tag = EPOCH_TAG(ep);
    float s1 = 0.f, s2 = 0.f;
#pragma unroll
    for (int i = 0; i < 4; ++i)
#pragma unroll
        for (int e = 0; e < 4; ++e) { s1 = dot2(xv[i][e], ONES, s1); s2 = dot2(xv[i][e], xv[i][e], s2); }
    s1 = row16_add(s1); s2 = row16_add(s2);
    { const float mu = s1 * (1.0f / DM), var = fmaxf(s2 * (1.0f / DM) - mu * mu, 0.f);
      if (cc == 0) st8_wt(ws + WS_RS + (size_t)row * 8, __builtin_bit_cast(u64, (f32x2){mu, __builtin_amdgcn_rsqf(var + LN_EPS)})); }
    if (j < 52) {
        float cs[8] = {0.f, 0.f, 0.f, 0.f, 0.f, 0.f, 0.f, 0.f};
        unsigned sel_lo = 0x00003f80u, sel_hi = 0x3f800000u;
        asm volatile("" : "+v"(sel_lo), "+v"(sel_hi));
#pragma unroll
        for (int i = 0; i < 8; ++i)
#pragma unroll
            for (int e = 0; e < 4; ++e) { cs[2 * e] = dot2(w[i][e], sel_lo, cs[2 * e]); cs[2 * e + 1] = dot2(w[i][e], sel_hi, cs[2 * e + 1]); }
        float* red = (float*)lds;
        *(f32x4*)(red + (wid * 4 + rr) * 128 + cc * 8) = (f32x4){cs[0], cs[1], cs[2], cs[3]}; *(f32x4*)(red + (wid * 4 + rr) * 128 + cc * 8 + 4) = (f32x4){cs[4], cs[5], cs[6], cs[7]};
        __syncthreads();
        if (tid < 64) { f32x2 a = {0.f, 0.f};
#pragma unroll
            for (int q = 0; q < 32; ++q) a += *(const f32x2*)(red + q * 128 + 2 * tid);
            st8_wt(ws + WS_CS + (size_t)((j & 1) * 3328 + cbk * 128 + 2 * tid) * 4, __builtin_bit_cast(u64, a)); }
    }
    STAMP(11);
    WAITV(0); __syncthreads();
    STAMP(1);
    if (tid == 0) { __hip_atomic_store(flagp(ws, FL_RS + j), tag, RLX_AGENT); if (j < 52) __hip_atomic_store(flagp(ws, FL_CS + j), tag, RLX_AGENT); }
}

constexpr int P_SLOT = 16384, P_ST = 4 * P_SLOT, P_LDS = 4 * P_SLOT + 4096;
struct PFrag { bf16x8 x[2], w; };
template <bool SWAP>
__device__ __forceinline__ void proj_main(const uint16_t* __restrict__ x, const uint16_t* __restrict__ W, int ldw, int rb, int col0, int cblk, char* __restrict__ ws, char* lds, int rot, u64& tag, f32x16 (&acc)[2]) {
    u64 ep; EPOCH_REQ(ep);
    const int tid = threadIdx.x, lane = tid & 63, wid = __builtin_amdgcn_readfirstlane(tid >> 6), wr = wid >> 2, wc = wid & 3, l31 = lane & 31, hh = lane >> 5;
    const unsigned lds0 = (unsigned)(uintptr_t)lds;
    const int xr = 16 * wid + (lane >> 2), wk = 4 * wid + (lane >> 4);
    const char* xb = (const char*)(x + (size_t)rb * 128 * DM); const char* wb = (const char*)(W + col0);
    const unsigned xvo = (xr * DM + ((lane & 3) ^ ((xr >> 2) & 3)) * 8) * 2, wvo = (wk * ldw + ((lane & 15) ^ wsw(wk)) * 8) * 2;
    const unsigned xd = lds0 + wid * 1024, wd = lds0 + 8192 + wid * 1024;
    auto issue1 = [&](int it, int which) { const unsigned so = (it & 3) * P_SLOT; const int kk = (it + rot) & 15;
        if (which == 0) { set_m0(xd + so); glds_s<0, false>(xb + kk * 64, xvo); } else { set_m0(wd + so); glds_s<0, false>(wb + (size_t)kk * 64 * ldw, wvo); } SBAR(); };
#pragma unroll
    for (int it = 0; it < 4; ++it) { issue1(it, 0); issue1(it, 1); }
    unsigned xoff[2][2], woff[2][2];
    const int blk = (lane >> 4) & 1, q = (lane & 15) >> 2, p = lane & 3;
#pragma unroll
    for (int ks = 0; ks < 2; ++ks) {
#pragma unroll
        for (int rt = 0; rt < 2; ++rt) { const int r = wr * 64 + rt * 32 + l31; xoff[rt][ks] = r * 64 + (((2 * ks + hh) ^ ((r >> 2) & 3)) * 16); }
        const int kr = 8 * hh + q, ch = 4 * wc + 2 * blk + (p >> 1);
        woff[0][ks] = 8192 + ks * 4096 + kr * 256 + ((ch ^ wsw(kr)) * 16) + 8 * (p & 1); woff[1][ks] = (woff[0][ks] + 1024) ^ 16; }
    auto rd = [&](PFrag& f, int slot, int j) { lds_cptr sb = (lds_cptr)lds + slot * P_SLOT;
        f.x[0] = ldsfrag(sb + xoff[0][j]); f.x[1] = ldsfrag(sb + xoff[1][j]); f.w = cat(trread(sb + woff[0][j]), trread(sb + woff[1][j])); };
    auto mm = [&](const PFrag& f) {
#pragma unroll
        for (int rt = 0; rt < 2; ++rt) acc[rt] = SWAP ? MFMA(f.x[rt], f.w, acc[rt]) : MFMA(f.w, f.x[rt], acc[rt]); };
    acc[0] = f32x16{}; acc[1] = f32x16{};
    PFrag fa[2], fb[2];
    asm volatile("s_waitcnt vmcnt(6)\n\ts_barrier" : "+v"(ep) :: "memory");
    tag = EPOCH_TAG(ep);
    STAMP(10);
    rd(fa[0], 0, 0); rd(fa[1], 0, 1);
    auto mm1 = [&](const PFrag& f, int rt) { acc[rt] = SWAP ? MFMA(f.x[rt], f.w, acc[rt]) : MFMA(f.w, f.x[rt], acc[rt]); SBAR(); };
    auto step = [&](int it, int sl, PFrag (&cur)[2], PFrag (&nxt)[2]) {
        __builtin_amdgcn_s_waitcnt(0xC07F);
        if (it < 13) WAITV(4); else if (it == 13) WAITV(2); else WAITV(0);
        __builtin_amdgcn_s_barrier(); asm volatile("" ::: "memory");
        lds_cptr sb = (lds_cptr)lds + ((sl + 1) & 3) * P_SLOT;
        mm1(cur[0], 0);
        nxt[0].x[0] = ldsfrag(sb + xoff[0][0]); nxt[0].x[1] = ldsfrag(sb + xoff[1][0]); SBAR();
        mm1(cur[0], 1);
        nxt[0].w = cat(trread(sb + woff[0][0]), trread(sb + woff[1][0])); SBAR();
        if (it + 4 < 16) issue1(it + 4, 0);
        mm1(cur[1], 0);
        nxt[1].x[0] = ldsfrag(sb + xoff[0][1]); nxt[1].x[1] = ldsfrag(sb + xoff[1][1]); SBAR();
        mm1(cur[1], 1);
        nxt[1].w = cat(trread(sb + woff[0][1]), trread(sb + woff[1][1])); SBAR();
        if (it + 4 < 16) issue1(it + 4, 1); };
#pragma unroll 1
    for (int io = 0; io < 2; ++io) { step(io * 4, 0, fa, fb); step(io * 4 + 1, 1, fb, fa); step(io * 4 + 2, 2, fa, fb); step(io * 4 + 3, 3, fb, fa); }
    const char* fa_ = (const char*)(lane < 4 ? flagp(ws, FL_RS + rb * 4 + lane) : flagp(ws, FL_CS + 2 * cblk + (lane & 1)));
    const char* sa_ = tid < 128 ? ws + WS_RS + (size_t)(rb * 128 + tid) * 8 : ws + WS_CS + (size_t)(cblk * 128 + 2 * (tid & 63)) * 4;
    const char* sb_ = ws + WS_CS + (size_t)(3328 + cblk * 128 + 2 * (tid & 63)) * 4;
    u64 fl, stv, stw;
    step(8, 0, fa, fb); step(9, 1, fb, fa);
    asm volatile("global_load_dwordx2 %0, %1, off sc1" : "=v"(fl) : "v"(fa_) : "memory");
    step(10, 2, fa, fb); step(11, 3, fb, fa); step(12, 0, fa, fb);
    asm volatile("" : "+v"(fl));
    const bool got = __all(lane >= 6 || fl == tag);
    asm volatile("global_load_dwordx2 %0, %2, off sc1\n\tglobal_load_dwordx2 %1, %3, off sc1" : "=&v"(stv), "=&v"(stw) : "v"(sa_), "v"(sb_) : "memory");
    step(13, 1, fb, fa); step(14, 2, fa, fb); step(15, 3, fb, fa);
    asm volatile("" : "+v"(stv), "+v"(stw));
    if (!got) { wait_flags((GAS u64*)fa_, lane < 6, tag); stv = ld8_wt(sa_); stw = ld8_wt(sb_); }
    STAMP(11);
    float* st = (float*)(lds + P_ST);
    { const f32x2 v = __builtin_bit_cast(f32x2, stv);
      if (tid < 128) { st[tid] = v[0]; st[128 + tid] = v[1]; } else if (tid < 192) *(f32x2*)(st + 256 + 2 * (tid - 128)) = v + __builtin_bit_cast(f32x2, stw); }
    __syncthreads();
}

__device__ __forceinline__ void qkv_role(const uint16_t* __restrict__ x, const uint16_t* __restrict__ wqkv, char* __restrict__ ws, char* lds, int b, u64& tag) {
    const int xcd = b & 7, i = b >> 3;
    const int rb = 4 * (xcd >> 1) + (i & 3), cb = 6 * (xcd & 1) + (i >> 2);
    const int tid = threadIdx.x, lane = tid & 63, wid = __builtin_amdgcn_readfirstlane(tid >> 6), wr = wid >> 2, wc = wid & 3, l31 = lane & 31, hh = lane >> 5;
    f32x16 acc[2];
    const int rot = ((i & 3) * 4 + (i >> 2) * 3) & 15;
    if (cb >= 8) proj_main<true>(x, wqkv, NQKV, rb, cb * 128, cb, ws, lds, rot, tag, acc); else proj_main<false>(x, wqkv, NQKV, rb, cb * 128, cb, ws, lds, rot, tag, acc);
    STAMP(1);
    const float* st = (const float*)(lds + P_ST);
    if (cb >= 8) {
        const float cs = st[256 + wc * 32 + l31];
        const int head = (cb - 8) * 8 + wc * 2 + (l31 >> 4);
#pragma unroll
        for (int rt = 0; rt < 2; ++rt) { const int tr = wr * 64 + rt * 32;
            unsigned pk[8];
#pragma unroll
            for (int q = 0; q < 4; ++q) { const f32x4 m = *(const f32x4*)(st + tr + 8 * q + 4 * hh), r = *(const f32x4*)(st + 128 + tr + 8 * q + 4 * hh);
                pk[2 * q] = cvtpk((acc[rt][4 * q] - m[0] * cs) * r[0], (acc[rt][4 * q + 1] - m[1] * cs) * r[1]);
                pk[2 * q + 1] = cvtpk((acc[rt][4 * q + 2] - m[2] * cs) * r[2], (acc[rt][4 * q + 3] - m[3] * cs) * r[3]); }
            const int tile = rb * 4 + (tr >> 5);
            char* dst = ws + WS_KV + (size_t)head * 131072 + (tile >> 3) * 16384 + 8192 + (tile & 7) * 1024 + hh * 256 + (l31 & 15) * 16;
            st16_wt(dst, (u32x4){pk[0], pk[1], pk[2], pk[3]}); st16_wt(dst + 512, (u32x4){pk[4], pk[5], pk[6], pk[7]}); }
    } else {
        const bool isK = cb >= 4; const float sc = isK ? 1.0f : QSCALE;
        float kmx[2] = {0.f, 0.f};
#pragma unroll
        for (int rt = 0; rt < 2; ++rt) { const int tr = wr * 64 + rt * 32, tile = rb * 4 + (tr >> 5);
            const float mu = st[tr + l31], rs = st[128 + tr + l31] * sc;
#pragma unroll
            for (int hb = 0; hb < 2; ++hb) {
                float v[8];
#pragma unroll
                for (int q = 0; q < 2; ++q) { const f32x4 c = *(const f32x4*)(st + 256 + wc * 32 + 16 * hb + 8 * q + 4 * hh);
#pragma unroll
                    for (int e = 0; e < 4; ++e) v[4 * q + e] = (acc[rt][8 * hb + 4 * q + e] - mu * c[e]) * rs; }
                u32x4 pk;
#pragma unroll
                for (int e = 0; e < 4; ++e) pk[e] = cvtpk(v[2 * e], v[2 * e + 1]);
                const int head = (cb & 3) * 8 + wc * 2 + hb;
                char* dst = isK ? ws + WS_KV + (size_t)head * 131072 + (tile >> 3) * 16384 + (tile & 7) * 1024 + lane * 16 : ws + WS_QF + (size_t)head * 65536 + tile * 1024 + lane * 16;
                st16_wt(dst, pk);
                float n2 = 0.f;
#pragma unroll
                for (int e = 0; e < 4; ++e) n2 = dot2(pk[e], pk[e], n2);
                kmx[hb] = fmaxf(kmx[hb], swap32_add(n2));
            } }
        if (isK) {
#pragma unroll
            for (int hb = 0; hb < 2; ++hb) { const float m = swap16_max(row16_max(kmx[hb]));
                if (lane == 0) __hip_atomic_store((GAS unsigned*)(ws + WS_KM) + ((cb & 3) * 8 + wc * 2 + hb) * 32 + rb * 2 + wr, __float_as_uint(m), RLX_AGENT); } }
    }
    STAMP(12);
    PUBLISH(FL_QKV + cb * 16 + rb);
    STAMP(13);
}

constexpr int A_ONES = 3 * 16384, A_LDS = A_ONES + 8192 + 512;
__device__ __forceinline__ void attn_role(char* __restrict__ ws, char* lds, int b, u64 tag) {
    const int tid = threadIdx.x, lane = tid & 63, wid = __builtin_amdgcn_readfirstlane(tid >> 6);
    const int head = (b & 7) * 4 + ((b >> 3) & 3), jb = b >> 5;
    const unsigned lds0 = (unsigned)(uintptr_t)lds;
    const char* kvb = ws + WS_KV + (size_t)head * 131072;
    if (wid == 0) {
        const int hb = head >> 3;
        const int idx = lane < 16 ? (4 + hb) * 16 + lane : lane < 32 ? (8 + hb) * 16 + (lane - 16) : hb * 16 + 2 * jb + (lane & 1);
        wait_flags(flagp(ws, FL_QKV + idx), lane < 34, tag);
        STAMP(14);
        }
    __syncthreads();
    STAMP(2);
    unsigned goff = wid * 2048 + lane * 16;
    const int tile0 = jb * 8 + wid;
    u32x4 qa; unsigned kmu;
    asm volatile("global_load_dwordx4 %0, %2, off sc1\n\tglobal_load_dword %1, %3, off sc1" : "=&v"(qa), "=&v"(kmu)
                 : "v"(ws + WS_QF + (size_t)head * 65536 + tile0 * 1024 + lane * 16), "v"(ws + WS_KM + (size_t)(head * 32 + (lane & 31)) * 4) : "memory");
#pragma unroll
    for (int c = 0; c < 2; ++c) { set_m0(lds0 + c * 16384 + wid * 2048); glds_s<0, true>(kvb + c * 16384, goff); glds_s<1024, true>(kvb + c * 16384, goff); }
    asm volatile("s_waitcnt vmcnt(4)" : "+v"(qa), "+v"(kmu) :: "memory");
    const float km = swap16_max(row16_max(__uint_as_float(kmu)));
    float na = 0.f;
#pragma unroll
    for (int e = 0; e < 4; ++e) na = dot2(qa[e], qa[e], na);
    na = swap32_add(na);
    float ma = __builtin_amdgcn_sqrtf(na * km);
    if (__any(!(ma <= 40.0f))) {
        float xa = -3.0e38f;
        for (int t = 0; t < 64; ++t) {
            const bf16x8 kf = __builtin_bit_cast(bf16x8, ld16_wt(kvb + (t >> 3) * 16384 + (t & 7) * 1024 + lane * 16));
            const f32x16 sa = MFMA(kf, __builtin_bit_cast(bf16x8, qa), f32x16{});
#pragma unroll
            for (int r = 0; r < 16; ++r) xa = fmaxf(xa, sa[r]); }
        ma = swap32_max(xa);
    }
    const float nma = -ma;
    const unsigned kaddr = lds0 + lane * 16;
    const bool ones = (lane & 16) != 0;
    const unsigned vaddr = ones ? lds0 + A_ONES + (lane & 15) * 16 : lds0 + 8192 + (lane >> 5) * 256 + (lane & 15) * 16;
    const unsigned vsel = ones ? 0u : 0xffffffffu;
    goff += 2 * 16384;
    unsigned dbeg = lds0 + wid * 2048, dend = dbeg + 3 * 16384, dslot = dbeg + 2 * 16384, nslot = 0x4000, cnt, m0save;
    unsigned oa0, oa1, oa2, oa3;
    STAMP(3);
    asm volatile(
    "s_mov_b32 %[m0save], m0\n"
    "v_mov_b32 v96, %[qa0]\n"
    "v_mov_b32 v97, %[qa1]\n"
    "v_mov_b32 v98, %[qa2]\n"
    "v_mov_b32 v99, %[qa3]\n"
    "v_mov_b32 v72, %[nma]\n"
    "v_mov_b32 v56, 0\n"
    "v_mov_b32 v73, %[nma]\n"
    "v_mov_b32 v57, 0\n"
    "v_mov_b32 v74, %[nma]\n"
    "v_mov_b32 v58, 0\n"
    "v_mov_b32 v75, %[nma]\n"
    "v_mov_b32 v59, 0\n"
    "v_mov_b32 v76, %[nma]\n"
    "v_mov_b32 v60, 0\n"
    "v_mov_b32 v77, %[nma]\n"
    "v_mov_b32 v61, 0\n"
    "v_mov_b32 v78, %[nma]\n"
    "v_mov_b32 v62, 0\n"
    "v_mov_b32 v79, %[nma]\n"
    "v_mov_b32 v63, 0\n"
    "v_mov_b32 v80, %[nma]\n"
    "v_mov_b32 v64, 0\n"
    "v_mov_b32 v81, %[nma]\n"
    "v_mov_b32 v65, 0\n"
    "v_mov_b32 v82, %[nma]\n"
    "v_mov_b32 v66, 0\n"
    "v_mov_b32 v83, %[nma]\n"
    "v_mov_b32 v67, 0\n"
    "v_mov_b32 v84, %[nma]\n"
    "v_mov_b32 v68, 0\n"
    "v_mov_b32 v85, %[nma]\n"
    "v_mov_b32 v69, 0\n"
    "v_mov_b32 v86, %[nma]\n"
    "v_mov_b32 v70, 0\n"
    "v_mov_b32 v87, %[nma]\n"
    "v_mov_b32 v71, 0\n"
    "v_mov_b32 v120, 0\n"
    "v_mov_b32 v121, 0\n"
    "v_mov_b32 v122, 0\n"
    "v_mov_b32 v123, 0\n"
    "v_mov_b32 v88, 0\n"
    "v_mov_b32 v89, 0\n"
    "v_mov_b32 v90, 0\n"
    "v_mov_b32 v91, 0\n"
    "v_mov_b32 v92, 0\n"
    "v_mov_b32 v93, 0\n"
    "v_mov_b32 v94, 0\n"
    "v_mov_b32 v95, 0\n"
    "v_mov_b32 v124, %[kaddr]\n"
    "v_mov_b32 v125, %[vaddr]\n"
    "v_add_u32 v126, 0x4000, v124\n"
    "v_and_b32 v127, 0x4000, %[vsel]\n"
    "v_add_u32 v127, v127, v125\n"
    "s_waitcnt vmcnt(2) lgkmcnt(0)\n"
    "s_barrier\n"
    "ds_read_b128 v[100:103], v124\n"
    "ds_read_b128 v[104:107], v124 offset:1024\n"
    "ds_read_b128 v[108:111], v125\n"
    "ds_read_b128 v[112:115], v125 offset:512\n"
    "s_mov_b32 %[cnt], 0\n"
    "s_waitcnt lgkmcnt(0)\n"
    "v_mfma_f32_32x32x16_bf16 v[24:39], v[100:103], v[96:99], v[72:87]\n"
    "s_nop 15\n"
    ".Lloop%=:\n"
    "v_exp_f32 v24, v24\n"
    "v_exp_f32 v25, v25\n"
    "v_mfma_f32_32x32x16_bf16 v[56:71], v[120:123], v[92:95], v[56:71]\n"
    "v_exp_f32 v26, v26\n"
    "v_exp_f32 v27, v27\n"
    "v_cvt_pk_bf16_f32 v88, v24, v25\n"
    "v_exp_f32 v28, v28\n"
    "s_waitcnt lgkmcnt(0)\n"
    "v_mfma_f32_32x32x16_bf16 v[40:55], v[104:107], v[96:99], v[72:87]\n"
    "v_exp_f32 v29, v29\n"
    "v_cvt_pk_bf16_f32 v89, v26, v27\n"
    "ds_read_b128 v[100:103], v124 offset:2048\n"
    "v_exp_f32 v30, v30\n"
    "ds_read_b128 v[116:119], v125 offset:1024\n"
    "v_exp_f32 v31, v31\n"
    "v_cvt_pk_bf16_f32 v90, v28, v29\n"
    "ds_read_b128 v[120:123], v125 offset:1536\n"
    "v_exp_f32 v32, v32\n"
    "v_exp_f32 v33, v33\n"
    "v_cvt_pk_bf16_f32 v91, v30, v31\n"
    "v_exp_f32 v34, v34\n"
    "v_exp_f32 v35, v35\n"
    "v_mfma_f32_32x32x16_bf16 v[56:71], v[108:111], v[88:91], v[56:71]\n"
    "v_cvt_pk_bf16_f32 v92, v32, v33\n"
    "v_exp_f32 v36, v36\n"
    "v_exp_f32 v37, v37\n"
    "v_cvt_pk_bf16_f32 v93, v34, v35\n"
    "v_exp_f32 v38, v38\n"
    "v_exp_f32 v39, v39\n"
    "v_cvt_pk_bf16_f32 v94, v36, v37\n"
    "v_cvt_pk_bf16_f32 v95, v38, v39\n"
    "v_exp_f32 v40, v40\n"
    "v_exp_f32 v41, v41\n"
    "v_mfma_f32_32x32x16_bf16 v[56:71], v[112:115], v[92:95], v[56:71]\n"
    "v_exp_f32 v42, v42\n"
    "v_exp_f32 v43, v43\n"
    "v_cvt_pk_bf16_f32 v88, v40, v41\n"
    "v_exp_f32 v44, v44\n"
    "s_waitcnt lgkmcnt(0)\n"
    "v_mfma_f32_32x32x16_bf16 v[24:39], v[100:103], v[96:99], v[72:87]\n"
    "v_exp_f32 v45, v45\n"
    "v_cvt_pk_bf16_f32 v89, v42, v43\n"
    "ds_read_b128 v[104:107], v124 offset:3072\n"
    "v_exp_f32 v46, v46\n"
    "ds_read_b128 v[108:111], v125 offset:2048\n"
    "v_exp_f32 v47, v47\n"
    "v_cvt_pk_bf16_f32 v90, v44, v45\n"
    "ds_read_b128 v[112:115], v125 offset:2560\n"
    "v_exp_f32 v48, v48\n"
    "v_exp_f32 v49, v49\n"
    "v_cvt_pk_bf16_f32 v91, v46, v47\n"
    "v_exp_f32 v50, v50\n"
    "v_exp_f32 v51, v51\n"
    "v_mfma_f32_32x32x16_bf16 v[56:71], v[116:119], v[88:91], v[56:71]\n"
    "v_cvt_pk_bf16_f32 v92, v48, v49\n"
    "v_exp_f32 v52, v52\n"
    "v_exp_f32 v53, v53\n"
    "v_cvt_pk_bf16_f32 v93, v50, v51\n"
    "v_exp_f32 v54, v54\n"
    "v_exp_f32 v55, v55\n"
    "v_cvt_pk_bf16_f32 v94, v52, v53\n"
    "v_cvt_pk_bf16_f32 v95, v54, v55\n"
    "v_exp_f32 v24, v24\n"
    "v_exp_f32 v25, v25\n"
    "v_mfma_f32_32x32x16_bf16 v[56:71], v[120:123], v[92:95], v[56:71]\n"
    "v_exp_f32 v26, v26\n"
    "v_exp_f32 v27, v27\n"
    "v_cvt_pk_bf16_f32 v88, v24, v25\n"
    "v_exp_f32 v28, v28\n"
    "s_waitcnt lgkmcnt(0)\n"
    "v_mfma_f32_32x32x16_bf16 v[40:55], v[104:107], v[96:99], v[72:87]\n"
    "v_exp_f32 v29, v29\n"
    "v_cvt_pk_bf16_f32 v89, v26, v27\n"
    "ds_read_b128 v[100:103], v124 offset:4096\n"
    "v_exp_f32 v30, v30\n"
    "ds_read_b128 v[116:119], v125 offset:3072\n"
    "v_exp_f32 v31, v31\n"
    "v_cvt_pk_bf16_f32 v90, v28, v29\n"
    "ds_read_b128 v[120:123], v125 offset:3584\n"
    "v_exp_f32 v32, v32\n"
    "v_exp_f32 v33, v33\n"
    "v_cvt_pk_bf16_f32 v91, v30, v31\n"
    "v_exp_f32 v34, v34\n"
    "v_exp_f32 v35, v35\n"
    "v_mfma_f32_32x32x16_bf16 v[56:71], v[108:111], v[88:91], v[56:71]\n"
    "v_cvt_pk_bf16_f32 v92, v32, v33\n"
    "v_exp_f32 v36, v36\n"
    "v_exp_f32 v37, v37\n"
    "v_cvt_pk_bf16_f32 v93, v34, v35\n"
    "v_exp_f32 v38, v38\n"
    "v_exp_f32 v39, v39\n"
    "v_cvt_pk_bf16_f32 v94, v36, v37\n"
    "v_cvt_pk_bf16_f32 v95, v38, v39\n"
    "v_exp_f32 v40, v40\n"
    "v_exp_f32 v41, v41\n"
    "v_mfma_f32_32x32x16_bf16 v[56:71], v[112:115], v[92:95], v[56:71]\n"
    "v_exp_f32 v42, v42\n"
    "v_exp_f32 v43, v43\n"
    "v_cvt_pk_bf16_f32 v88, v40, v41\n"
    "v_exp_f32 v44, v44\n"
    "s_waitcnt lgkmcnt(0)\n"
    "v_mfma_f32_32x32x16_bf16 v[24:39], v[100:103], v[96:99], v[72:87]\n"
    "v_exp_f32 v45, v45\n"
    "v_cvt_pk_bf16_f32 v89, v42, v43\n"
    "ds_read_b128 v[104:107], v124 offset:5120\n"
    "v_exp_f32 v46, v46\n"
    "ds_read_b128 v[108:111], v125 offset:4096\n"
    "v_exp_f32 v47, v47\n"
    "v_cvt_pk_bf16_f32 v90, v44, v45\n"
    "ds_read_b128 v[112:115], v125 offset:4608\n"
    "v_exp_f32 v48, v48\n"
    "v_exp_f32 v49, v49\n"
    "v_cvt_pk_bf16_f32 v91, v46, v47\n"
    "v_exp_f32 v50, v50\n"
    "v_exp_f32 v51, v51\n"
    "v_mfma_f32_32x32x16_bf16 v[56:71], v[116:119], v[88:91], v[56:71]\n"
    "v_cvt_pk_bf16_f32 v92, v48, v49\n"
    "v_exp_f32 v52, v52\n"
    "v_exp_f32 v53, v53\n"
    "v_cvt_pk_bf16_f32 v93, v50, v51\n"
    "v_exp_f32 v54, v54\n"
    "v_exp_f32 v55, v55\n"
    "v_cvt_pk_bf16_f32 v94, v52, v53\n"
    "v_cvt_pk_bf16_f32 v95, v54, v55\n"
    "v_exp_f32 v24, v24\n"
    "v_exp_f32 v25, v25\n"
    "v_mfma_f32_32x32x16_bf16 v[56:71], v[120:123], v[92:95], v[56:71]\n"
    "v_exp_f32 v26, v26\n"
    "v_exp_f32 v27, v27\n"
    "v_cvt_pk_bf16_f32 v88, v24, v25\n"
    "v_exp_f32 v28, v28\n"
    "s_waitcnt lgkmcnt(0)\n"
    "v_mfma_f32_32x32x16_bf16 v[40:55], v[104:107], v[96:99], v[72:87]\n"
    "v_exp_f32 v29, v29\n"
    "v_cvt_pk_bf16_f32 v89, v26, v27\n"
    "ds_read_b128 v[100:103], v124 offset:6144\n"
    "v_exp_f32 v30, v30\n"
    "ds_read_b128 v[116:119], v125 offset:5120\n"
    "v_exp_f32 v31, v31\n"
    "v_cvt_pk_bf16_f32 v90, v28, v29\n"
    "ds_read_b128 v[120:123], v125 offset:5632\n"
    "v_exp_f32 v32, v32\n"
    "v_exp_f32 v33, v33\n"
    "v_cvt_pk_bf16_f32 v91, v30, v31\n"
    "v_exp_f32 v34, v34\n"
    "v_exp_f32 v35, v35\n"
    "v_mfma_f32_32x32x16_bf16 v[56:71], v[108:111], v[88:91], v[56:71]\n"
    "v_cvt_pk_bf16_f32 v92, v32, v33\n"
    "v_exp_f32 v36, v36\n"
    "v_exp_f32 v37, v37\n"
    "v_cvt_pk_bf16_f32 v93, v34, v35\n"
    "v_exp_f32 v38, v38\n"
    "v_exp_f32 v39, v39\n"
    "v_cvt_pk_bf16_f32 v94, v36, v37\n"
    "v_cvt_pk_bf16_f32 v95, v38, v39\n"
    "v_exp_f32 v40, v40\n"
    "v_exp_f32 v41, v41\n"
    "v_mfma_f32_32x32x16_bf16 v[56:71], v[112:115], v[92:95], v[56:71]\n"
    "v_exp_f32 v42, v42\n"
    "v_exp_f32 v43, v43\n"
    "v_cvt_pk_bf16_f32 v88, v40, v41\n"
    "v_exp_f32 v44, v44\n"
    "s_waitcnt lgkmcnt(0)\n"
    "v_mfma_f32_32x32x16_bf16 v[24:39], v[100:103], v[96:99], v[72:87]\n"
    "v_exp_f32 v45, v45\n"
    "v_cvt_pk_bf16_f32 v89, v42, v43\n"
    "ds_read_b128 v[104:107], v124 offset:7168\n"
    "v_exp_f32 v46, v46\n"
    "ds_read_b128 v[108:111], v125 offset:6144\n"
    "v_exp_f32 v47, v47\n"
    "v_cvt_pk_bf16_f32 v90, v44, v45\n"
    "ds_read_b128 v[112:115], v125 offset:6656\n"
    "v_exp_f32 v48, v48\n"
    "v_exp_f32 v49, v49\n"
    "v_cvt_pk_bf16_f32 v91, v46, v47\n"
    "v_exp_f32 v50, v50\n"
    "v_exp_f32 v51, v51\n"
    "v_mfma_f32_32x32x16_bf16 v[56:71], v[116:119], v[88:91], v[56:71]\n"
    "v_cvt_pk_bf16_f32 v92, v48, v49\n"
    "v_exp_f32 v52, v52\n"
    "v_exp_f32 v53, v53\n"
    "v_cvt_pk_bf16_f32 v93, v50, v51\n"
    "v_exp_f32 v54, v54\n"
    "v_exp_f32 v55, v55\n"
    "v_cvt_pk_bf16_f32 v94, v52, v53\n"
    "v_cvt_pk_bf16_f32 v95, v54, v55\n"
    "v_exp_f32 v24, v24\n"
    "v_exp_f32 v25, v25\n"
    "v_mfma_f32_32x32x16_bf16 v[56:71], v[120:123], v[92:95], v[56:71]\n"
    "v_exp_f32 v26, v26\n"
    "v_exp_f32 v27, v27\n"
    "v_cvt_pk_bf16_f32 v88, v24, v25\n"
    "v_exp_f32 v28, v28\n"
    "s_waitcnt lgkmcnt(0)\n"
    "v_mfma_f32_32x32x16_bf16 v[40:55], v[104:107], v[96:99], v[72:87]\n"
    "v_exp_f32 v29, v29\n"
    "v_cvt_pk_bf16_f32 v89, v26, v27\n"
    "s_waitcnt vmcnt(0)\n"
    "s_barrier\n"
    "ds_read_b128 v[100:103], v126 offset:0\n"
    "v_exp_f32 v30, v30\n"
    "ds_read_b128 v[116:119], v125 offset:7168\n"
    "v_exp_f32 v31, v31\n"
    "v_cvt_pk_bf16_f32 v90, v28, v29\n"
    "ds_read_b128 v[120:123], v125 offset:7680\n"
    "v_exp_f32 v32, v32\n"
    "v_exp_f32 v33, v33\n"
    "v_cvt_pk_bf16_f32 v91, v30, v31\n"
    "v_exp_f32 v34, v34\n"
    "v_exp_f32 v35, v35\n"
    "v_mfma_f32_32x32x16_bf16 v[56:71], v[108:111], v[88:91], v[56:71]\n"
    "s_cmp_ge_u32 %[cnt], 6\n"
    "s_cbranch_scc1 .Lnodma%=\n"
    "s_mov_b32 m0, %[dslot]\n"
    "s_nop 0\n"
    "global_load_lds_dwordx4 %[goff], %[gbase] offset:0\n"
    "global_load_lds_dwordx4 %[goff], %[gbase] offset:1024\n"
    ".Lnodma%=:\n"
    "v_cvt_pk_bf16_f32 v92, v32, v33\n"
    "v_exp_f32 v36, v36\n"
    "v_exp_f32 v37, v37\n"
    "v_cvt_pk_bf16_f32 v93, v34, v35\n"
    "v_exp_f32 v38, v38\n"
    "v_exp_f32 v39, v39\n"
    "v_cvt_pk_bf16_f32 v94, v36, v37\n"
    "v_cvt_pk_bf16_f32 v95, v38, v39\n"
    "v_exp_f32 v40, v40\n"
    "v_exp_f32 v41, v41\n"
    "v_mfma_f32_32x32x16_bf16 v[56:71], v[112:115], v[92:95], v[56:71]\n"
    "v_exp_f32 v42, v42\n"
    "v_exp_f32 v43, v43\n"
    "v_cvt_pk_bf16_f32 v88, v40, v41\n"
    "v_exp_f32 v44, v44\n"
    "s_waitcnt lgkmcnt(0)\n"
    "v_mfma_f32_32x32x16_bf16 v[24:39], v[100:103], v[96:99], v[72:87]\n"
    "v_exp_f32 v45, v45\n"
    "v_cvt_pk_bf16_f32 v89, v42, v43\n"
    "ds_read_b128 v[104:107], v126 offset:1024\n"
    "v_exp_f32 v46, v46\n"
    "ds_read_b128 v[108:111], v127 offset:0\n"
    "v_exp_f32 v47, v47\n"
    "v_cvt_pk_bf16_f32 v90, v44, v45\n"
    "ds_read_b128 v[112:115], v127 offset:512\n"
    "v_exp_f32 v48, v48\n"
    "v_exp_f32 v49, v49\n"
    "v_cvt_pk_bf16_f32 v91, v46, v47\n"
    "v_exp_f32 v50, v50\n"
    "v_exp_f32 v51, v51\n"
    "v_mfma_f32_32x32x16_bf16 v[56:71], v[116:119], v[88:91], v[56:71]\n"
    "v_cvt_pk_bf16_f32 v92, v48, v49\n"
    "v_exp_f32 v52, v52\n"
    "v_exp_f32 v53, v53\n"
    "v_cvt_pk_bf16_f32 v93, v50, v51\n"
    "v_exp_f32 v54, v54\n"
    "v_exp_f32 v55, v55\n"
    "v_cvt_pk_bf16_f32 v94, v52, v53\n"
    "v_cvt_pk_bf16_f32 v95, v54, v55\n"
    "v_mov_b32 v124, v126\n"
    "v_mov_b32 v125, v127\n"
    "s_add_u32 %[nslot], %[nslot], 0x4000\n"
    "s_cmp_eq_u32 %[nslot], 0xc000\n"
    "s_cselect_b32 %[nslot], 0, %[nslot]\n"
    "v_add_u32 v126, %[nslot], %[kaddr]\n"
    "v_and_b32 v127, %[nslot], %[vsel]\n"
    "v_add_u32 v127, v127, %[vaddr]\n"
    "s_add_u32 %[dslot], %[dslot], 0x4000\n"
    "s_cmp_eq_u32 %[dslot], %[dend]\n"
    "s_cselect_b32 %[dslot], %[dbeg], %[dslot]\n"
    "v_add_u32 %[goff], 0x4000, %[goff]\n"
    "s_add_u32 %[cnt], %[cnt], 1\n"
    "s_cmp_lt_u32 %[cnt], 8\n"
    "s_cbranch_scc1 .Lloop%=\n"
    "s_nop 1\n"
    "v_mfma_f32_32x32x16_bf16 v[56:71], v[120:123], v[92:95], v[56:71]\n"
    "s_nop 15\n"
    "s_nop 3\n"
    "v_rcp_f32 v24, v64\n"
    "s_nop 0\n"
    "v_mul_f32 v56, v56, v24\n"
    "v_mul_f32 v57, v57, v24\n"
    "v_mul_f32 v58, v58, v24\n"
    "v_mul_f32 v59, v59, v24\n"
    "v_mul_f32 v60, v60, v24\n"
    "v_mul_f32 v61, v61, v24\n"
    "v_mul_f32 v62, v62, v24\n"
    "v_mul_f32 v63, v63, v24\n"
    "v_cvt_pk_bf16_f32 %[oa0], v56, v57\n"
    "v_cvt_pk_bf16_f32 %[oa1], v58, v59\n"
    "v_cvt_pk_bf16_f32 %[oa2], v60, v61\n"
    "v_cvt_pk_bf16_f32 %[oa3], v62, v63\n"
    "s_waitcnt lgkmcnt(0)\n"
    "s_mov_b32 m0, %[m0save]\n"
        : [oa0] "=&v"(oa0), [oa1] "=&v"(oa1), [oa2] "=&v"(oa2), [oa3] "=&v"(oa3),
          [goff] "+v"(goff), [dslot] "+s"(dslot), [nslot] "+s"(nslot), [cnt] "=&s"(cnt), [m0save] "=&s"(m0save)
        : [qa0] "v"(qa[0]), [qa1] "v"(qa[1]), [qa2] "v"(qa[2]), [qa3] "v"(qa[3]),
          [nma] "v"(nma), [kaddr] "v"(kaddr), [vaddr] "v"(vaddr), [vsel] "v"(vsel), [gbase] "s"(kvb), [dbeg] "s"(dbeg), [dend] "s"(dend), [psel] "s"(0x07060302u), [vmask] "s"(0x0000ffff0000ffffull), [vone] "v"(((lane & 27) == 16) ? 0x3f803f80u : 0u), [dbgaddr] "s"(lds0 + A_LDS + wid * 64)
        : "memory", "scc", "vcc",
          "v24", "v25", "v26", "v27", "v28", "v29", "v30", "v31", "v32", "v33", "v34", "v35", "v36", "v37", "v38", "v39", "v40", "v41", "v42", "v43", "v44", "v45", "v46", "v47", "v48", "v49", "v50", "v51", "v52", "v53", "v54", "v55", "v56", "v57", "v58", "v59", "v60", "v61", "v62", "v63", "v64", "v65", "v66", "v67", "v68", "v69", "v70", "v71", "v72", "v73", "v74", "v75", "v76", "v77", "v78", "v79", "v80", "v81", "v82", "v83", "v84", "v85", "v86", "v87", "v88", "v89", "v90", "v91", "v92", "v93", "v94", "v95", "v96", "v97", "v98", "v99", "v100", "v101", "v102", "v103", "v104", "v105", "v106", "v107", "v108", "v109", "v110", "v111", "v112", "v113", "v114", "v115", "v116", "v117", "v118", "v119", "v120", "v121", "v122", "v123", "v124", "v125", "v126", "v127"
    );
    STAMP(4);
    st16_wt(ws + WS_OF + ((size_t)tile0 * 32 + head) * 1024 + lane * 16, (u32x4){oa0, oa1, oa2, oa3});
    WAITV(0);
    if (lane == 0) __hip_atomic_store(flagp(ws, FL_O + tile0 * 32 + head), tag, RLX_AGENT);
}

__device__ __forceinline__ void z_role(const uint16_t* __restrict__ x, const uint16_t* __restrict__ win, char* __restrict__ ws, char* lds, int g, u64& tag) {
    const int tid = threadIdx.x, lane = tid & 63, wid = __builtin_amdgcn_readfirstlane(tid >> 6), wr = wid >> 2, wc = wid & 3, l31 = lane & 31, hh = lane >> 5;
    const int xcd = g & 7, i = g >> 3;
    const int rb = 4 * (xcd >> 1) + (i & 3), cbz = 7 * ((xcd & 1) ^ 1) + (i >> 2);
    f32x16 acc[2];
    proj_main<false>(x, win, NIN, rb, cbz * 128, 12 + cbz, ws, lds, ((i & 3) * 4 + (i >> 2) * 3) & 15, tag, acc);
    STAMP(1);
    const float* st = (const float*)(lds + P_ST);
    unsigned zp[2][8];
#pragma unroll
    for (int rt = 0; rt < 2; ++rt) { const int tr = wr * 64 + rt * 32; const float mu = st[tr + l31], rs = st[128 + tr + l31];
#pragma unroll
        for (int q = 0; q < 4; ++q) { const f32x4 c = *(const f32x4*)(st + 256 + wc * 32 + 8 * q + 4 * hh);
            zp[rt][2 * q] = cvtpk(gelu_t((acc[rt][4 * q] - mu * c[0]) * rs), gelu_t((acc[rt][4 * q + 1] - mu * c[1]) * rs));
            zp[rt][2 * q + 1] = cvtpk(gelu_t((acc[rt][4 * q + 2] - mu * c[2]) * rs), gelu_t((acc[rt][4 * q + 3] - mu * c[3]) * rs)); } }
    if (cbz < 7) {
#pragma unroll
        for (int rt = 0; rt < 2; ++rt) { const int tt = rb * 4 + wr * 2 + rt;
            char* dst = ws + WS_Z1 + ((size_t)tt * 28 + cbz * 4 + wc) * 2048 + lane * 32;
            st16_wt(dst, (u32x4){zp[rt][0], zp[rt][1], zp[rt][2], zp[rt][3]}); st16_wt(dst + 16, (u32x4){zp[rt][4], zp[rt][5], zp[rt][6], zp[rt][7]}); }
    } else {
        const int cj = cbz - 7;
        float* pst = (float*)lds;
        const unsigned ONES = 0x3f803f80u;
#pragma unroll
        for (int rt = 0; rt < 2; ++rt) { float s1 = 0.f, s2 = 0.f;
#pragma unroll
            for (int e = 0; e < 8; ++e) { s1 = dot2(zp[rt][e], ONES, s1); s2 = dot2(zp[rt][e], zp[rt][e], s2); }
            s1 = swap32_add(s1); s2 = swap32_add(s2);
            if (hh == 0) *(f32x2*)(pst + (wc * 128 + wr * 64 + rt * 32 + l31) * 2) = (f32x2){s1, s2}; }
        __syncthreads();
        char* gst = ws + WS_ST + (size_t)rb * 7 * 2048;
        float* fin = (float*)(lds + 4096);
        if (tid < 128) { f32x2 s = *(const f32x2*)(pst + tid * 2);
#pragma unroll
            for (int j = 1; j < 4; ++j) s += *(const f32x2*)(pst + (j * 128 + tid) * 2);
            st16_wt(gst + (cj * 128 + tid) * 16, (u32x4){__float_as_uint(s[0]), __float_as_uint(s[1]), (unsigned)tag, (unsigned)(tag >> 32)});
            u32x4 gr[7];
            for (unsigned spins = 0; spins < (1u << 18); ++spins) {
                asm volatile("global_load_dwordx4 %0, %7, off sc1\n\tglobal_load_dwordx4 %1, %7, off offset:2048 sc1\n\tglobal_load_dwordx4 %2, %8, off sc1\n\tglobal_load_dwordx4 %3, %8, off offset:2048 sc1\n\t"
                             "global_load_dwordx4 %4, %9, off sc1\n\tglobal_load_dwordx4 %5, %9, off offset:2048 sc1\n\tglobal_load_dwordx4 %6, %10, off sc1\n\ts_waitcnt vmcnt(0)"
                             : "=&v"(gr[0]), "=&v"(gr[1]), "=&v"(gr[2]), "=&v"(gr[3]), "=&v"(gr[4]), "=&v"(gr[5]), "=&v"(gr[6])
                             : "v"(gst + tid * 16), "v"(gst + 4096 + tid * 16), "v"(gst + 8192 + tid * 16), "v"(gst + 12288 + tid * 16) : "memory");
                bool ok = true;
#pragma unroll
                for (int j = 0; j < 7; ++j) ok &= gr[j][2] == (unsigned)tag && gr[j][3] == (unsigned)(tag >> 32);
                if (__all(ok)) break;
                __builtin_amdgcn_s_sleep(POLL_SLEEP); }
            float s1 = 0.f, s2 = 0.f;
#pragma unroll
            for (int j = 0; j < 7; ++j) { s1 += __uint_as_float(gr[j][0]); s2 += __uint_as_float(gr[j][1]); }
            const float mu = s1 * (1.0f / FH); const float var = fmaxf(s2 * (1.0f / FH) - mu * mu, 0.f);
            fin[tid] = mu; fin[128 + tid] = __builtin_amdgcn_rsqf(var + LN_EPS); }
        __syncthreads();
        char* img = lds + 8192;
#pragma unroll
        for (int rt = 0; rt < 2; ++rt) { const int tok = wr * 64 + rt * 32 + l31; const float mu = fin[tok], rs = fin[128 + tok];
#pragma unroll
            for (int q = 0; q < 4; ++q) { const unsigned a = zp[rt][2 * q], c = zp[rt][2 * q + 1];
                const u32x2 o = {cvtpk((bflo(a) - mu) * rs, (bfhi(a) - mu) * rs), cvtpk((bflo(c) - mu) * rs, (bfhi(c) - mu) * rs)};
                const int f = wc * 32 + 8 * q + 4 * hh;
                *(u32x2*)(img + tok * 256 + (((f >> 3) ^ wsw(tok)) * 16) + (f & 4) * 2) = o; } }
        __syncthreads();
        { const int blk = (lane >> 4) & 1, q = (lane & 15) >> 2, p = lane & 3;
#pragma unroll
          for (int kq = 0; kq < 4; ++kq) { const int ks = wr * 4 + kq; s16x4 t2[2];
#pragma unroll
              for (int t = 0; t < 2; ++t) { const int kr = 16 * ks + 8 * hh + 4 * t + q; const int ch = 4 * wc + 2 * blk + (p >> 1);
                  t2[t] = trread((lds_cptr)img + kr * 256 + ((ch ^ wsw(kr)) * 16) + 8 * (p & 1)); }
              st16_wt(ws + WS_Z2N + ((size_t)(cj * 4 + wc) * 128 + rb * 8 + ks) * 1024 + lane * 16, __builtin_bit_cast(u32x4, cat(t2[0], t2[1]))); } }
    }
    PUBLISH(FL_Z + rb * 14 + cbz);
}

constexpr int S_SLOT = 24576, S_LDS = 3 * S_SLOT;
__device__ __forceinline__ void spatial_role(const uint16_t* __restrict__ wsp, const uint16_t* __restrict__ bsp, char* __restrict__ ws, char* lds, int g, u64 tag) {
    const int tid = threadIdx.x, lane = tid & 63, wid8 = __builtin_amdgcn_readfirstlane(tid >> 6), wid = wid8 & 3, l31 = lane & 31, hh = lane >> 5;
    const int i = g >> 3, tb = 4 * (g & 7) + (i & 3), fb = i >> 2;
    const unsigned lds0 = (unsigned)(uintptr_t)lds;
    STAMP(3);
    if (wid8 >= 4) {
        const int r0 = 16 * wid + (lane >> 3), r1 = r0 + 8;
        const char* sb_ = (const char*)(wsp + (size_t)tb * 64 * SEQ);
        const unsigned vo0 = (r0 * SEQ + ((lane & 7) ^ ((r0 >> 1) & 7)) * 8) * 2, vo1 = (r1 * SEQ + ((lane & 7) ^ ((r1 >> 1) & 7)) * 8) * 2;
        const char* zs = ws + WS_Z2N + (size_t)(fb * 4 + wid) * 131072; const unsigned zvo = lane * 16;
        unsigned ready = 0;
        GAS u64* zf = flagp(ws, FL_Z + (lane & 15) * 14 + 7 + fb);
        auto need = [&](int rbk) {
            for (unsigned spins = 0; !((ready >> rbk) & 1) && spins < (1u << 18); ++spins) {
                ready = (unsigned)__ballot(__hip_atomic_load(zf, RLX_AGENT) == tag) & 0xffffu;
                if (!((ready >> rbk) & 1)) __builtin_amdgcn_s_sleep(POLL_SLEEP); } };
        auto issue = [&](int it, int slot) { const unsigned so = lds0 + slot * S_SLOT;
            need(it >> 1);
            set_m0(so + wid * 2048); glds_s<0, false>(sb_ + it * 128, vo0);
            set_m0(so + wid * 2048 + 1024); glds_s<0, false>(sb_ + it * 128, vo1);
            set_m0(so + 8192 + wid * 4096);
            glds_s<0, true>(zs + it * 4096, zvo); glds_s<1024, true>(zs + it * 4096, zvo); glds_s<2048, true>(zs + it * 4096, zvo); glds_s<3072, true>(zs + it * 4096, zvo); };
        issue(0, 0); issue(1, 1); issue(2, 2);
        WAIT_BAR(12);
        int slot = 0;
#pragma unroll 1
        for (int it = 1; it < 31; ++it) { WAIT_BAR(6); if (it + 2 < 32) issue(it + 2, slot); slot = slot == 2 ? 0 : slot + 1; }
        WAIT_BAR(0);
        BAR();
    } else {
        unsigned boff[2][4];
#pragma unroll
        for (int rt = 0; rt < 2; ++rt)
#pragma unroll
            for (int ks = 0; ks < 4; ++ks) { const int r = rt * 32 + l31; boff[rt][ks] = (r * 128 + ((hh ^ ((r >> 1) & 7)) * 16)) ^ (ks * 32); }
        const unsigned aoff = 8192 + wid * 4096 + lane * 16;
        struct SF { bf16x8 a, b[2]; };
        auto rd = [&](SF& f, int slot, int ks) { lds_cptr sb = (lds_cptr)lds + slot * S_SLOT;
            f.a = ldsfrag(sb + aoff + ks * 1024); f.b[0] = ldsfrag(sb + boff[0][ks]); f.b[1] = ldsfrag(sb + boff[1][ks]); };
        f32x16 acc[2] = {f32x16{}, f32x16{}};
        auto mm = [&](const SF& f) { acc[0] = MFMA(f.a, f.b[0], acc[0]); acc[1] = MFMA(f.a, f.b[1], acc[1]); };
        SF f[4];
        BAR();
#pragma unroll
        for (int ks = 0; ks < 4; ++ks) rd(f[ks], 0, ks);
        auto step = [&](int nsl) {
            LGKM_BAR();
#pragma unroll
            for (int ks = 0; ks < 4; ++ks) { lds_cptr sb = (lds_cptr)lds + nsl * S_SLOT;
                acc[0] = MFMA(f[ks].a, f[ks].b[0], acc[0]); SBAR();
                f[ks].b[0] = ldsfrag(sb + boff[0][ks]); SBAR();
                PACE();
                acc[1] = MFMA(f[ks].a, f[ks].b[1], acc[1]); SBAR();
                f[ks].b[1] = ldsfrag(sb + boff[1][ks]); f[ks].a = ldsfrag(sb + aoff + ks * 1024); SBAR();
                PACE(); } };
#pragma unroll 1
        for (int io = 0; io < 10; ++io) { step(1); step(2); step(0); }
        step(1); step(2);
        wait_flags(flagp(ws, FL_Z + (tb >> 1) * 14 + fb), lane == 0, tag);
#pragma unroll
        for (int rt = 0; rt < 2; ++rt) { const int tt = tb * 2 + rt; const float bias = __uint_as_float((unsigned)bsp[tt * 32 + l31] << 16);
            const char* z1p = ws + WS_Z1 + ((size_t)tt * 28 + fb * 4 + wid) * 2048 + lane * 32;
            const u32x4 za = ld16_wt(z1p), zb = ld16_wt(z1p + 16);
            u32x4 ua, ub;
#pragma unroll
            for (int e = 0; e < 4; ++e) {
                ua[e] = cvtpk(bflo(za[e]) * (acc[rt][2 * e] + bias), bfhi(za[e]) * (acc[rt][2 * e + 1] + bias));
                ub[e] = cvtpk(bflo(zb[e]) * (acc[rt][8 + 2 * e] + bias), bfhi(zb[e]) * (acc[rt][8 + 2 * e + 1] + bias)); }
            char* ud = ws + WS_U + ((size_t)tt * 56 + (fb * 4 + wid) * 2) * 1024 + lane * 16;
            st16_wt(ud, ua); st16_wt(ud + 1024, ub); }
    }
    PUBLISH(FL_U + tb * 7 + fb);
}

constexpr int F_SLOT = 8192 + 8192, F_LDS = 4 * F_SLOT;
__device__ __forceinline__ void final_role(const uint16_t* __restrict__ x, const uint16_t* __restrict__ wo, const uint16_t* __restrict__ wout, char* __restrict__ ws, uint16_t* __restrict__ out, char* lds, int g, u64 tag) {
    const int tb = 4 * (g & 7) + ((g >> 3) & 3), db = g >> 5;
    const int tid = threadIdx.x, lane = tid & 63, wid8 = __builtin_amdgcn_readfirstlane(tid >> 6), wid = wid8 & 3, wr = wid >> 1, wc = wid & 1, l31 = lane & 31, hh = lane >> 5;
    const unsigned lds0 = (unsigned)(uintptr_t)lds;
    auto wswz = [](int r) { return ((r & 2) << 1) | ((r >> 2) & 3); };
    f32x16 as = f32x16{}, ag = f32x16{};
    const int k0 = 16 * wid + (lane >> 3), k1 = k0 + 8;
    const unsigned wo0 = (k0 * DM + db * 64 + ((lane & 7) ^ wswz(k0)) * 8) * 2, wo1 = (k1 * DM + db * 64 + ((lane & 7) ^ wswz(k1)) * 8) * 2;
    const int att = tb * 2 + (wid & 1), aks = 2 * (wid >> 1);
    unsigned woff[2];
    { const int blk = (lane >> 4) & 1, q = (lane & 15) >> 2, p = lane & 3; const int kr = 4 * hh + q; const int ch = 4 * wc + 2 * blk + (p >> 1);
      woff[0] = kr * 128 + ((ch ^ wswz(kr)) * 16) + 8 * (p & 1); woff[1] = (woff[0] + 1024) ^ 32; }
    const unsigned aoff = 8192 + wr * 4096 + lane * 16;
    struct FF { bf16x8 w, a; };
    auto rd = [&](FF& f, int slot, int ks) { lds_cptr sb = (lds_cptr)lds + slot * F_SLOT;
        f.w = cat(trread(sb + woff[0] + ks * 2048), trread(sb + woff[1] + ks * 2048)); f.a = ldsfrag(sb + aoff + ks * 1024); };
    auto pass = [&](const uint16_t* wb, const char* ab, int nfr, int n, f32x16& acc, bool pace) {
        if (wid8 >= 4) {
            auto issue = [&](int it) { const unsigned so = lds0 + (it & 3) * F_SLOT; const char* w = (const char*)(wb + (size_t)it * 64 * DM);
                set_m0(so + wid * 2048); glds_s<0, false>(w, wo0);
                set_m0(so + wid * 2048 + 1024); glds_s<0, false>(w, wo1);
                const char* a = ab + ((size_t)att * nfr + it * 4 + aks) * 1024;
                set_m0(so + 8192 + (wid & 1) * 4096 + aks * 1024); glds_s<0, true>(a, lane * 16); glds_s<1024, true>(a, lane * 16); };
            issue(0); issue(1); issue(2); issue(3);
            WAIT_BAR(12);
#pragma unroll 1
            for (int i = 1; i < n - 2; ++i) { WAIT_BAR(8); if (i + 3 < n) issue(i + 3); }
            WAIT_BAR(4);
            WAIT_BAR(0);
            BAR();
        } else {
            FF fa[4], fb[4];
            BAR();
#pragma unroll
            for (int ks = 0; ks < 4; ++ks) rd(fa[ks], 0, ks);
            auto step = [&](int nsl, FF (&cur)[4], FF (&nxt)[4]) {
                LGKM_BAR();
#pragma unroll
                for (int ks = 0; ks < 4; ++ks) { acc = MFMA(cur[ks].w, cur[ks].a, acc); SBAR(); rd(nxt[ks], nsl, ks); SBAR(); if (pace) PACE(); } };
#pragma unroll 1
            for (int it = 0; it < n; it += 2) { step((it + 1) & 3, fa, fb); step((it + 2) & 3, fb, fa); }
        } };
    if (wid8 == 0) wait_flags(flagp(ws, FL_U + tb * 7 + lane), lane < 7, tag);
    __syncthreads();
    STAMP(5);
    pass(wout, ws + WS_U, 56, 14, ag, true);
    STAMP(6);
    __syncthreads();
#pragma unroll
    for (int q = 0; q < 8; ++q) {
        const int kr = 8 * wid8 + (lane >> 3);
        set_m0(lds0 + q * 8192 + wid8 * 1024);
        glds_s<0, false>((const char*)(wo + (size_t)q * 64 * DM), (kr * DM + db * 64 + ((lane & 7) ^ wswz(kr)) * 8) * 2); }
    const int tok = tb * 64 + wr * 32 + l31;
    float gate[16]; u32x2 xv[4];
    if (wid8 < 4) {
#pragma unroll
        for (int q = 0; q < 4; ++q) xv[q] = *(const u32x2*)(x + (size_t)tok * DM + db * 64 + wc * 32 + 8 * q + 4 * hh);
#pragma unroll
        for (int r = 0; r < 16; ++r) gate[r] = silu_f(ag[r]); }
    if (wid8 == 0) wait_flags(flagp(ws, FL_O + tb * 64 + lane), true, tag);
    WAITV(0);
    __syncthreads();
    STAMP(7);
    const int kh = wid8 >> 2;
    u32x4 of[16];
    { const char* ob = ws + WS_OF + ((size_t)(tb * 2 + wr) * 32 + 16 * kh) * 1024; const unsigned v0 = lane * 16, v1 = v0 + 4096, v2 = v0 + 8192, v3 = v0 + 12288;
      asm volatile(
        "global_load_dwordx4 %0, %16, %20 sc1\n\tglobal_load_dwordx4 %1, %16, %20 offset:1024 sc1\n\tglobal_load_dwordx4 %2, %16, %20 offset:2048 sc1\n\tglobal_load_dwordx4 %3, %16, %20 offset:3072 sc1\n\t"
        "global_load_dwordx4 %4, %17, %20 sc1\n\tglobal_load_dwordx4 %5, %17, %20 offset:1024 sc1\n\tglobal_load_dwordx4 %6, %17, %20 offset:2048 sc1\n\tglobal_load_dwordx4 %7, %17, %20 offset:3072 sc1\n\t"
        "global_load_dwordx4 %8, %18, %20 sc1\n\tglobal_load_dwordx4 %9, %18, %20 offset:1024 sc1\n\tglobal_load_dwordx4 %10, %18, %20 offset:2048 sc1\n\tglobal_load_dwordx4 %11, %18, %20 offset:3072 sc1\n\t"
        "global_load_dwordx4 %12, %19, %20 sc1\n\tglobal_load_dwordx4 %13, %19, %20 offset:1024 sc1\n\tglobal_load_dwordx4 %14, %19, %20 offset:2048 sc1\n\tglobal_load_dwordx4 %15, %19, %20 offset:3072 sc1\n\t"
        "s_waitcnt vmcnt(0)"
        : "=&v"(of[0]), "=&v"(of[1]), "=&v"(of[2]), "=&v"(of[3]), "=&v"(of[4]), "=&v"(of[5]), "=&v"(of[6]), "=&v"(of[7]),
          "=&v"(of[8]), "=&v"(of[9]), "=&v"(of[10]), "=&v"(of[11]), "=&v"(of[12]), "=&v"(of[13]), "=&v"(of[14]), "=&v"(of[15])
        : "v"(v0), "v"(v1), "v"(v2), "v"(v3), "s"(ob) : "memory"); }
    { lds_cptr wb_ = (lds_cptr)lds + kh * 32768;
#pragma unroll
      for (int ks = 0; ks < 16; ++ks)
          as = MFMA(cat(trread(wb_ + woff[0] + ks * 2048), trread(wb_ + woff[1] + ks * 2048)), __builtin_bit_cast(bf16x8, of[ks]), as); }
    __syncthreads();
    if (kh == 1) {
#pragma unroll
        for (int q = 0; q < 4; ++q) *(f32x4*)(lds + (wid * 4 + q) * 1024 + lane * 16) = (f32x4){as[4 * q], as[4 * q + 1], as[4 * q + 2], as[4 * q + 3]}; }
    __syncthreads();
    STAMP(8);
    if (kh == 0) {
#pragma unroll
        for (int q = 0; q < 4; ++q) { const f32x4 p = *(const f32x4*)(lds + (wid * 4 + q) * 1024 + lane * 16);
            const float r0 = bflo(xv[q][0]) + (as[4 * q] + p[0]) * gate[4 * q], r1 = bfhi(xv[q][0]) + (as[4 * q + 1] + p[1]) * gate[4 * q + 1];
            const float r2 = bflo(xv[q][1]) + (as[4 * q + 2] + p[2]) * gate[4 * q + 2], r3 = bfhi(xv[q][1]) + (as[4 * q + 3] + p[3]) * gate[4 * q + 3];
            *(u32x2*)(out + (size_t)tok * DM + db * 64 + wc * 32 + 8 * q + 4 * hh) = (u32x2){cvtpk(r0, r1), cvtpk(r2, r3)}; }
    }
}

#define ADELAY 0
#define APRIO 3
#define GDELAY 0
#define GPRIO 2
constexpr int ALL_LDS = S_LDS;
__global__ __launch_bounds__(512, 4) void k_all(const uint16_t* __restrict__ x, const uint16_t* __restrict__ wqkv, const uint16_t* __restrict__ wo, const uint16_t* __restrict__ win,
                                                const uint16_t* __restrict__ wsp, const uint16_t* __restrict__ bsp, const uint16_t* __restrict__ wout, char* __restrict__ ws, uint16_t* __restrict__ out) {
    extern __shared__ __attribute__((aligned(16))) char lds[];
    const int b = blockIdx.x;
    u64 tag;
    STAMP(0);
    if (b < 256) {
        __builtin_amdgcn_s_setprio(APRIO);
        if (ADELAY > 0 && b < 192) {
            const u64 t0 = __builtin_amdgcn_s_memrealtime();
            const int i_ = b >> 3, cb_ = 6 * (b & 1) + (i_ >> 2), line = (i_ & 3) * 256 + (threadIdx.x & 255);
            unsigned junk = 0;
            while (__builtin_amdgcn_s_memrealtime() - t0 < ADELAY) __builtin_amdgcn_s_sleep(8);
            asm volatile("s_waitcnt vmcnt(0)" : "+v"(junk) :: "memory"); }
        if (b < 192) qkv_role(x, wqkv, ws, lds, b, tag); else stats_role(x, wqkv, win, ws, lds, b - 192, tag);
        __builtin_amdgcn_s_setprio(0);
        __syncthreads();
        attn_role(ws, lds, b, tag);
        STAMP(5);
        if (b == 0 && threadIdx.x < 64) {
            const int lane = threadIdx.x;
            for (unsigned spins = 0; spins < (1u << 18); ++spins) {
                u64 v[8];
#pragma unroll
                for (int c = 0; c < 4; ++c) { v[c] = __hip_atomic_load(flagp(ws, FL_O + (c * 2 + (lane >> 5)) * 256 + (lane & 31)), RLX_AGENT); v[4 + c] = __hip_atomic_load(flagp(ws, FL_G + c * 64 + lane), RLX_AGENT); }
                bool ok = true;
#pragma unroll
                for (int c = 0; c < 8; ++c) ok &= v[c] == tag;
                if (__all(ok)) break;
                __builtin_amdgcn_s_sleep(8); }
            if (lane == 0) __hip_atomic_store(flagp(ws, FL_EPOCH), tag, RLX_AGENT);
        }
    } else {
        const int g = b - 256;
        __builtin_amdgcn_s_setprio(GPRIO);
            if (GDELAY > 0) { const u64 t0 = __builtin_amdgcn_s_memrealtime(); while (__builtin_amdgcn_s_memrealtime() - t0 < GDELAY) __builtin_amdgcn_s_sleep(8); }
        if (g < 224) { z_role(x, win, ws, lds, g, tag); STAMP(2); __syncthreads(); spatial_role(wsp, bsp, ws, lds, g, tag); STAMP(4); __syncthreads(); }
        else { u64 ep; EPOCH_REQ(ep);
            { const u64 t0 = __builtin_amdgcn_s_memrealtime(); while (__builtin_amdgcn_s_memrealtime() - t0 < WDELAY) __builtin_amdgcn_s_sleep(8); }
            { const int wv = (g - 224) * 8 + (threadIdx.x >> 6), lane = threadIdx.x & 63; unsigned junk = 0;
#pragma unroll
              for (int q = 0; q < 4; ++q) asm volatile("global_load_dword %0, %1, off" : "+v"(junk) : "v"((const char*)wsp + ((size_t)(q * 256 + wv) * 64 + lane) * 128) : "memory");
              if (wv < 112) asm volatile("global_load_dword %0, %1, off" : "+v"(junk) : "v"((const char*)wout + ((size_t)wv * 64 + lane) * 128) : "memory");
              else if (wv < 176) asm volatile("global_load_dword %0, %1, off" : "+v"(junk) : "v"((const char*)wo + ((size_t)(wv - 112) * 64 + lane) * 128) : "memory");
              asm volatile("s_waitcnt vmcnt(0)" : "+v"(junk) :: "memory"); }
            asm volatile("s_waitcnt vmcnt(0)" : "+v"(ep) :: "memory"); tag = EPOCH_TAG(ep); }
        if (threadIdx.x == 0) __hip_atomic_store(flagp(ws, FL_G + g), tag, RLX_AGENT);
        final_role(x, wo, wout, ws, out, lds, g, tag);
        STAMP(9);
    }
}

extern "C" void kernel_launch(void* const* d_in, const int* in_sizes, int n_in, void* d_out, int out_size, void* d_ws, size_t ws_size, hipStream_t stream) {
    const uint16_t* x = (const uint16_t*)d_in[0]; const uint16_t* wqkv = (const uint16_t*)d_in[1]; const uint16_t* wo = (const uint16_t*)d_in[2];
    const uint16_t* win = (const uint16_t*)d_in[4]; const uint16_t* wsp = (const uint16_t*)d_in[5]; const uint16_t* bsp = (const uint16_t*)d_in[6]; const uint16_t* wout = (const uint16_t*)d_in[7];
    (void)hipFuncSetAttribute((const void*)k_all, hipFuncAttributeMaxDynamicSharedMemorySize, ALL_LDS);
    char* wsb = (char*)d_ws;
    k_all<<<512, 512, ALL_LDS, stream>>>(x, wqkv, wo, win, wsp, bsp, wout, wsb, (uint16_t*)d_out);
}
```

```cpp
#include <hip/hip_runtime.h>
#include <hip/hip_bf16.h>
#include <stdint.h>
#define WDELAY 500

typedef short bf16x8 __attribute__((ext_vector_type(8)));
typedef short s16x4 __attribute__((ext_vector_type(4)));
typedef __bf16 bf16x2_t __attribute__((ext_vector_type(2)));
typedef float f32x16 __attribute__((ext_vector_type(16)));
typedef float f32x4 __attribute__((ext_vector_type(4)));
typedef float f32x2 __attribute__((ext_vector_type(2)));
typedef unsigned u32x4 __attribute__((ext_vector_type(4)));
typedef unsigned u32x2 __attribute__((ext_vector_type(2)));
typedef unsigned long long u64;
#define GAS __attribute__((address_space(1)))
#define LAS __attribute__((address_space(3)))
typedef LAS const char* lds_cptr;
typedef short v4i16_t __attribute__((ext_vector_type(4)));
#define RLX_AGENT __ATOMIC_RELAXED, __HIP_MEMORY_SCOPE_AGENT

constexpr int SEQ = 2048, DM = 512, NQKV = 1536, NIN = 1792, FH = 896;
constexpr float QSCALE = 0.25f * 1.4426950408889634f;
constexpr float LN_EPS = 1e-5f;
constexpr size_t MB = 1024 * 1024;
constexpr size_t WS_QF = 0, WS_KV = 2 * MB, WS_Z1 = 6 * MB, WS_Z2N = 10 * MB, WS_U = 14 * MB, WS_OF = 18 * MB;
constexpr size_t WS_KM = 20 * MB, WS_ST = 20 * MB + 65536, WS_RS = 20 * MB + 65536 + 229376, WS_CS = 20 * MB + 65536 + 245760  , WS_FLAG = 21 * MB;
constexpr int FL_RS = 0, FL_CS = 64, FL_QKV = 128, FL_ST = 320, FL_Z = 432, FL_U = 656, FL_G = 880, FL_EPOCH = 1136, FL_O = 1152;

#define MFMA(a, b, c) __builtin_amdgcn_mfma_f32_32x32x16_bf16(a, b, c, 0, 0, 0)
#define SBAR() __builtin_amdgcn_sched_barrier(0)
__device__ __forceinline__ unsigned cvtpk(float lo, float hi) { unsigned r; asm("v_cvt_pk_bf16_f32 %0, %1, %2" : "=v"(r) : "v"(lo), "v"(hi)); return r; }
__device__ __forceinline__ float bflo(unsigned u) { return __uint_as_float(u << 16); }
__device__ __forceinline__ float bfhi(unsigned u) { return __uint_as_float(u & 0xffff0000u); }
__device__ __forceinline__ float dot2(unsigned a, unsigned b, float acc) {
    return __builtin_amdgcn_fdot2_f32_bf16(__builtin_bit_cast(bf16x2_t, a), __builtin_bit_cast(bf16x2_t, b), acc, false); }
template <int IMM, bool WT> __device__ __forceinline__ void glds_s(const void* sbase, unsigned voff) {
    if (WT) asm volatile("global_load_lds_dwordx4 %0, %1 offset:%2 sc1" :: "v"(voff), "s"(sbase), "n"(IMM) : "memory");
    else asm volatile("global_load_lds_dwordx4 %0, %1 offset:%2" :: "v"(voff), "s"(sbase), "n"(IMM) : "memory"); }
__device__ __forceinline__ void set_m0(unsigned v) { asm volatile("s_mov_b32 m0, %0\n\ts_nop 0" :: "s"(v) : "memory", "m0"); }
__device__ __forceinline__ s16x4 trread(lds_cptr p) { return __builtin_bit_cast(s16x4, __builtin_amdgcn_ds_read_tr16_b64_v4i16((LAS v4i16_t*)p)); }
__device__ __forceinline__ bf16x8 ldsfrag(lds_cptr p) { return *(LAS const bf16x8*)p; }
__device__ __forceinline__ bf16x8 cat(s16x4 a, s16x4 b) { return (bf16x8){a[0], a[1], a[2], a[3], b[0], b[1], b[2], b[3]}; }
__device__ __forceinline__ float swap32_add(float v) { auto rr = __builtin_amdgcn_permlane32_swap(__float_as_uint(v), __float_as_uint(v), false, false); return __uint_as_float(rr[0]) + __uint_as_float(rr[1]); }
__device__ __forceinline__ float swap32_max(float v) { auto rr = __builtin_amdgcn_permlane32_swap(__float_as_uint(v), __float_as_uint(v), false, false); return fmaxf(__uint_as_float(rr[0]), __uint_as_float(rr[1])); }
template <int CTRL> __device__ __forceinline__ float dppf(float v) { return __int_as_float(__builtin_amdgcn_update_dpp(0, __float_as_int(v), CTRL, 0xf, 0xf, false)); }
__device__ __forceinline__ float row16_add(float v) { v += dppf<0x128>(v); v += dppf<0x124>(v); v += dppf<0x122>(v); v += dppf<0x121>(v); return v; }
__device__ __forceinline__ float row16_max(float v) { v = fmaxf(v, dppf<0x128>(v)); v = fmaxf(v, dppf<0x124>(v)); v = fmaxf(v, dppf<0x122>(v)); v = fmaxf(v, dppf<0x121>(v)); return v; }
__device__ __forceinline__ float swap16_max(float v) { auto rr = __builtin_amdgcn_permlane16_swap(__float_as_uint(v), __float_as_uint(v), false, false); return fmaxf(__uint_as_float(rr[0]), __uint_as_float(rr[1])); }
__device__ __forceinline__ float gelu_t(float x) {
    const float c1 = -2.3022082f, c2 = -0.10294324f;
    float e = __builtin_amdgcn_exp2f(x * __builtin_fmaf(x * x, c2, c1));
    return x * __builtin_amdgcn_rcpf(1.0f + e); }
__device__ __forceinline__ float silu_f(float x) { return x * __builtin_amdgcn_rcpf(1.0f + __builtin_amdgcn_exp2f(-1.4426950408889634f * x)); }
#define PACE_N 2
#define PACE() do { asm volatile("s_nop %0" :: "n"(PACE_N)); SBAR(); } while (0)
#define POLL_SLEEP 2
#define WAITV(n) asm volatile("s_waitcnt vmcnt(" #n ")" ::: "memory")
#define WAIT_BAR(n) asm volatile("s_waitcnt vmcnt(" #n ")\n\ts_barrier" ::: "memory")
#define BAR() asm volatile("s_barrier" ::: "memory")
#define LGKM_BAR() do { __builtin_amdgcn_s_waitcnt(0xC07F); asm volatile("" ::: "memory"); __builtin_amdgcn_s_barrier(); asm volatile("" ::: "memory"); } while (0)
__device__ __forceinline__ unsigned wsw(unsigned row) { return ((row & 3) << 2) | ((row >> 2) & 3); }
__device__ __forceinline__ void st16_wt(void* p, u32x4 v) {
    asm volatile("global_store_dwordx4 %0, %1, off sc1\n\ts_nop 1" :: "v"(p), "v"(v)); }
__device__ __forceinline__ void st8_wt(void* p, u64 v) { __hip_atomic_store((GAS u64*)p, v, RLX_AGENT); }
__device__ __forceinline__ u64 ld8_wt(const void* p) { return __hip_atomic_load((GAS u64*)p, RLX_AGENT); }
__device__ __forceinline__ u32x4 ld16_wt(const void* p) { u32x4 v; asm volatile("global_load_dwordx4 %0, %1, off sc1\n\ts_waitcnt vmcnt(0)" : "=&v"(v) : "v"(p) : "memory"); return v; }
__device__ __forceinline__ GAS u64* flagp(char* ws, int idx) { return (GAS u64*)(ws + WS_FLAG) + idx * 16; }
__device__ __forceinline__ void wait_flags(GAS u64* f, bool want, u64 tag) {
    for (unsigned spins = 0; spins < (1u << 18); ++spins) {
        const u64 v = want ? __hip_atomic_load(f, RLX_AGENT) : tag;
        if (__all(v == tag)) return;
        __builtin_amdgcn_s_sleep(POLL_SLEEP); } }
#define EPOCH_REQ(ep) asm volatile("global_load_dwordx2 %0, %1, off sc1" : "=v"(ep) : "v"(flagp(ws, FL_EPOCH)) : "memory")
#define EPOCH_TAG(ep) ((((u64)(unsigned)__builtin_amdgcn_readfirstlane((unsigned)((ep) >> 32)) << 32) | (unsigned)__builtin_amdgcn_readfirstlane((unsigned)(ep))) + 1)
#define PUBLISH(idx) do { WAITV(0); __syncthreads(); if (threadIdx.x == 0) __hip_atomic_store(flagp(ws, idx), tag, RLX_AGENT); } while (0)

__device__ __forceinline__ void stats_role(const uint16_t* __restrict__ x, const uint16_t* __restrict__ wqkv, const uint16_t* __restrict__ win, char* __restrict__ ws, char* lds, int j, u64& tag) {
    u64 ep; EPOCH_REQ(ep);
    const int tid = threadIdx.x, lane = tid & 63, wid = tid >> 6, rr = lane >> 4, cc = lane & 15;
    const unsigned ONES = 0x3f803f80u;
    u32x4 xv[4], w[8];
    const int row = 32 * j + 4 * wid + rr;
#pragma unroll
    for (int i = 0; i < 4; ++i) xv[i] = *(const u32x4*)(x + (size_t)row * DM + i * 128 + cc * 8);
    const int cbk = j >> 1;
    if (j < 52) {
        const uint16_t* W = cbk < 12 ? wqkv + cbk * 128 : win + (cbk - 12) * 128; const int ldw = cbk < 12 ? NQKV : NIN;
        const uint16_t* p = W + (size_t)((j & 1) * 256 + 32 * wid + rr) * ldw + cc * 8;
#pragma unroll
        for (int i = 0; i < 8; ++i) w[i] = *(const u32x4*)(p + (size_t)(i * 4) * ldw);
    }
    asm volatile("s_waitcnt vmcnt(0)" : "+v"(ep) :: "memory");
    tag = EPOCH_TAG(ep);
    float s1 = 0.f, s2 = 0.f;
#pragma unroll
    for (int i = 0; i < 4; ++i)
#pragma unroll
        for (int e = 0; e < 4; ++e) { s1 = dot2(xv[i][e], ONES, s1); s2 = dot2(xv[i][e], xv[i][e], s2); }
    s1 = row16_add(s1); s2 = row16_add(s2);
    { const float mu = s1 * (1.0f / DM), var = fmaxf(s2 * (1.0f / DM) - mu * mu, 0.f);
      if (cc == 0) st8_wt(ws + WS_RS + (size_t)row * 8, __builtin_bit_cast(u64, (f32x2){mu, __builtin_amdgcn_rsqf(var + LN_EPS)})); }
    if (j < 52) {
        float cs[8] = {0.f, 0.f, 0.f, 0.f, 0.f, 0.f, 0.f, 0.f};
        unsigned sel_lo = 0x00003f80u, sel_hi = 0x3f800000u;
        asm volatile("" : "+v"(sel_lo), "+v"(sel_hi));
#pragma unroll
        for (int i = 0; i < 8; ++i)
#pragma unroll
            for (int e = 0; e < 4; ++e) { cs[2 * e] = dot2(w[i][e], sel_lo, cs[2 * e]); cs[2 * e + 1] = dot2(w[i][e], sel_hi, cs[2 * e + 1]); }
        float* red = (float*)lds;
        *(f32x4*)(red + (wid * 4 + rr) * 128 + cc * 8) = (f32x4){cs[0], cs[1], cs[2], cs[3]}; *(f32x4*)(red + (wid * 4 + rr) * 128 + cc * 8 + 4) = (f32x4){cs[4], cs[5], cs[6], cs[7]};
        __syncthreads();
        if (tid < 64) { f32x2 a = {0.f, 0.f};
#pragma unroll
            for (int q = 0; q < 32; ++q) a += *(const f32x2*)(red + q * 128 + 2 * tid);
            st8_wt(ws + WS_CS + (size_t)((j & 1) * 3328 + cbk * 128 + 2 * tid) * 4, __builtin_bit_cast(u64, a)); }
    }
    WAITV(0); __syncthreads();
    if (tid == 0) { __hip_atomic_store(flagp(ws, FL_RS + j), tag, RLX_AGENT); if (j < 52) __hip_atomic_store(flagp(ws, FL_CS + j), tag, RLX_AGENT); }
}

constexpr int P_SLOT = 16384, P_ST = 4 * P_SLOT, P_LDS = 4 * P_SLOT + 4096;
struct PFrag { bf16x8 x[2], w; };
template <bool SWAP>
__device__ __forceinline__ void proj_main(const uint16_t* __restrict__ x, const uint16_t* __restrict__ W, int ldw, int rb, int col0, int cblk, char* __restrict__ ws, char* lds, int rot, u64& tag, f32x16 (&acc)[2]) {
    u64 ep; EPOCH_REQ(ep);
    const int tid = threadIdx.x, lane = tid & 63, wid = __builtin_amdgcn_readfirstlane(tid >> 6), wr = wid >> 2, wc = wid & 3, l31 = lane & 31, hh = lane >> 5;
    const unsigned lds0 = (unsigned)(uintptr_t)lds;
    const int xr = 16 * wid + (lane >> 2), wk = 4 * wid + (lane >> 4);
    const char* xb = (const char*)(x + (size_t)rb * 128 * DM); const char* wb = (const char*)(W + col0);
    const unsigned xvo = (xr * DM + ((lane & 3) ^ ((xr >> 2) & 3)) * 8) * 2, wvo = (wk * ldw + ((lane & 15) ^ wsw(wk)) * 8) * 2;
    const unsigned xd = lds0 + wid * 1024, wd = lds0 + 8192 + wid * 1024;
    auto issue1 = [&](int it, int which) { const unsigned so = (it & 3) * P_SLOT; const int kk = (it + rot) & 15;
        if (which == 0) { set_m0(xd + so); glds_s<0, false>(xb + kk * 64, xvo); } else { set_m0(wd + so); glds_s<0, false>(wb + (size_t)kk * 64 * ldw, wvo); } SBAR(); };
#pragma unroll
    for (int it = 0; it < 4; ++it) { issue1(it, 0); issue1(it, 1); }
    unsigned xoff[2][2], woff[2][2];
    const int blk = (lane >> 4) & 1, q = (lane & 15) >> 2, p = lane & 3;
#pragma unroll
    for (int ks = 0; ks < 2; ++ks) {
#pragma unroll
        for (int rt = 0; rt < 2; ++rt) { const int r = wr * 64 + rt * 32 + l31; xoff[rt][ks] = r * 64 + (((2 * ks + hh) ^ ((r >> 2) & 3)) * 16); }
        const int kr = 8 * hh + q, ch = 4 * wc + 2 * blk + (p >> 1);
        woff[0][ks] = 8192 + ks * 4096 + kr * 256 + ((ch ^ wsw(kr)) * 16) + 8 * (p & 1); woff[1][ks] = (woff[0][ks] + 1024) ^ 16; }
    auto rd = [&](PFrag& f, int slot, int j) { lds_cptr sb = (lds_cptr)lds + slot * P_SLOT;
        f.x[0] = ldsfrag(sb + xoff[0][j]); f.x[1] = ldsfrag(sb + xoff[1][j]); f.w = cat(trread(sb + woff[0][j]), trread(sb + woff[1][j])); };
    auto mm = [&](const PFrag& f) {
#pragma unroll
        for (int rt = 0; rt < 2; ++rt) acc[rt] = SWAP ? MFMA(f.x[rt], f.w, acc[rt]) : MFMA(f.w, f.x[rt], acc[rt]); };
    acc[0] = f32x16{}; acc[1] = f32x16{};
    PFrag fa[2], fb[2];
    asm volatile("s_waitcnt vmcnt(6)\n\ts_barrier" : "+v"(ep) :: "memory");
    tag = EPOCH_TAG(ep);
    rd(fa[0], 0, 0); rd(fa[1], 0, 1);
    auto mm1 = [&](const PFrag& f, int rt) { acc[rt] = SWAP ? MFMA(f.x[rt], f.w, acc[rt]) : MFMA(f.w, f.x[rt], acc[rt]); SBAR(); };
    auto step = [&](int it, int sl, PFrag (&cur)[2], PFrag (&nxt)[2]) {
        __builtin_amdgcn_s_waitcnt(0xC07F);
        if (it < 13) WAITV(4); else if (it == 13) WAITV(2); else WAITV(0);
        __builtin_amdgcn_s_barrier(); asm volatile("" ::: "memory");
        lds_cptr sb = (lds_cptr)lds + ((sl + 1) & 3) * P_SLOT;
        mm1(cur[0], 0);
        nxt[0].x[0] = ldsfrag(sb + xoff[0][0]); nxt[0].x[1] = ldsfrag(sb + xoff[1][0]); SBAR();
        mm1(cur[0], 1);
        nxt[0].w = cat(trread(sb + woff[0][0]), trread(sb + woff[1][0])); SBAR();
        if (it + 4 < 16) issue1(it + 4, 0);
        mm1(cur[1], 0);
        nxt[1].x[0] = ldsfrag(sb + xoff[0][1]); nxt[1].x[1] = ldsfrag(sb + xoff[1][1]); SBAR();
        mm1(cur[1], 1);
        nxt[1].w = cat(trread(sb + woff[0][1]), trread(sb + woff[1][1])); SBAR();
        if (it + 4 < 16) issue1(it + 4, 1); };
#pragma unroll 1
    for (int io = 0; io < 2; ++io) { step(io * 4, 0, fa, fb); step(io * 4 + 1, 1, fb, fa); step(io * 4 + 2, 2, fa, fb); step(io * 4 + 3, 3, fb, fa); }
    const char* fa_ = (const char*)(lane < 4 ? flagp(ws, FL_RS + rb * 4 + lane) : flagp(ws, FL_CS + 2 * cblk + (lane & 1)));
    const char* sa_ = tid < 128 ? ws + WS_RS + (size_t)(rb * 128 + tid) * 8 : ws + WS_CS + (size_t)(cblk * 128 + 2 * (tid & 63)) * 4;
    const char* sb_ = ws + WS_CS + (size_t)(3328 + cblk * 128 + 2 * (tid & 63)) * 4;
    u64 fl, stv, stw;
    step(8, 0, fa, fb); step(9, 1, fb, fa);
    asm volatile("global_load_dwordx2 %0, %1, off sc1" : "=v"(fl) : "v"(fa_) : "memory");
    step(10, 2, fa, fb); step(11, 3, fb, fa); step(12, 0, fa, fb);
    asm volatile("" : "+v"(fl));
    const bool got = __all(lane >= 6 || fl == tag);
    asm volatile("global_load_dwordx2 %0, %2, off sc1\n\tglobal_load_dwordx2 %1, %3, off sc1" : "=&v"(stv), "=&v"(stw) : "v"(sa_), "v"(sb_) : "memory");
    step(13, 1, fb, fa); step(14, 2, fa, fb); step(15, 3, fb, fa);
    asm volatile("" : "+v"(stv), "+v"(stw));
    if (!got) { wait_flags((GAS u64*)fa_, lane < 6, tag); stv = ld8_wt(sa_); stw = ld8_wt(sb_); }
    float* st = (float*)(lds + P_ST);
    { const f32x2 v = __builtin_bit_cast(f32x2, stv);
      if (tid < 128) { st[tid] = v[0]; st[128 + tid] = v[1]; } else if (tid < 192) *(f32x2*)(st + 256 + 2 * (tid - 128)) = v + __builtin_bit_cast(f32x2, stw); }
    __syncthreads();
}

__device__ __forceinline__ void qkv_role(const uint16_t* __restrict__ x, const uint16_t* __restrict__ wqkv, char* __restrict__ ws, char* lds, int b, u64& tag) {
    const int xcd = b & 7, i = b >> 3;
    const int rb = 4 * (xcd >> 1) + (i & 3), cb = 6 * (xcd & 1) + (i >> 2);
    const int tid = threadIdx.x, lane = tid & 63, wid = __builtin_amdgcn_readfirstlane(tid >> 6), wr = wid >> 2, wc = wid & 3, l31 = lane & 31, hh = lane >> 5;
    f32x16 acc[2];
    const int rot = ((i & 3) * 4 + (i >> 2) * 3) & 15;
    if (cb >= 8) proj_main<true>(x, wqkv, NQKV, rb, cb * 128, cb, ws, lds, rot, tag, acc); else proj_main<false>(x, wqkv, NQKV, rb, cb * 128, cb, ws, lds, rot, tag, acc);
    const float* st = (const float*)(lds + P_ST);
    if (cb >= 8) {
        const float cs = st[256 + wc * 32 + l31];
        const int head = (cb - 8) * 8 + wc * 2 + (l31 >> 4);
#pragma unroll
        for (int rt = 0; rt < 2; ++rt) { const int tr = wr * 64 + rt * 32;
            unsigned pk[8];
#pragma unroll
            for (int q = 0; q < 4; ++q) { const f32x4 m = *(const f32x4*)(st + tr + 8 * q + 4 * hh), r = *(const f32x4*)(st + 128 + tr + 8 * q + 4 * hh);
                pk[2 * q] = cvtpk((acc[rt][4 * q] - m[0] * cs) * r[0], (acc[rt][4 * q + 1] - m[1] * cs) * r[1]);
                pk[2 * q + 1] = cvtpk((acc[rt][4 * q + 2] - m[2] * cs) * r[2], (acc[rt][4 * q + 3] - m[3] * cs) * r[3]); }
            const int tile = rb * 4 + (tr >> 5);
            char* dst = ws + WS_KV + (size_t)head * 131072 + (tile >> 3) * 16384 + 8192 + (tile & 7) * 1024 + hh * 256 + (l31 & 15) * 16;
            st16_wt(dst, (u32x4){pk[0], pk[1], pk[2], pk[3]}); st16_wt(dst + 512, (u32x4){pk[4], pk[5], pk[6], pk[7]}); }
    } else {
        const bool isK = cb >= 4; const float sc = isK ? 1.0f : QSCALE;
        float kmx[2] = {0.f, 0.f};
#pragma unroll
        for (int rt = 0; rt < 2; ++rt) { const int tr = wr * 64 + rt * 32, tile = rb * 4 + (tr >> 5);
            const float mu = st[tr + l31], rs = st[128 + tr + l31] * sc;
#pragma unroll
            for (int hb = 0; hb < 2; ++hb) {
                float v[8];
#pragma unroll
                for (int q = 0; q < 2; ++q) { const f32x4 c = *(const f32x4*)(st + 256 + wc * 32 + 16 * hb + 8 * q + 4 * hh);
#pragma unroll
                    for (int e = 0; e < 4; ++e) v[4 * q + e] = (acc[rt][8 * hb + 4 * q + e] - mu * c[e]) * rs; }
                u32x4 pk;
#pragma unroll
                for (int e = 0; e < 4; ++e) pk[e] = cvtpk(v[2 * e], v[2 * e + 1]);
                const int head = (cb & 3) * 8 + wc * 2 + hb;
                char* dst = isK ? ws + WS_KV + (size_t)head * 131072 + (tile >> 3) * 16384 + (tile & 7) * 1024 + lane * 16 : ws + WS_QF + (size_t)head * 65536 + tile * 1024 + lane * 16;
                st16_wt(dst, pk);
                float n2 = 0.f;
#pragma unroll
                for (int e = 0; e < 4; ++e) n2 = dot2(pk[e], pk[e], n2);
                kmx[hb] = fmaxf(kmx[hb], swap32_add(n2));
            } }
        if (isK) {
#pragma unroll
            for (int hb = 0; hb < 2; ++hb) { const float m = swap16_max(row16_max(kmx[hb]));
                if (lane == 0) __hip_atomic_store((GAS unsigned*)(ws + WS_KM) + ((cb & 3) * 8 + wc * 2 + hb) * 32 + rb * 2 + wr, __float_as_uint(m), RLX_AGENT); } }
    }
    PUBLISH(FL_QKV + cb * 16 + rb);
}

constexpr int A_ONES = 3 * 16384, A_LDS = A_ONES + 8192 + 512;
__device__ __forceinline__ void attn_role(char* __restrict__ ws, char* lds, int b, u64 tag) {
    const int tid = threadIdx.x, lane = tid & 63, wid = __builtin_amdgcn_readfirstlane(tid >> 6);
    const int head = (b & 7) * 4 + ((b >> 3) & 3), jb = b >> 5;
    const unsigned lds0 = (unsigned)(uintptr_t)lds;
    const char* kvb = ws + WS_KV + (size_t)head * 131072;
    if (wid == 0) {
        const int hb = head >> 3;
        const int idx = lane < 16 ? (4 + hb) * 16 + lane : lane < 32 ? (8 + hb) * 16 + (lane - 16) : hb * 16 + 2 * jb + (lane & 1);
        wait_flags(flagp(ws, FL_QKV + idx), lane < 34, tag);
        }
    __syncthreads();
    unsigned goff = wid * 2048 + lane * 16;
    const int tile0 = jb * 8 + wid;
    u32x4 qa; unsigned kmu;
    asm volatile("global_load_dwordx4 %0, %2, off sc1\n\tglobal_load_dword %1, %3, off sc1" : "=&v"(qa), "=&v"(kmu)
                 : "v"(ws + WS_QF + (size_t)head * 65536 + tile0 * 1024 + lane * 16), "v"(ws + WS_KM + (size_t)(head * 32 + (lane & 31)) * 4) : "memory");
#pragma unroll
    for (int c = 0; c < 2; ++c) { set_m0(lds0 + c * 16384 + wid * 2048); glds_s<0, true>(kvb + c * 16384, goff); glds_s<1024, true>(kvb + c * 16384, goff); }
    asm volatile("s_waitcnt vmcnt(4)" : "+v"(qa), "+v"(kmu) :: "memory");
    const float km = swap16_max(row16_max(__uint_as_float(kmu)));
    float na = 0.f;
#pragma unroll
    for (int e = 0; e < 4; ++e) na = dot2(qa[e], qa[e], na);
    na = swap32_add(na);
    float ma = __builtin_amdgcn_sqrtf(na * km);
    if (__any(!(ma <= 40.0f))) {
        float xa = -3.0e38f;
        for (int t = 0; t < 64; ++t) {
            const bf16x8 kf = __builtin_bit_cast(bf16x8, ld16_wt(kvb + (t >> 3) * 16384 + (t & 7) * 1024 + lane * 16));
            const f32x16 sa = MFMA(kf, __builtin_bit_cast(bf16x8, qa), f32x16{});
#pragma unroll
            for (int r = 0; r < 16; ++r) xa = fmaxf(xa, sa[r]); }
        ma = swap32_max(xa);
    }
    const float nma = -ma;
    const unsigned kaddr = lds0 + lane * 16;
    const bool ones = (lane & 16) != 0;
    const unsigned vaddr = ones ? lds0 + A_ONES + (lane & 15) * 16 : lds0 + 8192 + (lane >> 5) * 256 + (lane & 15) * 16;
    const unsigned vsel = ones ? 0u : 0xffffffffu;
    goff += 2 * 16384;
    unsigned dbeg = lds0 + wid * 2048, dend = dbeg + 3 * 16384, dslot = dbeg + 2 * 16384, nslot = 0x4000, cnt, m0save;
    unsigned oa0, oa1, oa2, oa3;
    asm volatile(
    "s_mov_b32 %[m0save], m0\n"
    "v_mov_b32 v96, %[qa0]\n"
    "v_mov_b32 v97, %[qa1]\n"
    "v_mov_b32 v98, %[qa2]\n"
    "v_mov_b32 v99, %[qa3]\n"
    "v_mov_b32 v72, %[nma]\n"
    "v_mov_b32 v56, 0\n"
    "v_mov_b32 v73, %[nma]\n"
    "v_mov_b32 v57, 0\n"
    "v_mov_b32 v74, %[nma]\n"
    "v_mov_b32 v58, 0\n"
    "v_mov_b32 v75, %[nma]\n"
    "v_mov_b32 v59, 0\n"
    "v_mov_b32 v76, %[nma]\n"
    "v_mov_b32 v60, 0\n"
    "v_mov_b32 v77, %[nma]\n"
    "v_mov_b32 v61, 0\n"
    "v_mov_b32 v78, %[nma]\n"
    "v_mov_b32 v62, 0\n"
    "v_mov_b32 v79, %[nma]\n"
    "v_mov_b32 v63, 0\n"
    "v_mov_b32 v80, %[nma]\n"
    "v_mov_b32 v64, 0\n"
    "v_mov_b32 v81, %[nma]\n"
    "v_mov_b32 v65, 0\n"
    "v_mov_b32 v82, %[nma]\n"
    "v_mov_b32 v66, 0\n"
    "v_mov_b32 v83, %[nma]\n"
    "v_mov_b32 v67, 0\n"
    "v_mov_b32 v84, %[nma]\n"
    "v_mov_b32 v68, 0\n"
    "v_mov_b32 v85, %[nma]\n"
    "v_mov_b32 v69, 0\n"
    "v_mov_b32 v86, %[nma]\n"
    "v_mov_b32 v70, 0\n"
    "v_mov_b32 v87, %[nma]\n"
    "v_mov_b32 v71, 0\n"
    "v_mov_b32 v108, %[vone]\n"
    "v_mov_b32 v109, %[vone]\n"
    "v_mov_b32 v110, %[vone]\n"
    "v_mov_b32 v111, %[vone]\n"
    "v_mov_b32 v112, %[vone]\n"
    "v_mov_b32 v113, %[vone]\n"
    "v_mov_b32 v114, %[vone]\n"
    "v_mov_b32 v115, %[vone]\n"
    "v_mov_b32 v116, %[vone]\n"
    "v_mov_b32 v117, %[vone]\n"
    "v_mov_b32 v118, %[vone]\n"
    "v_mov_b32 v119, %[vone]\n"
    "v_mov_b32 v120, %[vone]\n"
    "v_mov_b32 v121, %[vone]\n"
    "v_mov_b32 v122, %[vone]\n"
    "v_mov_b32 v123, %[vone]\n"
    "v_mov_b32 v88, 0\n"
    "v_mov_b32 v89, 0\n"
    "v_mov_b32 v90, 0\n"
    "v_mov_b32 v91, 0\n"
    "v_mov_b32 v92, 0\n"
    "v_mov_b32 v93, 0\n"
    "v_mov_b32 v94, 0\n"
    "v_mov_b32 v95, 0\n"
    "v_mov_b32 v124, %[kaddr]\n"
    "v_mov_b32 v125, %[vaddr]\n"
    "v_add_u32 v126, 0x4000, v124\n"
    "v_and_b32 v127, 0x4000, %[vsel]\n"
    "v_add_u32 v127, v127, v125\n"
    "s_waitcnt vmcnt(2) lgkmcnt(0)\n"
    "s_barrier\n"
    "ds_read_b128 v[100:103], v124\n"
    "ds_read_b128 v[104:107], v124 offset:1024\n"
    "s_mov_b64 exec, %[vmask]\n"
    "ds_read_b128 v[108:111], v125\n"
    "ds_read_b128 v[112:115], v125 offset:512\n"
    "s_mov_b64 exec, -1\n"
    "s_mov_b32 %[cnt], 0\n"
    "s_waitcnt lgkmcnt(0)\n"
    "v_mfma_f32_32x32x16_bf16 v[24:39], v[100:103], v[96:99], v[72:87]\n"
    "s_nop 15\n"
    ".Lloop%=:\n"
    "v_exp_f32 v24, v24\n"
    "v_exp_f32 v25, v25\n"
    "v_mfma_f32_32x32x16_bf16 v[56:71], v[120:123], v[92:95], v[56:71]\n"
    "v_exp_f32 v26, v26\n"
    "v_exp_f32 v27, v27\n"
    "v_cvt_pk_bf16_f32 v88, v24, v25\n"
    "v_exp_f32 v28, v28\n"
    "s_waitcnt lgkmcnt(0)\n"
    "v_mfma_f32_32x32x16_bf16 v[40:55], v[104:107], v[96:99], v[72:87]\n"
    "v_exp_f32 v29, v29\n"
    "v_cvt_pk_bf16_f32 v89, v26, v27\n"
    "ds_read_b128 v[100:103], v124 offset:2048\n"
    "v_exp_f32 v30, v30\n"
    "v_exp_f32 v31, v31\n"
    "v_cvt_pk_bf16_f32 v90, v28, v29\n"
    "s_mov_b64 exec, %[vmask]\n"
    "ds_read_b128 v[116:119], v125 offset:1024\n"
    "ds_read_b128 v[120:123], v125 offset:1536\n"
    "s_mov_b64 exec, -1\n"
    "v_exp_f32 v32, v32\n"
    "v_exp_f32 v33, v33\n"
    "v_cvt_pk_bf16_f32 v91, v30, v31\n"
    "v_exp_f32 v34, v34\n"
    "v_exp_f32 v35, v35\n"
    "v_mfma_f32_32x32x16_bf16 v[56:71], v[108:111], v[88:91], v[56:71]\n"
    "v_cvt_pk_bf16_f32 v92, v32, v33\n"
    "v_exp_f32 v36, v36\n"
    "v_exp_f32 v37, v37\n"
    "v_cvt_pk_bf16_f32 v93, v34, v35\n"
    "v_exp_f32 v38, v38\n"
    "v_exp_f32 v39, v39\n"
    "v_cvt_pk_bf16_f32 v94, v36, v37\n"
    "v_cvt_pk_bf16_f32 v95, v38, v39\n"
    "v_exp_f32 v40, v40\n"
    "v_exp_f32 v41, v41\n"
    "v_mfma_f32_32x32x16_bf16 v[56:71], v[112:115], v[92:95], v[56:71]\n"
    "v_exp_f32 v42, v42\n"
    "v_exp_f32 v43, v43\n"
    "v_cvt_pk_bf16_f32 v88, v40, v41\n"
    "v_exp_f32 v44, v44\n"
    "s_waitcnt lgkmcnt(0)\n"
    "v_mfma_f32_32x32x16_bf16 v[24:39], v[100:103], v[96:99], v[72:87]\n"
    "v_exp_f32 v45, v45\n"
    "v_cvt_pk_bf16_f32 v89, v42, v43\n"
    "ds_read_b128 v[104:107], v124 offset:3072\n"
    "v_exp_f32 v46, v46\n"
    "v_exp_f32 v47, v47\n"
    "v_cvt_pk_bf16_f32 v90, v44, v45\n"
    "s_mov_b64 exec, %[vmask]\n"
    "ds_read_b128 v[108:111], v125 offset:2048\n"
    "ds_read_b128 v[112:115], v125 offset:2560\n"
    "s_mov_b64 exec, -1\n"
    "v_exp_f32 v48, v48\n"
    "v_exp_f32 v49, v49\n"
    "v_cvt_pk_bf16_f32 v91, v46, v47\n"
    "v_exp_f32 v50, v50\n"
    "v_exp_f32 v51, v51\n"
    "v_mfma_f32_32x32x16_bf16 v[56:71], v[116:119], v[88:91], v[56:71]\n"
    "v_cvt_pk_bf16_f32 v92, v48, v49\n"
    "v_exp_f32 v52, v52\n"
    "v_exp_f32 v53, v53\n"
    "v_cvt_pk_bf16_f32 v93, v50, v51\n"
    "v_exp_f32 v54, v54\n"
    "v_exp_f32 v55, v55\n"
    "v_cvt_pk_bf16_f32 v94, v52, v53\n"
    "v_cvt_pk_bf16_f32 v95, v54, v55\n"
    "v_exp_f32 v24, v24\n"
    "v_exp_f32 v25, v25\n"
    "v_mfma_f32_32x32x16_bf16 v[56:71], v[120:123], v[92:95], v[56:71]\n"
    "v_exp_f32 v26, v26\n"
    "v_exp_f32 v27, v27\n"
    "v_cvt_pk_bf16_f32 v88, v24, v25\n"
    "v_exp_f32 v28, v28\n"
    "s_waitcnt lgkmcnt(0)\n"
    "v_mfma_f32_32x32x16_bf16 v[40:55], v[104:107], v[96:99], v[72:87]\n"
    "v_exp_f32 v29, v29\n"
    "v_cvt_pk_bf16_f32 v89, v26, v27\n"
    "ds_read_b128 v[100:103], v124 offset:4096\n"
    "v_exp_f32 v30, v30\n"
    "v_exp_f32 v31, v31\n"
    "v_cvt_pk_bf16_f32 v90, v28, v29\n"
    "s_mov_b64 exec, %[vmask]\n"
    "ds_read_b128 v[116:119], v125 offset:3072\n"
    "ds_read_b128 v[120:123], v125 offset:3584\n"
    "s_mov_b64 exec, -1\n"
    "v_exp_f32 v32, v32\n"
    "v_exp_f32 v33, v33\n"
    "v_cvt_pk_bf16_f32 v91, v30, v31\n"
    "v_exp_f32 v34, v34\n"
    "v_exp_f32 v35, v35\n"
    "v_mfma_f32_32x32x16_bf16 v[56:71], v[108:111], v[88:91], v[56:71]\n"
    "v_cvt_pk_bf16_f32 v92, v32, v33\n"
    "v_exp_f32 v36, v36\n"
    "v_exp_f32 v37, v37\n"
    "v_cvt_pk_bf16_f32 v93, v34, v35\n"
    "v_exp_f32 v38, v38\n"
    "v_exp_f32 v39, v39\n"
    "v_cvt_pk_bf16_f32 v94, v36, v37\n"
    "v_cvt_pk_bf16_f32 v95, v38, v39\n"
    "v_exp_f32 v40, v40\n"
    "v_exp_f32 v41, v41\n"
    "v_mfma_f32_32x32x16_bf16 v[56:71], v[112:115], v[92:95], v[56:71]\n"
    "v_exp_f32 v42, v42\n"
    "v_exp_f32 v43, v43\n"
    "v_cvt_pk_bf16_f32 v88, v40, v41\n"
    "v_exp_f32 v44, v44\n"
    "s_waitcnt lgkmcnt(0)\n"
    "v_mfma_f32_32x32x16_bf16 v[24:39], v[100:103], v[96:99], v[72:87]\n"
    "v_exp_f32 v45, v45\n"
    "v_cvt_pk_bf16_f32 v89, v42, v43\n"
    "ds_read_b128 v[104:107], v124 offset:5120\n"
    "v_exp_f32 v46, v46\n"
    "v_exp_f32 v47, v47\n"
    "v_cvt_pk_bf16_f32 v90, v44, v45\n"
    "s_mov_b64 exec, %[vmask]\n"
    "ds_read_b128 v[108:111], v125 offset:4096\n"
    "ds_read_b128 v[112:115], v125 offset:4608\n"
    "s_mov_b64 exec, -1\n"
    "v_exp_f32 v48, v48\n"
    "v_exp_f32 v49, v49\n"
    "v_cvt_pk_bf16_f32 v91, v46, v47\n"
    "v_exp_f32 v50, v50\n"
    "v_exp_f32 v51, v51\n"
    "v_mfma_f32_32x32x16_bf16 v[56:71], v[116:119], v[88:91], v[56:71]\n"
    "v_cvt_pk_bf16_f32 v92, v48, v49\n"
    "v_exp_f32 v52, v52\n"
    "v_exp_f32 v53, v53\n"
    "v_cvt_pk_bf16_f32 v93, v50, v51\n"
    "v_exp_f32 v54, v54\n"
    "v_exp_f32 v55, v55\n"
    "v_cvt_pk_bf16_f32 v94, v52, v53\n"
    "v_cvt_pk_bf16_f32 v95, v54, v55\n"
    "v_exp_f32 v24, v24\n"
    "v_exp_f32 v25, v25\n"
    "v_mfma_f32_32x32x16_bf16 v[56:71], v[120:123], v[92:95], v[56:71]\n"
    "v_exp_f32 v26, v26\n"
    "v_exp_f32 v27, v27\n"
    "v_cvt_pk_bf16_f32 v88, v24, v25\n"
    "v_exp_f32 v28, v28\n"
    "s_waitcnt lgkmcnt(0)\n"
    "v_mfma_f32_32x32x16_bf16 v[40:55], v[104:107], v[96:99], v[72:87]\n"
    "v_exp_f32 v29, v29\n"
    "v_cvt_pk_bf16_f32 v89, v26, v27\n"
    "ds_read_b128 v[100:103], v124 offset:6144\n"
    "v_exp_f32 v30, v30\n"
    "v_exp_f32 v31, v31\n"
    "v_cvt_pk_bf16_f32 v90, v28, v29\n"
    "s_mov_b64 exec, %[vmask]\n"
    "ds_read_b128 v[116:119], v125 offset:5120\n"
    "ds_read_b128 v[120:123], v125 offset:5632\n"
    "s_mov_b64 exec, -1\n"
    "v_exp_f32 v32, v32\n"
    "v_exp_f32 v33, v33\n"
    "v_cvt_pk_bf16_f32 v91, v30, v31\n"
    "v_exp_f32 v34, v34\n"
    "v_exp_f32 v35, v35\n"
    "v_mfma_f32_32x32x16_bf16 v[56:71], v[108:111], v[88:91], v[56:71]\n"
    "v_cvt_pk_bf16_f32 v92, v32, v33\n"
    "v_exp_f32 v36, v36\n"
    "v_exp_f32 v37, v37\n"
    "v_cvt_pk_bf16_f32 v93, v34, v35\n"
    "v_exp_f32 v38, v38\n"
    "v_exp_f32 v39, v39\n"
    "v_cvt_pk_bf16_f32 v94, v36, v37\n"
    "v_cvt_pk_bf16_f32 v95, v38, v39\n"
    "v_exp_f32 v40, v40\n"
    "v_exp_f32 v41, v41\n"
    "v_mfma_f32_32x32x16_bf16 v[56:71], v[112:115], v[92:95], v[56:71]\n"
    "v_exp_f32 v42, v42\n"
    "v_exp_f32 v43, v43\n"
    "v_cvt_pk_bf16_f32 v88, v40, v41\n"
    "v_exp_f32 v44, v44\n"
    "s_waitcnt lgkmcnt(0)\n"
    "v_mfma_f32_32x32x16_bf16 v[24:39], v[100:103], v[96:99], v[72:87]\n"
    "v_exp_f32 v45, v45\n"
    "v_cvt_pk_bf16_f32 v89, v42, v43\n"
    "ds_read_b128 v[104:107], v124 offset:7168\n"
    "v_exp_f32 v46, v46\n"
    "v_exp_f32 v47, v47\n"
    "v_cvt_pk_bf16_f32 v90, v44, v45\n"
    "s_mov_b64 exec, %[vmask]\n"
    "ds_read_b128 v[108:111], v125 offset:6144\n"
    "ds_read_b128 v[112:115], v125 offset:6656\n"
    "s_mov_b64 exec, -1\n"
    "v_exp_f32 v48, v48\n"
    "v_exp_f32 v49, v49\n"
    "v_cvt_pk_bf16_f32 v91, v46, v47\n"
    "v_exp_f32 v50, v50\n"
    "v_exp_f32 v51, v51\n"
    "v_mfma_f32_32x32x16_bf16 v[56:71], v[116:119], v[88:91], v[56:71]\n"
    "v_cvt_pk_bf16_f32 v92, v48, v49\n"
    "v_exp_f32 v52, v52\n"
    "v_exp_f32 v53, v53\n"
    "v_cvt_pk_bf16_f32 v93, v50, v51\n"
    "v_exp_f32 v54, v54\n"
    "v_exp_f32 v55, v55\n"
    "v_cvt_pk_bf16_f32 v94, v52, v53\n"
    "v_cvt_pk_bf16_f32 v95, v54, v55\n"
    "v_exp_f32 v24, v24\n"
    "v_exp_f32 v25, v25\n"
    "v_mfma_f32_32x32x16_bf16 v[56:71], v[120:123], v[92:95], v[56:71]\n"
    "v_exp_f32 v26, v26\n"
    "v_exp_f32 v27, v27\n"
    "v_cvt_pk_bf16_f32 v88, v24, v25\n"
    "v_exp_f32 v28, v28\n"
    "s_waitcnt lgkmcnt(0)\n"
    "v_mfma_f32_32x32x16_bf16 v[40:55], v[104:107], v[96:99], v[72:87]\n"
    "v_exp_f32 v29, v29\n"
    "v_cvt_pk_bf16_f32 v89, v26, v27\n"
    "s_waitcnt vmcnt(0)\n"
    "s_barrier\n"
    "ds_read_b128 v[100:103], v126 offset:0\n"
    "v_exp_f32 v30, v30\n"
    "v_exp_f32 v31, v31\n"
    "v_cvt_pk_bf16_f32 v90, v28, v29\n"
    "s_mov_b64 exec, %[vmask]\n"
    "ds_read_b128 v[116:119], v125 offset:7168\n"
    "ds_read_b128 v[120:123], v125 offset:7680\n"
    "s_mov_b64 exec, -1\n"
    "v_exp_f32 v32, v32\n"
    "v_exp_f32 v33, v33\n"
    "v_cvt_pk_bf16_f32 v91, v30, v31\n"
    "v_exp_f32 v34, v34\n"
    "v_exp_f32 v35, v35\n"
    "v_mfma_f32_32x32x16_bf16 v[56:71], v[108:111], v[88:91], v[56:71]\n"
    "s_cmp_ge_u32 %[cnt], 6\n"
    "s_cbranch_scc1 .Lnodma%=\n"
    "s_mov_b32 m0, %[dslot]\n"
    "s_nop 0\n"
    "global_load_lds_dwordx4 %[goff], %[gbase] offset:0 sc1\n"
    "global_load_lds_dwordx4 %[goff], %[gbase] offset:1024 sc1\n"
    ".Lnodma%=:\n"
    "v_cvt_pk_bf16_f32 v92, v32, v33\n"
    "v_exp_f32 v36, v36\n"
    "v_exp_f32 v37, v37\n"
    "v_cvt_pk_bf16_f32 v93, v34, v35\n"
    "v_exp_f32 v38, v38\n"
    "v_exp_f32 v39, v39\n"
    "v_cvt_pk_bf16_f32 v94, v36, v37\n"
    "v_cvt_pk_bf16_f32 v95, v38, v39\n"
    "v_exp_f32 v40, v40\n"
    "v_exp_f32 v41, v41\n"
    "v_mfma_f32_32x32x16_bf16 v[56:71], v[112:115], v[92:95], v[56:71]\n"
    "v_exp_f32 v42, v42\n"
    "v_exp_f32 v43, v43\n"
    "v_cvt_pk_bf16_f32 v88, v40, v41\n"
    "v_exp_f32 v44, v44\n"
    "s_waitcnt lgkmcnt(0)\n"
    "v_mfma_f32_32x32x16_bf16 v[24:39], v[100:103], v[96:99], v[72:87]\n"
    "v_exp_f32 v45, v45\n"
    "v_cvt_pk_bf16_f32 v89, v42, v43\n"
    "ds_read_b128 v[104:107], v126 offset:1024\n"
    "v_exp_f32 v46, v46\n"
    "v_exp_f32 v47, v47\n"
    "v_cvt_pk_bf16_f32 v90, v44, v45\n"
    "s_mov_b64 exec, %[vmask]\n"
    "ds_read_b128 v[108:111], v127 offset:0\n"
    "ds_read_b128 v[112:115], v127 offset:512\n"
    "s_mov_b64 exec, -1\n"
    "v_exp_f32 v48, v48\n"
    "v_exp_f32 v49, v49\n"
    "v_cvt_pk_bf16_f32 v91, v46, v47\n"
    "v_exp_f32 v50, v50\n"
    "v_exp_f32 v51, v51\n"
    "v_mfma_f32_32x32x16_bf16 v[56:71], v[116:119], v[88:91], v[56:71]\n"
    "v_cvt_pk_bf16_f32 v92, v48, v49\n"
    "v_exp_f32 v52, v52\n"
    "v_exp_f32 v53, v53\n"
    "v_cvt_pk_bf16_f32 v93, v50, v51\n"
    "v_exp_f32 v54, v54\n"
    "v_exp_f32 v55, v55\n"
    "v_cvt_pk_bf16_f32 v94, v52, v53\n"
    "v_cvt_pk_bf16_f32 v95, v54, v55\n"
    "v_mov_b32 v124, v126\n"
    "v_mov_b32 v125, v127\n"
    "s_add_u32 %[nslot], %[nslot], 0x4000\n"
    "s_cmp_eq_u32 %[nslot], 0xc000\n"
    "s_cselect_b32 %[nslot], 0, %[nslot]\n"
    "v_add_u32 v126, %[nslot], %[kaddr]\n"
    "v_and_b32 v127, %[nslot], %[vsel]\n"
    "v_add_u32 v127, v127, %[vaddr]\n"
    "s_add_u32 %[dslot], %[dslot], 0x4000\n"
    "s_cmp_eq_u32 %[dslot], %[dend]\n"
    "s_cselect_b32 %[dslot], %[dbeg], %[dslot]\n"
    "v_add_u32 %[goff], 0x4000, %[goff]\n"
    "s_add_u32 %[cnt], %[cnt], 1\n"
    "s_cmp_lt_u32 %[cnt], 8\n"
    "s_cbranch_scc1 .Lloop%=\n"
    "s_nop 1\n"
    "v_mfma_f32_32x32x16_bf16 v[56:71], v[120:123], v[92:95], v[56:71]\n"
    "s_nop 15\n"
    "s_nop 3\n"
    "v_rcp_f32 v24, v64\n"
    "s_nop 0\n"
    "v_mul_f32 v56, v56, v24\n"
    "v_mul_f32 v57, v57, v24\n"
    "v_mul_f32 v58, v58, v24\n"
    "v_mul_f32 v59, v59, v24\n"
    "v_mul_f32 v60, v60, v24\n"
    "v_mul_f32 v61, v61, v24\n"
    "v_mul_f32 v62, v62, v24\n"
    "v_mul_f32 v63, v63, v24\n"
    "v_cvt_pk_bf16_f32 %[oa0], v56, v57\n"
    "v_cvt_pk_bf16_f32 %[oa1], v58, v59\n"
    "v_cvt_pk_bf16_f32 %[oa2], v60, v61\n"
    "v_cvt_pk_bf16_f32 %[oa3], v62, v63\n"
    "s_waitcnt lgkmcnt(0)\n"
    "s_mov_b32 m0, %[m0save]\n"
        : [oa0] "=&v"(oa0), [oa1] "=&v"(oa1), [oa2] "=&v"(oa2), [oa3] "=&v"(oa3),
          [goff] "+v"(goff), [dslot] "+s"(dslot), [nslot] "+s"(nslot), [cnt] "=&s"(cnt), [m0save] "=&s"(m0save)
        : [qa0] "v"(qa[0]), [qa1] "v"(qa[1]), [qa2] "v"(qa[2]), [qa3] "v"(qa[3]),
          [nma] "v"(nma), [kaddr] "v"(kaddr), [vaddr] "v"(vaddr), [vsel] "v"(vsel), [gbase] "s"(kvb), [dbeg] "s"(dbeg), [dend] "s"(dend), [psel] "s"(0x07060302u), [vmask] "s"(0x0000ffff0000ffffull), [vone] "v"(((lane & 27) == 16) ? 0x3f803f80u : 0u)
        : "memory", "scc", "vcc",
          "v24", "v25", "v26", "v27", "v28", "v29", "v30", "v31", "v32", "v33", "v34", "v35", "v36", "v37", "v38", "v39", "v40", "v41", "v42", "v43", "v44", "v45", "v46", "v47", "v48", "v49", "v50", "v51", "v52", "v53", "v54", "v55", "v56", "v57", "v58", "v59", "v60", "v61", "v62", "v63", "v64", "v65", "v66", "v67", "v68", "v69", "v70", "v71", "v72", "v73", "v74", "v75", "v76", "v77", "v78", "v79", "v80", "v81", "v82", "v83", "v84", "v85", "v86", "v87", "v88", "v89", "v90", "v91", "v92", "v93", "v94", "v95", "v96", "v97", "v98", "v99", "v100", "v101", "v102", "v103", "v104", "v105", "v106", "v107", "v108", "v109", "v110", "v111", "v112", "v113", "v114", "v115", "v116", "v117", "v118", "v119", "v120", "v121", "v122", "v123", "v124", "v125", "v126", "v127"
    );
    st16_wt(ws + WS_OF + ((size_t)tile0 * 32 + head) * 1024 + lane * 16, (u32x4){oa0, oa1, oa2, oa3});
    WAITV(0);
    if (lane == 0) __hip_atomic_store(flagp(ws, FL_O + tile0 * 32 + head), tag, RLX_AGENT);
}

__device__ __forceinline__ void z_role(const uint16_t* __restrict__ x, const uint16_t* __restrict__ win, char* __restrict__ ws, char* lds, int g, u64& tag) {
    const int tid = threadIdx.x, lane = tid & 63, wid = __builtin_amdgcn_readfirstlane(tid >> 6), wr = wid >> 2, wc = wid & 3, l31 = lane & 31, hh = lane >> 5;
    const int xcd = g & 7, i = g >> 3;
    const int rb = 4 * (xcd >> 1) + (i & 3), cbz = 7 * ((xcd & 1) ^ 1) + (i >> 2);
    f32x16 acc[2];
    proj_main<false>(x, win, NIN, rb, cbz * 128, 12 + cbz, ws, lds, ((i & 3) * 4 + (i >> 2) * 3) & 15, tag, acc);
    const float* st = (const float*)(lds + P_ST);
    unsigned zp[2][8];
#pragma unroll
    for (int rt = 0; rt < 2; ++rt) { const int tr = wr * 64 + rt * 32; const float mu = st[tr + l31], rs = st[128 + tr + l31];
#pragma unroll
        for (int q = 0; q < 4; ++q) { const f32x4 c = *(const f32x4*)(st + 256 + wc * 32 + 8 * q + 4 * hh);
            zp[rt][2 * q] = cvtpk(gelu_t((acc[rt][4 * q] - mu * c[0]) * rs), gelu_t((acc[rt][4 * q + 1] - mu * c[1]) * rs));
            zp[rt][2 * q + 1] = cvtpk(gelu_t((acc[rt][4 * q + 2] - mu * c[2]) * rs), gelu_t((acc[rt][4 * q + 3] - mu * c[3]) * rs)); } }
    if (cbz < 7) {
#pragma unroll
        for (int rt = 0; rt < 2; ++rt) { const int tt = rb * 4 + wr * 2 + rt;
            char* dst = ws + WS_Z1 + ((size_t)tt * 28 + cbz * 4 + wc) * 2048 + lane * 32;
            st16_wt(dst, (u32x4){zp[rt][0], zp[rt][1], zp[rt][2], zp[rt][3]}); st16_wt(dst + 16, (u32x4){zp[rt][4], zp[rt][5], zp[rt][6], zp[rt][7]}); }
    } else {
        const int cj = cbz - 7;
        float* pst = (float*)lds;
        const unsigned ONES = 0x3f803f80u;
#pragma unroll
        for (int rt = 0; rt < 2; ++rt) { float s1 = 0.f, s2 = 0.f;
#pragma unroll
            for (int e = 0; e < 8; ++e) { s1 = dot2(zp[rt][e], ONES, s1); s2 = dot2(zp[rt][e], zp[rt][e], s2); }
            s1 = swap32_add(s1); s2 = swap32_add(s2);
            if (hh == 0) *(f32x2*)(pst + (wc * 128 + wr * 64 + rt * 32 + l31) * 2) = (f32x2){s1, s2}; }
        __syncthreads();
        char* gst = ws + WS_ST + (size_t)rb * 7 * 2048;
        float* fin = (float*)(lds + 4096);
        if (tid < 128) { f32x2 s = *(const f32x2*)(pst + tid * 2);
#pragma unroll
            for (int j = 1; j < 4; ++j) s += *(const f32x2*)(pst + (j * 128 + tid) * 2);
            st16_wt(gst + (cj * 128 + tid) * 16, (u32x4){__float_as_uint(s[0]), __float_as_uint(s[1]), (unsigned)tag, (unsigned)(tag >> 32)});
            u32x4 gr[7];
            for (unsigned spins = 0; spins < (1u << 18); ++spins) {
                asm volatile("global_load_dwordx4 %0, %7, off sc1\n\tglobal_load_dwordx4 %1, %7, off offset:2048 sc1\n\tglobal_load_dwordx4 %2, %8, off sc1\n\tglobal_load_dwordx4 %3, %8, off offset:2048 sc1\n\t"
                             "global_load_dwordx4 %4, %9, off sc1\n\tglobal_load_dwordx4 %5, %9, off offset:2048 sc1\n\tglobal_load_dwordx4 %6, %10, off sc1\n\ts_waitcnt vmcnt(0)"
                             : "=&v"(gr[0]), "=&v"(gr[1]), "=&v"(gr[2]), "=&v"(gr[3]), "=&v"(gr[4]), "=&v"(gr[5]), "=&v"(gr[6])
                             : "v"(gst + tid * 16), "v"(gst + 4096 + tid * 16), "v"(gst + 8192 + tid * 16), "v"(gst + 12288 + tid * 16) : "memory");
                bool ok = true;
#pragma unroll
                for (int j = 0; j < 7; ++j) ok &= gr[j][2] == (unsigned)tag && gr[j][3] == (unsigned)(tag >> 32);
                if (__all(ok)) break;
                __builtin_amdgcn_s_sleep(POLL_SLEEP); }
            float s1 = 0.f, s2 = 0.f;
#pragma unroll
            for (int j = 0; j < 7; ++j) { s1 += __uint_as_float(gr[j][0]); s2 += __uint_as_float(gr[j][1]); }
            const float mu = s1 * (1.0f / FH); const float var = fmaxf(s2 * (1.0f / FH) - mu * mu, 0.f);
            fin[tid] = mu; fin[128 + tid] = __builtin_amdgcn_rsqf(var + LN_EPS); }
        __syncthreads();
        char* img = lds + 8192;
#pragma unroll
        for (int rt = 0; rt < 2; ++rt) { const int tok = wr * 64 + rt * 32 + l31; const float mu = fin[tok], rs = fin[128 + tok];
#pragma unroll
            for (int q = 0; q < 4; ++q) { const unsigned a = zp[rt][2 * q], c = zp[rt][2 * q + 1];
                const u32x2 o = {cvtpk((bflo(a) - mu) * rs, (bfhi(a) - mu) * rs), cvtpk((bflo(c) - mu) * rs, (bfhi(c) - mu) * rs)};
                const int f = wc * 32 + 8 * q + 4 * hh;
                *(u32x2*)(img + tok * 256 + (((f >> 3) ^ wsw(tok)) * 16) + (f & 4) * 2) = o; } }
        __syncthreads();
        { const int blk = (lane >> 4) & 1, q = (lane & 15) >> 2, p = lane & 3;
#pragma unroll
          for (int kq = 0; kq < 4; ++kq) { const int ks = wr * 4 + kq; s16x4 t2[2];
#pragma unroll
              for (int t = 0; t < 2; ++t) { const int kr = 16 * ks + 8 * hh + 4 * t + q; const int ch = 4 * wc + 2 * blk + (p >> 1);
                  t2[t] = trread((lds_cptr)img + kr * 256 + ((ch ^ wsw(kr)) * 16) + 8 * (p & 1)); }
              st16_wt(ws + WS_Z2N + ((size_t)(cj * 4 + wc) * 128 + rb * 8 + ks) * 1024 + lane * 16, __builtin_bit_cast(u32x4, cat(t2[0], t2[1]))); } }
    }
    PUBLISH(FL_Z + rb * 14 + cbz);
}

constexpr int S_SLOT = 24576, S_LDS = 3 * S_SLOT;
__device__ __forceinline__ void spatial_role(const uint16_t* __restrict__ wsp, const uint16_t* __restrict__ bsp, char* __restrict__ ws, char* lds, int g, u64 tag) {
    const int tid = threadIdx.x, lane = tid & 63, wid8 = __builtin_amdgcn_readfirstlane(tid >> 6), wid = wid8 & 3, l31 = lane & 31, hh = lane >> 5;
    const int i = g >> 3, tb = 4 * (g & 7) + (i & 3), fb = i >> 2;
    const unsigned lds0 = (unsigned)(uintptr_t)lds;
    if (wid8 >= 4) {
        const int r0 = 16 * wid + (lane >> 3), r1 = r0 + 8;
        const char* sb_ = (const char*)(wsp + (size_t)tb * 64 * SEQ);
        const unsigned vo0 = (r0 * SEQ + ((lane & 7) ^ ((r0 >> 1) & 7)) * 8) * 2, vo1 = (r1 * SEQ + ((lane & 7) ^ ((r1 >> 1) & 7)) * 8) * 2;
        const char* zs = ws + WS_Z2N + (size_t)(fb * 4 + wid) * 131072; const unsigned zvo = lane * 16;
        unsigned ready = 0;
        GAS u64* zf = flagp(ws, FL_Z + (lane & 15) * 14 + 7 + fb);
        auto need = [&](int rbk) {
            for (unsigned spins = 0; !((ready >> rbk) & 1) && spins < (1u << 18); ++spins) {
                ready = (unsigned)__ballot(__hip_atomic_load(zf, RLX_AGENT) == tag) & 0xffffu;
                if (!((ready >> rbk) & 1)) __builtin_amdgcn_s_sleep(POLL_SLEEP); } };
        auto issue = [&](int it, int slot) { const unsigned so = lds0 + slot * S_SLOT;
            need(it >> 1);
            set_m0(so + wid * 2048); glds_s<0, false>(sb_ + it * 128, vo0);
            set_m0(so + wid * 2048 + 1024); glds_s<0, false>(sb_ + it * 128, vo1);
            set_m0(so + 8192 + wid * 4096);
            glds_s<0, true>(zs + it * 4096, zvo); glds_s<1024, true>(zs + it * 4096, zvo); glds_s<2048, true>(zs + it * 4096, zvo); glds_s<3072, true>(zs + it * 4096, zvo); };
        issue(0, 0); issue(1, 1); issue(2, 2);
        WAIT_BAR(12);
        int slot = 0;
#pragma unroll 1
        for (int it = 1; it < 31; ++it) { WAIT_BAR(6); if (it + 2 < 32) issue(it + 2, slot); slot = slot == 2 ? 0 : slot + 1; }
        WAIT_BAR(0);
        BAR();
    } else {
        unsigned boff[2][4];
#pragma unroll
        for (int rt = 0; rt < 2; ++rt)
#pragma unroll
            for (int ks = 0; ks < 4; ++ks) { const int r = rt * 32 + l31; boff[rt][ks] = (r * 128 + ((hh ^ ((r >> 1) & 7)) * 16)) ^ (ks * 32); }
        const unsigned aoff = 8192 + wid * 4096 + lane * 16;
        struct SF { bf16x8 a, b[2]; };
        auto rd = [&](SF& f, int slot, int ks) { lds_cptr sb = (lds_cptr)lds + slot * S_SLOT;
            f.a = ldsfrag(sb + aoff + ks * 1024); f.b[0] = ldsfrag(sb + boff[0][ks]); f.b[1] = ldsfrag(sb + boff[1][ks]); };
        f32x16 acc[2] = {f32x16{}, f32x16{}};
        auto mm = [&](const SF& f) { acc[0] = MFMA(f.a, f.b[0], acc[0]); acc[1] = MFMA(f.a, f.b[1], acc[1]); };
        SF f[4];
        BAR();
#pragma unroll
        for (int ks = 0; ks < 4; ++ks) rd(f[ks], 0, ks);
        auto step = [&](int nsl) {
            LGKM_BAR();
#pragma unroll
            for (int ks = 0; ks < 4; ++ks) { lds_cptr sb = (lds_cptr)lds + nsl * S_SLOT;
                acc[0] = MFMA(f[ks].a, f[ks].b[0], acc[0]); SBAR();
                f[ks].b[0] = ldsfrag(sb + boff[0][ks]); SBAR();
                PACE();
                acc[1] = MFMA(f[ks].a, f[ks].b[1], acc[1]); SBAR();
                f[ks].b[1] = ldsfrag(sb + boff[1][ks]); f[ks].a = ldsfrag(sb + aoff + ks * 1024); SBAR();
                PACE(); } };
#pragma unroll 1
        for (int io = 0; io < 10; ++io) { step(1); step(2); step(0); }
        step(1); step(2);
        wait_flags(flagp(ws, FL_Z + (tb >> 1) * 14 + fb), lane == 0, tag);
#pragma unroll
        for (int rt = 0; rt < 2; ++rt) { const int tt = tb * 2 + rt; const float bias = __uint_as_float((unsigned)bsp[tt * 32 + l31] << 16);
            const char* z1p = ws + WS_Z1 + ((size_t)tt * 28 + fb * 4 + wid) * 2048 + lane * 32;
            const u32x4 za = ld16_wt(z1p), zb = ld16_wt(z1p + 16);
            u32x4 ua, ub;
#pragma unroll
            for (int e = 0; e < 4; ++e) {
                ua[e] = cvtpk(bflo(za[e]) * (acc[rt][2 * e] + bias), bfhi(za[e]) * (acc[rt][2 * e + 1] + bias));
                ub[e] = cvtpk(bflo(zb[e]) * (acc[rt][8 + 2 * e] + bias), bfhi(zb[e]) * (acc[rt][8 + 2 * e + 1] + bias)); }
            char* ud = ws + WS_U + ((size_t)tt * 56 + (fb * 4 + wid) * 2) * 1024 + lane * 16;
            st16_wt(ud, ua); st16_wt(ud + 1024, ub); }
    }
    PUBLISH(FL_U + tb * 7 + fb);
}

constexpr int F_SLOT = 8192 + 8192, F_LDS = 4 * F_SLOT;
__device__ __forceinline__ void final_role(const uint16_t* __restrict__ x, const uint16_t* __restrict__ wo, const uint16_t* __restrict__ wout, char* __restrict__ ws, uint16_t* __restrict__ out, char* lds, int g, u64 tag) {
    const int tb = 4 * (g & 7) + ((g >> 3) & 3), db = g >> 5;
    const int tid = threadIdx.x, lane = tid & 63, wid8 = __builtin_amdgcn_readfirstlane(tid >> 6), wid = wid8 & 3, wr = wid >> 1, wc = wid & 1, l31 = lane & 31, hh = lane >> 5;
    const unsigned lds0 = (unsigned)(uintptr_t)lds;
    auto wswz = [](int r) { return ((r & 2) << 1) | ((r >> 2) & 3); };
    f32x16 as = f32x16{}, ag = f32x16{};
    const int k0 = 16 * wid + (lane >> 3), k1 = k0 + 8;
    const unsigned wo0 = (k0 * DM + db * 64 + ((lane & 7) ^ wswz(k0)) * 8) * 2, wo1 = (k1 * DM + db * 64 + ((lane & 7) ^ wswz(k1)) * 8) * 2;
    const int att = tb * 2 + (wid & 1), aks = 2 * (wid >> 1);
    unsigned woff[2];
    { const int blk = (lane >> 4) & 1, q = (lane & 15) >> 2, p = lane & 3; const int kr = 4 * hh + q; const int ch = 4 * wc + 2 * blk + (p >> 1);
      woff[0] = kr * 128 + ((ch ^ wswz(kr)) * 16) + 8 * (p & 1); woff[1] = (woff[0] + 1024) ^ 32; }
    const unsigned aoff = 8192 + wr * 4096 + lane * 16;
    struct FF { bf16x8 w, a; };
    auto rd = [&](FF& f, int slot, int ks) { lds_cptr sb = (lds_cptr)lds + slot * F_SLOT;
        f.w = cat(trread(sb + woff[0] + ks * 2048), trread(sb + woff[1] + ks * 2048)); f.a = ldsfrag(sb + aoff + ks * 1024); };
    auto pass = [&](const uint16_t* wb, const char* ab, int nfr, int n, f32x16& acc, bool pace) {
        if (wid8 >= 4) {
            auto issue = [&](int it) { const unsigned so = lds0 + (it & 3) * F_SLOT; const char* w = (const char*)(wb + (size_t)it * 64 * DM);
                set_m0(so + wid * 2048); glds_s<0, false>(w, wo0);
                set_m0(so + wid * 2048 + 1024); glds_s<0, false>(w, wo1);
                const char* a = ab + ((size_t)att * nfr + it * 4 + aks) * 1024;
                set_m0(so + 8192 + (wid & 1) * 4096 + aks * 1024); glds_s<0, true>(a, lane * 16); glds_s<1024, true>(a, lane * 16); };
            issue(0); issue(1); issue(2); issue(3);
            WAIT_BAR(12);
#pragma unroll 1
            for (int i = 1; i < n - 2; ++i) { WAIT_BAR(8); if (i + 3 < n) issue(i + 3); }
            WAIT_BAR(4);
            WAIT_BAR(0);
            BAR();
        } else {
            FF fa[4], fb[4];
            BAR();
#pragma unroll
            for (int ks = 0; ks < 4; ++ks) rd(fa[ks], 0, ks);
            auto step = [&](int nsl, FF (&cur)[4], FF (&nxt)[4]) {
                LGKM_BAR();
#pragma unroll
                for (int ks = 0; ks < 4; ++ks) { acc = MFMA(cur[ks].w, cur[ks].a, acc); SBAR(); rd(nxt[ks], nsl, ks); SBAR(); if (pace) PACE(); } };
#pragma unroll 1
            for (int it = 0; it < n; it += 2) { step((it + 1) & 3, fa, fb); step((it + 2) & 3, fb, fa); }
        } };
    if (wid8 == 0) wait_flags(flagp(ws, FL_U + tb * 7 + lane), lane < 7, tag);
    __syncthreads();
    pass(wout, ws + WS_U, 56, 14, ag, true);
    __syncthreads();
#pragma unroll
    for (int q = 0; q < 8; ++q) {
        const int kr = 8 * wid8 + (lane >> 3);
        set_m0(lds0 + q * 8192 + wid8 * 1024);
        glds_s<0, false>((const char*)(wo + (size_t)q * 64 * DM), (kr * DM + db * 64 + ((lane & 7) ^ wswz(kr)) * 8) * 2); }
    const int tok = tb * 64 + wr * 32 + l31;
    float gate[16]; u32x2 xv[4];
    if (wid8 < 4) {
#pragma unroll
        for (int q = 0; q < 4; ++q) xv[q] = *(const u32x2*)(x + (size_t)tok * DM + db * 64 + wc * 32 + 8 * q + 4 * hh);
#pragma unroll
        for (int r = 0; r < 16; ++r) gate[r] = silu_f(ag[r]); }
    if (wid8 == 0) wait_flags(flagp(ws, FL_O + tb * 64 + lane), true, tag);
    WAITV(0);
    __syncthreads();
    const int kh = wid8 >> 2;
    u32x4 of[16];
    { const char* ob = ws + WS_OF + ((size_t)(tb * 2 + wr) * 32 + 16 * kh) * 1024; const unsigned v0 = lane * 16, v1 = v0 + 4096, v2 = v0 + 8192, v3 = v0 + 12288;
      asm volatile(
        "global_load_dwordx4 %0, %16, %20 sc1\n\tglobal_load_dwordx4 %1, %16, %20 offset:1024 sc1\n\tglobal_load_dwordx4 %2, %16, %20 offset:2048 sc1\n\tglobal_load_dwordx4 %3, %16, %20 offset:3072 sc1\n\t"
        "global_load_dwordx4 %4, %17, %20 sc1\n\tglobal_load_dwordx4 %5, %17, %20 offset:1024 sc1\n\tglobal_load_dwordx4 %6, %17, %20 offset:2048 sc1\n\tglobal_load_dwordx4 %7, %17, %20 offset:3072 sc1\n\t"
        "global_load_dwordx4 %8, %18, %20 sc1\n\tglobal_load_dwordx4 %9, %18, %20 offset:1024 sc1\n\tglobal_load_dwordx4 %10, %18, %20 offset:2048 sc1\n\tglobal_load_dwordx4 %11, %18, %20 offset:3072 sc1\n\t"
        "global_load_dwordx4 %12, %19, %20 sc1\n\tglobal_load_dwordx4 %13, %19, %20 offset:1024 sc1\n\tglobal_load_dwordx4 %14, %19, %20 offset:2048 sc1\n\tglobal_load_dwordx4 %15, %19, %20 offset:3072 sc1\n\t"
        "s_waitcnt vmcnt(0)"
        : "=&v"(of[0]), "=&v"(of[1]), "=&v"(of[2]), "=&v"(of[3]), "=&v"(of[4]), "=&v"(of[5]), "=&v"(of[6]), "=&v"(of[7]),
          "=&v"(of[8]), "=&v"(of[9]), "=&v"(of[10]), "=&v"(of[11]), "=&v"(of[12]), "=&v"(of[13]), "=&v"(of[14]), "=&v"(of[15])
        : "v"(v0), "v"(v1), "v"(v2), "v"(v3), "s"(ob) : "memory"); }
    { lds_cptr wb_ = (lds_cptr)lds + kh * 32768;
#pragma unroll
      for (int ks = 0; ks < 16; ++ks)
          as = MFMA(cat(trread(wb_ + woff[0] + ks * 2048), trread(wb_ + woff[1] + ks * 2048)), __builtin_bit_cast(bf16x8, of[ks]), as); }
    __syncthreads();
    if (kh == 1) {
#pragma unroll
        for (int q = 0; q < 4; ++q) *(f32x4*)(lds + (wid * 4 + q) * 1024 + lane * 16) = (f32x4){as[4 * q], as[4 * q + 1], as[4 * q + 2], as[4 * q + 3]}; }
    __syncthreads();
    if (kh == 0) {
#pragma unroll
        for (int q = 0; q < 4; ++q) { const f32x4 p = *(const f32x4*)(lds + (wid * 4 + q) * 1024 + lane * 16);
            const float r0 = bflo(xv[q][0]) + (as[4 * q] + p[0]) * gate[4 * q], r1 = bfhi(xv[q][0]) + (as[4 * q + 1] + p[1]) * gate[4 * q + 1];
            const float r2 = bflo(xv[q][1]) + (as[4 * q + 2] + p[2]) * gate[4 * q + 2], r3 = bfhi(xv[q][1]) + (as[4 * q + 3] + p[3]) * gate[4 * q + 3];
            *(u32x2*)(out + (size_t)tok * DM + db * 64 + wc * 32 + 8 * q + 4 * hh) = (u32x2){cvtpk(r0, r1), cvtpk(r2, r3)}; }
    }
}

#define ADELAY 0
#define APRIO 3
#define GDELAY 0
#define GPRIO 2
constexpr int ALL_LDS = S_LDS;
__global__ __launch_bounds__(512, 4) void k_all(const uint16_t* __restrict__ x, const uint16_t* __restrict__ wqkv, const uint16_t* __restrict__ wo, const uint16_t* __restrict__ win,
                                                const uint16_t* __restrict__ wsp, const uint16_t* __restrict__ bsp, const uint16_t* __restrict__ wout, char* __restrict__ ws, uint16_t* __restrict__ out) {
    extern __shared__ __attribute__((aligned(16))) char lds[];
    const int b = blockIdx.x;
    u64 tag;
    if (b < 256) {
        __builtin_amdgcn_s_setprio(APRIO);
        if (ADELAY > 0 && b < 192) {
            const u64 t0 = __builtin_amdgcn_s_memrealtime();
            const int i_ = b >> 3, cb_ = 6 * (b & 1) + (i_ >> 2), line = (i_ & 3) * 256 + (threadIdx.x & 255);
            unsigned junk = 0;
            while (__builtin_amdgcn_s_memrealtime() - t0 < ADELAY) __builtin_amdgcn_s_sleep(8);
            asm volatile("s_waitcnt vmcnt(0)" : "+v"(junk) :: "memory"); }
        if (b < 192) qkv_role(x, wqkv, ws, lds, b, tag); else stats_role(x, wqkv, win, ws, lds, b - 192, tag);
        __builtin_amdgcn_s_setprio(0);
        __syncthreads();
        attn_role(ws, lds, b, tag);
        if (b == 0 && threadIdx.x < 64) {
            const int lane = threadIdx.x;
            for (unsigned spins = 0; spins < (1u << 18); ++spins) {
                u64 v[8];
#pragma unroll
                for (int c = 0; c < 4; ++c) { v[c] = __hip_atomic_load(flagp(ws, FL_O + (c * 2 + (lane >> 5)) * 256 + (lane & 31)), RLX_AGENT); v[4 + c] = __hip_atomic_load(flagp(ws, FL_G + c * 64 + lane), RLX_AGENT); }
                bool ok = true;
#pragma unroll
                for (int c = 0; c < 8; ++c) ok &= v[c] == tag;
                if (__all(ok)) break;
                __builtin_amdgcn_s_sleep(8); }
            if (lane == 0) __hip_atomic_store(flagp(ws, FL_EPOCH), tag, RLX_AGENT);
        }
    } else {
        const int g = b - 256;
        __builtin_amdgcn_s_setprio(GPRIO);
            if (GDELAY > 0) { const u64 t0 = __builtin_amdgcn_s_memrealtime(); while (__builtin_amdgcn_s_memrealtime() - t0 < GDELAY) __builtin_amdgcn_s_sleep(8); }
        if (g < 224) { z_role(x, win, ws, lds, g, tag); __syncthreads(); spatial_role(wsp, bsp, ws, lds, g, tag); __syncthreads(); }
        else { u64 ep; EPOCH_REQ(ep);
            { const u64 t0 = __builtin_amdgcn_s_memrealtime(); while (__builtin_amdgcn_s_memrealtime() - t0 < WDELAY) __builtin_amdgcn_s_sleep(8); }
            { const int wv = (g - 224) * 8 + (threadIdx.x >> 6), lane = threadIdx.x & 63; unsigned junk = 0;
#pragma unroll
              for (int q = 0; q < 4; ++q) asm volatile("global_load_dword %0, %1, off" : "+v"(junk) : "v"((const char*)wsp + ((size_t)(q * 256 + wv) * 64 + lane) * 128) : "memory");
              if (wv < 112) asm volatile("global_load_dword %0, %1, off" : "+v"(junk) : "v"((const char*)wout + ((size_t)wv * 64 + lane) * 128) : "memory");
              else if (wv < 176) asm volatile("global_load_dword %0, %1, off" : "+v"(junk) : "v"((const char*)wo + ((size_t)(wv - 112) * 64 + lane) * 128) : "memory");
              asm volatile("s_waitcnt vmcnt(0)" : "+v"(junk) :: "memory"); }
            asm volatile("s_waitcnt vmcnt(0)" : "+v"(ep) :: "memory"); tag = EPOCH_TAG(ep); }
        if (threadIdx.x == 0) __hip_atomic_store(flagp(ws, FL_G + g), tag, RLX_AGENT);
        final_role(x, wo, wout, ws, out, lds, g, tag);
    }
}

extern "C" void kernel_launch(void* const* d_in, const int* in_sizes, int n_in, void* d_out, int out_size, void* d_ws, size_t ws_size, hipStream_t stream) {
    const uint16_t* x = (const uint16_t*)d_in[0]; const uint16_t* wqkv = (const uint16_t*)d_in[1]; const uint16_t* wo = (const uint16_t*)d_in[2];
    const uint16_t* win = (const uint16_t*)d_in[4]; const uint16_t* wsp = (const uint16_t*)d_in[5]; const uint16_t* bsp = (const uint16_t*)d_in[6]; const uint16_t* wout = (const uint16_t*)d_in[7];
    (void)hipFuncSetAttribute((const void*)k_all, hipFuncAttributeMaxDynamicSharedMemorySize, ALL_LDS);
    char* wsb = (char*)d_ws;
    k_all<<<512, 512, ALL_LDS, stream>>>(x, wqkv, wo, win, wsp, bsp, wout, wsb, (uint16_t*)d_out);
}
```
